```python
import torch

def _softmax(x, axis=-1):
    return torch.softmax(x, dim=axis)
def _gelu(x):
    return torch.nn.functional.gelu(x, approximate="tanh")
def _silu(x):
    return torch.nn.functional.silu(x)
def _sigmoid(x):
    return torch.sigmoid(x)
def _layernorm(x, axis=-1, eps=1e-5):
    m = x.mean(dim=axis, keepdim=True)
    v = x.var(dim=axis, keepdim=True, unbiased=False)
    return (x - m) / torch.sqrt(v + eps)
def _scatter_add(tgt, idx, src, axis=0):
    return tgt.clone().index_add_(axis, idx, src)
def _segment_sum(src, seg, n):
    out = torch.zeros((n,) + src.shape[1:], dtype=src.dtype)
    return out.index_add_(0, seg, src)
def _pad(x, pw):
    flat = tuple(v for pair in reversed(pw) for v in pair)
    return torch.nn.functional.pad(x, flat)

kernel_name = 'recurrent_scan__compressed_kv_cla__glu_gated_glu__prompt'

def setup_inputs():

    _w = (lambda a: (lambda t: t.to(torch.bfloat16) if t.is_floating_point() else t)(torch.as_tensor(a)))
    def _A_setup():
        B, H, Sq, Sk, D, M = 1, 32, 2048, 2048, 128, 512
        Dc = 4 * D
        return {
            "x": _w((lambda *s: torch.randn(*s, dtype=torch.bfloat16) * 0.05)(B, Sq, M)),
            "c_kv": _w((lambda *s: torch.randn(*s, dtype=torch.bfloat16) * 0.2)(B, Sk, Dc)),
            "w_q": _w((lambda *s: torch.randn(*s, dtype=torch.bfloat16) * 3.2)(M, H, D) * (M ** -0.5)),
            "w_uk": _w((lambda *s: torch.randn(*s, dtype=torch.bfloat16) * 3.2)(Dc, H, D) * (Dc ** -0.5)),
            "w_uv": _w((lambda *s: torch.randn(*s, dtype=torch.bfloat16) * 0.2)(Dc, H, D) * (Dc ** -0.5)),
            "w_o": _w((lambda *s: torch.randn(*s, dtype=torch.bfloat16) * 0.2)(H, D, M) * ((H * D) ** -0.5)),
        }
    _A_in = _A_setup()

    def _B_setup():
        B, S, D, F = 1, 2048, 512, 1792
        return {
            "x": _w((lambda *s: torch.randn(*s, dtype=torch.bfloat16) * 0.05)(B, S, D)),
            "w_gate": _w((lambda *s: torch.randn(*s, dtype=torch.bfloat16) * 0.2)(D, F)),
            "w_up": _w((lambda *s: torch.randn(*s, dtype=torch.bfloat16) * 0.1)(D, F)),
            "w_down": _w((lambda *s: torch.randn(*s, dtype=torch.bfloat16) * 0.1)(F, D)),
        }
    _B_in = _B_setup()

    return {
        "A_x": _A_in["x"],
        "A_c_kv": _A_in["c_kv"],
        "A_w_q": _A_in["w_q"],
        "A_w_uk": _A_in["w_uk"],
        "A_w_uv": _A_in["w_uv"],
        "A_w_o": _A_in["w_o"],
        "B_x": _B_in["x"],
        "B_w_gate": _B_in["w_gate"],
        "B_w_up": _B_in["w_up"],
        "B_w_down": _B_in["w_down"],
    }

def reference(A_x, A_c_kv, A_w_q, A_w_uk, A_w_uv, A_w_o, B_x, B_w_gate, B_w_up, B_w_down):
    def _A_ref(x, c_kv, w_q, w_uk, w_uv, w_o):
        B, Sq, M = x.shape[0], x.shape[1], x.shape[2]
        Sk = c_kv.shape[1]
        H, D = w_q.shape[1], w_q.shape[2]
        q = torch.einsum("bqm,mhd->bhqd", x, w_q)
        k = torch.einsum("bkc,chd->bhkd", c_kv, w_uk)
        v = torch.einsum("bkc,chd->bhkd", c_kv, w_uv)
        scores = torch.einsum("bhqd,bhkd->bhqk", q, k) * (D ** -0.5)
        off = Sk - Sq
        qi = torch.arange(Sq)[:, None]
        kj = torch.arange(Sk)[None, :]
        cmask = kj <= (qi + off)
        scores = torch.where(cmask[None, None], scores, -1e9)
        p = _softmax(scores, axis=-1)
        ctx = torch.einsum("bhqk,bhkd->bhqd", p, v)
        return torch.einsum("bhqd,hdm->bqm", ctx, w_o)

    def _B_ref(x, w_gate, w_up, w_down):
        g = torch.einsum("bsd,df->bsf", x, w_gate)
        u = torch.einsum("bsd,df->bsf", x, w_up)
        h = _sigmoid(g) * u
        return torch.einsum("bsf,fd->bsd", h, w_down)

    x = A_x
    xn = _layernorm(x, axis=-1)
    s = _A_ref(xn, A_c_kv, A_w_q, A_w_uk, A_w_uv, A_w_o)
    g = _silu(_B_ref(xn, B_w_gate, B_w_up, B_w_down))
    return x + s * g
```

```cpp
#include <hip/hip_runtime.h>
#include <hip/hip_bf16.h>
#include <stdint.h>

typedef unsigned short u16;
typedef short bf16x8 __attribute__((ext_vector_type(8)));
typedef short s16x4 __attribute__((ext_vector_type(4)));
typedef float f32x16 __attribute__((ext_vector_type(16)));
typedef float f32x4 __attribute__((ext_vector_type(4)));
typedef float f32x2 __attribute__((ext_vector_type(2)));
typedef unsigned u32x4 __attribute__((ext_vector_type(4)));
typedef unsigned u32x2 __attribute__((ext_vector_type(2)));
typedef __bf16 bf16x2_t __attribute__((ext_vector_type(2)));
#define DI __device__ __forceinline__
#define LAS __attribute__((address_space(3)))
#define MFMA32(a, b, c) __builtin_amdgcn_mfma_f32_32x32x16_bf16((a), (b), (c), 0, 0, 0)

constexpr int SEQ = 2048, DM = 512, NH = 32, HD = 128, FF = 1792;
constexpr size_t WS_Q = 0, WS_K = 16u << 20, WS_V = 32u << 20, WS_C = 48u << 20, WS_H = 64u << 20;
constexpr float QSCALE = 0.08838834764831845f * 1.4426950408889634f;

DI unsigned cvtpk(float lo, float hi) {
  f32x2 v = {lo, hi};
  bf16x2_t b = __builtin_convertvector(v, bf16x2_t);
  return __builtin_bit_cast(unsigned, b);
}
DI float bf2f(u16 v) { return __uint_as_float(((unsigned)v) << 16); }
DI float bfround(float x) { return __uint_as_float(cvtpk(x, 0.f) << 16); }
DI void glds16(const void* g, unsigned lds_base) {
  unsigned sv;
  asm volatile("s_mov_b32 %0, m0\n\ts_mov_b32 m0, %2\n\ts_nop 0\n\tglobal_load_lds_dwordx4 %1, off\n\ts_mov_b32 m0, %0"
               : "=&s"(sv) : "v"(g), "s"(lds_base) : "memory");
}
typedef short v4i16_t __attribute__((ext_vector_type(4)));
DI s16x4 trread(const LAS char* p) {
  return __builtin_bit_cast(s16x4, __builtin_amdgcn_ds_read_tr16_b64_v4i16((LAS v4i16_t*)p));
}
DI bf16x8 cat8(s16x4 lo, s16x4 hi) { return __builtin_shufflevector(lo, hi, 0, 1, 2, 3, 4, 5, 6, 7); }
DI u32x4 pack8(const f32x16& x, int s) {
  u32x4 w;
  w[0] = cvtpk(x[8 * s + 0], x[8 * s + 1]); w[1] = cvtpk(x[8 * s + 2], x[8 * s + 3]);
  w[2] = cvtpk(x[8 * s + 4], x[8 * s + 5]); w[3] = cvtpk(x[8 * s + 6], x[8 * s + 7]);
  return w;
}
DI int crow(int r, int h) { return (r & 3) + 8 * (r >> 2) + 4 * h; }
DI float dot2(unsigned a, unsigned b, float c) {
  return __builtin_amdgcn_fdot2_f32_bf16(__builtin_bit_cast(bf16x2_t, a), __builtin_bit_cast(bf16x2_t, b), c, false);
}

template <int TYPE>
DI void k1_tile(const u16* act, const u16* W0, const u16* W1, int ldw, int rb, int cbl, char* ws, char* lds) {
  const int tid = threadIdx.x, lane = tid & 63, wid = __builtin_amdgcn_readfirstlane(tid >> 6), wr = wid >> 1, wc = wid & 1;
  const int r32 = lane & 31, h = lane >> 5;
  const unsigned lds0 = (unsigned)(uintptr_t)lds;
  const u16* asrc[2];
#pragma unroll
  for (int par = 0; par < 2; ++par) {
    const int row = lane >> 3, c = (lane & 7) ^ ((4 * par + (lane >> 4)) & 7);
    asrc[par] = act + (size_t)(rb * 256 + wid * 64 + par * 8 + row) * DM + c * 8;
  }
  const u16* wsrc;
  {
    const int row = (lane >> 2) & 7, nbl = lane >> 5, c = lane & 3;
    if (TYPE == 3) wsrc = ((wid & 1) ? W1 : W0) + (size_t)row * ldw + cbl * 128 + (wid >> 1) * 64 + nbl * 32 + c * 8;
    else wsrc = W0 + (size_t)row * ldw + cbl * 256 + wid * 64 + nbl * 32 + c * 8;
  }
  auto stage = [&](int kt, int buf) {
    const unsigned ab = lds0 + buf * 65536, wb = ab + 32768;
#pragma unroll
    for (int i = 0; i < 8; ++i)
      glds16(asrc[i & 1] + (size_t)(i >> 1) * 16 * DM + kt * 64, __builtin_amdgcn_readfirstlane(ab + (wid * 8 + i) * 1024));
#pragma unroll
    for (int kg = 0; kg < 8; ++kg)
      glds16(wsrc + (size_t)(kt * 64 + kg * 8) * ldw, __builtin_amdgcn_readfirstlane(wb + (kg * 4 + wid) * 1024));
  };
  f32x16 acc[4][4];
#pragma unroll
  for (int a = 0; a < 4; ++a)
#pragma unroll
    for (int b = 0; b < 4; ++b) acc[a][b] = f32x16{};
  float sx[2] = {0.f, 0.f}, sq[2] = {0.f, 0.f}, cs[2] = {0.f, 0.f};
  int aoff[4];
#pragma unroll
  for (int s = 0; s < 4; ++s) aoff[s] = (wr * 128 + r32) * 128 + (((2 * s + h) ^ ((r32 >> 1) & 7)) << 4);
  const int woff = 32768 + h * 4096 + ((lane & 15) >> 2) * 64 + ((lane >> 4) & 1) * 32 + (lane & 3) * 8 + wc * 2048;
  stage(0, 0);
  for (int kt = 0; kt < 8; ++kt) {
    asm volatile("s_waitcnt vmcnt(0)\n\ts_barrier" ::: "memory");
    if (kt + 1 < 8) stage(kt + 1, (kt + 1) & 1);
    const LAS char* base = (const LAS char*)lds + (kt & 1) * 65536;
#pragma unroll
    for (int s = 0; s < 4; ++s) {
      bf16x8 xf[4], wf[4];
#pragma unroll
      for (int t = 0; t < 4; ++t) xf[t] = *(const LAS bf16x8*)(base + aoff[s] + (t ^ (2 * wc)) * 4096);
#pragma unroll
      for (int n = 0; n < 4; ++n) {
        const LAS char* p = base + woff + s * 8192 + (n >> 1) * 1024 + ((n & 1) ^ wr) * 512;
        wf[n] = cat8(trread(p), trread(p + 256));
      }
      if (TYPE >= 2) {
#pragma unroll
        for (int i = 0; i < 2; ++i) {
          u32x4 xv = __builtin_bit_cast(u32x4, xf[i]);
          u32x4 wv = __builtin_bit_cast(u32x4, wf[2 * i]);
#pragma unroll
          for (int e = 0; e < 4; ++e) {
            sx[i] = dot2(xv[e], 0x3f803f80u, sx[i]);
            sq[i] = dot2(xv[e], xv[e], sq[i]);
            cs[i] = dot2(wv[e], 0x3f803f80u, cs[i]);
          }
        }
      }
#pragma unroll
      for (int n = 0; n < 4; ++n)
#pragma unroll
        for (int t = 0; t < 4; ++t)
          acc[n][t] = (TYPE == 1) ? MFMA32(xf[t], wf[n], acc[n][t]) : MFMA32(wf[n], xf[t], acc[n][t]);
    }
    asm volatile("s_waitcnt lgkmcnt(0)" ::: "memory");
  }
  __syncthreads();
  const int TB0 = rb * 8 + wr * 4;
  if (TYPE == 0 || TYPE == 1) {
    char* dst = ws + (TYPE == 0 ? WS_K : WS_V) + (size_t)((cbl * 2 + wc) * 64 + TB0) * 8192 + lane * 16;
#pragma unroll
    for (int t = 0; t < 4; ++t)
#pragma unroll
      for (int n = 0; n < 4; ++n)
#pragma unroll
        for (int s = 0; s < 2; ++s)
          *(u32x4*)(dst + (t ^ (2 * wc)) * 8192 + (TYPE == 0 ? (2 * (n ^ wr) + s) : (4 * s + (n ^ wr))) * 1024) = pack8(acc[n][t], s);
  } else {
    float* st = (float*)lds;
    float* csl = (float*)lds + 512;
#pragma unroll
    for (int i = 0; i < 2; ++i) {
      float s1 = sx[i] + __shfl_xor(sx[i], 32), s2 = sq[i] + __shfl_xor(sq[i], 32), c1 = cs[i] + __shfl_xor(cs[i], 32);
      const float mean = s1 * (1.f / DM);
      float var = s2 * (1.f / DM) - mean * mean;
      var = fmaxf(var, 0.f);
      const float sd = bfround(sqrtf(bfround(bfround(var) + 1e-5f)));
      const float rstd = 1.f / sd;
      if (h == 0) {
        const int tok = wr * 128 + (wc * 2 + i) * 32 + r32;
        st[tok * 2] = rstd; st[tok * 2 + 1] = -bfround(mean) * rstd;
        csl[wc * 128 + ((2 * i) ^ wr) * 32 + r32] = c1;
      }
    }
    __syncthreads();
    float ra[4], rbv[4];
#pragma unroll
    for (int t = 0; t < 4; ++t) { f32x2 v = *(f32x2*)(st + (wr * 128 + (t ^ (2 * wc)) * 32 + r32) * 2); ra[t] = v[0]; rbv[t] = v[1]; }
    if (TYPE == 2) {
      char* dst = ws + WS_Q + (size_t)((cbl * 2 + wc) * 64 + TB0) * 8192 + lane * 16;
#pragma unroll
      for (int n = 0; n < 4; ++n) {
        f32x16 cv;
#pragma unroll
        for (int g = 0; g < 4; ++g) { f32x4 c4 = *(f32x4*)(csl + wc * 128 + (n ^ wr) * 32 + 8 * g + 4 * h); cv[4 * g] = c4[0]; cv[4 * g + 1] = c4[1]; cv[4 * g + 2] = c4[2]; cv[4 * g + 3] = c4[3]; }
#pragma unroll
        for (int t = 0; t < 4; ++t) {
          f32x16 v;
          const float a = ra[t] * QSCALE, b = rbv[t] * QSCALE;
#pragma unroll
          for (int r = 0; r < 16; ++r) v[r] = fmaf(acc[n][t][r], a, b * cv[r]);
#pragma unroll
          for (int s = 0; s < 2; ++s) *(u32x4*)(dst + (t ^ (2 * wc)) * 8192 + (2 * (n ^ wr) + s) * 1024) = pack8(v, s);
          __builtin_amdgcn_sched_barrier(0);
        }
      }
    } else {
      char* dst = ws + WS_H + (size_t)(TB0 * 112 + cbl * 8 + wc * 4) * 1024 + lane * 16;
#pragma unroll
      for (int n = 0; n < 2; ++n) {
        f32x16 cg, cu;
#pragma unroll
        for (int g = 0; g < 4; ++g) {
          f32x4 c4 = *(f32x4*)(csl + wc * 128 + (n ^ wr) * 32 + 8 * g + 4 * h); cg[4 * g] = c4[0]; cg[4 * g + 1] = c4[1]; cg[4 * g + 2] = c4[2]; cg[4 * g + 3] = c4[3];
          f32x4 d4 = *(f32x4*)(csl + wc * 128 + ((n ^ wr) + 2) * 32 + 8 * g + 4 * h); cu[4 * g] = d4[0]; cu[4 * g + 1] = d4[1]; cu[4 * g + 2] = d4[2]; cu[4 * g + 3] = d4[3];
        }
#pragma unroll
        for (int t = 0; t < 4; ++t) {
          f32x16 v;
#pragma unroll
          for (int r = 0; r < 16; ++r) {
            const float g = fmaf(acc[n][t][r], ra[t], rbv[t] * cg[r]);
            const float u = fmaf(acc[n + 2][t][r], ra[t], rbv[t] * cu[r]);
            v[r] = u * __builtin_amdgcn_rcpf(1.f + __builtin_amdgcn_exp2f(-1.4426950408889634f * g));
          }
#pragma unroll
          for (int s = 0; s < 2; ++s) *(u32x4*)(dst + (size_t)(t ^ (2 * wc)) * 112 * 1024 + (2 * (n ^ wr) + s) * 1024) = pack8(v, s);
          __builtin_amdgcn_sched_barrier(0);
        }
      }
    }
    __syncthreads();
  }
}

__global__ __launch_bounds__(256, 1) void k1_proj(const u16* x, const u16* c, const u16* wq, const u16* wuk, const u16* wuv,
                                                  const u16* wg, const u16* wu, char* ws) {
  extern __shared__ __attribute__((aligned(16))) char lds[];
  const int b = blockIdx.x & 255, xcd = b & 7, i = b >> 3;
  const int cb = xcd * 4 + (i >> 3), rb = i & 7;
  if (blockIdx.x < 256) {
    if (cb < 16) k1_tile<0>(c, wuk, wuk, NH * HD, rb, cb, ws, lds);
    else k1_tile<1>(c, wuv, wuv, NH * HD, rb, cb - 16, ws, lds);
  } else {
    if (cb < 16) k1_tile<2>(x, wq, wq, NH * HD, rb, cb, ws, lds);
    else if (cb < 30) k1_tile<3>(x, wg, wu, FF, rb, cb - 16, ws, lds);
  }
}

DI void attn_block_safe(int head, int xq, char* ws, char* lds) {
  const int tid = threadIdx.x, lane = tid & 63, wid = __builtin_amdgcn_readfirstlane(tid >> 6), rh = wid & 1, kh = wid >> 1;
  const int r32 = lane & 31, h = lane >> 5;
  const char* Qb = ws + WS_Q + (size_t)(head * 64 + xq * 4 + rh * 2) * 8192 + lane * 16;
  const char* Kb = ws + WS_K + (size_t)(head * 64) * 8192 + lane * 16;
  const char* Vb = ws + WS_V + (size_t)(head * 64) * 8192 + lane * 16;
  float* xo = (float*)lds;
#pragma unroll 1
  for (int qb = 0; qb < 2; ++qb) {
    bf16x8 qf[8];
#pragma unroll
    for (int ks = 0; ks < 8; ++ks) qf[ks] = *(const bf16x8*)(Qb + qb * 8192 + ks * 1024);
    f32x16 o[4];
#pragma unroll
    for (int d = 0; d < 4; ++d) o[d] = f32x16{};
    float m = -1e30f, l = 0.f;
    const int qrow = xq * 128 + rh * 64 + qb * 32;
    const int nkb = qrow / 32 + 1;
#pragma unroll 1
    for (int kb = 0; kb < nkb; ++kb) {
      if (((kb >> 1) & 1) != kh) continue;
      f32x16 s = f32x16{};
#pragma unroll
      for (int ks = 0; ks < 8; ++ks) s = MFMA32(*(const bf16x8*)(Kb + (size_t)kb * 8192 + ks * 1024), qf[ks], s);
      if (kb == nkb - 1) {
#pragma unroll
        for (int r = 0; r < 16; ++r) if (crow(r, h) > r32) s[r] = -__builtin_inff();
      }
      float mx = s[0];
#pragma unroll
      for (int r = 1; r < 16; ++r) mx = fmaxf(mx, s[r]);
      mx = fmaxf(mx, __shfl_xor(mx, 32));
      const float mn = fmaxf(m, mx), al = __builtin_amdgcn_exp2f(m - mn);
      m = mn;
      float ps = 0.f;
#pragma unroll
      for (int r = 0; r < 16; ++r) { s[r] = __builtin_amdgcn_exp2f(s[r] - mn); ps += s[r]; }
      l = l * al + ps;
#pragma unroll
      for (int d = 0; d < 4; ++d)
#pragma unroll
        for (int r = 0; r < 16; ++r) o[d][r] *= al;
#pragma unroll
      for (int st = 0; st < 2; ++st) {
        const bf16x8 p = __builtin_bit_cast(bf16x8, pack8(s, st));
#pragma unroll
        for (int d = 0; d < 4; ++d) o[d] = MFMA32(*(const bf16x8*)(Vb + (size_t)kb * 8192 + (st * 4 + d) * 1024), p, o[d]);
      }
    }
    l += __shfl_xor(l, 32);
    float* xw = xo + rh * (64 * 66);
    if (kh == 1) {
#pragma unroll
      for (int d = 0; d < 4; ++d)
#pragma unroll
        for (int r = 0; r < 16; ++r) xw[(d * 16 + r) * 64 + lane] = o[d][r];
      xw[64 * 64 + lane] = m; xw[65 * 64 + lane] = l;
    }
    __syncthreads();
    if (kh == 0) {
      const float m2 = xw[64 * 64 + lane], l2 = xw[65 * 64 + lane];
      const float mm = fmaxf(m, m2), a1 = __builtin_amdgcn_exp2f(m - mm), a2 = __builtin_amdgcn_exp2f(m2 - mm);
      const float inv = 1.f / (l * a1 + l2 * a2), f1 = a1 * inv, f2 = a2 * inv;
      char* dst = ws + WS_C + (size_t)((qrow / 32) * 256 + head * 8) * 1024 + lane * 16;
#pragma unroll
      for (int d = 0; d < 4; ++d) {
        f32x16 v;
#pragma unroll
        for (int r = 0; r < 16; ++r) v[r] = o[d][r] * f1 + xw[(d * 16 + r) * 64 + lane] * f2;
#pragma unroll
        for (int st = 0; st < 2; ++st) *(u32x4*)(dst + (2 * d + st) * 1024) = pack8(v, st);
      }
    }
    __syncthreads();
  }
}

__global__ __launch_bounds__(256, 1) void k2_attn(char* ws) {
  extern __shared__ __attribute__((aligned(16))) char lds[];
  const int b = blockIdx.x, xcd = b & 7, i = b >> 3;
  const int head = xcd * 4 + (i >> 3), x = i & 7;
  attn_block_safe(head, 15 - x, ws, lds);
  attn_block_safe(head, x, ws, lds);
}

__global__ __launch_bounds__(256, 1) void k3_out(const u16* x, const u16* wo, const u16* wd, const char* ws, u16* out) {
  extern __shared__ __attribute__((aligned(16))) char lds[];
  const int tid = threadIdx.x, lane = tid & 63, wid = __builtin_amdgcn_readfirstlane(tid >> 6);
  const int h = lane >> 5;
  const int b = blockIdx.x, xcd = b & 7, i = b >> 3;
  const int rb = xcd * 4 + (i >> 3), cb = i & 7;
  const unsigned lds0 = (unsigned)(uintptr_t)lds + wid * 8192;
  const LAS char* lw = (const LAS char*)lds + wid * 8192 + (4 * h + ((lane & 15) >> 2)) * 64 + ((lane >> 4) & 1) * 32 + (lane & 3) * 8;
  f32x16 acc[2][2][2];
#pragma unroll
  for (int p = 0; p < 2; ++p)
#pragma unroll
    for (int n = 0; n < 2; ++n)
#pragma unroll
      for (int t = 0; t < 2; ++t) acc[p][n][t] = f32x16{};
  const int wrow = (lane >> 2) & 7, wcol = cb * 64 + (lane >> 5) * 32 + (lane & 3) * 8;
#pragma unroll
  for (int p = 0; p < 2; ++p) {
    const int nks = p ? 112 : 256;
    const char* A = ws + (p ? WS_H : WS_C) + (size_t)(rb * 2) * nks * 1024 + lane * 16;
    const u16* W = (p ? wd : wo) + (size_t)wrow * DM + wcol;
#pragma unroll 1
    for (int g = wid; g < nks / 4; g += 4) {
#pragma unroll
      for (int kg = 0; kg < 8; ++kg) glds16(W + (size_t)(g * 64 + kg * 8) * DM, __builtin_amdgcn_readfirstlane(lds0 + kg * 1024));
      bf16x8 af[4][2];
#pragma unroll
      for (int s = 0; s < 4; ++s)
#pragma unroll
        for (int t = 0; t < 2; ++t) af[s][t] = *(const bf16x8*)(A + (size_t)t * nks * 1024 + (g * 4 + s) * 1024);
      asm volatile("s_waitcnt vmcnt(0)" ::: "memory");
#pragma unroll
      for (int s = 0; s < 4; ++s)
#pragma unroll
        for (int n = 0; n < 2; ++n) {
          const bf16x8 wf = cat8(trread(lw + s * 2048 + n * 512), trread(lw + s * 2048 + 1024 + n * 512));
#pragma unroll
          for (int t = 0; t < 2; ++t) acc[p][n][t] = MFMA32(wf, af[s][t], acc[p][n][t]);
        }
      asm volatile("s_waitcnt lgkmcnt(0)" ::: "memory");
    }
  }
  __syncthreads();
  float* red = (float*)lds;
#pragma unroll
  for (int p = 0; p < 2; ++p)
#pragma unroll
    for (int n = 0; n < 2; ++n)
#pragma unroll
      for (int t = 0; t < 2; ++t)
#pragma unroll
        for (int g = 0; g < 4; ++g) {
          f32x4 v = {acc[p][n][t][4 * g], acc[p][n][t][4 * g + 1], acc[p][n][t][4 * g + 2], acc[p][n][t][4 * g + 3]};
          *(f32x4*)(red + ((wid * 2 + p) * 64 + t * 32 + (lane & 31)) * 68 + n * 32 + 8 * g + 4 * h) = v;
        }
  __syncthreads();
#pragma unroll
  for (int it = 0; it < 2; ++it) {
    const int row = it * 32 + (tid >> 3), c8 = (tid & 7) * 8;
    float s[8], d[8];
#pragma unroll
    for (int e = 0; e < 8; ++e) { s[e] = 0.f; d[e] = 0.f; }
#pragma unroll
    for (int w = 0; w < 4; ++w)
#pragma unroll
      for (int e = 0; e < 8; ++e) { s[e] += red[((w * 2) * 64 + row) * 68 + c8 + e]; d[e] += red[((w * 2 + 1) * 64 + row) * 68 + c8 + e]; }
    const size_t off = (size_t)(rb * 64 + row) * DM + cb * 64 + c8;
    const u32x4 xv = *(const u32x4*)(x + off);
    float r[8];
#pragma unroll
    for (int e = 0; e < 8; ++e) {
      const float xe = __uint_as_float((e & 1) ? (xv[e >> 1] & 0xffff0000u) : (xv[e >> 1] << 16));
      const float g = d[e] * __builtin_amdgcn_rcpf(1.f + __builtin_amdgcn_exp2f(-1.4426950408889634f * d[e]));
      r[e] = xe + s[e] * g;
    }
    u32x4 w = {cvtpk(r[0], r[1]), cvtpk(r[2], r[3]), cvtpk(r[4], r[5]), cvtpk(r[6], r[7])};
    *(u32x4*)(out + off) = w;
  }
}

extern "C" void kernel_launch(void* const* d_in, const int* in_sizes, int n_in, void* d_out, int out_size, void* d_ws,
                              size_t ws_size, hipStream_t stream) {
  const u16* x = (const u16*)d_in[0];
  const u16* c = (const u16*)d_in[1];
  const u16* wq = (const u16*)d_in[2];
  const u16* wuk = (const u16*)d_in[3];
  const u16* wuv = (const u16*)d_in[4];
  const u16* wo = (const u16*)d_in[5];
  const u16* wg = (const u16*)d_in[7];
  const u16* wu = (const u16*)d_in[8];
  const u16* wd = (const u16*)d_in[9];
  char* ws = (char*)d_ws;
  (void)hipFuncSetAttribute((const void*)k1_proj, hipFuncAttributeMaxDynamicSharedMemorySize, 131072);
  (void)hipFuncSetAttribute((const void*)k2_attn, hipFuncAttributeMaxDynamicSharedMemorySize, 65536);
  (void)hipFuncSetAttribute((const void*)k3_out, hipFuncAttributeMaxDynamicSharedMemorySize, 139264);
  k1_proj<<<512, 256, 131072, stream>>>(x, c, wq, wuk, wuv, wg, wu, ws);
  k2_attn<<<256, 256, 65536, stream>>>(ws);
  k3_out<<<256, 256, 139264, stream>>>(x, wo, wd, ws, (u16*)d_out);
}
```

```cpp
#include <hip/hip_runtime.h>
#include <hip/hip_bf16.h>
#include <stdint.h>

typedef unsigned short u16;
constexpr size_t WS_K = 16u << 20, WS_V = 32u << 20, WS_C = 48u << 20, WS_H = 64u << 20, WS_XN = 80u << 20, WS_END = 97u << 20;

__global__ __launch_bounds__(256, 1) void k_mega(const u16* x, const u16* c, const u16* wq, const u16* wuk, const u16* wuv, const u16* wo,
                                                 const u16* wg, const u16* wu, const u16* wd, char* ws, u16* out) {
  extern __shared__ __attribute__((aligned(16))) char lds[];
  const int b = blockIdx.x, xcd = b & 7, i = b >> 3;
  const int head = xcd * 4 + (i >> 3), xq = i & 7;
  const int wid = __builtin_amdgcn_readfirstlane(threadIdx.x >> 6);
  const unsigned lds0 = (unsigned)(uintptr_t)lds;
  asm volatile("s_getpc_b64 s[96:97]\n v_mbcnt_lo_u32_b32 v251, -1, 0\n v_mbcnt_hi_u32_b32 v251, -1, v251\n v_lshlrev_b32 v251, 7, v251\n global_load_dword v251, v251, s[96:97]\n" ::: "s96", "s97", "v251");
  asm volatile(
    ".Lcode0_%=:\n"
    "s_mov_b32 s70, m0\n"
    "s_mov_b32 s66, %11\n"
    "s_mov_b32 s67, %12\n"
    "s_mov_b32 s68, %13\n"
    "s_mov_b32 s69, %14\n"
    "s_lshr_b32 s71, s68, 1\n"
    "s_and_b32 s72, s68, 1\n"
    "v_mbcnt_lo_u32_b32 v242, -1, 0\n"
    "v_mbcnt_hi_u32_b32 v242, -1, v242\n"
    "v_lshlrev_b32 v8, 4, v242\n"
    "s_mov_b64 s[48:49], %1\n"
    "s_mov_b32 s50, 0x200000\n"
    "s_mov_b32 s51, 0x20000\n"
    "s_cmp_eq_u32 s71, 0\n"
    "s_cselect_b64 s[52:53], %3, %4\n"
    "s_mov_b32 s54, 0x400000\n"
    "s_mov_b32 s55, 0x20000\n"
    "s_mov_b32 s73, 0xe00\n"
    "s_cselect_b32 s83, 0x2000, s73\n"
    "s_lshl_b32 s77, s66, 8\n"
    "s_lshl_b32 s73, s72, 7\n"
    "s_add_u32 s77, s77, s73\n"
    "s_mov_b32 s79, 0x80000\n"
    "s_add_u32 s78, s77, 0x40000\n"
    "s_lshl_b32 s75, s67, 18\n"
    "s_lshl_b32 s73, s68, 16\n"
    "s_add_u32 s75, s75, s73\n"
    "s_add_u32 s76, s75, 0x8000\n"
    "v_lshrrev_b32 v245, 3, v242\n"
    "v_lshlrev_b32 v245, 10, v245\n"
    "v_lshrrev_b32 v246, 4, v242\n"
    "v_and_b32 v247, 7, v242\n"
    "v_xor_b32 v246, v246, v247\n"
    "v_lshlrev_b32 v246, 4, v246\n"
    "v_add_u32 v230, v245, v246\n"
    "v_xor_b32 v231, 64, v230\n"
    "v_add_u32 v231, 0x1c00, v231\n"
    "v_add_u32 v232, 0x3800, v230\n"
    "v_add_u32 v233, 0x3800, v231\n"
    "s_lshl_b32 s73, s68, 13\n"
    "s_add_u32 s80, s73, s69\n"
    "s_mov_b32 s74, 0x2000\n"
    "v_lshrrev_b32 v245, 2, v242\n"
    "v_and_b32 v245, 7, v245\n"
    "v_mul_lo_u32 v245, v245, s74\n"
    "v_lshrrev_b32 v246, 5, v242\n"
    "v_lshlrev_b32 v246, 6, v246\n"
    "v_add_u32 v245, v245, v246\n"
    "v_and_b32 v246, 3, v242\n"
    "v_lshlrev_b32 v246, 4, v246\n"
    "v_xor_b32 v247, 32, v246\n"
    "s_lshl_b32 s73, s74, 3\n"
    "s_sub_u32 s73, s73, 0x400\n"
    "v_add_u32 v234, v245, v246\n"
    "v_add_u32 v245, s73, v245\n"
    "v_add_u32 v235, v245, v247\n"
    "v_add_u32 v245, s73, v245\n"
    "v_add_u32 v236, v245, v246\n"
    "v_add_u32 v245, s73, v245\n"
    "v_add_u32 v237, v245, v247\n"
    "s_mov_b64 s[56:57], %9\n"
    "s_add_u32 s56, s56, 0x6000000\n"
    "s_addc_u32 s57, s57, 0\n"
    "s_mov_b32 s58, 0x10000\n"
    "s_mov_b32 s59, 0x20000\n"
    "s_mov_b64 s[60:61], %0\n"
    "s_mov_b32 s62, 0x200000\n"
    "s_mov_b32 s63, 0x20000\n"
    "s_lshl_b32 s84, s66, 3\n"
    "s_add_u32 s84, s84, s67\n"
    "s_lshl_b32 s84, s84, 2\n"
    "s_add_u32 s84, s84, s68\n"
    "s_lshl_b32 s84, s84, 11\n"
    "v_mov_b32 v5, 0\n"
    "buffer_load_dwordx2 v[6:7], v5, s[56:59], 0 offen sc1\n"
    "buffer_load_dwordx4 v[160:163], v8, s[60:63], s84 offen\n"
    "buffer_load_dwordx4 v[164:167], v8, s[60:63], s84 offen offset:1024\n"
    "s_add_u32 m0, s80, 0x0\n"
    "s_nop 0\n"
    "buffer_load_dwordx4 v230, s[48:51], s75 offen offset:0 lds\n"
    "buffer_load_dwordx4 v231, s[48:51], s75 offen offset:1024 lds\n"
    "buffer_load_dwordx4 v232, s[48:51], s75 offen offset:2048 lds\n"
    "buffer_load_dwordx4 v233, s[48:51], s75 offen offset:3072 lds\n"
    "s_add_u32 m0, s80, 0x1000\n"
    "s_nop 0\n"
    "buffer_load_dwordx4 v230, s[48:51], s76 offen offset:0 lds\n"
    "buffer_load_dwordx4 v231, s[48:51], s76 offen offset:1024 lds\n"
    "buffer_load_dwordx4 v232, s[48:51], s76 offen offset:2048 lds\n"
    "buffer_load_dwordx4 v233, s[48:51], s76 offen offset:3072 lds\n"
    "s_add_u32 s75, s75, 0x80\n"
    "s_add_u32 s76, s76, 0x80\n"
    "s_add_u32 m0, s80, 0x10000\n"
    "s_nop 0\n"
    "buffer_load_dwordx4 v234, s[52:55], s77 offen offset:0 nt lds\n"
    "buffer_load_dwordx4 v235, s[52:55], s77 offen offset:1024 nt lds\n"
    "buffer_load_dwordx4 v236, s[52:55], s77 offen offset:2048 nt lds\n"
    "buffer_load_dwordx4 v237, s[52:55], s77 offen offset:3072 nt lds\n"
    "s_add_u32 m0, s80, 0x11000\n"
    "s_nop 0\n"
    "buffer_load_dwordx4 v234, s[52:55], s78 offen offset:0 nt lds\n"
    "buffer_load_dwordx4 v235, s[52:55], s78 offen offset:1024 nt lds\n"
    "buffer_load_dwordx4 v236, s[52:55], s78 offen offset:2048 nt lds\n"
    "buffer_load_dwordx4 v237, s[52:55], s78 offen offset:3072 nt lds\n"
    "s_add_u32 s77, s77, s79\n"
    "s_add_u32 s78, s78, s79\n"
    "s_getpc_b64 s[96:97]\n"
    ".Lpc4_%=:\n"
    "s_add_u32 s96, s96, .Lcode0_%=-.Lpc4_%=\n"
    "s_addc_u32 s97, s97, -1\n"
    "s_lshl_b32 s73, s67, 2\n"
    "s_add_u32 s73, s73, s68\n"
    "s_and_b32 s73, s73, 15\n"
    "s_lshl_b32 s73, s73, 13\n"
    "v_lshlrev_b32 v250, 3, v8\n"
    "v_add_u32 v250, s73, v250\n"
    "v_min_u32 v250, .Lcode1_%=-.Lcode0_%=-4, v250\n"
    "global_load_dword v250, v250, s[96:97]\n"
    "v_lshrrev_b32 v245, 2, v242\n"
    "v_and_b32 v245, 7, v245\n"
    "v_mul_lo_u32 v245, v245, s83\n"
    "v_lshrrev_b32 v246, 5, v242\n"
    "v_lshlrev_b32 v246, 6, v246\n"
    "v_add_u32 v245, v245, v246\n"
    "v_and_b32 v246, 3, v242\n"
    "v_lshlrev_b32 v246, 4, v246\n"
    "v_xor_b32 v247, 32, v246\n"
    "s_lshl_b32 s73, s83, 3\n"
    "s_sub_u32 s73, s73, 0x400\n"
    "v_add_u32 v238, v245, v246\n"
    "v_add_u32 v245, s73, v245\n"
    "v_add_u32 v239, v245, v247\n"
    "v_add_u32 v245, s73, v245\n"
    "v_add_u32 v240, v245, v246\n"
    "v_add_u32 v245, s73, v245\n"
    "v_add_u32 v241, v245, v247\n"
    "v_and_b32 v245, 15, v242\n"
    "v_lshrrev_b32 v246, 4, v242\n"
    "v_lshrrev_b32 v247, 1, v245\n"
    "v_xor_b32 v247, v247, v246\n"
    "v_lshlrev_b32 v247, 4, v247\n"
    "v_lshlrev_b32 v224, 7, v245\n"
    "v_add_u32 v224, v224, v247\n"
    "s_lshl_b32 s73, s71, 14\n"
    "s_add_u32 s73, s73, s69\n"
    "v_add_u32 v224, s73, v224\n"
    "v_xor_b32 v225, 64, v224\n"
    "s_lshl_b32 s73, s72, 10\n"
    "v_add_u32 v243, s73, v8\n"
    "v_add_u32 v244, s73, v243\n"
    "v_and_b32 v248, 1, v246\n"
    "v_lshlrev_b32 v226, 10, v246\n"
    "v_lshrrev_b32 v247, 2, v245\n"
    "v_lshlrev_b32 v247, 6, v247\n"
    "v_add_u32 v226, v226, v247\n"
    "v_lshlrev_b32 v248, 5, v248\n"
    "v_add_u32 v226, v226, v248\n"
    "v_and_b32 v247, 3, v245\n"
    "v_lshlrev_b32 v247, 3, v247\n"
    "v_add_u32 v226, v226, v247\n"
    "s_lshl_b32 s73, s72, 9\n"
    "s_add_u32 s73, s73, s69\n"
    "s_add_u32 s73, s73, 0x10000\n"
    "v_add_u32 v226, s73, v226\n"
    "v_xor_b32 v227, 32, v226\n"
    "v_add_u32 v228, 0x0, v226\n"
    "v_add_u32 v229, 0x0, v227\n"
    "s_lshl_b32 s73, s66, 19\n"
    "s_mov_b64 s[40:41], %9\n"
    "s_add_u32 s74, s73, 0x1000000\n"
    "s_add_u32 s40, s40, s74\n"
    "s_addc_u32 s41, s41, 0\n"
    "s_mov_b32 s42, 0x80000\n"
    "s_mov_b32 s43, 0x20000\n"
    "s_mov_b64 s[44:45], %9\n"
    "s_add_u32 s74, s73, 0x2000000\n"
    "s_add_u32 s44, s44, s74\n"
    "s_addc_u32 s45, s45, 0\n"
    "s_mov_b32 s46, 0x80000\n"
    "s_mov_b32 s47, 0x20000\n"
    "s_lshl_b32 s81, s67, 3\n"
    "s_lshl_b32 s73, s71, 2\n"
    "s_add_u32 s81, s81, s73\n"
    "s_lshl_b32 s81, s81, 13\n"
    "s_sub_u32 s73, 15, s67\n"
    "s_and_b32 s74, s66, 1\n"
    "s_cmp_eq_u32 s74, 0\n"
    "s_cselect_b32 s38, s73, s67\n"
    "s_cselect_b32 s39, s67, s73\n"
    "s_waitcnt vmcnt(17)\n"
    "v_readfirstlane_b32 s64, v6\n"
    "v_readfirstlane_b32 s65, v7\n"
    "s_mov_b64 s[60:61], %9\n"
    "s_add_u32 s60, s60, 0x5000000\n"
    "s_addc_u32 s61, s61, 0\n"
    "s_mov_b32 s62, 0x200000\n"
    "s_mov_b32 s63, 0x20000\n"
    "s_add_u32 s64, s64, 0x7f4a7c15\n"
    "s_addc_u32 s65, s65, 0x9e3779b9\n"
    "v_lshlrev_b32 v170, 16, v160\n"
    "v_and_b32 v171, 0xffff0000, v160\n"
    "v_lshlrev_b32 v172, 16, v161\n"
    "v_and_b32 v173, 0xffff0000, v161\n"
    "v_lshlrev_b32 v174, 16, v162\n"
    "v_and_b32 v175, 0xffff0000, v162\n"
    "v_lshlrev_b32 v176, 16, v163\n"
    "v_and_b32 v177, 0xffff0000, v163\n"
    "v_add_f32 v190, v170, v171\n"
    "v_add_f32 v190, v190, v172\n"
    "v_add_f32 v190, v190, v173\n"
    "v_add_f32 v190, v190, v174\n"
    "v_add_f32 v190, v190, v175\n"
    "v_add_f32 v190, v190, v176\n"
    "v_add_f32 v190, v190, v177\n"
    "v_lshlrev_b32 v180, 16, v164\n"
    "v_and_b32 v181, 0xffff0000, v164\n"
    "v_lshlrev_b32 v182, 16, v165\n"
    "v_and_b32 v183, 0xffff0000, v165\n"
    "v_lshlrev_b32 v184, 16, v166\n"
    "v_and_b32 v185, 0xffff0000, v166\n"
    "v_lshlrev_b32 v186, 16, v167\n"
    "v_and_b32 v187, 0xffff0000, v167\n"
    "v_add_f32 v191, v180, v181\n"
    "v_add_f32 v191, v191, v182\n"
    "v_add_f32 v191, v191, v183\n"
    "v_add_f32 v191, v191, v184\n"
    "v_add_f32 v191, v191, v185\n"
    "v_add_f32 v191, v191, v186\n"
    "v_add_f32 v191, v191, v187\n"
    "s_nop 1\n"
    "v_add_f32_dpp v190, v190, v190 quad_perm:[1,0,3,2] row_mask:0xf bank_mask:0xf\n"
    "v_add_f32_dpp v191, v191, v191 quad_perm:[1,0,3,2] row_mask:0xf bank_mask:0xf\n"
    "s_nop 1\n"
    "v_add_f32_dpp v190, v190, v190 quad_perm:[2,3,0,1] row_mask:0xf bank_mask:0xf\n"
    "v_add_f32_dpp v191, v191, v191 quad_perm:[2,3,0,1] row_mask:0xf bank_mask:0xf\n"
    "s_nop 1\n"
    "v_add_f32_dpp v190, v190, v190 row_half_mirror row_mask:0xf bank_mask:0xf\n"
    "v_add_f32_dpp v191, v191, v191 row_half_mirror row_mask:0xf bank_mask:0xf\n"
    "s_nop 1\n"
    "v_add_f32_dpp v190, v190, v190 row_mirror row_mask:0xf bank_mask:0xf\n"
    "v_add_f32_dpp v191, v191, v191 row_mirror row_mask:0xf bank_mask:0xf\n"
    "v_mov_b32 v192, v190\n"
    "v_mov_b32 v193, v191\n"
    "s_nop 1\n"
    "v_permlane16_swap_b32 v190, v192\n"
    "v_permlane16_swap_b32 v191, v193\n"
    "v_add_f32 v190, v190, v192\n"
    "v_add_f32 v191, v191, v193\n"
    "v_mov_b32 v192, v190\n"
    "v_mov_b32 v193, v191\n"
    "s_nop 1\n"
    "v_permlane32_swap_b32 v190, v192\n"
    "v_permlane32_swap_b32 v191, v193\n"
    "v_add_f32 v190, v190, v192\n"
    "v_add_f32 v191, v191, v193\n"
    "v_mul_f32 v190, 0x3b000000, v190\n"
    "v_sub_f32 v198, v170, v190\n"
    "v_mul_f32 v194, v198, v198\n"
    "v_sub_f32 v198, v171, v190\n"
    "v_fmac_f32 v194, v198, v198\n"
    "v_sub_f32 v198, v172, v190\n"
    "v_fmac_f32 v194, v198, v198\n"
    "v_sub_f32 v198, v173, v190\n"
    "v_fmac_f32 v194, v198, v198\n"
    "v_sub_f32 v198, v174, v190\n"
    "v_fmac_f32 v194, v198, v198\n"
    "v_sub_f32 v198, v175, v190\n"
    "v_fmac_f32 v194, v198, v198\n"
    "v_sub_f32 v198, v176, v190\n"
    "v_fmac_f32 v194, v198, v198\n"
    "v_sub_f32 v198, v177, v190\n"
    "v_fmac_f32 v194, v198, v198\n"
    "v_mul_f32 v191, 0x3b000000, v191\n"
    "v_sub_f32 v198, v180, v191\n"
    "v_mul_f32 v195, v198, v198\n"
    "v_sub_f32 v198, v181, v191\n"
    "v_fmac_f32 v195, v198, v198\n"
    "s_add_u32 m0, s80, 0x8000\n"
    "v_sub_f32 v198, v182, v191\n"
    "v_fmac_f32 v195, v198, v198\n"
    "v_sub_f32 v198, v183, v191\n"
    "v_fmac_f32 v195, v198, v198\n"
    "v_sub_f32 v198, v184, v191\n"
    "buffer_load_dwordx4 v230, s[48:51], s75 offen offset:0 lds\n"
    "v_fmac_f32 v195, v198, v198\n"
    "v_sub_f32 v198, v185, v191\n"
    "v_fmac_f32 v195, v198, v198\n"
    "v_sub_f32 v198, v186, v191\n"
    "v_fmac_f32 v195, v198, v198\n"
    "buffer_load_dwordx4 v231, s[48:51], s75 offen offset:1024 lds\n"
    "v_sub_f32 v198, v187, v191\n"
    "v_fmac_f32 v195, v198, v198\n"
    "s_nop 1\n"
    "v_add_f32_dpp v194, v194, v194 quad_perm:[1,0,3,2] row_mask:0xf bank_mask:0xf\n"
    "v_add_f32_dpp v195, v195, v195 quad_perm:[1,0,3,2] row_mask:0xf bank_mask:0xf\n"
    "buffer_load_dwordx4 v232, s[48:51], s75 offen offset:2048 lds\n"
    "s_nop 1\n"
    "v_add_f32_dpp v194, v194, v194 quad_perm:[2,3,0,1] row_mask:0xf bank_mask:0xf\n"
    "v_add_f32_dpp v195, v195, v195 quad_perm:[2,3,0,1] row_mask:0xf bank_mask:0xf\n"
    "s_nop 1\n"
    "v_add_f32_dpp v194, v194, v194 row_half_mirror row_mask:0xf bank_mask:0xf\n"
    "buffer_load_dwordx4 v233, s[48:51], s75 offen offset:3072 lds\n"
    "v_add_f32_dpp v195, v195, v195 row_half_mirror row_mask:0xf bank_mask:0xf\n"
    "s_nop 1\n"
    "v_add_f32_dpp v194, v194, v194 row_mirror row_mask:0xf bank_mask:0xf\n"
    "v_add_f32_dpp v195, v195, v195 row_mirror row_mask:0xf bank_mask:0xf\n"
    "v_mov_b32 v196, v194\n"
    "s_add_u32 m0, s80, 0x9000\n"
    "v_mov_b32 v197, v195\n"
    "s_nop 1\n"
    "v_permlane16_swap_b32 v194, v196\n"
    "v_permlane16_swap_b32 v195, v197\n"
    "v_add_f32 v194, v194, v196\n"
    "buffer_load_dwordx4 v230, s[48:51], s76 offen offset:0 lds\n"
    "v_add_f32 v195, v195, v197\n"
    "v_mov_b32 v196, v194\n"
    "v_mov_b32 v197, v195\n"
    "s_nop 1\n"
    "v_permlane32_swap_b32 v194, v196\n"
    "buffer_load_dwordx4 v231, s[48:51], s76 offen offset:1024 lds\n"
    "v_permlane32_swap_b32 v195, v197\n"
    "v_add_f32 v194, v194, v196\n"
    "v_add_f32 v195, v195, v197\n"
    "v_mul_f32 v194, 0x3b000000, v194\n"
    "v_cvt_pk_bf16_f32 v194, v194, v194\n"
    "buffer_load_dwordx4 v232, s[48:51], s76 offen offset:2048 lds\n"
    "v_lshlrev_b32 v194, 16, v194\n"
    "v_add_f32 v194, 0x3727c5ac, v194\n"
    "v_cvt_pk_bf16_f32 v194, v194, v194\n"
    "v_lshlrev_b32 v194, 16, v194\n"
    "v_sqrt_f32 v194, v194\n"
    "buffer_load_dwordx4 v233, s[48:51], s76 offen offset:3072 lds\n"
    "s_nop 0\n"
    "v_cvt_pk_bf16_f32 v194, v194, v194\n"
    "v_lshlrev_b32 v194, 16, v194\n"
    "v_rcp_f32 v194, v194\n"
    "v_cvt_pk_bf16_f32 v190, v190, v190\n"
    "s_add_u32 s75, s75, 0x80\n"
    "v_lshlrev_b32 v190, 16, v190\n"
    "v_mul_f32 v194, 0x3e0293ee, v194\n"
    "v_sub_f32 v170, v170, v190\n"
    "v_sub_f32 v171, v171, v190\n"
    "v_sub_f32 v172, v172, v190\n"
    "s_add_u32 s76, s76, 0x80\n"
    "v_sub_f32 v173, v173, v190\n"
    "v_sub_f32 v174, v174, v190\n"
    "v_sub_f32 v175, v175, v190\n"
    "v_sub_f32 v176, v176, v190\n"
    "v_sub_f32 v177, v177, v190\n"
    "s_add_u32 m0, s80, 0x18000\n"
    "v_cvt_pk_bf16_f32 v160, v170, v171\n"
    "v_lshlrev_b32 v170, 16, v160\n"
    "v_and_b32 v171, 0xffff0000, v160\n"
    "v_mul_f32 v170, v170, v194\n"
    "v_mul_f32 v171, v171, v194\n"
    "buffer_load_dwordx4 v234, s[52:55], s77 offen offset:0 nt lds\n"
    "v_cvt_pk_bf16_f32 v216, v170, v171\n"
    "v_cvt_pk_bf16_f32 v161, v172, v173\n"
    "v_lshlrev_b32 v172, 16, v161\n"
    "v_and_b32 v173, 0xffff0000, v161\n"
    "v_mul_f32 v172, v172, v194\n"
    "buffer_load_dwordx4 v235, s[52:55], s77 offen offset:1024 nt lds\n"
    "v_mul_f32 v173, v173, v194\n"
    "v_cvt_pk_bf16_f32 v217, v172, v173\n"
    "v_cvt_pk_bf16_f32 v162, v174, v175\n"
    "v_lshlrev_b32 v174, 16, v162\n"
    "v_and_b32 v175, 0xffff0000, v162\n"
    "buffer_load_dwordx4 v236, s[52:55], s77 offen offset:2048 nt lds\n"
    "v_mul_f32 v174, v174, v194\n"
    "v_mul_f32 v175, v175, v194\n"
    "v_cvt_pk_bf16_f32 v218, v174, v175\n"
    "v_cvt_pk_bf16_f32 v163, v176, v177\n"
    "v_lshlrev_b32 v176, 16, v163\n"
    "buffer_load_dwordx4 v237, s[52:55], s77 offen offset:3072 nt lds\n"
    "v_and_b32 v177, 0xffff0000, v163\n"
    "v_mul_f32 v176, v176, v194\n"
    "v_mul_f32 v177, v177, v194\n"
    "v_cvt_pk_bf16_f32 v219, v176, v177\n"
    "v_mul_f32 v195, 0x3b000000, v195\n"
    "s_add_u32 m0, s80, 0x19000\n"
    "v_cvt_pk_bf16_f32 v195, v195, v195\n"
    "v_lshlrev_b32 v195, 16, v195\n"
    "v_add_f32 v195, 0x3727c5ac, v195\n"
    "v_cvt_pk_bf16_f32 v195, v195, v195\n"
    "v_lshlrev_b32 v195, 16, v195\n"
    "buffer_load_dwordx4 v234, s[52:55], s78 offen offset:0 nt lds\n"
    "v_sqrt_f32 v195, v195\n"
    "s_nop 0\n"
    "v_cvt_pk_bf16_f32 v195, v195, v195\n"
    "v_lshlrev_b32 v195, 16, v195\n"
    "v_rcp_f32 v195, v195\n"
    "buffer_load_dwordx4 v235, s[52:55], s78 offen offset:1024 nt lds\n"
    "v_cvt_pk_bf16_f32 v191, v191, v191\n"
    "v_lshlrev_b32 v191, 16, v191\n"
    "v_mul_f32 v195, 0x3e0293ee, v195\n"
    "v_sub_f32 v180, v180, v191\n"
    "v_sub_f32 v181, v181, v191\n"
    "buffer_load_dwordx4 v236, s[52:55], s78 offen offset:2048 nt lds\n"
    "v_sub_f32 v182, v182, v191\n"
    "v_sub_f32 v183, v183, v191\n"
    "v_sub_f32 v184, v184, v191\n"
    "v_sub_f32 v185, v185, v191\n"
    "v_sub_f32 v186, v186, v191\n"
    "buffer_load_dwordx4 v237, s[52:55], s78 offen offset:3072 nt lds\n"
    "v_sub_f32 v187, v187, v191\n"
    "v_cvt_pk_bf16_f32 v164, v180, v181\n"
    "v_lshlrev_b32 v180, 16, v164\n"
    "v_and_b32 v181, 0xffff0000, v164\n"
    "v_mul_f32 v180, v180, v195\n"
    "s_add_u32 s77, s77, s79\n"
    "v_mul_f32 v181, v181, v195\n"
    "v_cvt_pk_bf16_f32 v220, v180, v181\n"
    "v_cvt_pk_bf16_f32 v165, v182, v183\n"
    "v_lshlrev_b32 v182, 16, v165\n"
    "v_and_b32 v183, 0xffff0000, v165\n"
    "s_add_u32 s78, s78, s79\n"
    "v_mul_f32 v182, v182, v195\n"
    "v_mul_f32 v183, v183, v195\n"
    "v_cvt_pk_bf16_f32 v221, v182, v183\n"
    "v_cvt_pk_bf16_f32 v166, v184, v185\n"
    "v_lshlrev_b32 v184, 16, v166\n"
    "v_and_b32 v185, 0xffff0000, v166\n"
    "v_mul_f32 v184, v184, v195\n"
    "v_mul_f32 v185, v185, v195\n"
    "v_cvt_pk_bf16_f32 v222, v184, v185\n"
    "v_cvt_pk_bf16_f32 v167, v186, v187\n"
    "v_lshlrev_b32 v186, 16, v167\n"
    "v_and_b32 v187, 0xffff0000, v167\n"
    "v_mul_f32 v186, v186, v195\n"
    "v_mul_f32 v187, v187, v195\n"
    "v_cvt_pk_bf16_f32 v223, v186, v187\n"
    "s_waitcnt vmcnt(17)\n"
    "s_barrier\n"
    "ds_read_b128 a[0:3], v224 offset:0\n"
    "ds_read_b128 a[4:7], v225 offset:0\n"
    "ds_read_b64_tr_b16 a[72:73], v228 offset:0\n"
    "ds_read_b64_tr_b16 a[74:75], v228 offset:256\n"
    "ds_read_b64_tr_b16 a[76:77], v228 offset:4096\n"
    "ds_read_b64_tr_b16 a[78:79], v228 offset:4352\n"
    "ds_read_b64_tr_b16 a[80:81], v229 offset:0\n"
    "ds_read_b64_tr_b16 a[82:83], v229 offset:256\n"
    "ds_read_b64_tr_b16 a[84:85], v229 offset:4096\n"
    "ds_read_b64_tr_b16 a[86:87], v229 offset:4352\n"
    "ds_read_b64_tr_b16 a[88:89], v228 offset:8192\n"
    "ds_read_b64_tr_b16 a[90:91], v228 offset:8448\n"
    "ds_read_b64_tr_b16 a[92:93], v228 offset:12288\n"
    "ds_read_b64_tr_b16 a[94:95], v228 offset:12544\n"
    "ds_read_b64_tr_b16 a[96:97], v229 offset:8192\n"
    "ds_read_b64_tr_b16 a[98:99], v229 offset:8448\n"
    "ds_read_b64_tr_b16 a[100:101], v229 offset:12288\n"
    "ds_read_b64_tr_b16 a[102:103], v229 offset:12544\n"
    "ds_read_b64_tr_b16 a[104:105], v228 offset:16384\n"
    "ds_read_b64_tr_b16 a[106:107], v228 offset:16640\n"
    "ds_read_b64_tr_b16 a[108:109], v228 offset:20480\n"
    "ds_read_b64_tr_b16 a[110:111], v228 offset:20736\n"
    "ds_read_b128 a[8:11], v224 offset:2048\n"
    "ds_read_b128 a[12:15], v225 offset:2048\n"
    "ds_read_b64_tr_b16 a[112:113], v229 offset:16384\n"
    "ds_read_b64_tr_b16 a[114:115], v229 offset:16640\n"
    "ds_read_b64_tr_b16 a[116:117], v229 offset:20480\n"
    "ds_read_b64_tr_b16 a[118:119], v229 offset:20736\n"
    "ds_read_b64_tr_b16 a[120:121], v228 offset:24576\n"
    "ds_read_b64_tr_b16 a[122:123], v228 offset:24832\n"
    "ds_read_b64_tr_b16 a[124:125], v228 offset:28672\n"
    "ds_read_b64_tr_b16 a[126:127], v228 offset:28928\n"
    "ds_read_b64_tr_b16 a[128:129], v229 offset:24576\n"
    "ds_read_b64_tr_b16 a[130:131], v229 offset:24832\n"
    "ds_read_b64_tr_b16 a[132:133], v229 offset:28672\n"
    "ds_read_b64_tr_b16 a[134:135], v229 offset:28928\n"
    "s_waitcnt lgkmcnt(15)\n"
    "v_mfma_f32_16x16x32_bf16 a[200:203], a[72:75], a[0:3], 0\n"
    "ds_read_b128 a[16:19], v224 offset:4096\n"
    "v_mfma_f32_16x16x32_bf16 a[200:203], a[76:79], a[4:7], a[200:203]\n"
    "s_add_u32 m0, s80, 0x20000\n"
    "v_mfma_f32_16x16x32_bf16 a[204:207], a[80:83], a[0:3], 0\n"
    "ds_read_b128 a[20:23], v225 offset:4096\n"
    "v_mfma_f32_16x16x32_bf16 a[204:207], a[84:87], a[4:7], a[204:207]\n"
    "buffer_load_dwordx4 v234, s[52:55], s77 offen offset:0 nt lds\n"
    "v_mfma_f32_16x16x32_bf16 a[208:211], a[88:91], a[0:3], 0\n"
    "ds_read_b128 a[24:27], v224 offset:6144\n"
    "v_mfma_f32_16x16x32_bf16 a[208:211], a[92:95], a[4:7], a[208:211]\n"
    "v_mfma_f32_16x16x32_bf16 a[212:215], a[96:99], a[0:3], 0\n"
    "ds_read_b128 a[28:31], v225 offset:6144\n"
    "v_mfma_f32_16x16x32_bf16 a[212:215], a[100:103], a[4:7], a[212:215]\n"
    "buffer_load_dwordx4 v235, s[52:55], s77 offen offset:1024 nt lds\n"
    "v_mfma_f32_16x16x32_bf16 v[16:19], a[0:3], a[104:107], 0\n"
    "ds_read_b128 a[32:35], v224 offset:8192\n"
    "s_waitcnt lgkmcnt(15)\n"
    "v_mfma_f32_16x16x32_bf16 v[16:19], a[4:7], a[108:111], v[16:19]\n"
    "v_mfma_f32_16x16x32_bf16 v[20:23], a[0:3], a[112:115], 0\n"
    "ds_read_b128 a[36:39], v225 offset:8192\n"
    "s_waitcnt lgkmcnt(14)\n"
    "v_mfma_f32_16x16x32_bf16 v[20:23], a[4:7], a[116:119], v[20:23]\n"
    "buffer_load_dwordx4 v236, s[52:55], s77 offen offset:2048 nt lds\n"
    "s_waitcnt lgkmcnt(12)\n"
    "v_mfma_f32_16x16x32_bf16 v[24:27], a[0:3], a[120:123], 0\n"
    "ds_read_b128 a[40:43], v224 offset:10240\n"
    "s_waitcnt lgkmcnt(11)\n"
    "v_mfma_f32_16x16x32_bf16 v[24:27], a[4:7], a[124:127], v[24:27]\n"
    "s_waitcnt lgkmcnt(9)\n"
    "v_mfma_f32_16x16x32_bf16 v[28:31], a[0:3], a[128:131], 0\n"
    "ds_read_b128 a[44:47], v225 offset:10240\n"
    "s_waitcnt lgkmcnt(8)\n"
    "v_mfma_f32_16x16x32_bf16 v[28:31], a[4:7], a[132:135], v[28:31]\n"
    "buffer_load_dwordx4 v237, s[52:55], s77 offen offset:3072 nt lds\n"
    "v_mfma_f32_16x16x32_bf16 a[216:219], a[72:75], a[8:11], 0\n"
    "ds_read_b128 a[48:51], v224 offset:12288\n"
    "v_mfma_f32_16x16x32_bf16 a[216:219], a[76:79], a[12:15], a[216:219]\n"
    "v_mfma_f32_16x16x32_bf16 a[220:223], a[80:83], a[8:11], 0\n"
    "ds_read_b128 a[52:55], v225 offset:12288\n"
    "v_mfma_f32_16x16x32_bf16 a[220:223], a[84:87], a[12:15], a[220:223]\n"
    "s_add_u32 m0, s80, 0x21000\n"
    "v_mfma_f32_16x16x32_bf16 a[224:227], a[88:91], a[8:11], 0\n"
    "ds_read_b128 a[56:59], v224 offset:14336\n"
    "v_mfma_f32_16x16x32_bf16 a[224:227], a[92:95], a[12:15], a[224:227]\n"
    "buffer_load_dwordx4 v234, s[52:55], s78 offen offset:0 nt lds\n"
    "v_mfma_f32_16x16x32_bf16 a[228:231], a[96:99], a[8:11], 0\n"
    "ds_read_b128 a[60:63], v225 offset:14336\n"
    "v_mfma_f32_16x16x32_bf16 a[228:231], a[100:103], a[12:15], a[228:231]\n"
    "v_mfma_f32_16x16x32_bf16 v[32:35], a[8:11], a[104:107], 0\n"
    "v_mfma_f32_16x16x32_bf16 v[32:35], a[12:15], a[108:111], v[32:35]\n"
    "buffer_load_dwordx4 v235, s[52:55], s78 offen offset:1024 nt lds\n"
    "v_mfma_f32_16x16x32_bf16 v[36:39], a[8:11], a[112:115], 0\n"
    "v_mfma_f32_16x16x32_bf16 v[36:39], a[12:15], a[116:119], v[36:39]\n"
    "v_mfma_f32_16x16x32_bf16 v[40:43], a[8:11], a[120:123], 0\n"
    "v_mfma_f32_16x16x32_bf16 v[40:43], a[12:15], a[124:127], v[40:43]\n"
    "buffer_load_dwordx4 v236, s[52:55], s78 offen offset:2048 nt lds\n"
    "v_mfma_f32_16x16x32_bf16 v[44:47], a[8:11], a[128:131], 0\n"
    "v_mfma_f32_16x16x32_bf16 v[44:47], a[12:15], a[132:135], v[44:47]\n"
    "s_waitcnt lgkmcnt(11)\n"
    "v_mfma_f32_16x16x32_bf16 a[232:235], a[72:75], a[16:19], 0\n"
    "s_waitcnt lgkmcnt(10)\n"
    "v_mfma_f32_16x16x32_bf16 a[232:235], a[76:79], a[20:23], a[232:235]\n"
    "buffer_load_dwordx4 v237, s[52:55], s78 offen offset:3072 nt lds\n"
    "v_mfma_f32_16x16x32_bf16 a[236:239], a[80:83], a[16:19], 0\n"
    "v_mfma_f32_16x16x32_bf16 a[236:239], a[84:87], a[20:23], a[236:239]\n"
    "v_mfma_f32_16x16x32_bf16 a[240:243], a[88:91], a[16:19], 0\n"
    "v_mfma_f32_16x16x32_bf16 a[240:243], a[92:95], a[20:23], a[240:243]\n"
    "s_add_u32 s77, s77, s79\n"
    "v_mfma_f32_16x16x32_bf16 a[244:247], a[96:99], a[16:19], 0\n"
    "s_add_u32 s78, s78, s79\n"
    "v_mfma_f32_16x16x32_bf16 a[244:247], a[100:103], a[20:23], a[244:247]\n"
    "buffer_store_dwordx4 v[216:219], v8, s[60:63], s84 offen offset:0 sc1\n"
    "v_mfma_f32_16x16x32_bf16 v[48:51], a[16:19], a[104:107], 0\n"
    "v_mfma_f32_16x16x32_bf16 v[48:51], a[20:23], a[108:111], v[48:51]\n"
    "v_mfma_f32_16x16x32_bf16 v[52:55], a[16:19], a[112:115], 0\n"
    "v_mfma_f32_16x16x32_bf16 v[52:55], a[20:23], a[116:119], v[52:55]\n"
    "buffer_store_dwordx4 v[220:223], v8, s[60:63], s84 offen offset:1024 sc1\n"
    "v_mfma_f32_16x16x32_bf16 v[56:59], a[16:19], a[120:123], 0\n"
    "v_mfma_f32_16x16x32_bf16 v[56:59], a[20:23], a[124:127], v[56:59]\n"
    "v_mfma_f32_16x16x32_bf16 v[60:63], a[16:19], a[128:131], 0\n"
    "v_mfma_f32_16x16x32_bf16 v[60:63], a[20:23], a[132:135], v[60:63]\n"
    "s_waitcnt lgkmcnt(9)\n"
    "v_mfma_f32_16x16x32_bf16 v[64:67], a[72:75], a[24:27], 0\n"
    "s_waitcnt lgkmcnt(8)\n"
    "v_mfma_f32_16x16x32_bf16 v[64:67], a[76:79], a[28:31], v[64:67]\n"
    "v_mfma_f32_16x16x32_bf16 v[68:71], a[80:83], a[24:27], 0\n"
    "v_mfma_f32_16x16x32_bf16 v[68:71], a[84:87], a[28:31], v[68:71]\n"
    "v_mfma_f32_16x16x32_bf16 v[72:75], a[88:91], a[24:27], 0\n"
    "v_mfma_f32_16x16x32_bf16 v[72:75], a[92:95], a[28:31], v[72:75]\n"
    "v_mfma_f32_16x16x32_bf16 v[76:79], a[96:99], a[24:27], 0\n"
    "v_mfma_f32_16x16x32_bf16 v[76:79], a[100:103], a[28:31], v[76:79]\n"
    "v_mfma_f32_16x16x32_bf16 v[80:83], a[24:27], a[104:107], 0\n"
    "v_add_u32 v228, 0x8000, v226\n"
    "v_mfma_f32_16x16x32_bf16 v[80:83], a[28:31], a[108:111], v[80:83]\n"
    "v_add_u32 v229, 0x8000, v227\n"
    "v_mfma_f32_16x16x32_bf16 v[84:87], a[24:27], a[112:115], 0\n"
    "v_mfma_f32_16x16x32_bf16 v[84:87], a[28:31], a[116:119], v[84:87]\n"
    "v_mfma_f32_16x16x32_bf16 v[88:91], a[24:27], a[120:123], 0\n"
    "s_waitcnt vmcnt(10) lgkmcnt(0)\n"
    "s_barrier\n"
    "v_mfma_f32_16x16x32_bf16 v[88:91], a[28:31], a[124:127], v[88:91]\n"
    "ds_read_b64_tr_b16 a[136:137], v228 offset:0\n"
    "s_add_u32 m0, s80, 0x0\n"
    "v_mfma_f32_16x16x32_bf16 v[92:95], a[24:27], a[128:131], 0\n"
    "buffer_load_dwordx4 v230, s[48:51], s75 offen offset:0 lds\n"
    "v_mfma_f32_16x16x32_bf16 v[92:95], a[28:31], a[132:135], v[92:95]\n"
    "ds_read_b64_tr_b16 a[138:139], v228 offset:256\n"
    "v_mfma_f32_16x16x32_bf16 v[96:99], a[72:75], a[32:35], 0\n"
    "ds_read_b64_tr_b16 a[140:141], v228 offset:4096\n"
    "v_mfma_f32_16x16x32_bf16 v[96:99], a[76:79], a[36:39], v[96:99]\n"
    "ds_read_b64_tr_b16 a[142:143], v228 offset:4352\n"
    "v_mfma_f32_16x16x32_bf16 v[100:103], a[80:83], a[32:35], 0\n"
    "buffer_load_dwordx4 v231, s[48:51], s75 offen offset:1024 lds\n"
    "v_mfma_f32_16x16x32_bf16 v[100:103], a[84:87], a[36:39], v[100:103]\n"
    "ds_read_b64_tr_b16 a[144:145], v229 offset:0\n"
    "v_mfma_f32_16x16x32_bf16 v[104:107], a[88:91], a[32:35], 0\n"
    "ds_read_b64_tr_b16 a[146:147], v229 offset:256\n"
    "v_mfma_f32_16x16x32_bf16 v[104:107], a[92:95], a[36:39], v[104:107]\n"
    "ds_read_b64_tr_b16 a[148:149], v229 offset:4096\n"
    "v_mfma_f32_16x16x32_bf16 v[108:111], a[96:99], a[32:35], 0\n"
    "buffer_load_dwordx4 v232, s[48:51], s75 offen offset:2048 lds\n"
    "v_mfma_f32_16x16x32_bf16 v[108:111], a[100:103], a[36:39], v[108:111]\n"
    "ds_read_b64_tr_b16 a[150:151], v229 offset:4352\n"
    "v_mfma_f32_16x16x32_bf16 v[112:115], a[32:35], a[104:107], 0\n"
    "ds_read_b64_tr_b16 a[152:153], v228 offset:8192\n"
    "v_mfma_f32_16x16x32_bf16 v[112:115], a[36:39], a[108:111], v[112:115]\n"
    "ds_read_b64_tr_b16 a[154:155], v228 offset:8448\n"
    "v_mfma_f32_16x16x32_bf16 v[116:119], a[32:35], a[112:115], 0\n"
    "buffer_load_dwordx4 v233, s[48:51], s75 offen offset:3072 lds\n"
    "v_mfma_f32_16x16x32_bf16 v[116:119], a[36:39], a[116:119], v[116:119]\n"
    "ds_read_b64_tr_b16 a[156:157], v228 offset:12288\n"
    "v_mfma_f32_16x16x32_bf16 v[120:123], a[32:35], a[120:123], 0\n"
    "ds_read_b64_tr_b16 a[158:159], v228 offset:12544\n"
    "v_mfma_f32_16x16x32_bf16 v[120:123], a[36:39], a[124:127], v[120:123]\n"
    "ds_read_b64_tr_b16 a[160:161], v229 offset:8192\n"
    "v_mfma_f32_16x16x32_bf16 v[124:127], a[32:35], a[128:131], 0\n"
    "ds_read_b64_tr_b16 a[162:163], v229 offset:8448\n"
    "s_add_u32 m0, s80, 0x1000\n"
    "v_mfma_f32_16x16x32_bf16 v[124:127], a[36:39], a[132:135], v[124:127]\n"
    "buffer_load_dwordx4 v230, s[48:51], s76 offen offset:0 lds\n"
    "v_mfma_f32_16x16x32_bf16 v[128:131], a[72:75], a[40:43], 0\n"
    "ds_read_b128 a[32:35], v224 offset:40960\n"
    "v_mfma_f32_16x16x32_bf16 v[128:131], a[76:79], a[44:47], v[128:131]\n"
    "ds_read_b64_tr_b16 a[164:165], v229 offset:12288\n"
    "v_mfma_f32_16x16x32_bf16 v[132:135], a[80:83], a[40:43], 0\n"
    "ds_read_b128 a[36:39], v225 offset:40960\n"
    "v_mfma_f32_16x16x32_bf16 v[132:135], a[84:87], a[44:47], v[132:135]\n"
    "buffer_load_dwordx4 v231, s[48:51], s76 offen offset:1024 lds\n"
    "v_mfma_f32_16x16x32_bf16 v[136:139], a[88:91], a[40:43], 0\n"
    "ds_read_b64_tr_b16 a[166:167], v229 offset:12544\n"
    "v_mfma_f32_16x16x32_bf16 v[136:139], a[92:95], a[44:47], v[136:139]\n"
    "ds_read_b64_tr_b16 a[168:169], v228 offset:16384\n"
    "v_mfma_f32_16x16x32_bf16 v[140:143], a[96:99], a[40:43], 0\n"
    "ds_read_b64_tr_b16 a[170:171], v228 offset:16640\n"
    "v_mfma_f32_16x16x32_bf16 v[140:143], a[100:103], a[44:47], v[140:143]\n"
    "buffer_load_dwordx4 v232, s[48:51], s76 offen offset:2048 lds\n"
    "v_mfma_f32_16x16x32_bf16 v[144:147], a[40:43], a[104:107], 0\n"
    "ds_read_b64_tr_b16 a[172:173], v228 offset:20480\n"
    "v_mfma_f32_16x16x32_bf16 v[144:147], a[44:47], a[108:111], v[144:147]\n"
    "ds_read_b64_tr_b16 a[174:175], v228 offset:20736\n"
    "v_mfma_f32_16x16x32_bf16 v[148:151], a[40:43], a[112:115], 0\n"
    "ds_read_b64_tr_b16 a[176:177], v229 offset:16384\n"
    "v_mfma_f32_16x16x32_bf16 v[148:151], a[44:47], a[116:119], v[148:151]\n"
    "buffer_load_dwordx4 v233, s[48:51], s76 offen offset:3072 lds\n"
    "v_mfma_f32_16x16x32_bf16 v[152:155], a[40:43], a[120:123], 0\n"
    "ds_read_b64_tr_b16 a[178:179], v229 offset:16640\n"
    "v_mfma_f32_16x16x32_bf16 v[152:155], a[44:47], a[124:127], v[152:155]\n"
    "ds_read_b64_tr_b16 a[180:181], v229 offset:20480\n"
    "v_mfma_f32_16x16x32_bf16 v[156:159], a[40:43], a[128:131], 0\n"
    "ds_read_b64_tr_b16 a[182:183], v229 offset:20736\n"
    "v_mfma_f32_16x16x32_bf16 v[156:159], a[44:47], a[132:135], v[156:159]\n"
    "ds_read_b64_tr_b16 a[184:185], v228 offset:24576\n"
    "s_add_u32 s75, s75, 0x80\n"
    "v_mfma_f32_16x16x32_bf16 v[160:163], a[72:75], a[48:51], 0\n"
    "ds_read_b64_tr_b16 a[186:187], v228 offset:24832\n"
    "s_add_u32 s76, s76, 0x80\n"
    "v_mfma_f32_16x16x32_bf16 v[160:163], a[76:79], a[52:55], v[160:163]\n"
    "ds_read_b128 a[40:43], v224 offset:43008\n"
    "v_mfma_f32_16x16x32_bf16 v[164:167], a[80:83], a[48:51], 0\n"
    "ds_read_b128 a[44:47], v225 offset:43008\n"
    "v_mfma_f32_16x16x32_bf16 v[164:167], a[84:87], a[52:55], v[164:167]\n"
    "ds_read_b64_tr_b16 a[188:189], v228 offset:28672\n"
    "v_mfma_f32_16x16x32_bf16 v[168:171], a[88:91], a[48:51], 0\n"
    "ds_read_b64_tr_b16 a[190:191], v228 offset:28928\n"
    "v_mfma_f32_16x16x32_bf16 v[168:171], a[92:95], a[52:55], v[168:171]\n"
    "ds_read_b64_tr_b16 a[192:193], v229 offset:24576\n"
    "v_mfma_f32_16x16x32_bf16 v[172:175], a[96:99], a[48:51], 0\n"
    "ds_read_b64_tr_b16 a[194:195], v229 offset:24832\n"
    "v_mfma_f32_16x16x32_bf16 v[172:175], a[100:103], a[52:55], v[172:175]\n"
    "ds_read_b64_tr_b16 a[196:197], v229 offset:28672\n"
    "v_mfma_f32_16x16x32_bf16 v[176:179], a[48:51], a[104:107], 0\n"
    "ds_read_b64_tr_b16 a[198:199], v229 offset:28928\n"
    "v_mfma_f32_16x16x32_bf16 v[176:179], a[52:55], a[108:111], v[176:179]\n"
    "ds_read_b128 a[64:67], v224 offset:47104\n"
    "v_mfma_f32_16x16x32_bf16 v[180:183], a[48:51], a[112:115], 0\n"
    "ds_read_b128 a[68:71], v225 offset:47104\n"
    "v_mfma_f32_16x16x32_bf16 v[180:183], a[52:55], a[116:119], v[180:183]\n"
    "ds_read_b128 a[0:3], v224 offset:32768\n"
    "v_mfma_f32_16x16x32_bf16 v[184:187], a[48:51], a[120:123], 0\n"
    "ds_read_b128 a[4:7], v225 offset:32768\n"
    "v_mfma_f32_16x16x32_bf16 v[184:187], a[52:55], a[124:127], v[184:187]\n"
    "ds_read_b128 a[8:11], v224 offset:34816\n"
    "v_mfma_f32_16x16x32_bf16 v[188:191], a[48:51], a[128:131], 0\n"
    "ds_read_b128 a[12:15], v225 offset:34816\n"
    "v_mfma_f32_16x16x32_bf16 v[188:191], a[52:55], a[132:135], v[188:191]\n"
    "ds_read_b128 a[16:19], v224 offset:36864\n"
    "v_mfma_f32_16x16x32_bf16 v[192:195], a[72:75], a[56:59], 0\n"
    "ds_read_b128 a[48:51], v224 offset:45056\n"
    "v_mfma_f32_16x16x32_bf16 v[192:195], a[76:79], a[60:63], v[192:195]\n"
    "ds_read_b128 a[20:23], v225 offset:36864\n"
    "v_mfma_f32_16x16x32_bf16 v[196:199], a[80:83], a[56:59], 0\n"
    "ds_read_b128 a[52:55], v225 offset:45056\n"
    "v_mfma_f32_16x16x32_bf16 v[196:199], a[84:87], a[60:63], v[196:199]\n"
    "ds_read_b128 a[24:27], v224 offset:38912\n"
    "v_mfma_f32_16x16x32_bf16 v[200:203], a[88:91], a[56:59], 0\n"
    "ds_read_b128 a[28:31], v225 offset:38912\n"
    "v_mfma_f32_16x16x32_bf16 v[200:203], a[92:95], a[60:63], v[200:203]\n"
    "v_mfma_f32_16x16x32_bf16 v[204:207], a[96:99], a[56:59], 0\n"
    "v_mfma_f32_16x16x32_bf16 v[204:207], a[100:103], a[60:63], v[204:207]\n"
    "v_mfma_f32_16x16x32_bf16 v[208:211], a[56:59], a[104:107], 0\n"
    "v_mfma_f32_16x16x32_bf16 v[208:211], a[60:63], a[108:111], v[208:211]\n"
    "v_mfma_f32_16x16x32_bf16 v[212:215], a[56:59], a[112:115], 0\n"
    "v_mfma_f32_16x16x32_bf16 v[212:215], a[60:63], a[116:119], v[212:215]\n"
    "v_mfma_f32_16x16x32_bf16 v[216:219], a[56:59], a[120:123], 0\n"
    "v_mfma_f32_16x16x32_bf16 v[216:219], a[60:63], a[124:127], v[216:219]\n"
    "v_mfma_f32_16x16x32_bf16 v[220:223], a[56:59], a[128:131], 0\n"
    "v_mfma_f32_16x16x32_bf16 v[220:223], a[60:63], a[132:135], v[220:223]\n"
    "s_waitcnt lgkmcnt(0)\n"
    "v_mfma_f32_16x16x32_bf16 a[200:203], a[136:139], a[0:3], a[200:203]\n"
    "v_mfma_f32_16x16x32_bf16 a[200:203], a[140:143], a[4:7], a[200:203]\n"
    "s_add_u32 m0, s80, 0x10000\n"
    "v_mfma_f32_16x16x32_bf16 a[204:207], a[144:147], a[0:3], a[204:207]\n"
    "buffer_load_dwordx4 v234, s[52:55], s77 offen offset:0 nt lds\n"
    "v_mfma_f32_16x16x32_bf16 a[204:207], a[148:151], a[4:7], a[204:207]\n"
    "v_mfma_f32_16x16x32_bf16 a[208:211], a[152:155], a[0:3], a[208:211]\n"
    "v_mfma_f32_16x16x32_bf16 a[208:211], a[156:159], a[4:7], a[208:211]\n"
    "v_mfma_f32_16x16x32_bf16 a[212:215], a[160:163], a[0:3], a[212:215]\n"
    "buffer_load_dwordx4 v235, s[52:55], s77 offen offset:1024 nt lds\n"
    "v_mfma_f32_16x16x32_bf16 a[212:215], a[164:167], a[4:7], a[212:215]\n"
    "v_mfma_f32_16x16x32_bf16 v[16:19], a[0:3], a[168:171], v[16:19]\n"
    "v_mfma_f32_16x16x32_bf16 v[16:19], a[4:7], a[172:175], v[16:19]\n"
    "v_mfma_f32_16x16x32_bf16 v[20:23], a[0:3], a[176:179], v[20:23]\n"
    "buffer_load_dwordx4 v236, s[52:55], s77 offen offset:2048 nt lds\n"
    "v_mfma_f32_16x16x32_bf16 v[20:23], a[4:7], a[180:183], v[20:23]\n"
    "v_mfma_f32_16x16x32_bf16 v[24:27], a[0:3], a[184:187], v[24:27]\n"
    "v_mfma_f32_16x16x32_bf16 v[24:27], a[4:7], a[188:191], v[24:27]\n"
    "v_mfma_f32_16x16x32_bf16 v[28:31], a[0:3], a[192:195], v[28:31]\n"
    "buffer_load_dwordx4 v237, s[52:55], s77 offen offset:3072 nt lds\n"
    "v_mfma_f32_16x16x32_bf16 v[28:31], a[4:7], a[196:199], v[28:31]\n"
    "v_mfma_f32_16x16x32_bf16 a[216:219], a[136:139], a[8:11], a[216:219]\n"
    "v_mfma_f32_16x16x32_bf16 a[216:219], a[140:143], a[12:15], a[216:219]\n"
    "v_mfma_f32_16x16x32_bf16 a[220:223], a[144:147], a[8:11], a[220:223]\n"
    "s_add_u32 m0, s80, 0x11000\n"
    "v_mfma_f32_16x16x32_bf16 a[220:223], a[148:151], a[12:15], a[220:223]\n"
    "buffer_load_dwordx4 v234, s[52:55], s78 offen offset:0 nt lds\n"
    "v_mfma_f32_16x16x32_bf16 a[224:227], a[152:155], a[8:11], a[224:227]\n"
    "v_mfma_f32_16x16x32_bf16 a[224:227], a[156:159], a[12:15], a[224:227]\n"
    "v_mfma_f32_16x16x32_bf16 a[228:231], a[160:163], a[8:11], a[228:231]\n"
    "v_mfma_f32_16x16x32_bf16 a[228:231], a[164:167], a[12:15], a[228:231]\n"
    "buffer_load_dwordx4 v235, s[52:55], s78 offen offset:1024 nt lds\n"
    "v_mfma_f32_16x16x32_bf16 v[32:35], a[8:11], a[168:171], v[32:35]\n"
    "v_mfma_f32_16x16x32_bf16 v[32:35], a[12:15], a[172:175], v[32:35]\n"
    "v_mfma_f32_16x16x32_bf16 v[36:39], a[8:11], a[176:179], v[36:39]\n"
    "v_mfma_f32_16x16x32_bf16 v[36:39], a[12:15], a[180:183], v[36:39]\n"
    "buffer_load_dwordx4 v236, s[52:55], s78 offen offset:2048 nt lds\n"
    "v_mfma_f32_16x16x32_bf16 v[40:43], a[8:11], a[184:187], v[40:43]\n"
    "v_mfma_f32_16x16x32_bf16 v[40:43], a[12:15], a[188:191], v[40:43]\n"
    "v_mfma_f32_16x16x32_bf16 v[44:47], a[8:11], a[192:195], v[44:47]\n"
    "v_mfma_f32_16x16x32_bf16 v[44:47], a[12:15], a[196:199], v[44:47]\n"
    "buffer_load_dwordx4 v237, s[52:55], s78 offen offset:3072 nt lds\n"
    "v_mfma_f32_16x16x32_bf16 a[232:235], a[136:139], a[16:19], a[232:235]\n"
    "v_mfma_f32_16x16x32_bf16 a[232:235], a[140:143], a[20:23], a[232:235]\n"
    "v_mfma_f32_16x16x32_bf16 a[236:239], a[144:147], a[16:19], a[236:239]\n"
    "v_mfma_f32_16x16x32_bf16 a[236:239], a[148:151], a[20:23], a[236:239]\n"
    "s_add_u32 s77, s77, s79\n"
    "v_mfma_f32_16x16x32_bf16 a[240:243], a[152:155], a[16:19], a[240:243]\n"
    "s_add_u32 s78, s78, s79\n"
    "v_mfma_f32_16x16x32_bf16 a[240:243], a[156:159], a[20:23], a[240:243]\n"
    "s_cmp_eq_u32 s68, 2\n"
    "s_cselect_b64 s[60:61], %6, %7\n"
    "s_cmp_lt_u32 s66, 28\n"
    "s_cselect_b32 s74, 0x1c0000, 0\n"
    "s_cmp_eq_u32 s71, 0\n"
    "s_cselect_b64 s[60:61], %2, s[60:61]\n"
    "s_cselect_b32 s62, 0x400000, s74\n"
    "v_mfma_f32_16x16x32_bf16 a[244:247], a[160:163], a[16:19], a[244:247]\n"
    "s_lshl_b32 s84, s66, 8\n"
    "s_lshl_b32 s73, s72, 7\n"
    "s_add_u32 s84, s84, s73\n"
    "s_lshl_b32 s73, s66, 7\n"
    "s_cmp_eq_u32 s71, 0\n"
    "s_cselect_b32 s84, s84, s73\n"
    "v_mfma_f32_16x16x32_bf16 a[244:247], a[164:167], a[20:23], a[244:247]\n"
    "s_mul_i32 s73, s83, s67\n"
    "s_lshl_b32 s73, s73, 6\n"
    "s_add_u32 s84, s84, s73\n"
    "v_mfma_f32_16x16x32_bf16 v[48:51], a[16:19], a[168:171], v[48:51]\n"
    "v_and_b32 v249, 15, v242\n"
    "v_mul_lo_u32 v249, v249, s83\n"
    "v_mfma_f32_16x16x32_bf16 v[48:51], a[20:23], a[172:175], v[48:51]\n"
    "buffer_load_dword a248, v249, s[60:63], s84 offen\n"
    "s_lshl_b32 s73, s83, 4\n"
    "s_add_u32 s84, s84, s73\n"
    "v_mfma_f32_16x16x32_bf16 v[52:55], a[16:19], a[176:179], v[52:55]\n"
    "v_mfma_f32_16x16x32_bf16 v[52:55], a[20:23], a[180:183], v[52:55]\n"
    "v_mfma_f32_16x16x32_bf16 v[56:59], a[16:19], a[184:187], v[56:59]\n"
    "v_mfma_f32_16x16x32_bf16 v[56:59], a[20:23], a[188:191], v[56:59]\n"
    "v_mfma_f32_16x16x32_bf16 v[60:63], a[16:19], a[192:195], v[60:63]\n"
    "v_mfma_f32_16x16x32_bf16 v[60:63], a[20:23], a[196:199], v[60:63]\n"
    "v_mfma_f32_16x16x32_bf16 v[64:67], a[136:139], a[24:27], v[64:67]\n"
    "v_mfma_f32_16x16x32_bf16 v[64:67], a[140:143], a[28:31], v[64:67]\n"
    "v_mfma_f32_16x16x32_bf16 v[68:71], a[144:147], a[24:27], v[68:71]\n"
    "v_mfma_f32_16x16x32_bf16 v[68:71], a[148:151], a[28:31], v[68:71]\n"
    "v_mfma_f32_16x16x32_bf16 v[72:75], a[152:155], a[24:27], v[72:75]\n"
    "v_mfma_f32_16x16x32_bf16 v[72:75], a[156:159], a[28:31], v[72:75]\n"
    "v_mfma_f32_16x16x32_bf16 v[76:79], a[160:163], a[24:27], v[76:79]\n"
    "v_mfma_f32_16x16x32_bf16 v[76:79], a[164:167], a[28:31], v[76:79]\n"
    "v_mfma_f32_16x16x32_bf16 v[80:83], a[24:27], a[168:171], v[80:83]\n"
    "v_add_u32 v228, 0x10000, v226\n"
    "v_mfma_f32_16x16x32_bf16 v[80:83], a[28:31], a[172:175], v[80:83]\n"
    "v_add_u32 v229, 0x10000, v227\n"
    "v_mfma_f32_16x16x32_bf16 v[84:87], a[24:27], a[176:179], v[84:87]\n"
    "v_mfma_f32_16x16x32_bf16 v[84:87], a[28:31], a[180:183], v[84:87]\n"
    "v_mfma_f32_16x16x32_bf16 v[88:91], a[24:27], a[184:187], v[88:91]\n"
    "s_waitcnt vmcnt(9)\n"
    "s_barrier\n"
    "v_mfma_f32_16x16x32_bf16 v[88:91], a[28:31], a[188:191], v[88:91]\n"
    "s_cmp_lg_u32 s68, 0\n"
    "s_cbranch_scc1 .Lw5_%=\n"
    "s_lshl_b32 s73, s66, 3\n"
    "s_add_u32 s73, s73, s67\n"
    "s_lshl_b32 s73, s73, 3\n"
    "s_add_u32 s73, s73, 0x1000\n"
    "v_mov_b32 v5, s73\n"
    "v_mov_b32 v6, s64\n"
    "v_mov_b32 v7, s65\n"
    "s_mov_b64 exec, 1\n"
    "buffer_store_dwordx2 v[6:7], v5, s[56:59], 0 offen sc1\n"
    "s_mov_b64 exec, -1\n"
    ".Lw5_%=:\n"
    "v_mfma_f32_16x16x32_bf16 v[92:95], a[24:27], a[192:195], v[92:95]\n"
    "ds_read_b64_tr_b16 a[72:73], v228 offset:0\n"
    "s_add_u32 m0, s80, 0x8000\n"
    "v_mfma_f32_16x16x32_bf16 v[92:95], a[28:31], a[196:199], v[92:95]\n"
    "buffer_load_dwordx4 v230, s[48:51], s75 offen offset:0 lds\n"
    "v_mfma_f32_16x16x32_bf16 v[96:99], a[136:139], a[32:35], v[96:99]\n"
    "ds_read_b64_tr_b16 a[74:75], v228 offset:256\n"
    "v_mfma_f32_16x16x32_bf16 v[96:99], a[140:143], a[36:39], v[96:99]\n"
    "ds_read_b64_tr_b16 a[76:77], v228 offset:4096\n"
    "v_mfma_f32_16x16x32_bf16 v[100:103], a[144:147], a[32:35], v[100:103]\n"
    "ds_read_b64_tr_b16 a[78:79], v228 offset:4352\n"
    "v_mfma_f32_16x16x32_bf16 v[100:103], a[148:151], a[36:39], v[100:103]\n"
    "buffer_load_dwordx4 v231, s[48:51], s75 offen offset:1024 lds\n"
    "v_mfma_f32_16x16x32_bf16 v[104:107], a[152:155], a[32:35], v[104:107]\n"
    "ds_read_b64_tr_b16 a[80:81], v229 offset:0\n"
    "v_mfma_f32_16x16x32_bf16 v[104:107], a[156:159], a[36:39], v[104:107]\n"
    "ds_read_b64_tr_b16 a[82:83], v229 offset:256\n"
    "v_mfma_f32_16x16x32_bf16 v[108:111], a[160:163], a[32:35], v[108:111]\n"
    "ds_read_b64_tr_b16 a[84:85], v229 offset:4096\n"
    "v_mfma_f32_16x16x32_bf16 v[108:111], a[164:167], a[36:39], v[108:111]\n"
    "buffer_load_dwordx4 v232, s[48:51], s75 offen offset:2048 lds\n"
    "v_mfma_f32_16x16x32_bf16 v[112:115], a[32:35], a[168:171], v[112:115]\n"
    "ds_read_b64_tr_b16 a[86:87], v229 offset:4352\n"
    "v_mfma_f32_16x16x32_bf16 v[112:115], a[36:39], a[172:175], v[112:115]\n"
    "ds_read_b64_tr_b16 a[88:89], v228 offset:8192\n"
    "v_mfma_f32_16x16x32_bf16 v[116:119], a[32:35], a[176:179], v[116:119]\n"
    "ds_read_b64_tr_b16 a[90:91], v228 offset:8448\n"
    "v_mfma_f32_16x16x32_bf16 v[116:119], a[36:39], a[180:183], v[116:119]\n"
    "buffer_load_dwordx4 v233, s[48:51], s75 offen offset:3072 lds\n"
    "v_mfma_f32_16x16x32_bf16 v[120:123], a[32:35], a[184:187], v[120:123]\n"
    "ds_read_b64_tr_b16 a[92:93], v228 offset:12288\n"
    "v_mfma_f32_16x16x32_bf16 v[120:123], a[36:39], a[188:191], v[120:123]\n"
    "ds_read_b64_tr_b16 a[94:95], v228 offset:12544\n"
    "v_mfma_f32_16x16x32_bf16 v[124:127], a[32:35], a[192:195], v[124:127]\n"
    "ds_read_b64_tr_b16 a[96:97], v229 offset:8192\n"
    "v_mfma_f32_16x16x32_bf16 v[124:127], a[36:39], a[196:199], v[124:127]\n"
    "ds_read_b64_tr_b16 a[98:99], v229 offset:8448\n"
    "s_add_u32 m0, s80, 0x9000\n"
    "v_mfma_f32_16x16x32_bf16 v[128:131], a[136:139], a[40:43], v[128:131]\n"
    "buffer_load_dwordx4 v230, s[48:51], s76 offen offset:0 lds\n"
    "v_mfma_f32_16x16x32_bf16 v[128:131], a[140:143], a[44:47], v[128:131]\n"
    "ds_read_b128 a[32:35], v224 offset:8192\n"
    "v_mfma_f32_16x16x32_bf16 v[132:135], a[144:147], a[40:43], v[132:135]\n"
    "ds_read_b128 a[36:39], v225 offset:8192\n"
    "v_mfma_f32_16x16x32_bf16 v[132:135], a[148:151], a[44:47], v[132:135]\n"
    "ds_read_b64_tr_b16 a[100:101], v229 offset:12288\n"
    "v_mfma_f32_16x16x32_bf16 v[136:139], a[152:155], a[40:43], v[136:139]\n"
    "buffer_load_dwordx4 v231, s[48:51], s76 offen offset:1024 lds\n"
    "v_mfma_f32_16x16x32_bf16 v[136:139], a[156:159], a[44:47], v[136:139]\n"
    "ds_read_b64_tr_b16 a[102:103], v229 offset:12544\n"
    "v_mfma_f32_16x16x32_bf16 v[140:143], a[160:163], a[40:43], v[140:143]\n"
    "ds_read_b64_tr_b16 a[104:105], v228 offset:16384\n"
    "v_mfma_f32_16x16x32_bf16 v[140:143], a[164:167], a[44:47], v[140:143]\n"
    "ds_read_b64_tr_b16 a[106:107], v228 offset:16640\n"
    "v_mfma_f32_16x16x32_bf16 v[144:147], a[40:43], a[168:171], v[144:147]\n"
    "buffer_load_dwordx4 v232, s[48:51], s76 offen offset:2048 lds\n"
    "v_mfma_f32_16x16x32_bf16 v[144:147], a[44:47], a[172:175], v[144:147]\n"
    "ds_read_b64_tr_b16 a[108:109], v228 offset:20480\n"
    "v_mfma_f32_16x16x32_bf16 v[148:151], a[40:43], a[176:179], v[148:151]\n"
    "ds_read_b64_tr_b16 a[110:111], v228 offset:20736\n"
    "v_mfma_f32_16x16x32_bf16 v[148:151], a[44:47], a[180:183], v[148:151]\n"
    "ds_read_b64_tr_b16 a[112:113], v229 offset:16384\n"
    "v_mfma_f32_16x16x32_bf16 v[152:155], a[40:43], a[184:187], v[152:155]\n"
    "buffer_load_dwordx4 v233, s[48:51], s76 offen offset:3072 lds\n"
    "v_mfma_f32_16x16x32_bf16 v[152:155], a[44:47], a[188:191], v[152:155]\n"
    "ds_read_b64_tr_b16 a[114:115], v229 offset:16640\n"
    "v_mfma_f32_16x16x32_bf16 v[156:159], a[40:43], a[192:195], v[156:159]\n"
    "ds_read_b64_tr_b16 a[116:117], v229 offset:20480\n"
    "v_mfma_f32_16x16x32_bf16 v[156:159], a[44:47], a[196:199], v[156:159]\n"
    "ds_read_b64_tr_b16 a[118:119], v229 offset:20736\n"
    "v_mfma_f32_16x16x32_bf16 v[160:163], a[136:139], a[48:51], v[160:163]\n"
    "ds_read_b64_tr_b16 a[120:121], v228 offset:24576\n"
    "s_add_u32 s75, s75, 0x80\n"
    "v_mfma_f32_16x16x32_bf16 v[160:163], a[140:143], a[52:55], v[160:163]\n"
    "ds_read_b64_tr_b16 a[122:123], v228 offset:24832\n"
    "s_add_u32 s76, s76, 0x80\n"
    "v_mfma_f32_16x16x32_bf16 v[164:167], a[144:147], a[48:51], v[164:167]\n"
    "ds_read_b128 a[40:43], v224 offset:10240\n"
    "v_mfma_f32_16x16x32_bf16 v[164:167], a[148:151], a[52:55], v[164:167]\n"
    "ds_read_b128 a[44:47], v225 offset:10240\n"
    "v_mfma_f32_16x16x32_bf16 v[168:171], a[152:155], a[48:51], v[168:171]\n"
    "ds_read_b64_tr_b16 a[124:125], v228 offset:28672\n"
    "v_mfma_f32_16x16x32_bf16 v[168:171], a[156:159], a[52:55], v[168:171]\n"
    "ds_read_b64_tr_b16 a[126:127], v228 offset:28928\n"
    "v_mfma_f32_16x16x32_bf16 v[172:175], a[160:163], a[48:51], v[172:175]\n"
    "ds_read_b64_tr_b16 a[128:129], v229 offset:24576\n"
    "v_mfma_f32_16x16x32_bf16 v[172:175], a[164:167], a[52:55], v[172:175]\n"
    "ds_read_b64_tr_b16 a[130:131], v229 offset:24832\n"
    "v_mfma_f32_16x16x32_bf16 v[176:179], a[48:51], a[168:171], v[176:179]\n"
    "ds_read_b64_tr_b16 a[132:133], v229 offset:28672\n"
    "v_mfma_f32_16x16x32_bf16 v[176:179], a[52:55], a[172:175], v[176:179]\n"
    "ds_read_b64_tr_b16 a[134:135], v229 offset:28928\n"
    "v_mfma_f32_16x16x32_bf16 v[180:183], a[48:51], a[176:179], v[180:183]\n"
    "ds_read_b128 a[56:59], v224 offset:14336\n"
    "v_mfma_f32_16x16x32_bf16 v[180:183], a[52:55], a[180:183], v[180:183]\n"
    "ds_read_b128 a[60:63], v225 offset:14336\n"
    "v_mfma_f32_16x16x32_bf16 v[184:187], a[48:51], a[184:187], v[184:187]\n"
    "ds_read_b128 a[0:3], v224 offset:0\n"
    "v_mfma_f32_16x16x32_bf16 v[184:187], a[52:55], a[188:191], v[184:187]\n"
    "ds_read_b128 a[4:7], v225 offset:0\n"
    "v_mfma_f32_16x16x32_bf16 v[188:191], a[48:51], a[192:195], v[188:191]\n"
    "ds_read_b128 a[8:11], v224 offset:2048\n"
    "v_mfma_f32_16x16x32_bf16 v[188:191], a[52:55], a[196:199], v[188:191]\n"
    "ds_read_b128 a[12:15], v225 offset:2048\n"
    "v_mfma_f32_16x16x32_bf16 v[192:195], a[136:139], a[64:67], v[192:195]\n"
    "ds_read_b128 a[48:51], v224 offset:12288\n"
    "v_mfma_f32_16x16x32_bf16 v[192:195], a[140:143], a[68:71], v[192:195]\n"
    "ds_read_b128 a[16:19], v224 offset:4096\n"
    "v_mfma_f32_16x16x32_bf16 v[196:199], a[144:147], a[64:67], v[196:199]\n"
    "ds_read_b128 a[52:55], v225 offset:12288\n"
    "v_mfma_f32_16x16x32_bf16 v[196:199], a[148:151], a[68:71], v[196:199]\n"
    "ds_read_b128 a[20:23], v225 offset:4096\n"
    "v_mfma_f32_16x16x32_bf16 v[200:203], a[152:155], a[64:67], v[200:203]\n"
    "ds_read_b128 a[24:27], v224 offset:6144\n"
    "v_mfma_f32_16x16x32_bf16 v[200:203], a[156:159], a[68:71], v[200:203]\n"
    "ds_read_b128 a[28:31], v225 offset:6144\n"
    "v_mfma_f32_16x16x32_bf16 v[204:207], a[160:163], a[64:67], v[204:207]\n"
    "v_mfma_f32_16x16x32_bf16 v[204:207], a[164:167], a[68:71], v[204:207]\n"
    "v_mfma_f32_16x16x32_bf16 v[208:211], a[64:67], a[168:171], v[208:211]\n"
    "v_mfma_f32_16x16x32_bf16 v[208:211], a[68:71], a[172:175], v[208:211]\n"
    "v_mfma_f32_16x16x32_bf16 v[212:215], a[64:67], a[176:179], v[212:215]\n"
    "v_mfma_f32_16x16x32_bf16 v[212:215], a[68:71], a[180:183], v[212:215]\n"
    "v_mfma_f32_16x16x32_bf16 v[216:219], a[64:67], a[184:187], v[216:219]\n"
    "v_mfma_f32_16x16x32_bf16 v[216:219], a[68:71], a[188:191], v[216:219]\n"
    "v_mfma_f32_16x16x32_bf16 v[220:223], a[64:67], a[192:195], v[220:223]\n"
    "v_mfma_f32_16x16x32_bf16 v[220:223], a[68:71], a[196:199], v[220:223]\n"
    "s_waitcnt lgkmcnt(0)\n"
    "v_mfma_f32_16x16x32_bf16 a[200:203], a[72:75], a[0:3], a[200:203]\n"
    "v_mfma_f32_16x16x32_bf16 a[200:203], a[76:79], a[4:7], a[200:203]\n"
    "s_add_u32 m0, s80, 0x18000\n"
    "v_mfma_f32_16x16x32_bf16 a[204:207], a[80:83], a[0:3], a[204:207]\n"
    "buffer_load_dwordx4 v234, s[52:55], s77 offen offset:0 nt lds\n"
    "v_mfma_f32_16x16x32_bf16 a[204:207], a[84:87], a[4:7], a[204:207]\n"
    "v_mfma_f32_16x16x32_bf16 a[208:211], a[88:91], a[0:3], a[208:211]\n"
    "v_mfma_f32_16x16x32_bf16 a[208:211], a[92:95], a[4:7], a[208:211]\n"
    "v_mfma_f32_16x16x32_bf16 a[212:215], a[96:99], a[0:3], a[212:215]\n"
    "buffer_load_dwordx4 v235, s[52:55], s77 offen offset:1024 nt lds\n"
    "v_mfma_f32_16x16x32_bf16 a[212:215], a[100:103], a[4:7], a[212:215]\n"
    "v_mfma_f32_16x16x32_bf16 v[16:19], a[0:3], a[104:107], v[16:19]\n"
    "v_mfma_f32_16x16x32_bf16 v[16:19], a[4:7], a[108:111], v[16:19]\n"
    "v_mfma_f32_16x16x32_bf16 v[20:23], a[0:3], a[112:115], v[20:23]\n"
    "buffer_load_dwordx4 v236, s[52:55], s77 offen offset:2048 nt lds\n"
    "v_mfma_f32_16x16x32_bf16 v[20:23], a[4:7], a[116:119], v[20:23]\n"
    "v_mfma_f32_16x16x32_bf16 v[24:27], a[0:3], a[120:123], v[24:27]\n"
    "v_mfma_f32_16x16x32_bf16 v[24:27], a[4:7], a[124:127], v[24:27]\n"
    "v_mfma_f32_16x16x32_bf16 v[28:31], a[0:3], a[128:131], v[28:31]\n"
    "buffer_load_dwordx4 v237, s[52:55], s77 offen offset:3072 nt lds\n"
    "v_mfma_f32_16x16x32_bf16 v[28:31], a[4:7], a[132:135], v[28:31]\n"
    "v_mfma_f32_16x16x32_bf16 a[216:219], a[72:75], a[8:11], a[216:219]\n"
    "v_mfma_f32_16x16x32_bf16 a[216:219], a[76:79], a[12:15], a[216:219]\n"
    "v_mfma_f32_16x16x32_bf16 a[220:223], a[80:83], a[8:11], a[220:223]\n"
    "s_add_u32 m0, s80, 0x19000\n"
    "v_mfma_f32_16x16x32_bf16 a[220:223], a[84:87], a[12:15], a[220:223]\n"
    "buffer_load_dwordx4 v234, s[52:55], s78 offen offset:0 nt lds\n"
    "v_mfma_f32_16x16x32_bf16 a[224:227], a[88:91], a[8:11], a[224:227]\n"
    "v_mfma_f32_16x16x32_bf16 a[224:227], a[92:95], a[12:15], a[224:227]\n"
    "v_mfma_f32_16x16x32_bf16 a[228:231], a[96:99], a[8:11], a[228:231]\n"
    "v_mfma_f32_16x16x32_bf16 a[228:231], a[100:103], a[12:15], a[228:231]\n"
    "buffer_load_dwordx4 v235, s[52:55], s78 offen offset:1024 nt lds\n"
    "v_mfma_f32_16x16x32_bf16 v[32:35], a[8:11], a[104:107], v[32:35]\n"
    "v_mfma_f32_16x16x32_bf16 v[32:35], a[12:15], a[108:111], v[32:35]\n"
    "v_mfma_f32_16x16x32_bf16 v[36:39], a[8:11], a[112:115], v[36:39]\n"
    "v_mfma_f32_16x16x32_bf16 v[36:39], a[12:15], a[116:119], v[36:39]\n"
    "buffer_load_dwordx4 v236, s[52:55], s78 offen offset:2048 nt lds\n"
    "v_mfma_f32_16x16x32_bf16 v[40:43], a[8:11], a[120:123], v[40:43]\n"
    "v_mfma_f32_16x16x32_bf16 v[40:43], a[12:15], a[124:127], v[40:43]\n"
    "v_mfma_f32_16x16x32_bf16 v[44:47], a[8:11], a[128:131], v[44:47]\n"
    "v_mfma_f32_16x16x32_bf16 v[44:47], a[12:15], a[132:135], v[44:47]\n"
    "buffer_load_dwordx4 v237, s[52:55], s78 offen offset:3072 nt lds\n"
    "v_mfma_f32_16x16x32_bf16 a[232:235], a[72:75], a[16:19], a[232:235]\n"
    "v_mfma_f32_16x16x32_bf16 a[232:235], a[76:79], a[20:23], a[232:235]\n"
    "v_mfma_f32_16x16x32_bf16 a[236:239], a[80:83], a[16:19], a[236:239]\n"
    "v_mfma_f32_16x16x32_bf16 a[236:239], a[84:87], a[20:23], a[236:239]\n"
    "s_add_u32 s77, s77, s79\n"
    "v_mfma_f32_16x16x32_bf16 a[240:243], a[88:91], a[16:19], a[240:243]\n"
    "s_add_u32 s78, s78, s79\n"
    "v_mfma_f32_16x16x32_bf16 a[240:243], a[92:95], a[20:23], a[240:243]\n"
    "buffer_load_dword a249, v249, s[60:63], s84 offen\n"
    "s_lshl_b32 s73, s83, 4\n"
    "s_add_u32 s84, s84, s73\n"
    "v_mfma_f32_16x16x32_bf16 a[244:247], a[96:99], a[16:19], a[244:247]\n"
    "v_mfma_f32_16x16x32_bf16 a[244:247], a[100:103], a[20:23], a[244:247]\n"
    "v_mfma_f32_16x16x32_bf16 v[48:51], a[16:19], a[104:107], v[48:51]\n"
    "v_mfma_f32_16x16x32_bf16 v[48:51], a[20:23], a[108:111], v[48:51]\n"
    "v_mfma_f32_16x16x32_bf16 v[52:55], a[16:19], a[112:115], v[52:55]\n"
    "v_mfma_f32_16x16x32_bf16 v[52:55], a[20:23], a[116:119], v[52:55]\n"
    "v_mfma_f32_16x16x32_bf16 v[56:59], a[16:19], a[120:123], v[56:59]\n"
    "v_mfma_f32_16x16x32_bf16 v[56:59], a[20:23], a[124:127], v[56:59]\n"
    "v_mfma_f32_16x16x32_bf16 v[60:63], a[16:19], a[128:131], v[60:63]\n"
    "v_mfma_f32_16x16x32_bf16 v[60:63], a[20:23], a[132:135], v[60:63]\n"
    "v_mfma_f32_16x16x32_bf16 v[64:67], a[72:75], a[24:27], v[64:67]\n"
    "v_mfma_f32_16x16x32_bf16 v[64:67], a[76:79], a[28:31], v[64:67]\n"
    "v_mfma_f32_16x16x32_bf16 v[68:71], a[80:83], a[24:27], v[68:71]\n"
    "v_mfma_f32_16x16x32_bf16 v[68:71], a[84:87], a[28:31], v[68:71]\n"
    "v_mfma_f32_16x16x32_bf16 v[72:75], a[88:91], a[24:27], v[72:75]\n"
    "v_mfma_f32_16x16x32_bf16 v[72:75], a[92:95], a[28:31], v[72:75]\n"
    "v_mfma_f32_16x16x32_bf16 v[76:79], a[96:99], a[24:27], v[76:79]\n"
    "v_mfma_f32_16x16x32_bf16 v[76:79], a[100:103], a[28:31], v[76:79]\n"
    "v_mfma_f32_16x16x32_bf16 v[80:83], a[24:27], a[104:107], v[80:83]\n"
    "v_add_u32 v228, 0x0, v226\n"
    "v_mfma_f32_16x16x32_bf16 v[80:83], a[28:31], a[108:111], v[80:83]\n"
    "v_add_u32 v229, 0x0, v227\n"
    "v_mfma_f32_16x16x32_bf16 v[84:87], a[24:27], a[112:115], v[84:87]\n"
    "v_mfma_f32_16x16x32_bf16 v[84:87], a[28:31], a[116:119], v[84:87]\n"
    "v_mfma_f32_16x16x32_bf16 v[88:91], a[24:27], a[120:123], v[88:91]\n"
    "s_waitcnt vmcnt(9)\n"
    "s_barrier\n"
    "v_mfma_f32_16x16x32_bf16 v[88:91], a[28:31], a[124:127], v[88:91]\n"
    "ds_read_b64_tr_b16 a[136:137], v228 offset:0\n"
    "s_add_u32 m0, s80, 0x0\n"
    "v_mfma_f32_16x16x32_bf16 v[92:95], a[24:27], a[128:131], v[92:95]\n"
    "buffer_load_dwordx4 v230, s[48:51], s75 offen offset:0 lds\n"
    "v_mfma_f32_16x16x32_bf16 v[92:95], a[28:31], a[132:135], v[92:95]\n"
    "ds_read_b64_tr_b16 a[138:139], v228 offset:256\n"
    "v_mfma_f32_16x16x32_bf16 v[96:99], a[72:75], a[32:35], v[96:99]\n"
    "ds_read_b64_tr_b16 a[140:141], v228 offset:4096\n"
    "v_mfma_f32_16x16x32_bf16 v[96:99], a[76:79], a[36:39], v[96:99]\n"
    "ds_read_b64_tr_b16 a[142:143], v228 offset:4352\n"
    "v_mfma_f32_16x16x32_bf16 v[100:103], a[80:83], a[32:35], v[100:103]\n"
    "buffer_load_dwordx4 v231, s[48:51], s75 offen offset:1024 lds\n"
    "v_mfma_f32_16x16x32_bf16 v[100:103], a[84:87], a[36:39], v[100:103]\n"
    "ds_read_b64_tr_b16 a[144:145], v229 offset:0\n"
    "v_mfma_f32_16x16x32_bf16 v[104:107], a[88:91], a[32:35], v[104:107]\n"
    "ds_read_b64_tr_b16 a[146:147], v229 offset:256\n"
    "v_mfma_f32_16x16x32_bf16 v[104:107], a[92:95], a[36:39], v[104:107]\n"
    "ds_read_b64_tr_b16 a[148:149], v229 offset:4096\n"
    "v_mfma_f32_16x16x32_bf16 v[108:111], a[96:99], a[32:35], v[108:111]\n"
    "buffer_load_dwordx4 v232, s[48:51], s75 offen offset:2048 lds\n"
    "v_mfma_f32_16x16x32_bf16 v[108:111], a[100:103], a[36:39], v[108:111]\n"
    "ds_read_b64_tr_b16 a[150:151], v229 offset:4352\n"
    "v_mfma_f32_16x16x32_bf16 v[112:115], a[32:35], a[104:107], v[112:115]\n"
    "ds_read_b64_tr_b16 a[152:153], v228 offset:8192\n"
    "v_mfma_f32_16x16x32_bf16 v[112:115], a[36:39], a[108:111], v[112:115]\n"
    "ds_read_b64_tr_b16 a[154:155], v228 offset:8448\n"
    "v_mfma_f32_16x16x32_bf16 v[116:119], a[32:35], a[112:115], v[116:119]\n"
    "buffer_load_dwordx4 v233, s[48:51], s75 offen offset:3072 lds\n"
    "v_mfma_f32_16x16x32_bf16 v[116:119], a[36:39], a[116:119], v[116:119]\n"
    "ds_read_b64_tr_b16 a[156:157], v228 offset:12288\n"
    "v_mfma_f32_16x16x32_bf16 v[120:123], a[32:35], a[120:123], v[120:123]\n"
    "ds_read_b64_tr_b16 a[158:159], v228 offset:12544\n"
    "v_mfma_f32_16x16x32_bf16 v[120:123], a[36:39], a[124:127], v[120:123]\n"
    "ds_read_b64_tr_b16 a[160:161], v229 offset:8192\n"
    "v_mfma_f32_16x16x32_bf16 v[124:127], a[32:35], a[128:131], v[124:127]\n"
    "ds_read_b64_tr_b16 a[162:163], v229 offset:8448\n"
    "s_add_u32 m0, s80, 0x1000\n"
    "v_mfma_f32_16x16x32_bf16 v[124:127], a[36:39], a[132:135], v[124:127]\n"
    "buffer_load_dwordx4 v230, s[48:51], s76 offen offset:0 lds\n"
    "v_mfma_f32_16x16x32_bf16 v[128:131], a[72:75], a[40:43], v[128:131]\n"
    "ds_read_b128 a[32:35], v224 offset:40960\n"
    "v_mfma_f32_16x16x32_bf16 v[128:131], a[76:79], a[44:47], v[128:131]\n"
    "ds_read_b64_tr_b16 a[164:165], v229 offset:12288\n"
    "v_mfma_f32_16x16x32_bf16 v[132:135], a[80:83], a[40:43], v[132:135]\n"
    "ds_read_b128 a[36:39], v225 offset:40960\n"
    "v_mfma_f32_16x16x32_bf16 v[132:135], a[84:87], a[44:47], v[132:135]\n"
    "buffer_load_dwordx4 v231, s[48:51], s76 offen offset:1024 lds\n"
    "v_mfma_f32_16x16x32_bf16 v[136:139], a[88:91], a[40:43], v[136:139]\n"
    "ds_read_b64_tr_b16 a[166:167], v229 offset:12544\n"
    "v_mfma_f32_16x16x32_bf16 v[136:139], a[92:95], a[44:47], v[136:139]\n"
    "ds_read_b64_tr_b16 a[168:169], v228 offset:16384\n"
    "v_mfma_f32_16x16x32_bf16 v[140:143], a[96:99], a[40:43], v[140:143]\n"
    "ds_read_b64_tr_b16 a[170:171], v228 offset:16640\n"
    "v_mfma_f32_16x16x32_bf16 v[140:143], a[100:103], a[44:47], v[140:143]\n"
    "buffer_load_dwordx4 v232, s[48:51], s76 offen offset:2048 lds\n"
    "v_mfma_f32_16x16x32_bf16 v[144:147], a[40:43], a[104:107], v[144:147]\n"
    "ds_read_b64_tr_b16 a[172:173], v228 offset:20480\n"
    "v_mfma_f32_16x16x32_bf16 v[144:147], a[44:47], a[108:111], v[144:147]\n"
    "ds_read_b64_tr_b16 a[174:175], v228 offset:20736\n"
    "v_mfma_f32_16x16x32_bf16 v[148:151], a[40:43], a[112:115], v[148:151]\n"
    "ds_read_b64_tr_b16 a[176:177], v229 offset:16384\n"
    "v_mfma_f32_16x16x32_bf16 v[148:151], a[44:47], a[116:119], v[148:151]\n"
    "buffer_load_dwordx4 v233, s[48:51], s76 offen offset:3072 lds\n"
    "v_mfma_f32_16x16x32_bf16 v[152:155], a[40:43], a[120:123], v[152:155]\n"
    "ds_read_b64_tr_b16 a[178:179], v229 offset:16640\n"
    "v_mfma_f32_16x16x32_bf16 v[152:155], a[44:47], a[124:127], v[152:155]\n"
    "ds_read_b64_tr_b16 a[180:181], v229 offset:20480\n"
    "v_mfma_f32_16x16x32_bf16 v[156:159], a[40:43], a[128:131], v[156:159]\n"
    "ds_read_b64_tr_b16 a[182:183], v229 offset:20736\n"
    "v_mfma_f32_16x16x32_bf16 v[156:159], a[44:47], a[132:135], v[156:159]\n"
    "ds_read_b64_tr_b16 a[184:185], v228 offset:24576\n"
    "s_add_u32 s75, s75, 0x80\n"
    "v_mfma_f32_16x16x32_bf16 v[160:163], a[72:75], a[48:51], v[160:163]\n"
    "ds_read_b64_tr_b16 a[186:187], v228 offset:24832\n"
    "s_add_u32 s76, s76, 0x80\n"
    "v_mfma_f32_16x16x32_bf16 v[160:163], a[76:79], a[52:55], v[160:163]\n"
    "ds_read_b128 a[40:43], v224 offset:43008\n"
    "v_mfma_f32_16x16x32_bf16 v[164:167], a[80:83], a[48:51], v[164:167]\n"
    "ds_read_b128 a[44:47], v225 offset:43008\n"
    "v_mfma_f32_16x16x32_bf16 v[164:167], a[84:87], a[52:55], v[164:167]\n"
    "ds_read_b64_tr_b16 a[188:189], v228 offset:28672\n"
    "v_mfma_f32_16x16x32_bf16 v[168:171], a[88:91], a[48:51], v[168:171]\n"
    "ds_read_b64_tr_b16 a[190:191], v228 offset:28928\n"
    "v_mfma_f32_16x16x32_bf16 v[168:171], a[92:95], a[52:55], v[168:171]\n"
    "ds_read_b64_tr_b16 a[192:193], v229 offset:24576\n"
    "v_mfma_f32_16x16x32_bf16 v[172:175], a[96:99], a[48:51], v[172:175]\n"
    "ds_read_b64_tr_b16 a[194:195], v229 offset:24832\n"
    "v_mfma_f32_16x16x32_bf16 v[172:175], a[100:103], a[52:55], v[172:175]\n"
    "ds_read_b64_tr_b16 a[196:197], v229 offset:28672\n"
    "v_mfma_f32_16x16x32_bf16 v[176:179], a[48:51], a[104:107], v[176:179]\n"
    "ds_read_b64_tr_b16 a[198:199], v229 offset:28928\n"
    "v_mfma_f32_16x16x32_bf16 v[176:179], a[52:55], a[108:111], v[176:179]\n"
    "ds_read_b128 a[64:67], v224 offset:47104\n"
    "v_mfma_f32_16x16x32_bf16 v[180:183], a[48:51], a[112:115], v[180:183]\n"
    "ds_read_b128 a[68:71], v225 offset:47104\n"
    "v_mfma_f32_16x16x32_bf16 v[180:183], a[52:55], a[116:119], v[180:183]\n"
    "ds_read_b128 a[0:3], v224 offset:32768\n"
    "v_mfma_f32_16x16x32_bf16 v[184:187], a[48:51], a[120:123], v[184:187]\n"
    "ds_read_b128 a[4:7], v225 offset:32768\n"
    "v_mfma_f32_16x16x32_bf16 v[184:187], a[52:55], a[124:127], v[184:187]\n"
    "ds_read_b128 a[8:11], v224 offset:34816\n"
    "v_mfma_f32_16x16x32_bf16 v[188:191], a[48:51], a[128:131], v[188:191]\n"
    "ds_read_b128 a[12:15], v225 offset:34816\n"
    "v_mfma_f32_16x16x32_bf16 v[188:191], a[52:55], a[132:135], v[188:191]\n"
    "ds_read_b128 a[16:19], v224 offset:36864\n"
    "v_mfma_f32_16x16x32_bf16 v[192:195], a[72:75], a[56:59], v[192:195]\n"
    "ds_read_b128 a[48:51], v224 offset:45056\n"
    "v_mfma_f32_16x16x32_bf16 v[192:195], a[76:79], a[60:63], v[192:195]\n"
    "ds_read_b128 a[20:23], v225 offset:36864\n"
    "v_mfma_f32_16x16x32_bf16 v[196:199], a[80:83], a[56:59], v[196:199]\n"
    "ds_read_b128 a[52:55], v225 offset:45056\n"
    "v_mfma_f32_16x16x32_bf16 v[196:199], a[84:87], a[60:63], v[196:199]\n"
    "ds_read_b128 a[24:27], v224 offset:38912\n"
    "v_mfma_f32_16x16x32_bf16 v[200:203], a[88:91], a[56:59], v[200:203]\n"
    "ds_read_b128 a[28:31], v225 offset:38912\n"
    "v_mfma_f32_16x16x32_bf16 v[200:203], a[92:95], a[60:63], v[200:203]\n"
    "v_mfma_f32_16x16x32_bf16 v[204:207], a[96:99], a[56:59], v[204:207]\n"
    "v_mfma_f32_16x16x32_bf16 v[204:207], a[100:103], a[60:63], v[204:207]\n"
    "v_mfma_f32_16x16x32_bf16 v[208:211], a[56:59], a[104:107], v[208:211]\n"
    "v_mfma_f32_16x16x32_bf16 v[208:211], a[60:63], a[108:111], v[208:211]\n"
    "v_mfma_f32_16x16x32_bf16 v[212:215], a[56:59], a[112:115], v[212:215]\n"
    "v_mfma_f32_16x16x32_bf16 v[212:215], a[60:63], a[116:119], v[212:215]\n"
    "v_mfma_f32_16x16x32_bf16 v[216:219], a[56:59], a[120:123], v[216:219]\n"
    "v_mfma_f32_16x16x32_bf16 v[216:219], a[60:63], a[124:127], v[216:219]\n"
    "v_mfma_f32_16x16x32_bf16 v[220:223], a[56:59], a[128:131], v[220:223]\n"
    "v_mfma_f32_16x16x32_bf16 v[220:223], a[60:63], a[132:135], v[220:223]\n"
    "s_waitcnt lgkmcnt(0)\n"
    "v_mfma_f32_16x16x32_bf16 a[200:203], a[136:139], a[0:3], a[200:203]\n"
    "v_mfma_f32_16x16x32_bf16 a[200:203], a[140:143], a[4:7], a[200:203]\n"
    "s_add_u32 m0, s80, 0x20000\n"
    "v_mfma_f32_16x16x32_bf16 a[204:207], a[144:147], a[0:3], a[204:207]\n"
    "buffer_load_dwordx4 v234, s[52:55], s77 offen offset:0 nt lds\n"
    "v_mfma_f32_16x16x32_bf16 a[204:207], a[148:151], a[4:7], a[204:207]\n"
    "v_mfma_f32_16x16x32_bf16 a[208:211], a[152:155], a[0:3], a[208:211]\n"
    "v_mfma_f32_16x16x32_bf16 a[208:211], a[156:159], a[4:7], a[208:211]\n"
    "v_mfma_f32_16x16x32_bf16 a[212:215], a[160:163], a[0:3], a[212:215]\n"
    "buffer_load_dwordx4 v235, s[52:55], s77 offen offset:1024 nt lds\n"
    "v_mfma_f32_16x16x32_bf16 a[212:215], a[164:167], a[4:7], a[212:215]\n"
    "v_mfma_f32_16x16x32_bf16 v[16:19], a[0:3], a[168:171], v[16:19]\n"
    "v_mfma_f32_16x16x32_bf16 v[16:19], a[4:7], a[172:175], v[16:19]\n"
    "v_mfma_f32_16x16x32_bf16 v[20:23], a[0:3], a[176:179], v[20:23]\n"
    "buffer_load_dwordx4 v236, s[52:55], s77 offen offset:2048 nt lds\n"
    "v_mfma_f32_16x16x32_bf16 v[20:23], a[4:7], a[180:183], v[20:23]\n"
    "v_mfma_f32_16x16x32_bf16 v[24:27], a[0:3], a[184:187], v[24:27]\n"
    "v_mfma_f32_16x16x32_bf16 v[24:27], a[4:7], a[188:191], v[24:27]\n"
    "v_mfma_f32_16x16x32_bf16 v[28:31], a[0:3], a[192:195], v[28:31]\n"
    "buffer_load_dwordx4 v237, s[52:55], s77 offen offset:3072 nt lds\n"
    "v_mfma_f32_16x16x32_bf16 v[28:31], a[4:7], a[196:199], v[28:31]\n"
    "v_mfma_f32_16x16x32_bf16 a[216:219], a[136:139], a[8:11], a[216:219]\n"
    "v_mfma_f32_16x16x32_bf16 a[216:219], a[140:143], a[12:15], a[216:219]\n"
    "v_mfma_f32_16x16x32_bf16 a[220:223], a[144:147], a[8:11], a[220:223]\n"
    "s_add_u32 m0, s80, 0x21000\n"
    "v_mfma_f32_16x16x32_bf16 a[220:223], a[148:151], a[12:15], a[220:223]\n"
    "buffer_load_dwordx4 v234, s[52:55], s78 offen offset:0 nt lds\n"
    "v_mfma_f32_16x16x32_bf16 a[224:227], a[152:155], a[8:11], a[224:227]\n"
    "v_mfma_f32_16x16x32_bf16 a[224:227], a[156:159], a[12:15], a[224:227]\n"
    "v_mfma_f32_16x16x32_bf16 a[228:231], a[160:163], a[8:11], a[228:231]\n"
    "v_mfma_f32_16x16x32_bf16 a[228:231], a[164:167], a[12:15], a[228:231]\n"
    "buffer_load_dwordx4 v235, s[52:55], s78 offen offset:1024 nt lds\n"
    "v_mfma_f32_16x16x32_bf16 v[32:35], a[8:11], a[168:171], v[32:35]\n"
    "v_mfma_f32_16x16x32_bf16 v[32:35], a[12:15], a[172:175], v[32:35]\n"
    "v_mfma_f32_16x16x32_bf16 v[36:39], a[8:11], a[176:179], v[36:39]\n"
    "v_mfma_f32_16x16x32_bf16 v[36:39], a[12:15], a[180:183], v[36:39]\n"
    "buffer_load_dwordx4 v236, s[52:55], s78 offen offset:2048 nt lds\n"
    "v_mfma_f32_16x16x32_bf16 v[40:43], a[8:11], a[184:187], v[40:43]\n"
    "v_mfma_f32_16x16x32_bf16 v[40:43], a[12:15], a[188:191], v[40:43]\n"
    "v_mfma_f32_16x16x32_bf16 v[44:47], a[8:11], a[192:195], v[44:47]\n"
    "v_mfma_f32_16x16x32_bf16 v[44:47], a[12:15], a[196:199], v[44:47]\n"
    "buffer_load_dwordx4 v237, s[52:55], s78 offen offset:3072 nt lds\n"
    "v_mfma_f32_16x16x32_bf16 a[232:235], a[136:139], a[16:19], a[232:235]\n"
    "v_mfma_f32_16x16x32_bf16 a[232:235], a[140:143], a[20:23], a[232:235]\n"
    "v_mfma_f32_16x16x32_bf16 a[236:239], a[144:147], a[16:19], a[236:239]\n"
    "v_mfma_f32_16x16x32_bf16 a[236:239], a[148:151], a[20:23], a[236:239]\n"
    "s_add_u32 s77, s77, s79\n"
    "v_mfma_f32_16x16x32_bf16 a[240:243], a[152:155], a[16:19], a[240:243]\n"
    "s_add_u32 s78, s78, s79\n"
    "v_mfma_f32_16x16x32_bf16 a[240:243], a[156:159], a[20:23], a[240:243]\n"
    "buffer_load_dword a250, v249, s[60:63], s84 offen\n"
    "s_lshl_b32 s73, s83, 4\n"
    "s_add_u32 s84, s84, s73\n"
    "v_mfma_f32_16x16x32_bf16 a[244:247], a[160:163], a[16:19], a[244:247]\n"
    "v_mfma_f32_16x16x32_bf16 a[244:247], a[164:167], a[20:23], a[244:247]\n"
    "v_mfma_f32_16x16x32_bf16 v[48:51], a[16:19], a[168:171], v[48:51]\n"
    "v_mfma_f32_16x16x32_bf16 v[48:51], a[20:23], a[172:175], v[48:51]\n"
    "v_mfma_f32_16x16x32_bf16 v[52:55], a[16:19], a[176:179], v[52:55]\n"
    "v_mfma_f32_16x16x32_bf16 v[52:55], a[20:23], a[180:183], v[52:55]\n"
    "v_mfma_f32_16x16x32_bf16 v[56:59], a[16:19], a[184:187], v[56:59]\n"
    "v_mfma_f32_16x16x32_bf16 v[56:59], a[20:23], a[188:191], v[56:59]\n"
    "v_mfma_f32_16x16x32_bf16 v[60:63], a[16:19], a[192:195], v[60:63]\n"
    "v_mfma_f32_16x16x32_bf16 v[60:63], a[20:23], a[196:199], v[60:63]\n"
    "v_mfma_f32_16x16x32_bf16 v[64:67], a[136:139], a[24:27], v[64:67]\n"
    "v_mfma_f32_16x16x32_bf16 v[64:67], a[140:143], a[28:31], v[64:67]\n"
    "v_mfma_f32_16x16x32_bf16 v[68:71], a[144:147], a[24:27], v[68:71]\n"
    "v_mfma_f32_16x16x32_bf16 v[68:71], a[148:151], a[28:31], v[68:71]\n"
    "v_mfma_f32_16x16x32_bf16 v[72:75], a[152:155], a[24:27], v[72:75]\n"
    "v_mfma_f32_16x16x32_bf16 v[72:75], a[156:159], a[28:31], v[72:75]\n"
    "v_mfma_f32_16x16x32_bf16 v[76:79], a[160:163], a[24:27], v[76:79]\n"
    "v_mfma_f32_16x16x32_bf16 v[76:79], a[164:167], a[28:31], v[76:79]\n"
    "v_mfma_f32_16x16x32_bf16 v[80:83], a[24:27], a[168:171], v[80:83]\n"
    "v_add_u32 v228, 0x8000, v226\n"
    "v_mfma_f32_16x16x32_bf16 v[80:83], a[28:31], a[172:175], v[80:83]\n"
    "v_add_u32 v229, 0x8000, v227\n"
    "v_mfma_f32_16x16x32_bf16 v[84:87], a[24:27], a[176:179], v[84:87]\n"
    "v_mfma_f32_16x16x32_bf16 v[84:87], a[28:31], a[180:183], v[84:87]\n"
    "v_mfma_f32_16x16x32_bf16 v[88:91], a[24:27], a[184:187], v[88:91]\n"
    "s_waitcnt vmcnt(9)\n"
    "s_barrier\n"
    "v_mfma_f32_16x16x32_bf16 v[88:91], a[28:31], a[188:191], v[88:91]\n"
    "ds_read_b64_tr_b16 a[72:73], v228 offset:0\n"
    "s_add_u32 m0, s80, 0x8000\n"
    "v_mfma_f32_16x16x32_bf16 v[92:95], a[24:27], a[192:195], v[92:95]\n"
    "buffer_load_dwordx4 v230, s[48:51], s75 offen offset:0 lds\n"
    "v_mfma_f32_16x16x32_bf16 v[92:95], a[28:31], a[196:199], v[92:95]\n"
    "ds_read_b64_tr_b16 a[74:75], v228 offset:256\n"
    "v_mfma_f32_16x16x32_bf16 v[96:99], a[136:139], a[32:35], v[96:99]\n"
    "ds_read_b64_tr_b16 a[76:77], v228 offset:4096\n"
    "v_mfma_f32_16x16x32_bf16 v[96:99], a[140:143], a[36:39], v[96:99]\n"
    "ds_read_b64_tr_b16 a[78:79], v228 offset:4352\n"
    "v_mfma_f32_16x16x32_bf16 v[100:103], a[144:147], a[32:35], v[100:103]\n"
    "buffer_load_dwordx4 v231, s[48:51], s75 offen offset:1024 lds\n"
    "v_mfma_f32_16x16x32_bf16 v[100:103], a[148:151], a[36:39], v[100:103]\n"
    "ds_read_b64_tr_b16 a[80:81], v229 offset:0\n"
    "v_mfma_f32_16x16x32_bf16 v[104:107], a[152:155], a[32:35], v[104:107]\n"
    "ds_read_b64_tr_b16 a[82:83], v229 offset:256\n"
    "v_mfma_f32_16x16x32_bf16 v[104:107], a[156:159], a[36:39], v[104:107]\n"
    "ds_read_b64_tr_b16 a[84:85], v229 offset:4096\n"
    "v_mfma_f32_16x16x32_bf16 v[108:111], a[160:163], a[32:35], v[108:111]\n"
    "buffer_load_dwordx4 v232, s[48:51], s75 offen offset:2048 lds\n"
    "v_mfma_f32_16x16x32_bf16 v[108:111], a[164:167], a[36:39], v[108:111]\n"
    "ds_read_b64_tr_b16 a[86:87], v229 offset:4352\n"
    "v_mfma_f32_16x16x32_bf16 v[112:115], a[32:35], a[168:171], v[112:115]\n"
    "ds_read_b64_tr_b16 a[88:89], v228 offset:8192\n"
    "v_mfma_f32_16x16x32_bf16 v[112:115], a[36:39], a[172:175], v[112:115]\n"
    "ds_read_b64_tr_b16 a[90:91], v228 offset:8448\n"
    "v_mfma_f32_16x16x32_bf16 v[116:119], a[32:35], a[176:179], v[116:119]\n"
    "buffer_load_dwordx4 v233, s[48:51], s75 offen offset:3072 lds\n"
    "v_mfma_f32_16x16x32_bf16 v[116:119], a[36:39], a[180:183], v[116:119]\n"
    "ds_read_b64_tr_b16 a[92:93], v228 offset:12288\n"
    "v_mfma_f32_16x16x32_bf16 v[120:123], a[32:35], a[184:187], v[120:123]\n"
    "ds_read_b64_tr_b16 a[94:95], v228 offset:12544\n"
    "v_mfma_f32_16x16x32_bf16 v[120:123], a[36:39], a[188:191], v[120:123]\n"
    "ds_read_b64_tr_b16 a[96:97], v229 offset:8192\n"
    "v_mfma_f32_16x16x32_bf16 v[124:127], a[32:35], a[192:195], v[124:127]\n"
    "ds_read_b64_tr_b16 a[98:99], v229 offset:8448\n"
    "s_add_u32 m0, s80, 0x9000\n"
    "v_mfma_f32_16x16x32_bf16 v[124:127], a[36:39], a[196:199], v[124:127]\n"
    "buffer_load_dwordx4 v230, s[48:51], s76 offen offset:0 lds\n"
    "v_mfma_f32_16x16x32_bf16 v[128:131], a[136:139], a[40:43], v[128:131]\n"
    "ds_read_b128 a[32:35], v224 offset:8192\n"
    "v_mfma_f32_16x16x32_bf16 v[128:131], a[140:143], a[44:47], v[128:131]\n"
    "ds_read_b64_tr_b16 a[100:101], v229 offset:12288\n"
    "v_mfma_f32_16x16x32_bf16 v[132:135], a[144:147], a[40:43], v[132:135]\n"
    "ds_read_b128 a[36:39], v225 offset:8192\n"
    "v_mfma_f32_16x16x32_bf16 v[132:135], a[148:151], a[44:47], v[132:135]\n"
    "buffer_load_dwordx4 v231, s[48:51], s76 offen offset:1024 lds\n"
    "v_mfma_f32_16x16x32_bf16 v[136:139], a[152:155], a[40:43], v[136:139]\n"
    "ds_read_b64_tr_b16 a[102:103], v229 offset:12544\n"
    "v_mfma_f32_16x16x32_bf16 v[136:139], a[156:159], a[44:47], v[136:139]\n"
    "ds_read_b64_tr_b16 a[104:105], v228 offset:16384\n"
    "v_mfma_f32_16x16x32_bf16 v[140:143], a[160:163], a[40:43], v[140:143]\n"
    "ds_read_b64_tr_b16 a[106:107], v228 offset:16640\n"
    "v_mfma_f32_16x16x32_bf16 v[140:143], a[164:167], a[44:47], v[140:143]\n"
    "buffer_load_dwordx4 v232, s[48:51], s76 offen offset:2048 lds\n"
    "v_mfma_f32_16x16x32_bf16 v[144:147], a[40:43], a[168:171], v[144:147]\n"
    "ds_read_b64_tr_b16 a[108:109], v228 offset:20480\n"
    "v_mfma_f32_16x16x32_bf16 v[144:147], a[44:47], a[172:175], v[144:147]\n"
    "ds_read_b64_tr_b16 a[110:111], v228 offset:20736\n"
    "v_mfma_f32_16x16x32_bf16 v[148:151], a[40:43], a[176:179], v[148:151]\n"
    "ds_read_b64_tr_b16 a[112:113], v229 offset:16384\n"
    "v_mfma_f32_16x16x32_bf16 v[148:151], a[44:47], a[180:183], v[148:151]\n"
    "buffer_load_dwordx4 v233, s[48:51], s76 offen offset:3072 lds\n"
    "v_mfma_f32_16x16x32_bf16 v[152:155], a[40:43], a[184:187], v[152:155]\n"
    "ds_read_b64_tr_b16 a[114:115], v229 offset:16640\n"
    "v_mfma_f32_16x16x32_bf16 v[152:155], a[44:47], a[188:191], v[152:155]\n"
    "ds_read_b64_tr_b16 a[116:117], v229 offset:20480\n"
    "v_mfma_f32_16x16x32_bf16 v[156:159], a[40:43], a[192:195], v[156:159]\n"
    "ds_read_b64_tr_b16 a[118:119], v229 offset:20736\n"
    "v_mfma_f32_16x16x32_bf16 v[156:159], a[44:47], a[196:199], v[156:159]\n"
    "ds_read_b64_tr_b16 a[120:121], v228 offset:24576\n"
    "s_add_u32 s75, s75, 0x80\n"
    "v_mfma_f32_16x16x32_bf16 v[160:163], a[136:139], a[48:51], v[160:163]\n"
    "ds_read_b64_tr_b16 a[122:123], v228 offset:24832\n"
    "s_add_u32 s76, s76, 0x80\n"
    "v_mfma_f32_16x16x32_bf16 v[160:163], a[140:143], a[52:55], v[160:163]\n"
    "ds_read_b128 a[40:43], v224 offset:10240\n"
    "v_mfma_f32_16x16x32_bf16 v[164:167], a[144:147], a[48:51], v[164:167]\n"
    "ds_read_b128 a[44:47], v225 offset:10240\n"
    "v_mfma_f32_16x16x32_bf16 v[164:167], a[148:151], a[52:55], v[164:167]\n"
    "ds_read_b64_tr_b16 a[124:125], v228 offset:28672\n"
    "v_mfma_f32_16x16x32_bf16 v[168:171], a[152:155], a[48:51], v[168:171]\n"
    "ds_read_b64_tr_b16 a[126:127], v228 offset:28928\n"
    "v_mfma_f32_16x16x32_bf16 v[168:171], a[156:159], a[52:55], v[168:171]\n"
    "ds_read_b64_tr_b16 a[128:129], v229 offset:24576\n"
    "v_mfma_f32_16x16x32_bf16 v[172:175], a[160:163], a[48:51], v[172:175]\n"
    "ds_read_b64_tr_b16 a[130:131], v229 offset:24832\n"
    "v_mfma_f32_16x16x32_bf16 v[172:175], a[164:167], a[52:55], v[172:175]\n"
    "ds_read_b64_tr_b16 a[132:133], v229 offset:28672\n"
    "v_mfma_f32_16x16x32_bf16 v[176:179], a[48:51], a[168:171], v[176:179]\n"
    "ds_read_b64_tr_b16 a[134:135], v229 offset:28928\n"
    "v_mfma_f32_16x16x32_bf16 v[176:179], a[52:55], a[172:175], v[176:179]\n"
    "ds_read_b128 a[56:59], v224 offset:14336\n"
    "v_mfma_f32_16x16x32_bf16 v[180:183], a[48:51], a[176:179], v[180:183]\n"
    "ds_read_b128 a[60:63], v225 offset:14336\n"
    "v_mfma_f32_16x16x32_bf16 v[180:183], a[52:55], a[180:183], v[180:183]\n"
    "ds_read_b128 a[0:3], v224 offset:0\n"
    "v_mfma_f32_16x16x32_bf16 v[184:187], a[48:51], a[184:187], v[184:187]\n"
    "ds_read_b128 a[4:7], v225 offset:0\n"
    "v_mfma_f32_16x16x32_bf16 v[184:187], a[52:55], a[188:191], v[184:187]\n"
    "ds_read_b128 a[8:11], v224 offset:2048\n"
    "v_mfma_f32_16x16x32_bf16 v[188:191], a[48:51], a[192:195], v[188:191]\n"
    "ds_read_b128 a[12:15], v225 offset:2048\n"
    "v_mfma_f32_16x16x32_bf16 v[188:191], a[52:55], a[196:199], v[188:191]\n"
    "ds_read_b128 a[16:19], v224 offset:4096\n"
    "v_mfma_f32_16x16x32_bf16 v[192:195], a[136:139], a[64:67], v[192:195]\n"
    "ds_read_b128 a[48:51], v224 offset:12288\n"
    "v_mfma_f32_16x16x32_bf16 v[192:195], a[140:143], a[68:71], v[192:195]\n"
    "ds_read_b128 a[20:23], v225 offset:4096\n"
    "v_mfma_f32_16x16x32_bf16 v[196:199], a[144:147], a[64:67], v[196:199]\n"
    "ds_read_b128 a[52:55], v225 offset:12288\n"
    "v_mfma_f32_16x16x32_bf16 v[196:199], a[148:151], a[68:71], v[196:199]\n"
    "ds_read_b128 a[24:27], v224 offset:6144\n"
    "v_mfma_f32_16x16x32_bf16 v[200:203], a[152:155], a[64:67], v[200:203]\n"
    "ds_read_b128 a[28:31], v225 offset:6144\n"
    "v_mfma_f32_16x16x32_bf16 v[200:203], a[156:159], a[68:71], v[200:203]\n"
    "v_mfma_f32_16x16x32_bf16 v[204:207], a[160:163], a[64:67], v[204:207]\n"
    "v_mfma_f32_16x16x32_bf16 v[204:207], a[164:167], a[68:71], v[204:207]\n"
    "v_mfma_f32_16x16x32_bf16 v[208:211], a[64:67], a[168:171], v[208:211]\n"
    "v_mfma_f32_16x16x32_bf16 v[208:211], a[68:71], a[172:175], v[208:211]\n"
    "v_mfma_f32_16x16x32_bf16 v[212:215], a[64:67], a[176:179], v[212:215]\n"
    "v_mfma_f32_16x16x32_bf16 v[212:215], a[68:71], a[180:183], v[212:215]\n"
    "v_mfma_f32_16x16x32_bf16 v[216:219], a[64:67], a[184:187], v[216:219]\n"
    "v_mfma_f32_16x16x32_bf16 v[216:219], a[68:71], a[188:191], v[216:219]\n"
    "v_mfma_f32_16x16x32_bf16 v[220:223], a[64:67], a[192:195], v[220:223]\n"
    "v_mfma_f32_16x16x32_bf16 v[220:223], a[68:71], a[196:199], v[220:223]\n"
    "s_waitcnt lgkmcnt(0)\n"
    "v_mfma_f32_16x16x32_bf16 a[200:203], a[72:75], a[0:3], a[200:203]\n"
    "v_mfma_f32_16x16x32_bf16 a[200:203], a[76:79], a[4:7], a[200:203]\n"
    "s_add_u32 m0, s80, 0x10000\n"
    "v_mfma_f32_16x16x32_bf16 a[204:207], a[80:83], a[0:3], a[204:207]\n"
    "buffer_load_dwordx4 v234, s[52:55], s77 offen offset:0 nt lds\n"
    "v_mfma_f32_16x16x32_bf16 a[204:207], a[84:87], a[4:7], a[204:207]\n"
    "v_mfma_f32_16x16x32_bf16 a[208:211], a[88:91], a[0:3], a[208:211]\n"
    "v_mfma_f32_16x16x32_bf16 a[208:211], a[92:95], a[4:7], a[208:211]\n"
    "v_mfma_f32_16x16x32_bf16 a[212:215], a[96:99], a[0:3], a[212:215]\n"
    "buffer_load_dwordx4 v235, s[52:55], s77 offen offset:1024 nt lds\n"
    "v_mfma_f32_16x16x32_bf16 a[212:215], a[100:103], a[4:7], a[212:215]\n"
    "v_mfma_f32_16x16x32_bf16 v[16:19], a[0:3], a[104:107], v[16:19]\n"
    "v_mfma_f32_16x16x32_bf16 v[16:19], a[4:7], a[108:111], v[16:19]\n"
    "v_mfma_f32_16x16x32_bf16 v[20:23], a[0:3], a[112:115], v[20:23]\n"
    "buffer_load_dwordx4 v236, s[52:55], s77 offen offset:2048 nt lds\n"
    "v_mfma_f32_16x16x32_bf16 v[20:23], a[4:7], a[116:119], v[20:23]\n"
    "v_mfma_f32_16x16x32_bf16 v[24:27], a[0:3], a[120:123], v[24:27]\n"
    "v_mfma_f32_16x16x32_bf16 v[24:27], a[4:7], a[124:127], v[24:27]\n"
    "v_mfma_f32_16x16x32_bf16 v[28:31], a[0:3], a[128:131], v[28:31]\n"
    "buffer_load_dwordx4 v237, s[52:55], s77 offen offset:3072 nt lds\n"
    "v_mfma_f32_16x16x32_bf16 v[28:31], a[4:7], a[132:135], v[28:31]\n"
    "v_mfma_f32_16x16x32_bf16 a[216:219], a[72:75], a[8:11], a[216:219]\n"
    "v_mfma_f32_16x16x32_bf16 a[216:219], a[76:79], a[12:15], a[216:219]\n"
    "v_mfma_f32_16x16x32_bf16 a[220:223], a[80:83], a[8:11], a[220:223]\n"
    "s_add_u32 m0, s80, 0x11000\n"
    "v_mfma_f32_16x16x32_bf16 a[220:223], a[84:87], a[12:15], a[220:223]\n"
    "buffer_load_dwordx4 v234, s[52:55], s78 offen offset:0 nt lds\n"
    "v_mfma_f32_16x16x32_bf16 a[224:227], a[88:91], a[8:11], a[224:227]\n"
    "v_mfma_f32_16x16x32_bf16 a[224:227], a[92:95], a[12:15], a[224:227]\n"
    "v_mfma_f32_16x16x32_bf16 a[228:231], a[96:99], a[8:11], a[228:231]\n"
    "v_mfma_f32_16x16x32_bf16 a[228:231], a[100:103], a[12:15], a[228:231]\n"
    "buffer_load_dwordx4 v235, s[52:55], s78 offen offset:1024 nt lds\n"
    "v_mfma_f32_16x16x32_bf16 v[32:35], a[8:11], a[104:107], v[32:35]\n"
    "v_mfma_f32_16x16x32_bf16 v[32:35], a[12:15], a[108:111], v[32:35]\n"
    "v_mfma_f32_16x16x32_bf16 v[36:39], a[8:11], a[112:115], v[36:39]\n"
    "v_mfma_f32_16x16x32_bf16 v[36:39], a[12:15], a[116:119], v[36:39]\n"
    "buffer_load_dwordx4 v236, s[52:55], s78 offen offset:2048 nt lds\n"
    "v_mfma_f32_16x16x32_bf16 v[40:43], a[8:11], a[120:123], v[40:43]\n"
    "v_mfma_f32_16x16x32_bf16 v[40:43], a[12:15], a[124:127], v[40:43]\n"
    "v_mfma_f32_16x16x32_bf16 v[44:47], a[8:11], a[128:131], v[44:47]\n"
    "v_mfma_f32_16x16x32_bf16 v[44:47], a[12:15], a[132:135], v[44:47]\n"
    "buffer_load_dwordx4 v237, s[52:55], s78 offen offset:3072 nt lds\n"
    "v_mfma_f32_16x16x32_bf16 a[232:235], a[72:75], a[16:19], a[232:235]\n"
    "v_mfma_f32_16x16x32_bf16 a[232:235], a[76:79], a[20:23], a[232:235]\n"
    "v_mfma_f32_16x16x32_bf16 a[236:239], a[80:83], a[16:19], a[236:239]\n"
    "v_mfma_f32_16x16x32_bf16 a[236:239], a[84:87], a[20:23], a[236:239]\n"
    "s_add_u32 s77, s77, s79\n"
    "v_mfma_f32_16x16x32_bf16 a[240:243], a[88:91], a[16:19], a[240:243]\n"
    "s_add_u32 s78, s78, s79\n"
    "v_mfma_f32_16x16x32_bf16 a[240:243], a[92:95], a[20:23], a[240:243]\n"
    "buffer_load_dword a251, v249, s[60:63], s84 offen\n"
    "s_lshl_b32 s73, s83, 4\n"
    "s_add_u32 s84, s84, s73\n"
    "v_mfma_f32_16x16x32_bf16 a[244:247], a[96:99], a[16:19], a[244:247]\n"
    "v_mfma_f32_16x16x32_bf16 a[244:247], a[100:103], a[20:23], a[244:247]\n"
    "v_mfma_f32_16x16x32_bf16 v[48:51], a[16:19], a[104:107], v[48:51]\n"
    "v_mfma_f32_16x16x32_bf16 v[48:51], a[20:23], a[108:111], v[48:51]\n"
    "v_mfma_f32_16x16x32_bf16 v[52:55], a[16:19], a[112:115], v[52:55]\n"
    "v_mfma_f32_16x16x32_bf16 v[52:55], a[20:23], a[116:119], v[52:55]\n"
    "v_mfma_f32_16x16x32_bf16 v[56:59], a[16:19], a[120:123], v[56:59]\n"
    "v_mfma_f32_16x16x32_bf16 v[56:59], a[20:23], a[124:127], v[56:59]\n"
    "v_mfma_f32_16x16x32_bf16 v[60:63], a[16:19], a[128:131], v[60:63]\n"
    "v_mfma_f32_16x16x32_bf16 v[60:63], a[20:23], a[132:135], v[60:63]\n"
    "v_mfma_f32_16x16x32_bf16 v[64:67], a[72:75], a[24:27], v[64:67]\n"
    "v_mfma_f32_16x16x32_bf16 v[64:67], a[76:79], a[28:31], v[64:67]\n"
    "v_mfma_f32_16x16x32_bf16 v[68:71], a[80:83], a[24:27], v[68:71]\n"
    "v_mfma_f32_16x16x32_bf16 v[68:71], a[84:87], a[28:31], v[68:71]\n"
    "v_mfma_f32_16x16x32_bf16 v[72:75], a[88:91], a[24:27], v[72:75]\n"
    "v_mfma_f32_16x16x32_bf16 v[72:75], a[92:95], a[28:31], v[72:75]\n"
    "v_mfma_f32_16x16x32_bf16 v[76:79], a[96:99], a[24:27], v[76:79]\n"
    "v_mfma_f32_16x16x32_bf16 v[76:79], a[100:103], a[28:31], v[76:79]\n"
    "v_mfma_f32_16x16x32_bf16 v[80:83], a[24:27], a[104:107], v[80:83]\n"
    "v_add_u32 v228, 0x10000, v226\n"
    "v_mfma_f32_16x16x32_bf16 v[80:83], a[28:31], a[108:111], v[80:83]\n"
    "v_add_u32 v229, 0x10000, v227\n"
    "v_mfma_f32_16x16x32_bf16 v[84:87], a[24:27], a[112:115], v[84:87]\n"
    "v_mfma_f32_16x16x32_bf16 v[84:87], a[28:31], a[116:119], v[84:87]\n"
    "v_mfma_f32_16x16x32_bf16 v[88:91], a[24:27], a[120:123], v[88:91]\n"
    "s_waitcnt vmcnt(9)\n"
    "s_barrier\n"
    "v_mfma_f32_16x16x32_bf16 v[88:91], a[28:31], a[124:127], v[88:91]\n"
    "ds_read_b64_tr_b16 a[136:137], v228 offset:0\n"
    "s_add_u32 m0, s80, 0x0\n"
    "v_mfma_f32_16x16x32_bf16 v[92:95], a[24:27], a[128:131], v[92:95]\n"
    "buffer_load_dwordx4 v230, s[48:51], s75 offen offset:0 lds\n"
    "v_mfma_f32_16x16x32_bf16 v[92:95], a[28:31], a[132:135], v[92:95]\n"
    "ds_read_b64_tr_b16 a[138:139], v228 offset:256\n"
    "v_mfma_f32_16x16x32_bf16 v[96:99], a[72:75], a[32:35], v[96:99]\n"
    "ds_read_b64_tr_b16 a[140:141], v228 offset:4096\n"
    "v_mfma_f32_16x16x32_bf16 v[96:99], a[76:79], a[36:39], v[96:99]\n"
    "ds_read_b64_tr_b16 a[142:143], v228 offset:4352\n"
    "v_mfma_f32_16x16x32_bf16 v[100:103], a[80:83], a[32:35], v[100:103]\n"
    "buffer_load_dwordx4 v231, s[48:51], s75 offen offset:1024 lds\n"
    "v_mfma_f32_16x16x32_bf16 v[100:103], a[84:87], a[36:39], v[100:103]\n"
    "ds_read_b64_tr_b16 a[144:145], v229 offset:0\n"
    "v_mfma_f32_16x16x32_bf16 v[104:107], a[88:91], a[32:35], v[104:107]\n"
    "ds_read_b64_tr_b16 a[146:147], v229 offset:256\n"
    "v_mfma_f32_16x16x32_bf16 v[104:107], a[92:95], a[36:39], v[104:107]\n"
    "ds_read_b64_tr_b16 a[148:149], v229 offset:4096\n"
    "v_mfma_f32_16x16x32_bf16 v[108:111], a[96:99], a[32:35], v[108:111]\n"
    "buffer_load_dwordx4 v232, s[48:51], s75 offen offset:2048 lds\n"
    "v_mfma_f32_16x16x32_bf16 v[108:111], a[100:103], a[36:39], v[108:111]\n"
    "ds_read_b64_tr_b16 a[150:151], v229 offset:4352\n"
    "v_mfma_f32_16x16x32_bf16 v[112:115], a[32:35], a[104:107], v[112:115]\n"
    "ds_read_b64_tr_b16 a[152:153], v228 offset:8192\n"
    "v_mfma_f32_16x16x32_bf16 v[112:115], a[36:39], a[108:111], v[112:115]\n"
    "ds_read_b64_tr_b16 a[154:155], v228 offset:8448\n"
    "v_mfma_f32_16x16x32_bf16 v[116:119], a[32:35], a[112:115], v[116:119]\n"
    "buffer_load_dwordx4 v233, s[48:51], s75 offen offset:3072 lds\n"
    "v_mfma_f32_16x16x32_bf16 v[116:119], a[36:39], a[116:119], v[116:119]\n"
    "ds_read_b64_tr_b16 a[156:157], v228 offset:12288\n"
    "v_mfma_f32_16x16x32_bf16 v[120:123], a[32:35], a[120:123], v[120:123]\n"
    "ds_read_b64_tr_b16 a[158:159], v228 offset:12544\n"
    "v_mfma_f32_16x16x32_bf16 v[120:123], a[36:39], a[124:127], v[120:123]\n"
    "ds_read_b64_tr_b16 a[160:161], v229 offset:8192\n"
    "v_mfma_f32_16x16x32_bf16 v[124:127], a[32:35], a[128:131], v[124:127]\n"
    "ds_read_b64_tr_b16 a[162:163], v229 offset:8448\n"
    "s_add_u32 m0, s80, 0x1000\n"
    "v_mfma_f32_16x16x32_bf16 v[124:127], a[36:39], a[132:135], v[124:127]\n"
    "buffer_load_dwordx4 v230, s[48:51], s76 offen offset:0 lds\n"
    "v_mfma_f32_16x16x32_bf16 v[128:131], a[72:75], a[40:43], v[128:131]\n"
    "ds_read_b128 a[32:35], v224 offset:40960\n"
    "v_mfma_f32_16x16x32_bf16 v[128:131], a[76:79], a[44:47], v[128:131]\n"
    "ds_read_b64_tr_b16 a[164:165], v229 offset:12288\n"
    "v_mfma_f32_16x16x32_bf16 v[132:135], a[80:83], a[40:43], v[132:135]\n"
    "ds_read_b128 a[36:39], v225 offset:40960\n"
    "v_mfma_f32_16x16x32_bf16 v[132:135], a[84:87], a[44:47], v[132:135]\n"
    "buffer_load_dwordx4 v231, s[48:51], s76 offen offset:1024 lds\n"
    "v_mfma_f32_16x16x32_bf16 v[136:139], a[88:91], a[40:43], v[136:139]\n"
    "ds_read_b64_tr_b16 a[166:167], v229 offset:12544\n"
    "v_mfma_f32_16x16x32_bf16 v[136:139], a[92:95], a[44:47], v[136:139]\n"
    "ds_read_b64_tr_b16 a[168:169], v228 offset:16384\n"
    "v_mfma_f32_16x16x32_bf16 v[140:143], a[96:99], a[40:43], v[140:143]\n"
    "ds_read_b64_tr_b16 a[170:171], v228 offset:16640\n"
    "v_mfma_f32_16x16x32_bf16 v[140:143], a[100:103], a[44:47], v[140:143]\n"
    "buffer_load_dwordx4 v232, s[48:51], s76 offen offset:2048 lds\n"
    "v_mfma_f32_16x16x32_bf16 v[144:147], a[40:43], a[104:107], v[144:147]\n"
    "ds_read_b64_tr_b16 a[172:173], v228 offset:20480\n"
    "v_mfma_f32_16x16x32_bf16 v[144:147], a[44:47], a[108:111], v[144:147]\n"
    "ds_read_b64_tr_b16 a[174:175], v228 offset:20736\n"
    "v_mfma_f32_16x16x32_bf16 v[148:151], a[40:43], a[112:115], v[148:151]\n"
    "ds_read_b64_tr_b16 a[176:177], v229 offset:16384\n"
    "v_mfma_f32_16x16x32_bf16 v[148:151], a[44:47], a[116:119], v[148:151]\n"
    "buffer_load_dwordx4 v233, s[48:51], s76 offen offset:3072 lds\n"
    "v_mfma_f32_16x16x32_bf16 v[152:155], a[40:43], a[120:123], v[152:155]\n"
    "ds_read_b64_tr_b16 a[178:179], v229 offset:16640\n"
    "v_mfma_f32_16x16x32_bf16 v[152:155], a[44:47], a[124:127], v[152:155]\n"
    "ds_read_b64_tr_b16 a[180:181], v229 offset:20480\n"
    "v_mfma_f32_16x16x32_bf16 v[156:159], a[40:43], a[128:131], v[156:159]\n"
    "ds_read_b64_tr_b16 a[182:183], v229 offset:20736\n"
    "v_mfma_f32_16x16x32_bf16 v[156:159], a[44:47], a[132:135], v[156:159]\n"
    "ds_read_b64_tr_b16 a[184:185], v228 offset:24576\n"
    "s_add_u32 s75, s75, 0x80\n"
    "v_mfma_f32_16x16x32_bf16 v[160:163], a[72:75], a[48:51], v[160:163]\n"
    "ds_read_b64_tr_b16 a[186:187], v228 offset:24832\n"
    "s_add_u32 s76, s76, 0x80\n"
    "v_mfma_f32_16x16x32_bf16 v[160:163], a[76:79], a[52:55], v[160:163]\n"
    "ds_read_b128 a[40:43], v224 offset:43008\n"
    "v_mfma_f32_16x16x32_bf16 v[164:167], a[80:83], a[48:51], v[164:167]\n"
    "ds_read_b128 a[44:47], v225 offset:43008\n"
    "v_mfma_f32_16x16x32_bf16 v[164:167], a[84:87], a[52:55], v[164:167]\n"
    "ds_read_b64_tr_b16 a[188:189], v228 offset:28672\n"
    "v_mfma_f32_16x16x32_bf16 v[168:171], a[88:91], a[48:51], v[168:171]\n"
    "ds_read_b64_tr_b16 a[190:191], v228 offset:28928\n"
    "v_mfma_f32_16x16x32_bf16 v[168:171], a[92:95], a[52:55], v[168:171]\n"
    "ds_read_b64_tr_b16 a[192:193], v229 offset:24576\n"
    "v_mfma_f32_16x16x32_bf16 v[172:175], a[96:99], a[48:51], v[172:175]\n"
    "ds_read_b64_tr_b16 a[194:195], v229 offset:24832\n"
    "v_mfma_f32_16x16x32_bf16 v[172:175], a[100:103], a[52:55], v[172:175]\n"
    "ds_read_b64_tr_b16 a[196:197], v229 offset:28672\n"
    "v_mfma_f32_16x16x32_bf16 v[176:179], a[48:51], a[104:107], v[176:179]\n"
    "ds_read_b64_tr_b16 a[198:199], v229 offset:28928\n"
    "v_mfma_f32_16x16x32_bf16 v[176:179], a[52:55], a[108:111], v[176:179]\n"
    "ds_read_b128 a[64:67], v224 offset:47104\n"
    "v_mfma_f32_16x16x32_bf16 v[180:183], a[48:51], a[112:115], v[180:183]\n"
    "ds_read_b128 a[68:71], v225 offset:47104\n"
    "v_mfma_f32_16x16x32_bf16 v[180:183], a[52:55], a[116:119], v[180:183]\n"
    "ds_read_b128 a[0:3], v224 offset:32768\n"
    "v_mfma_f32_16x16x32_bf16 v[184:187], a[48:51], a[120:123], v[184:187]\n"
    "ds_read_b128 a[4:7], v225 offset:32768\n"
    "v_mfma_f32_16x16x32_bf16 v[184:187], a[52:55], a[124:127], v[184:187]\n"
    "ds_read_b128 a[8:11], v224 offset:34816\n"
    "v_mfma_f32_16x16x32_bf16 v[188:191], a[48:51], a[128:131], v[188:191]\n"
    "ds_read_b128 a[12:15], v225 offset:34816\n"
    "v_mfma_f32_16x16x32_bf16 v[188:191], a[52:55], a[132:135], v[188:191]\n"
    "ds_read_b128 a[16:19], v224 offset:36864\n"
    "v_mfma_f32_16x16x32_bf16 v[192:195], a[72:75], a[56:59], v[192:195]\n"
    "ds_read_b128 a[48:51], v224 offset:45056\n"
    "v_mfma_f32_16x16x32_bf16 v[192:195], a[76:79], a[60:63], v[192:195]\n"
    "ds_read_b128 a[20:23], v225 offset:36864\n"
    "v_mfma_f32_16x16x32_bf16 v[196:199], a[80:83], a[56:59], v[196:199]\n"
    "ds_read_b128 a[52:55], v225 offset:45056\n"
    "v_mfma_f32_16x16x32_bf16 v[196:199], a[84:87], a[60:63], v[196:199]\n"
    "ds_read_b128 a[24:27], v224 offset:38912\n"
    "v_mfma_f32_16x16x32_bf16 v[200:203], a[88:91], a[56:59], v[200:203]\n"
    "ds_read_b128 a[28:31], v225 offset:38912\n"
    "v_mfma_f32_16x16x32_bf16 v[200:203], a[92:95], a[60:63], v[200:203]\n"
    "v_mfma_f32_16x16x32_bf16 v[204:207], a[96:99], a[56:59], v[204:207]\n"
    "v_mfma_f32_16x16x32_bf16 v[204:207], a[100:103], a[60:63], v[204:207]\n"
    "v_mfma_f32_16x16x32_bf16 v[208:211], a[56:59], a[104:107], v[208:211]\n"
    "v_mfma_f32_16x16x32_bf16 v[208:211], a[60:63], a[108:111], v[208:211]\n"
    "v_mfma_f32_16x16x32_bf16 v[212:215], a[56:59], a[112:115], v[212:215]\n"
    "v_mfma_f32_16x16x32_bf16 v[212:215], a[60:63], a[116:119], v[212:215]\n"
    "v_mfma_f32_16x16x32_bf16 v[216:219], a[56:59], a[120:123], v[216:219]\n"
    "v_mfma_f32_16x16x32_bf16 v[216:219], a[60:63], a[124:127], v[216:219]\n"
    "v_mfma_f32_16x16x32_bf16 v[220:223], a[56:59], a[128:131], v[220:223]\n"
    "v_mfma_f32_16x16x32_bf16 v[220:223], a[60:63], a[132:135], v[220:223]\n"
    "s_waitcnt lgkmcnt(0)\n"
    "v_mfma_f32_16x16x32_bf16 a[200:203], a[136:139], a[0:3], a[200:203]\n"
    "v_mfma_f32_16x16x32_bf16 a[200:203], a[140:143], a[4:7], a[200:203]\n"
    "s_add_u32 m0, s80, 0x18000\n"
    "v_mfma_f32_16x16x32_bf16 a[204:207], a[144:147], a[0:3], a[204:207]\n"
    "buffer_load_dwordx4 v234, s[52:55], s77 offen offset:0 nt lds\n"
    "v_mfma_f32_16x16x32_bf16 a[204:207], a[148:151], a[4:7], a[204:207]\n"
    "v_mfma_f32_16x16x32_bf16 a[208:211], a[152:155], a[0:3], a[208:211]\n"
    "v_mfma_f32_16x16x32_bf16 a[208:211], a[156:159], a[4:7], a[208:211]\n"
    "v_mfma_f32_16x16x32_bf16 a[212:215], a[160:163], a[0:3], a[212:215]\n"
    "buffer_load_dwordx4 v235, s[52:55], s77 offen offset:1024 nt lds\n"
    "v_mfma_f32_16x16x32_bf16 a[212:215], a[164:167], a[4:7], a[212:215]\n"
    "v_mfma_f32_16x16x32_bf16 v[16:19], a[0:3], a[168:171], v[16:19]\n"
    "v_mfma_f32_16x16x32_bf16 v[16:19], a[4:7], a[172:175], v[16:19]\n"
    "v_mfma_f32_16x16x32_bf16 v[20:23], a[0:3], a[176:179], v[20:23]\n"
    "buffer_load_dwordx4 v236, s[52:55], s77 offen offset:2048 nt lds\n"
    "v_mfma_f32_16x16x32_bf16 v[20:23], a[4:7], a[180:183], v[20:23]\n"
    "v_mfma_f32_16x16x32_bf16 v[24:27], a[0:3], a[184:187], v[24:27]\n"
    "v_mfma_f32_16x16x32_bf16 v[24:27], a[4:7], a[188:191], v[24:27]\n"
    "v_mfma_f32_16x16x32_bf16 v[28:31], a[0:3], a[192:195], v[28:31]\n"
    "buffer_load_dwordx4 v237, s[52:55], s77 offen offset:3072 nt lds\n"
    "v_mfma_f32_16x16x32_bf16 v[28:31], a[4:7], a[196:199], v[28:31]\n"
    "v_mfma_f32_16x16x32_bf16 a[216:219], a[136:139], a[8:11], a[216:219]\n"
    "v_mfma_f32_16x16x32_bf16 a[216:219], a[140:143], a[12:15], a[216:219]\n"
    "v_mfma_f32_16x16x32_bf16 a[220:223], a[144:147], a[8:11], a[220:223]\n"
    "s_add_u32 m0, s80, 0x19000\n"
    "v_mfma_f32_16x16x32_bf16 a[220:223], a[148:151], a[12:15], a[220:223]\n"
    "buffer_load_dwordx4 v234, s[52:55], s78 offen offset:0 nt lds\n"
    "v_mfma_f32_16x16x32_bf16 a[224:227], a[152:155], a[8:11], a[224:227]\n"
    "v_mfma_f32_16x16x32_bf16 a[224:227], a[156:159], a[12:15], a[224:227]\n"
    "v_mfma_f32_16x16x32_bf16 a[228:231], a[160:163], a[8:11], a[228:231]\n"
    "v_mfma_f32_16x16x32_bf16 a[228:231], a[164:167], a[12:15], a[228:231]\n"
    "buffer_load_dwordx4 v235, s[52:55], s78 offen offset:1024 nt lds\n"
    "v_mfma_f32_16x16x32_bf16 v[32:35], a[8:11], a[168:171], v[32:35]\n"
    "v_mfma_f32_16x16x32_bf16 v[32:35], a[12:15], a[172:175], v[32:35]\n"
    "v_mfma_f32_16x16x32_bf16 v[36:39], a[8:11], a[176:179], v[36:39]\n"
    "v_mfma_f32_16x16x32_bf16 v[36:39], a[12:15], a[180:183], v[36:39]\n"
    "buffer_load_dwordx4 v236, s[52:55], s78 offen offset:2048 nt lds\n"
    "v_mfma_f32_16x16x32_bf16 v[40:43], a[8:11], a[184:187], v[40:43]\n"
    "v_mfma_f32_16x16x32_bf16 v[40:43], a[12:15], a[188:191], v[40:43]\n"
    "v_mfma_f32_16x16x32_bf16 v[44:47], a[8:11], a[192:195], v[44:47]\n"
    "v_mfma_f32_16x16x32_bf16 v[44:47], a[12:15], a[196:199], v[44:47]\n"
    "buffer_load_dwordx4 v237, s[52:55], s78 offen offset:3072 nt lds\n"
    "v_mfma_f32_16x16x32_bf16 a[232:235], a[136:139], a[16:19], a[232:235]\n"
    "v_mfma_f32_16x16x32_bf16 a[232:235], a[140:143], a[20:23], a[232:235]\n"
    "v_mfma_f32_16x16x32_bf16 a[236:239], a[144:147], a[16:19], a[236:239]\n"
    "v_mfma_f32_16x16x32_bf16 a[236:239], a[148:151], a[20:23], a[236:239]\n"
    "s_add_u32 s77, s77, s79\n"
    "v_mfma_f32_16x16x32_bf16 a[240:243], a[152:155], a[16:19], a[240:243]\n"
    "s_add_u32 s78, s78, s79\n"
    "v_mfma_f32_16x16x32_bf16 a[240:243], a[156:159], a[20:23], a[240:243]\n"
    "s_lshl_b32 s73, s68, 12\n"
    "v_add_u32 v12, s73, v8\n"
    "s_add_u32 s88, s73, s69\n"
    "v_mfma_f32_16x16x32_bf16 a[244:247], a[160:163], a[16:19], a[244:247]\n"
    "s_lshl_b32 s73, s71, 13\n"
    "s_add_u32 s73, s73, s69\n"
    "v_add_u32 v9, s73, v8\n"
    "v_mfma_f32_16x16x32_bf16 a[244:247], a[164:167], a[20:23], a[244:247]\n"
    "s_lshl_b32 s89, s38, 15\n"
    "v_mfma_f32_16x16x32_bf16 v[48:51], a[16:19], a[168:171], v[48:51]\n"
    "s_add_u32 s89, s89, 0x4000\n"
    "v_mfma_f32_16x16x32_bf16 v[48:51], a[20:23], a[172:175], v[48:51]\n"
    "s_mov_b32 s90, s89\n"
    "v_mfma_f32_16x16x32_bf16 v[52:55], a[16:19], a[176:179], v[52:55]\n"
    "s_lshl_b32 s87, s39, 15\n"
    "v_mfma_f32_16x16x32_bf16 v[52:55], a[20:23], a[180:183], v[52:55]\n"
    "s_add_u32 s87, s87, 0x4000\n"
    "v_mfma_f32_16x16x32_bf16 v[56:59], a[16:19], a[184:187], v[56:59]\n"
    "s_mov_b32 s86, 0\n"
    "v_mfma_f32_16x16x32_bf16 v[56:59], a[20:23], a[188:191], v[56:59]\n"
    "s_mov_b32 s94, 0\n"
    "v_mfma_f32_16x16x32_bf16 v[60:63], a[16:19], a[192:195], v[60:63]\n"
    "s_mov_b32 s95, 21\n"
    "v_mfma_f32_16x16x32_bf16 v[60:63], a[20:23], a[196:199], v[60:63]\n"
    "v_mfma_f32_16x16x32_bf16 v[64:67], a[136:139], a[24:27], v[64:67]\n"
    "v_mfma_f32_16x16x32_bf16 v[64:67], a[140:143], a[28:31], v[64:67]\n"
    "v_mfma_f32_16x16x32_bf16 v[68:71], a[144:147], a[24:27], v[68:71]\n"
    "v_mfma_f32_16x16x32_bf16 v[68:71], a[148:151], a[28:31], v[68:71]\n"
    "v_mfma_f32_16x16x32_bf16 v[72:75], a[152:155], a[24:27], v[72:75]\n"
    "v_mfma_f32_16x16x32_bf16 v[72:75], a[156:159], a[28:31], v[72:75]\n"
    "v_mfma_f32_16x16x32_bf16 v[76:79], a[160:163], a[24:27], v[76:79]\n"
    "v_mfma_f32_16x16x32_bf16 v[76:79], a[164:167], a[28:31], v[76:79]\n"
    "v_mfma_f32_16x16x32_bf16 v[80:83], a[24:27], a[168:171], v[80:83]\n"
    "v_add_u32 v228, 0x0, v226\n"
    "v_mfma_f32_16x16x32_bf16 v[80:83], a[28:31], a[172:175], v[80:83]\n"
    "v_add_u32 v229, 0x0, v227\n"
    "v_mfma_f32_16x16x32_bf16 v[84:87], a[24:27], a[176:179], v[84:87]\n"
    "v_mfma_f32_16x16x32_bf16 v[84:87], a[28:31], a[180:183], v[84:87]\n"
    "v_mfma_f32_16x16x32_bf16 v[88:91], a[24:27], a[184:187], v[88:91]\n"
    "s_waitcnt vmcnt(8)\n"
    "s_barrier\n"
    "v_mfma_f32_16x16x32_bf16 v[88:91], a[28:31], a[188:191], v[88:91]\n"
    "s_cmp_lg_u32 s68, 0\n"
    "s_cbranch_scc1 .Lw6_%=\n"
    "v_lshrrev_b32 v5, 1, v8\n"
    "v_and_b32 v5, 0x7f, v5\n"
    "s_lshl_b32 s73, s67, 7\n"
    "s_sub_u32 s74, 0x1780, s73\n"
    "s_add_u32 s73, s73, 0x1000\n"
    "v_mov_b32 v6, s73\n"
    "v_mov_b32 v7, s74\n"
    "v_cmp_gt_u32 vcc, 0x200, v8\n"
    "s_nop 1\n"
    "v_cndmask_b32 v6, v6, v7, vcc\n"
    "v_add_u32 v5, v5, v6\n"
    "buffer_load_dwordx2 v[6:7], v5, s[56:59], 0 offen sc1\n"
    ".Lw6_%=:\n"
    "v_mfma_f32_16x16x32_bf16 v[92:95], a[24:27], a[192:195], v[92:95]\n"
    "ds_read_b64_tr_b16 a[72:73], v228 offset:0\n"
    "s_add_u32 m0, s80, 0x8000\n"
    "v_mfma_f32_16x16x32_bf16 v[92:95], a[28:31], a[196:199], v[92:95]\n"
    "buffer_load_dwordx4 v230, s[48:51], s75 offen offset:0 lds\n"
    "v_mfma_f32_16x16x32_bf16 v[96:99], a[136:139], a[32:35], v[96:99]\n"
    "ds_read_b64_tr_b16 a[74:75], v228 offset:256\n"
    "v_mfma_f32_16x16x32_bf16 v[96:99], a[140:143], a[36:39], v[96:99]\n"
    "ds_read_b64_tr_b16 a[76:77], v228 offset:4096\n"
    "v_mfma_f32_16x16x32_bf16 v[100:103], a[144:147], a[32:35], v[100:103]\n"
    "ds_read_b64_tr_b16 a[78:79], v228 offset:4352\n"
    "v_mfma_f32_16x16x32_bf16 v[100:103], a[148:151], a[36:39], v[100:103]\n"
    "buffer_load_dwordx4 v231, s[48:51], s75 offen offset:1024 lds\n"
    "v_mfma_f32_16x16x32_bf16 v[104:107], a[152:155], a[32:35], v[104:107]\n"
    "ds_read_b64_tr_b16 a[80:81], v229 offset:0\n"
    "v_mfma_f32_16x16x32_bf16 v[104:107], a[156:159], a[36:39], v[104:107]\n"
    "ds_read_b64_tr_b16 a[82:83], v229 offset:256\n"
    "v_mfma_f32_16x16x32_bf16 v[108:111], a[160:163], a[32:35], v[108:111]\n"
    "ds_read_b64_tr_b16 a[84:85], v229 offset:4096\n"
    "v_mfma_f32_16x16x32_bf16 v[108:111], a[164:167], a[36:39], v[108:111]\n"
    "buffer_load_dwordx4 v232, s[48:51], s75 offen offset:2048 lds\n"
    "v_mfma_f32_16x16x32_bf16 v[112:115], a[32:35], a[168:171], v[112:115]\n"
    "ds_read_b64_tr_b16 a[86:87], v229 offset:4352\n"
    "v_mfma_f32_16x16x32_bf16 v[112:115], a[36:39], a[172:175], v[112:115]\n"
    "ds_read_b64_tr_b16 a[88:89], v228 offset:8192\n"
    "v_mfma_f32_16x16x32_bf16 v[116:119], a[32:35], a[176:179], v[116:119]\n"
    "ds_read_b64_tr_b16 a[90:91], v228 offset:8448\n"
    "v_mfma_f32_16x16x32_bf16 v[116:119], a[36:39], a[180:183], v[116:119]\n"
    "buffer_load_dwordx4 v233, s[48:51], s75 offen offset:3072 lds\n"
    "v_mfma_f32_16x16x32_bf16 v[120:123], a[32:35], a[184:187], v[120:123]\n"
    "ds_read_b64_tr_b16 a[92:93], v228 offset:12288\n"
    "v_mfma_f32_16x16x32_bf16 v[120:123], a[36:39], a[188:191], v[120:123]\n"
    "ds_read_b64_tr_b16 a[94:95], v228 offset:12544\n"
    "v_mfma_f32_16x16x32_bf16 v[124:127], a[32:35], a[192:195], v[124:127]\n"
    "ds_read_b64_tr_b16 a[96:97], v229 offset:8192\n"
    "v_mfma_f32_16x16x32_bf16 v[124:127], a[36:39], a[196:199], v[124:127]\n"
    "ds_read_b64_tr_b16 a[98:99], v229 offset:8448\n"
    "s_add_u32 m0, s80, 0x9000\n"
    "v_mfma_f32_16x16x32_bf16 v[128:131], a[136:139], a[40:43], v[128:131]\n"
    "buffer_load_dwordx4 v230, s[48:51], s76 offen offset:0 lds\n"
    "v_mfma_f32_16x16x32_bf16 v[128:131], a[140:143], a[44:47], v[128:131]\n"
    "ds_read_b128 a[32:35], v224 offset:8192\n"
    "v_mfma_f32_16x16x32_bf16 v[132:135], a[144:147], a[40:43], v[132:135]\n"
    "ds_read_b128 a[36:39], v225 offset:8192\n"
    "v_mfma_f32_16x16x32_bf16 v[132:135], a[148:151], a[44:47], v[132:135]\n"
    "ds_read_b64_tr_b16 a[100:101], v229 offset:12288\n"
    "v_mfma_f32_16x16x32_bf16 v[136:139], a[152:155], a[40:43], v[136:139]\n"
    "buffer_load_dwordx4 v231, s[48:51], s76 offen offset:1024 lds\n"
    "v_mfma_f32_16x16x32_bf16 v[136:139], a[156:159], a[44:47], v[136:139]\n"
    "ds_read_b64_tr_b16 a[102:103], v229 offset:12544\n"
    "v_mfma_f32_16x16x32_bf16 v[140:143], a[160:163], a[40:43], v[140:143]\n"
    "ds_read_b64_tr_b16 a[104:105], v228 offset:16384\n"
    "v_mfma_f32_16x16x32_bf16 v[140:143], a[164:167], a[44:47], v[140:143]\n"
    "ds_read_b64_tr_b16 a[106:107], v228 offset:16640\n"
    "v_mfma_f32_16x16x32_bf16 v[144:147], a[40:43], a[168:171], v[144:147]\n"
    "buffer_load_dwordx4 v232, s[48:51], s76 offen offset:2048 lds\n"
    "v_mfma_f32_16x16x32_bf16 v[144:147], a[44:47], a[172:175], v[144:147]\n"
    "ds_read_b64_tr_b16 a[108:109], v228 offset:20480\n"
    "v_mfma_f32_16x16x32_bf16 v[148:151], a[40:43], a[176:179], v[148:151]\n"
    "ds_read_b64_tr_b16 a[110:111], v228 offset:20736\n"
    "v_mfma_f32_16x16x32_bf16 v[148:151], a[44:47], a[180:183], v[148:151]\n"
    "ds_read_b64_tr_b16 a[112:113], v229 offset:16384\n"
    "v_mfma_f32_16x16x32_bf16 v[152:155], a[40:43], a[184:187], v[152:155]\n"
    "buffer_load_dwordx4 v233, s[48:51], s76 offen offset:3072 lds\n"
    "v_mfma_f32_16x16x32_bf16 v[152:155], a[44:47], a[188:191], v[152:155]\n"
    "ds_read_b64_tr_b16 a[114:115], v229 offset:16640\n"
    "v_mfma_f32_16x16x32_bf16 v[156:159], a[40:43], a[192:195], v[156:159]\n"
    "ds_read_b64_tr_b16 a[116:117], v229 offset:20480\n"
    "v_mfma_f32_16x16x32_bf16 v[156:159], a[44:47], a[196:199], v[156:159]\n"
    "ds_read_b64_tr_b16 a[118:119], v229 offset:20736\n"
    "v_mfma_f32_16x16x32_bf16 v[160:163], a[136:139], a[48:51], v[160:163]\n"
    "ds_read_b64_tr_b16 a[120:121], v228 offset:24576\n"
    "s_add_u32 s75, s75, 0x80\n"
    "v_mfma_f32_16x16x32_bf16 v[160:163], a[140:143], a[52:55], v[160:163]\n"
    "ds_read_b64_tr_b16 a[122:123], v228 offset:24832\n"
    "s_add_u32 s76, s76, 0x80\n"
    "v_mfma_f32_16x16x32_bf16 v[164:167], a[144:147], a[48:51], v[164:167]\n"
    "ds_read_b128 a[40:43], v224 offset:10240\n"
    "v_mfma_f32_16x16x32_bf16 v[164:167], a[148:151], a[52:55], v[164:167]\n"
    "ds_read_b128 a[44:47], v225 offset:10240\n"
    "v_mfma_f32_16x16x32_bf16 v[168:171], a[152:155], a[48:51], v[168:171]\n"
    "ds_read_b64_tr_b16 a[124:125], v228 offset:28672\n"
    "v_mfma_f32_16x16x32_bf16 v[168:171], a[156:159], a[52:55], v[168:171]\n"
    "ds_read_b64_tr_b16 a[126:127], v228 offset:28928\n"
    "v_mfma_f32_16x16x32_bf16 v[172:175], a[160:163], a[48:51], v[172:175]\n"
    "ds_read_b64_tr_b16 a[128:129], v229 offset:24576\n"
    "v_mfma_f32_16x16x32_bf16 v[172:175], a[164:167], a[52:55], v[172:175]\n"
    "ds_read_b64_tr_b16 a[130:131], v229 offset:24832\n"
    "v_mfma_f32_16x16x32_bf16 v[176:179], a[48:51], a[168:171], v[176:179]\n"
    "ds_read_b64_tr_b16 a[132:133], v229 offset:28672\n"
    "v_mfma_f32_16x16x32_bf16 v[176:179], a[52:55], a[172:175], v[176:179]\n"
    "ds_read_b64_tr_b16 a[134:135], v229 offset:28928\n"
    "v_mfma_f32_16x16x32_bf16 v[180:183], a[48:51], a[176:179], v[180:183]\n"
    "ds_read_b128 a[56:59], v224 offset:14336\n"
    "v_mfma_f32_16x16x32_bf16 v[180:183], a[52:55], a[180:183], v[180:183]\n"
    "ds_read_b128 a[60:63], v225 offset:14336\n"
    "v_mfma_f32_16x16x32_bf16 v[184:187], a[48:51], a[184:187], v[184:187]\n"
    "ds_read_b128 a[0:3], v224 offset:0\n"
    "v_mfma_f32_16x16x32_bf16 v[184:187], a[52:55], a[188:191], v[184:187]\n"
    "ds_read_b128 a[4:7], v225 offset:0\n"
    "v_mfma_f32_16x16x32_bf16 v[188:191], a[48:51], a[192:195], v[188:191]\n"
    "ds_read_b128 a[8:11], v224 offset:2048\n"
    "v_mfma_f32_16x16x32_bf16 v[188:191], a[52:55], a[196:199], v[188:191]\n"
    "ds_read_b128 a[12:15], v225 offset:2048\n"
    "v_mfma_f32_16x16x32_bf16 v[192:195], a[136:139], a[64:67], v[192:195]\n"
    "ds_read_b128 a[48:51], v224 offset:12288\n"
    "v_mfma_f32_16x16x32_bf16 v[192:195], a[140:143], a[68:71], v[192:195]\n"
    "ds_read_b128 a[16:19], v224 offset:4096\n"
    "v_mfma_f32_16x16x32_bf16 v[196:199], a[144:147], a[64:67], v[196:199]\n"
    "ds_read_b128 a[52:55], v225 offset:12288\n"
    "v_mfma_f32_16x16x32_bf16 v[196:199], a[148:151], a[68:71], v[196:199]\n"
    "ds_read_b128 a[20:23], v225 offset:4096\n"
    "v_mfma_f32_16x16x32_bf16 v[200:203], a[152:155], a[64:67], v[200:203]\n"
    "ds_read_b128 a[24:27], v224 offset:6144\n"
    "v_mfma_f32_16x16x32_bf16 v[200:203], a[156:159], a[68:71], v[200:203]\n"
    "ds_read_b128 a[28:31], v225 offset:6144\n"
    "v_mfma_f32_16x16x32_bf16 v[204:207], a[160:163], a[64:67], v[204:207]\n"
    "v_mfma_f32_16x16x32_bf16 v[204:207], a[164:167], a[68:71], v[204:207]\n"
    "v_mfma_f32_16x16x32_bf16 v[208:211], a[64:67], a[168:171], v[208:211]\n"
    "v_mfma_f32_16x16x32_bf16 v[208:211], a[68:71], a[172:175], v[208:211]\n"
    "v_mfma_f32_16x16x32_bf16 v[212:215], a[64:67], a[176:179], v[212:215]\n"
    "v_mfma_f32_16x16x32_bf16 v[212:215], a[68:71], a[180:183], v[212:215]\n"
    "v_mfma_f32_16x16x32_bf16 v[216:219], a[64:67], a[184:187], v[216:219]\n"
    "v_mfma_f32_16x16x32_bf16 v[216:219], a[68:71], a[188:191], v[216:219]\n"
    "v_mfma_f32_16x16x32_bf16 v[220:223], a[64:67], a[192:195], v[220:223]\n"
    "v_mfma_f32_16x16x32_bf16 v[220:223], a[68:71], a[196:199], v[220:223]\n"
    "s_waitcnt lgkmcnt(0)\n"
    "v_mfma_f32_16x16x32_bf16 a[200:203], a[72:75], a[0:3], a[200:203]\n"
    "v_mfma_f32_16x16x32_bf16 a[200:203], a[76:79], a[4:7], a[200:203]\n"
    "s_cmp_eq_u32 s68, 2\n"
    "s_cselect_b64 s[52:53], %6, %7\n"
    "s_cmp_lt_u32 s66, 28\n"
    "s_cselect_b32 s74, 0x1c0000, 0\n"
    "s_cmp_eq_u32 s71, 0\n"
    "s_cselect_b64 s[52:53], %2, s[52:53]\n"
    "s_cselect_b32 s54, 0x400000, s74\n"
    "v_mfma_f32_16x16x32_bf16 a[204:207], a[80:83], a[0:3], a[204:207]\n"
    "s_lshl_b32 s77, s66, 8\n"
    "s_lshl_b32 s73, s72, 7\n"
    "s_add_u32 s77, s77, s73\n"
    "s_lshl_b32 s73, s66, 7\n"
    "s_cmp_eq_u32 s71, 0\n"
    "s_cselect_b32 s77, s77, s73\n"
    "v_mfma_f32_16x16x32_bf16 a[204:207], a[84:87], a[4:7], a[204:207]\n"
    "s_lshl_b32 s73, s83, 5\n"
    "s_add_u32 s78, s77, s73\n"
    "s_lshl_b32 s79, s83, 6\n"
    "v_mfma_f32_16x16x32_bf16 a[208:211], a[88:91], a[0:3], a[208:211]\n"
    "s_add_u32 m0, s80, 0x20000\n"
    "v_mfma_f32_16x16x32_bf16 a[208:211], a[92:95], a[4:7], a[208:211]\n"
    "buffer_load_dwordx4 v238, s[52:55], s77 offen offset:0 lds\n"
    "v_mfma_f32_16x16x32_bf16 a[212:215], a[96:99], a[0:3], a[212:215]\n"
    "v_mfma_f32_16x16x32_bf16 a[212:215], a[100:103], a[4:7], a[212:215]\n"
    "v_mfma_f32_16x16x32_bf16 v[16:19], a[0:3], a[104:107], v[16:19]\n"
    "v_mfma_f32_16x16x32_bf16 v[16:19], a[4:7], a[108:111], v[16:19]\n"
    "buffer_load_dwordx4 v239, s[52:55], s77 offen offset:1024 lds\n"
    "v_mfma_f32_16x16x32_bf16 v[20:23], a[0:3], a[112:115], v[20:23]\n"
    "v_mfma_f32_16x16x32_bf16 v[20:23], a[4:7], a[116:119], v[20:23]\n"
    "v_mfma_f32_16x16x32_bf16 v[24:27], a[0:3], a[120:123], v[24:27]\n"
    "v_mfma_f32_16x16x32_bf16 v[24:27], a[4:7], a[124:127], v[24:27]\n"
    "buffer_load_dwordx4 v240, s[52:55], s77 offen offset:2048 lds\n"
    "v_mfma_f32_16x16x32_bf16 v[28:31], a[0:3], a[128:131], v[28:31]\n"
    "v_mfma_f32_16x16x32_bf16 v[28:31], a[4:7], a[132:135], v[28:31]\n"
    "v_mfma_f32_16x16x32_bf16 a[216:219], a[72:75], a[8:11], a[216:219]\n"
    "v_mfma_f32_16x16x32_bf16 a[216:219], a[76:79], a[12:15], a[216:219]\n"
    "buffer_load_dwordx4 v241, s[52:55], s77 offen offset:3072 lds\n"
    "v_mfma_f32_16x16x32_bf16 a[220:223], a[80:83], a[8:11], a[220:223]\n"
    "v_mfma_f32_16x16x32_bf16 a[220:223], a[84:87], a[12:15], a[220:223]\n"
    "v_mfma_f32_16x16x32_bf16 a[224:227], a[88:91], a[8:11], a[224:227]\n"
    "v_mfma_f32_16x16x32_bf16 a[224:227], a[92:95], a[12:15], a[224:227]\n"
    "s_add_u32 m0, s80, 0x21000\n"
    "v_mfma_f32_16x16x32_bf16 a[228:231], a[96:99], a[8:11], a[228:231]\n"
    "buffer_load_dwordx4 v238, s[52:55], s78 offen offset:0 lds\n"
    "v_mfma_f32_16x16x32_bf16 a[228:231], a[100:103], a[12:15], a[228:231]\n"
    "v_mfma_f32_16x16x32_bf16 v[32:35], a[8:11], a[104:107], v[32:35]\n"
    "v_mfma_f32_16x16x32_bf16 v[32:35], a[12:15], a[108:111], v[32:35]\n"
    "v_mfma_f32_16x16x32_bf16 v[36:39], a[8:11], a[112:115], v[36:39]\n"
    "buffer_load_dwordx4 v239, s[52:55], s78 offen offset:1024 lds\n"
    "v_mfma_f32_16x16x32_bf16 v[36:39], a[12:15], a[116:119], v[36:39]\n"
    "v_mfma_f32_16x16x32_bf16 v[40:43], a[8:11], a[120:123], v[40:43]\n"
    "v_mfma_f32_16x16x32_bf16 v[40:43], a[12:15], a[124:127], v[40:43]\n"
    "v_mfma_f32_16x16x32_bf16 v[44:47], a[8:11], a[128:131], v[44:47]\n"
    "buffer_load_dwordx4 v240, s[52:55], s78 offen offset:2048 lds\n"
    "v_mfma_f32_16x16x32_bf16 v[44:47], a[12:15], a[132:135], v[44:47]\n"
    "v_mfma_f32_16x16x32_bf16 a[232:235], a[72:75], a[16:19], a[232:235]\n"
    "v_mfma_f32_16x16x32_bf16 a[232:235], a[76:79], a[20:23], a[232:235]\n"
    "v_mfma_f32_16x16x32_bf16 a[236:239], a[80:83], a[16:19], a[236:239]\n"
    "buffer_load_dwordx4 v241, s[52:55], s78 offen offset:3072 lds\n"
    "v_mfma_f32_16x16x32_bf16 a[236:239], a[84:87], a[20:23], a[236:239]\n"
    "v_mfma_f32_16x16x32_bf16 a[240:243], a[88:91], a[16:19], a[240:243]\n"
    "v_mfma_f32_16x16x32_bf16 a[240:243], a[92:95], a[20:23], a[240:243]\n"
    "v_mfma_f32_16x16x32_bf16 a[244:247], a[96:99], a[16:19], a[244:247]\n"
    "s_add_u32 s77, s77, s79\n"
    "v_mfma_f32_16x16x32_bf16 a[244:247], a[100:103], a[20:23], a[244:247]\n"
    "s_add_u32 s78, s78, s79\n"
    "v_mfma_f32_16x16x32_bf16 v[48:51], a[16:19], a[104:107], v[48:51]\n"
    "v_mfma_f32_16x16x32_bf16 v[48:51], a[20:23], a[108:111], v[48:51]\n"
    "v_mfma_f32_16x16x32_bf16 v[52:55], a[16:19], a[112:115], v[52:55]\n"
    "v_mfma_f32_16x16x32_bf16 v[52:55], a[20:23], a[116:119], v[52:55]\n"
    "v_mfma_f32_16x16x32_bf16 v[56:59], a[16:19], a[120:123], v[56:59]\n"
    "v_mfma_f32_16x16x32_bf16 v[56:59], a[20:23], a[124:127], v[56:59]\n"
    "v_mfma_f32_16x16x32_bf16 v[60:63], a[16:19], a[128:131], v[60:63]\n"
    "v_mfma_f32_16x16x32_bf16 v[60:63], a[20:23], a[132:135], v[60:63]\n"
    "v_mfma_f32_16x16x32_bf16 v[64:67], a[72:75], a[24:27], v[64:67]\n"
    "v_mfma_f32_16x16x32_bf16 v[64:67], a[76:79], a[28:31], v[64:67]\n"
    "v_mfma_f32_16x16x32_bf16 v[68:71], a[80:83], a[24:27], v[68:71]\n"
    "s_cmp_lg_u32 s68, 0\n"
    "s_cbranch_scc1 .Lw7_%=\n"
    "s_waitcnt vmcnt(16)\n"
    ".Lw7_%=:\n"
    "v_mfma_f32_16x16x32_bf16 v[68:71], a[84:87], a[28:31], v[68:71]\n"
    "s_cmp_lg_u32 s68, 0\n"
    "s_cbranch_scc1 .Lw9_%=\n"
    "s_mov_b32 s73, 0x40000\n"
    ".Lp8_%=:\n"
    "v_cmp_ne_u64 vcc, s[64:65], v[6:7]\n"
    "s_nop 4\n"
    "s_cbranch_vccz .Lq8_%=\n"
    "s_sleep 2\n"
    "buffer_load_dwordx2 v[6:7], v5, s[56:59], 0 offen sc1\n"
    "s_waitcnt vmcnt(0)\n"
    "s_sub_u32 s73, s73, 1\n"
    "s_cbranch_scc0 .Lp8_%=\n"
    ".Lq8_%=:\n"
    ".Lw9_%=:\n"
    "v_mfma_f32_16x16x32_bf16 v[72:75], a[88:91], a[24:27], v[72:75]\n"
    "v_mfma_f32_16x16x32_bf16 v[72:75], a[92:95], a[28:31], v[72:75]\n"
    "v_mfma_f32_16x16x32_bf16 v[76:79], a[96:99], a[24:27], v[76:79]\n"
    "v_mfma_f32_16x16x32_bf16 v[76:79], a[100:103], a[28:31], v[76:79]\n"
    "v_mfma_f32_16x16x32_bf16 v[80:83], a[24:27], a[104:107], v[80:83]\n"
    "v_add_u32 v228, 0x8000, v226\n"
    "v_mfma_f32_16x16x32_bf16 v[80:83], a[28:31], a[108:111], v[80:83]\n"
    "v_add_u32 v229, 0x8000, v227\n"
    "v_mfma_f32_16x16x32_bf16 v[84:87], a[24:27], a[112:115], v[84:87]\n"
    "v_mfma_f32_16x16x32_bf16 v[84:87], a[28:31], a[116:119], v[84:87]\n"
    "v_mfma_f32_16x16x32_bf16 v[88:91], a[24:27], a[120:123], v[88:91]\n"
    "s_waitcnt vmcnt(8)\n"
    "s_barrier\n"
    "v_mfma_f32_16x16x32_bf16 v[88:91], a[28:31], a[124:127], v[88:91]\n"
    "s_mov_b64 s[48:49], %9\n"
    "s_add_u32 s48, s48, 0x5000000\n"
    "s_addc_u32 s49, s49, 0\n"
    "s_mov_b32 s50, 0x200000\n"
    "s_mov_b32 s51, 0x20000\n"
    "v_mfma_f32_16x16x32_bf16 v[92:95], a[24:27], a[128:131], v[92:95]\n"
    "s_cmp_eq_u32 s71, 0\n"
    "s_cselect_b32 s73, s38, s39\n"
    "s_lshl_b32 s75, s73, 17\n"
    "v_mfma_f32_16x16x32_bf16 v[92:95], a[28:31], a[132:135], v[92:95]\n"
    "s_lshl_b32 s73, s72, 16\n"
    "s_add_u32 s75, s75, s73\n"
    "s_add_u32 s76, s75, 0x8000\n"
    "v_mfma_f32_16x16x32_bf16 v[96:99], a[72:75], a[32:35], v[96:99]\n"
    "ds_read_b64_tr_b16 a[136:137], v228 offset:0\n"
    "s_add_u32 m0, s80, 0x0\n"
    "v_mfma_f32_16x16x32_bf16 v[96:99], a[76:79], a[36:39], v[96:99]\n"
    "buffer_load_dwordx4 v230, s[48:51], s75 offen offset:0 sc1 lds\n"
    "v_mfma_f32_16x16x32_bf16 v[100:103], a[80:83], a[32:35], v[100:103]\n"
    "ds_read_b64_tr_b16 a[138:139], v228 offset:256\n"
    "v_mfma_f32_16x16x32_bf16 v[100:103], a[84:87], a[36:39], v[100:103]\n"
    "ds_read_b64_tr_b16 a[140:141], v228 offset:4096\n"
    "v_mfma_f32_16x16x32_bf16 v[104:107], a[88:91], a[32:35], v[104:107]\n"
    "ds_read_b64_tr_b16 a[142:143], v228 offset:4352\n"
    "v_mfma_f32_16x16x32_bf16 v[104:107], a[92:95], a[36:39], v[104:107]\n"
    "buffer_load_dwordx4 v231, s[48:51], s75 offen offset:1024 sc1 lds\n"
    "v_mfma_f32_16x16x32_bf16 v[108:111], a[96:99], a[32:35], v[108:111]\n"
    "ds_read_b64_tr_b16 a[144:145], v229 offset:0\n"
    "v_mfma_f32_16x16x32_bf16 v[108:111], a[100:103], a[36:39], v[108:111]\n"
    "ds_read_b64_tr_b16 a[146:147], v229 offset:256\n"
    "v_mfma_f32_16x16x32_bf16 v[112:115], a[32:35], a[104:107], v[112:115]\n"
    "ds_read_b64_tr_b16 a[148:149], v229 offset:4096\n"
    "v_mfma_f32_16x16x32_bf16 v[112:115], a[36:39], a[108:111], v[112:115]\n"
    "buffer_load_dwordx4 v232, s[48:51], s75 offen offset:2048 sc1 lds\n"
    "v_mfma_f32_16x16x32_bf16 v[116:119], a[32:35], a[112:115], v[116:119]\n"
    "ds_read_b64_tr_b16 a[150:151], v229 offset:4352\n"
    "v_mfma_f32_16x16x32_bf16 v[116:119], a[36:39], a[116:119], v[116:119]\n"
    "ds_read_b64_tr_b16 a[152:153], v228 offset:8192\n"
    "v_mfma_f32_16x16x32_bf16 v[120:123], a[32:35], a[120:123], v[120:123]\n"
    "ds_read_b64_tr_b16 a[154:155], v228 offset:8448\n"
    "v_mfma_f32_16x16x32_bf16 v[120:123], a[36:39], a[124:127], v[120:123]\n"
    "buffer_load_dwordx4 v233, s[48:51], s75 offen offset:3072 sc1 lds\n"
    "v_mfma_f32_16x16x32_bf16 v[124:127], a[32:35], a[128:131], v[124:127]\n"
    "ds_read_b64_tr_b16 a[156:157], v228 offset:12288\n"
    "v_mfma_f32_16x16x32_bf16 v[124:127], a[36:39], a[132:135], v[124:127]\n"
    "ds_read_b64_tr_b16 a[158:159], v228 offset:12544\n"
    "v_mfma_f32_16x16x32_bf16 v[128:131], a[72:75], a[40:43], v[128:131]\n"
    "ds_read_b128 a[32:35], v224 offset:40960\n"
    "v_mfma_f32_16x16x32_bf16 v[128:131], a[76:79], a[44:47], v[128:131]\n"
    "ds_read_b64_tr_b16 a[160:161], v229 offset:8192\n"
    "s_add_u32 m0, s80, 0x1000\n"
    "v_mfma_f32_16x16x32_bf16 v[132:135], a[80:83], a[40:43], v[132:135]\n"
    "buffer_load_dwordx4 v230, s[48:51], s76 offen offset:0 sc1 lds\n"
    "v_mfma_f32_16x16x32_bf16 v[132:135], a[84:87], a[44:47], v[132:135]\n"
    "ds_read_b128 a[36:39], v225 offset:40960\n"
    "v_mfma_f32_16x16x32_bf16 v[136:139], a[88:91], a[40:43], v[136:139]\n"
    "ds_read_b64_tr_b16 a[162:163], v229 offset:8448\n"
    "v_mfma_f32_16x16x32_bf16 v[136:139], a[92:95], a[44:47], v[136:139]\n"
    "ds_read_b64_tr_b16 a[164:165], v229 offset:12288\n"
    "v_mfma_f32_16x16x32_bf16 v[140:143], a[96:99], a[40:43], v[140:143]\n"
    "buffer_load_dwordx4 v231, s[48:51], s76 offen offset:1024 sc1 lds\n"
    "v_mfma_f32_16x16x32_bf16 v[140:143], a[100:103], a[44:47], v[140:143]\n"
    "ds_read_b64_tr_b16 a[166:167], v229 offset:12544\n"
    "v_mfma_f32_16x16x32_bf16 v[144:147], a[40:43], a[104:107], v[144:147]\n"
    "ds_read_b64_tr_b16 a[168:169], v228 offset:16384\n"
    "v_mfma_f32_16x16x32_bf16 v[144:147], a[44:47], a[108:111], v[144:147]\n"
    "ds_read_b64_tr_b16 a[170:171], v228 offset:16640\n"
    "v_mfma_f32_16x16x32_bf16 v[148:151], a[40:43], a[112:115], v[148:151]\n"
    "buffer_load_dwordx4 v232, s[48:51], s76 offen offset:2048 sc1 lds\n"
    "v_mfma_f32_16x16x32_bf16 v[148:151], a[44:47], a[116:119], v[148:151]\n"
    "ds_read_b64_tr_b16 a[172:173], v228 offset:20480\n"
    "v_mfma_f32_16x16x32_bf16 v[152:155], a[40:43], a[120:123], v[152:155]\n"
    "ds_read_b64_tr_b16 a[174:175], v228 offset:20736\n"
    "v_mfma_f32_16x16x32_bf16 v[152:155], a[44:47], a[124:127], v[152:155]\n"
    "ds_read_b64_tr_b16 a[176:177], v229 offset:16384\n"
    "v_mfma_f32_16x16x32_bf16 v[156:159], a[40:43], a[128:131], v[156:159]\n"
    "buffer_load_dwordx4 v233, s[48:51], s76 offen offset:3072 sc1 lds\n"
    "v_mfma_f32_16x16x32_bf16 v[156:159], a[44:47], a[132:135], v[156:159]\n"
    "ds_read_b64_tr_b16 a[178:179], v229 offset:16640\n"
    "v_mfma_f32_16x16x32_bf16 v[160:163], a[72:75], a[48:51], v[160:163]\n"
    "ds_read_b128 a[40:43], v224 offset:43008\n"
    "v_mfma_f32_16x16x32_bf16 v[160:163], a[76:79], a[52:55], v[160:163]\n"
    "ds_read_b64_tr_b16 a[180:181], v229 offset:20480\n"
    "v_mfma_f32_16x16x32_bf16 v[164:167], a[80:83], a[48:51], v[164:167]\n"
    "ds_read_b64_tr_b16 a[182:183], v229 offset:20736\n"
    "s_add_u32 s75, s75, 0x80\n"
    "v_mfma_f32_16x16x32_bf16 v[164:167], a[84:87], a[52:55], v[164:167]\n"
    "ds_read_b64_tr_b16 a[184:185], v228 offset:24576\n"
    "s_add_u32 s76, s76, 0x80\n"
    "v_mfma_f32_16x16x32_bf16 v[168:171], a[88:91], a[48:51], v[168:171]\n"
    "ds_read_b128 a[44:47], v225 offset:43008\n"
    "v_mfma_f32_16x16x32_bf16 v[168:171], a[92:95], a[52:55], v[168:171]\n"
    "ds_read_b64_tr_b16 a[186:187], v228 offset:24832\n"
    "v_mfma_f32_16x16x32_bf16 v[172:175], a[96:99], a[48:51], v[172:175]\n"
    "ds_read_b64_tr_b16 a[188:189], v228 offset:28672\n"
    "v_mfma_f32_16x16x32_bf16 v[172:175], a[100:103], a[52:55], v[172:175]\n"
    "ds_read_b64_tr_b16 a[190:191], v228 offset:28928\n"
    "v_mfma_f32_16x16x32_bf16 v[176:179], a[48:51], a[104:107], v[176:179]\n"
    "ds_read_b64_tr_b16 a[192:193], v229 offset:24576\n"
    "v_mfma_f32_16x16x32_bf16 v[176:179], a[52:55], a[108:111], v[176:179]\n"
    "ds_read_b64_tr_b16 a[194:195], v229 offset:24832\n"
    "v_mfma_f32_16x16x32_bf16 v[180:183], a[48:51], a[112:115], v[180:183]\n"
    "ds_read_b64_tr_b16 a[196:197], v229 offset:28672\n"
    "v_mfma_f32_16x16x32_bf16 v[180:183], a[52:55], a[116:119], v[180:183]\n"
    "ds_read_b64_tr_b16 a[198:199], v229 offset:28928\n"
    "v_mfma_f32_16x16x32_bf16 v[184:187], a[48:51], a[120:123], v[184:187]\n"
    "ds_read_b128 a[64:67], v224 offset:47104\n"
    "v_mfma_f32_16x16x32_bf16 v[184:187], a[52:55], a[124:127], v[184:187]\n"
    "ds_read_b128 a[68:71], v225 offset:47104\n"
    "v_mfma_f32_16x16x32_bf16 v[188:191], a[48:51], a[128:131], v[188:191]\n"
    "ds_read_b128 a[0:3], v224 offset:32768\n"
    "v_mfma_f32_16x16x32_bf16 v[188:191], a[52:55], a[132:135], v[188:191]\n"
    "ds_read_b128 a[4:7], v225 offset:32768\n"
    "v_mfma_f32_16x16x32_bf16 v[192:195], a[72:75], a[56:59], v[192:195]\n"
    "ds_read_b128 a[48:51], v224 offset:45056\n"
    "v_mfma_f32_16x16x32_bf16 v[192:195], a[76:79], a[60:63], v[192:195]\n"
    "ds_read_b128 a[8:11], v224 offset:34816\n"
    "v_mfma_f32_16x16x32_bf16 v[196:199], a[80:83], a[56:59], v[196:199]\n"
    "ds_read_b128 a[52:55], v225 offset:45056\n"
    "v_mfma_f32_16x16x32_bf16 v[196:199], a[84:87], a[60:63], v[196:199]\n"
    "ds_read_b128 a[12:15], v225 offset:34816\n"
    "v_mfma_f32_16x16x32_bf16 v[200:203], a[88:91], a[56:59], v[200:203]\n"
    "ds_read_b128 a[16:19], v224 offset:36864\n"
    "v_mfma_f32_16x16x32_bf16 v[200:203], a[92:95], a[60:63], v[200:203]\n"
    "ds_read_b128 a[20:23], v225 offset:36864\n"
    "v_mfma_f32_16x16x32_bf16 v[204:207], a[96:99], a[56:59], v[204:207]\n"
    "ds_read_b128 a[24:27], v224 offset:38912\n"
    "v_mfma_f32_16x16x32_bf16 v[204:207], a[100:103], a[60:63], v[204:207]\n"
    "ds_read_b128 a[28:31], v225 offset:38912\n"
    "v_mfma_f32_16x16x32_bf16 v[208:211], a[56:59], a[104:107], v[208:211]\n"
    "v_mfma_f32_16x16x32_bf16 v[208:211], a[60:63], a[108:111], v[208:211]\n"
    "v_mfma_f32_16x16x32_bf16 v[212:215], a[56:59], a[112:115], v[212:215]\n"
    "v_mfma_f32_16x16x32_bf16 v[212:215], a[60:63], a[116:119], v[212:215]\n"
    "v_mfma_f32_16x16x32_bf16 v[216:219], a[56:59], a[120:123], v[216:219]\n"
    "v_mfma_f32_16x16x32_bf16 v[216:219], a[60:63], a[124:127], v[216:219]\n"
    "v_mfma_f32_16x16x32_bf16 v[220:223], a[56:59], a[128:131], v[220:223]\n"
    "v_mfma_f32_16x16x32_bf16 v[220:223], a[60:63], a[132:135], v[220:223]\n"
    "s_waitcnt lgkmcnt(0)\n"
    "v_mfma_f32_16x16x32_bf16 a[200:203], a[136:139], a[0:3], a[200:203]\n"
    "v_mfma_f32_16x16x32_bf16 a[200:203], a[140:143], a[4:7], a[200:203]\n"
    "s_add_u32 m0, s80, 0x10000\n"
    "v_mfma_f32_16x16x32_bf16 a[204:207], a[144:147], a[0:3], a[204:207]\n"
    "buffer_load_dwordx4 v238, s[52:55], s77 offen offset:0 lds\n"
    "v_mfma_f32_16x16x32_bf16 a[204:207], a[148:151], a[4:7], a[204:207]\n"
    "v_mfma_f32_16x16x32_bf16 a[208:211], a[152:155], a[0:3], a[208:211]\n"
    "v_mfma_f32_16x16x32_bf16 a[208:211], a[156:159], a[4:7], a[208:211]\n"
    "buffer_load_dwordx4 v239, s[52:55], s77 offen offset:1024 lds\n"
    "v_mfma_f32_16x16x32_bf16 a[212:215], a[160:163], a[0:3], a[212:215]\n"
    "v_mfma_f32_16x16x32_bf16 a[212:215], a[164:167], a[4:7], a[212:215]\n"
    "v_mfma_f32_16x16x32_bf16 v[16:19], a[0:3], a[168:171], v[16:19]\n"
    "buffer_load_dwordx4 v240, s[52:55], s77 offen offset:2048 lds\n"
    "v_mfma_f32_16x16x32_bf16 v[16:19], a[4:7], a[172:175], v[16:19]\n"
    "v_mfma_f32_16x16x32_bf16 v[20:23], a[0:3], a[176:179], v[20:23]\n"
    "v_mfma_f32_16x16x32_bf16 v[20:23], a[4:7], a[180:183], v[20:23]\n"
    "buffer_load_dwordx4 v241, s[52:55], s77 offen offset:3072 lds\n"
    "v_mfma_f32_16x16x32_bf16 v[24:27], a[0:3], a[184:187], v[24:27]\n"
    "v_mfma_f32_16x16x32_bf16 v[24:27], a[4:7], a[188:191], v[24:27]\n"
    "v_mfma_f32_16x16x32_bf16 v[28:31], a[0:3], a[192:195], v[28:31]\n"
    "s_add_u32 m0, s80, 0x11000\n"
    "v_mfma_f32_16x16x32_bf16 v[28:31], a[4:7], a[196:199], v[28:31]\n"
    "buffer_load_dwordx4 v238, s[52:55], s78 offen offset:0 lds\n"
    "v_mfma_f32_16x16x32_bf16 a[216:219], a[136:139], a[8:11], a[216:219]\n"
    "v_mfma_f32_16x16x32_bf16 a[216:219], a[140:143], a[12:15], a[216:219]\n"
    "v_mfma_f32_16x16x32_bf16 a[220:223], a[144:147], a[8:11], a[220:223]\n"
    "buffer_load_dwordx4 v239, s[52:55], s78 offen offset:1024 lds\n"
    "v_mfma_f32_16x16x32_bf16 a[220:223], a[148:151], a[12:15], a[220:223]\n"
    "v_mfma_f32_16x16x32_bf16 a[224:227], a[152:155], a[8:11], a[224:227]\n"
    "v_mfma_f32_16x16x32_bf16 a[224:227], a[156:159], a[12:15], a[224:227]\n"
    "buffer_load_dwordx4 v240, s[52:55], s78 offen offset:2048 lds\n"
    "v_mfma_f32_16x16x32_bf16 a[228:231], a[160:163], a[8:11], a[228:231]\n"
    "v_mfma_f32_16x16x32_bf16 a[228:231], a[164:167], a[12:15], a[228:231]\n"
    "v_mfma_f32_16x16x32_bf16 v[32:35], a[8:11], a[168:171], v[32:35]\n"
    "buffer_load_dwordx4 v241, s[52:55], s78 offen offset:3072 lds\n"
    "v_mfma_f32_16x16x32_bf16 v[32:35], a[12:15], a[172:175], v[32:35]\n"
    "v_mfma_f32_16x16x32_bf16 v[36:39], a[8:11], a[176:179], v[36:39]\n"
    "v_mfma_f32_16x16x32_bf16 v[36:39], a[12:15], a[180:183], v[36:39]\n"
    "s_add_u32 s77, s77, s79\n"
    "v_mfma_f32_16x16x32_bf16 v[40:43], a[8:11], a[184:187], v[40:43]\n"
    "s_add_u32 s78, s78, s79\n"
    "v_mfma_f32_16x16x32_bf16 v[40:43], a[12:15], a[188:191], v[40:43]\n"
    "v_mfma_f32_16x16x32_bf16 v[44:47], a[8:11], a[192:195], v[44:47]\n"
    "v_mfma_f32_16x16x32_bf16 v[44:47], a[12:15], a[196:199], v[44:47]\n"
    "v_mfma_f32_16x16x32_bf16 a[232:235], a[136:139], a[16:19], a[232:235]\n"
    "v_mfma_f32_16x16x32_bf16 a[232:235], a[140:143], a[20:23], a[232:235]\n"
    "v_mfma_f32_16x16x32_bf16 a[236:239], a[144:147], a[16:19], a[236:239]\n"
    "v_mfma_f32_16x16x32_bf16 a[236:239], a[148:151], a[20:23], a[236:239]\n"
    "v_mfma_f32_16x16x32_bf16 a[240:243], a[152:155], a[16:19], a[240:243]\n"
    "v_accvgpr_read_b32 v245, a200\n"
    "v_accvgpr_read_b32 v246, a201\n"
    "v_cvt_pk_bf16_f32 v252, v245, v246\n"
    "v_mfma_f32_16x16x32_bf16 a[240:243], a[156:159], a[20:23], a[240:243]\n"
    "v_accvgpr_read_b32 v247, a202\n"
    "v_accvgpr_read_b32 v248, a203\n"
    "v_mfma_f32_16x16x32_bf16 a[244:247], a[160:163], a[16:19], a[244:247]\n"
    "v_cvt_pk_bf16_f32 v253, v247, v248\n"
    "v_accvgpr_read_b32 v245, a204\n"
    "v_accvgpr_read_b32 v246, a205\n"
    "v_mfma_f32_16x16x32_bf16 a[244:247], a[164:167], a[20:23], a[244:247]\n"
    "v_cvt_pk_bf16_f32 v254, v245, v246\n"
    "v_accvgpr_read_b32 v247, a206\n"
    "v_accvgpr_read_b32 v248, a207\n"
    "v_mfma_f32_16x16x32_bf16 v[48:51], a[16:19], a[168:171], v[48:51]\n"
    "v_cvt_pk_bf16_f32 v255, v247, v248\n"
    "s_add_u32 s82, s81, 0x0\n"
    "buffer_store_dwordx4 v[252:255], v243, s[40:43], s82 offen offset:0 sc1\n"
    "v_mfma_f32_16x16x32_bf16 v[48:51], a[20:23], a[172:175], v[48:51]\n"
    "v_accvgpr_read_b32 v245, a208\n"
    "v_accvgpr_read_b32 v246, a209\n"
    "v_mfma_f32_16x16x32_bf16 v[52:55], a[16:19], a[176:179], v[52:55]\n"
    "v_cvt_pk_bf16_f32 v234, v245, v246\n"
    "v_accvgpr_read_b32 v247, a210\n"
    "v_accvgpr_read_b32 v248, a211\n"
    "v_mfma_f32_16x16x32_bf16 v[52:55], a[20:23], a[180:183], v[52:55]\n"
    "v_cvt_pk_bf16_f32 v235, v247, v248\n"
    "v_accvgpr_read_b32 v245, a212\n"
    "v_mfma_f32_16x16x32_bf16 v[56:59], a[16:19], a[184:187], v[56:59]\n"
    "v_accvgpr_read_b32 v246, a213\n"
    "v_cvt_pk_bf16_f32 v236, v245, v246\n"
    "v_accvgpr_read_b32 v247, a214\n"
    "v_mfma_f32_16x16x32_bf16 v[56:59], a[20:23], a[188:191], v[56:59]\n"
    "v_accvgpr_read_b32 v248, a215\n"
    "v_cvt_pk_bf16_f32 v237, v247, v248\n"
    "s_add_u32 s82, s81, 0x0\n"
    "buffer_store_dwordx4 v[234:237], v243, s[40:43], s82 offen offset:2048 sc1\n"
    "v_mfma_f32_16x16x32_bf16 v[60:63], a[16:19], a[192:195], v[60:63]\n"
    "v_accvgpr_read_b32 v245, a216\n"
    "v_mfma_f32_16x16x32_bf16 v[60:63], a[20:23], a[196:199], v[60:63]\n"
    "v_accvgpr_read_b32 v246, a217\n"
    "v_cvt_pk_bf16_f32 v252, v245, v246\n"
    "v_accvgpr_read_b32 v247, a218\n"
    "v_mfma_f32_16x16x32_bf16 v[64:67], a[136:139], a[24:27], v[64:67]\n"
    "v_accvgpr_read_b32 v248, a219\n"
    "v_cvt_pk_bf16_f32 v253, v247, v248\n"
    "v_accvgpr_read_b32 v245, a220\n"
    "v_mfma_f32_16x16x32_bf16 v[64:67], a[140:143], a[28:31], v[64:67]\n"
    "v_accvgpr_read_b32 v246, a221\n"
    "v_cvt_pk_bf16_f32 v254, v245, v246\n"
    "v_mfma_f32_16x16x32_bf16 v[68:71], a[144:147], a[24:27], v[68:71]\n"
    "v_accvgpr_read_b32 v247, a222\n"
    "v_accvgpr_read_b32 v248, a223\n"
    "v_cvt_pk_bf16_f32 v255, v247, v248\n"
    "v_mfma_f32_16x16x32_bf16 v[68:71], a[148:151], a[28:31], v[68:71]\n"
    "s_add_u32 s82, s81, 0x1000\n"
    "buffer_store_dwordx4 v[252:255], v243, s[40:43], s82 offen offset:0 sc1\n"
    "v_mfma_f32_16x16x32_bf16 v[72:75], a[152:155], a[24:27], v[72:75]\n"
    "v_accvgpr_read_b32 v245, a224\n"
    "v_accvgpr_read_b32 v246, a225\n"
    "v_cvt_pk_bf16_f32 v234, v245, v246\n"
    "v_mfma_f32_16x16x32_bf16 v[72:75], a[156:159], a[28:31], v[72:75]\n"
    "v_accvgpr_read_b32 v247, a226\n"
    "v_accvgpr_read_b32 v248, a227\n"
    "v_cvt_pk_bf16_f32 v235, v247, v248\n"
    "v_mfma_f32_16x16x32_bf16 v[76:79], a[160:163], a[24:27], v[76:79]\n"
    "v_accvgpr_read_b32 v245, a228\n"
    "v_accvgpr_read_b32 v246, a229\n"
    "v_mfma_f32_16x16x32_bf16 v[76:79], a[164:167], a[28:31], v[76:79]\n"
    "v_cvt_pk_bf16_f32 v236, v245, v246\n"
    "v_accvgpr_read_b32 v247, a230\n"
    "v_accvgpr_read_b32 v248, a231\n"
    "v_mfma_f32_16x16x32_bf16 v[80:83], a[24:27], a[168:171], v[80:83]\n"
    "v_add_u32 v228, 0x10000, v226\n"
    "v_cvt_pk_bf16_f32 v237, v247, v248\n"
    "s_add_u32 s82, s81, 0x1000\n"
    "buffer_store_dwordx4 v[234:237], v243, s[40:43], s82 offen offset:2048 sc1\n"
    "v_mfma_f32_16x16x32_bf16 v[80:83], a[28:31], a[172:175], v[80:83]\n"
    "v_add_u32 v229, 0x10000, v227\n"
    "v_mfma_f32_16x16x32_bf16 v[84:87], a[24:27], a[176:179], v[84:87]\n"
    "v_cvt_pk_bf16_f32 v16, v16, v17\n"
    "v_cvt_pk_bf16_f32 v17, v18, v19\n"
    "v_cvt_pk_bf16_f32 v18, v32, v33\n"
    "v_mfma_f32_16x16x32_bf16 v[84:87], a[28:31], a[180:183], v[84:87]\n"
    "v_cvt_pk_bf16_f32 v19, v34, v35\n"
    "v_cvt_pk_bf16_f32 v20, v20, v21\n"
    "v_cvt_pk_bf16_f32 v21, v22, v23\n"
    "v_mfma_f32_16x16x32_bf16 v[88:91], a[24:27], a[184:187], v[88:91]\n"
    "s_waitcnt vmcnt(12)\n"
    "s_barrier\n"
    "v_mfma_f32_16x16x32_bf16 v[88:91], a[28:31], a[188:191], v[88:91]\n"
    "ds_read_b64_tr_b16 a[72:73], v228 offset:0\n"
    "s_add_u32 m0, s80, 0x8000\n"
    "v_cvt_pk_bf16_f32 v22, v36, v37\n"
    "v_mfma_f32_16x16x32_bf16 v[92:95], a[24:27], a[192:195], v[92:95]\n"
    "buffer_load_dwordx4 v230, s[48:51], s75 offen offset:0 sc1 lds\n"
    "v_cvt_pk_bf16_f32 v23, v38, v39\n"
    "v_mfma_f32_16x16x32_bf16 v[92:95], a[28:31], a[196:199], v[92:95]\n"
    "ds_read_b64_tr_b16 a[74:75], v228 offset:256\n"
    "v_cvt_pk_bf16_f32 v24, v24, v25\n"
    "v_mfma_f32_16x16x32_bf16 v[96:99], a[136:139], a[32:35], v[96:99]\n"
    "v_cvt_pk_bf16_f32 v25, v26, v27\n"
    "v_cvt_pk_bf16_f32 v26, v40, v41\n"
    "v_cvt_pk_bf16_f32 v27, v42, v43\n"
    "v_mfma_f32_16x16x32_bf16 v[96:99], a[140:143], a[36:39], v[96:99]\n"
    "ds_read_b64_tr_b16 a[76:77], v229 offset:0\n"
    "v_cvt_pk_bf16_f32 v28, v28, v29\n"
    "v_cvt_pk_bf16_f32 v29, v30, v31\n"
    "v_mfma_f32_16x16x32_bf16 v[100:103], a[144:147], a[32:35], v[100:103]\n"
    "buffer_load_dwordx4 v231, s[48:51], s75 offen offset:1024 sc1 lds\n"
    "v_mfma_f32_16x16x32_bf16 v[100:103], a[148:151], a[36:39], v[100:103]\n"
    "ds_read_b64_tr_b16 a[78:79], v229 offset:256\n"
    "v_cvt_pk_bf16_f32 v30, v44, v45\n"
    "v_cvt_pk_bf16_f32 v31, v46, v47\n"
    "v_mfma_f32_16x16x32_bf16 v[104:107], a[152:155], a[32:35], v[104:107]\n"
    "v_accvgpr_read_b32 v245, a232\n"
    "v_accvgpr_read_b32 v246, a233\n"
    "v_cvt_pk_bf16_f32 v252, v245, v246\n"
    "v_mfma_f32_16x16x32_bf16 v[104:107], a[156:159], a[36:39], v[104:107]\n"
    "ds_read_b64_tr_b16 a[80:81], v228 offset:8192\n"
    "v_accvgpr_read_b32 v247, a234\n"
    "v_accvgpr_read_b32 v248, a235\n"
    "v_cvt_pk_bf16_f32 v253, v247, v248\n"
    "v_mfma_f32_16x16x32_bf16 v[108:111], a[160:163], a[32:35], v[108:111]\n"
    "buffer_load_dwordx4 v232, s[48:51], s75 offen offset:2048 sc1 lds\n"
    "v_accvgpr_read_b32 v245, a236\n"
    "v_mfma_f32_16x16x32_bf16 v[108:111], a[164:167], a[36:39], v[108:111]\n"
    "ds_read_b64_tr_b16 a[82:83], v228 offset:8448\n"
    "v_accvgpr_read_b32 v246, a237\n"
    "v_cvt_pk_bf16_f32 v254, v245, v246\n"
    "v_accvgpr_read_b32 v247, a238\n"
    "v_mfma_f32_16x16x32_bf16 v[112:115], a[32:35], a[168:171], v[112:115]\n"
    "v_accvgpr_read_b32 v248, a239\n"
    "v_cvt_pk_bf16_f32 v255, v247, v248\n"
    "v_accvgpr_write_b32 a0, v252\n"
    "v_mfma_f32_16x16x32_bf16 v[112:115], a[36:39], a[172:175], v[112:115]\n"
    "ds_read_b64_tr_b16 a[84:85], v229 offset:8192\n"
    "v_accvgpr_write_b32 a1, v253\n"
    "v_accvgpr_write_b32 a2, v254\n"
    "v_accvgpr_write_b32 a3, v255\n"
    "v_mfma_f32_16x16x32_bf16 v[116:119], a[32:35], a[176:179], v[116:119]\n"
    "buffer_load_dwordx4 v233, s[48:51], s75 offen offset:3072 sc1 lds\n"
    "v_accvgpr_read_b32 v245, a240\n"
    "v_mfma_f32_16x16x32_bf16 v[116:119], a[36:39], a[180:183], v[116:119]\n"
    "ds_read_b64_tr_b16 a[86:87], v229 offset:8448\n"
    "v_accvgpr_read_b32 v246, a241\n"
    "v_cvt_pk_bf16_f32 v234, v245, v246\n"
    "v_accvgpr_read_b32 v247, a242\n"
    "v_mfma_f32_16x16x32_bf16 v[120:123], a[32:35], a[184:187], v[120:123]\n"
    "v_accvgpr_read_b32 v248, a243\n"
    "v_cvt_pk_bf16_f32 v235, v247, v248\n"
    "v_accvgpr_read_b32 v245, a244\n"
    "v_mfma_f32_16x16x32_bf16 v[120:123], a[36:39], a[188:191], v[120:123]\n"
    "ds_read_b64_tr_b16 a[88:89], v228 offset:16384\n"
    "v_accvgpr_read_b32 v246, a245\n"
    "v_cvt_pk_bf16_f32 v236, v245, v246\n"
    "v_accvgpr_read_b32 v247, a246\n"
    "v_mfma_f32_16x16x32_bf16 v[124:127], a[32:35], a[192:195], v[124:127]\n"
    "s_add_u32 m0, s80, 0x9000\n"
    "v_accvgpr_read_b32 v248, a247\n"
    "v_cvt_pk_bf16_f32 v237, v247, v248\n"
    "v_mfma_f32_16x16x32_bf16 v[124:127], a[36:39], a[196:199], v[124:127]\n"
    "buffer_load_dwordx4 v230, s[48:51], s76 offen offset:0 sc1 lds\n"
    "v_accvgpr_write_b32 a4, v234\n"
    "v_accvgpr_write_b32 a5, v235\n"
    "v_mfma_f32_16x16x32_bf16 v[128:131], a[136:139], a[40:43], v[128:131]\n"
    "ds_read_b64_tr_b16 a[90:91], v228 offset:16640\n"
    "v_accvgpr_write_b32 a6, v236\n"
    "v_accvgpr_write_b32 a7, v237\n"
    "v_mfma_f32_16x16x32_bf16 v[128:131], a[140:143], a[44:47], v[128:131]\n"
    "v_cvt_pk_bf16_f32 v64, v64, v65\n"
    "v_cvt_pk_bf16_f32 v65, v66, v67\n"
    "v_cvt_pk_bf16_f32 v66, v68, v69\n"
    "v_cvt_pk_bf16_f32 v67, v70, v71\n"
    "v_mfma_f32_16x16x32_bf16 v[132:135], a[144:147], a[40:43], v[132:135]\n"
    "ds_read_b64_tr_b16 a[92:93], v229 offset:16384\n"
    "v_accvgpr_write_b32 a8, v64\n"
    "v_accvgpr_write_b32 a9, v65\n"
    "v_mfma_f32_16x16x32_bf16 v[132:135], a[148:151], a[44:47], v[132:135]\n"
    "buffer_load_dwordx4 v231, s[48:51], s76 offen offset:1024 sc1 lds\n"
    "v_accvgpr_write_b32 a10, v66\n"
    "v_accvgpr_write_b32 a11, v67\n"
    "v_mfma_f32_16x16x32_bf16 v[136:139], a[152:155], a[40:43], v[136:139]\n"
    "ds_read_b64_tr_b16 a[94:95], v229 offset:16640\n"
    "v_cvt_pk_bf16_f32 v72, v72, v73\n"
    "v_cvt_pk_bf16_f32 v73, v74, v75\n"
    "v_mfma_f32_16x16x32_bf16 v[136:139], a[156:159], a[44:47], v[136:139]\n"
    "v_cvt_pk_bf16_f32 v74, v76, v77\n"
    "v_cvt_pk_bf16_f32 v75, v78, v79\n"
    "v_accvgpr_write_b32 a12, v72\n"
    "v_accvgpr_write_b32 a13, v73\n"
    "v_mfma_f32_16x16x32_bf16 v[140:143], a[160:163], a[40:43], v[140:143]\n"
    "ds_read_b64_tr_b16 a[96:97], v228 offset:24576\n"
    "v_accvgpr_write_b32 a14, v74\n"
    "v_accvgpr_write_b32 a15, v75\n"
    "v_mfma_f32_16x16x32_bf16 v[140:143], a[164:167], a[44:47], v[140:143]\n"
    "buffer_load_dwordx4 v232, s[48:51], s76 offen offset:2048 sc1 lds\n"
    "v_cvt_pk_bf16_f32 v48, v48, v49\n"
    "v_cvt_pk_bf16_f32 v49, v50, v51\n"
    "v_mfma_f32_16x16x32_bf16 v[144:147], a[40:43], a[168:171], v[144:147]\n"
    "ds_read_b64_tr_b16 a[98:99], v228 offset:24832\n"
    "v_cvt_pk_bf16_f32 v50, v80, v81\n"
    "v_cvt_pk_bf16_f32 v51, v82, v83\n"
    "v_mfma_f32_16x16x32_bf16 v[144:147], a[44:47], a[172:175], v[144:147]\n"
    "v_accvgpr_write_b32 a16, v48\n"
    "v_accvgpr_write_b32 a17, v49\n"
    "v_accvgpr_write_b32 a18, v50\n"
    "v_accvgpr_write_b32 a19, v51\n"
    "v_mfma_f32_16x16x32_bf16 v[148:151], a[40:43], a[176:179], v[148:151]\n"
    "ds_read_b64_tr_b16 a[100:101], v229 offset:24576\n"
    "v_cvt_pk_bf16_f32 v52, v52, v53\n"
    "v_cvt_pk_bf16_f32 v53, v54, v55\n"
    "v_mfma_f32_16x16x32_bf16 v[148:151], a[44:47], a[180:183], v[148:151]\n"
    "buffer_load_dwordx4 v233, s[48:51], s76 offen offset:3072 sc1 lds\n"
    "v_cvt_pk_bf16_f32 v54, v84, v85\n"
    "v_cvt_pk_bf16_f32 v55, v86, v87\n"
    "v_mfma_f32_16x16x32_bf16 v[152:155], a[40:43], a[184:187], v[152:155]\n"
    "ds_read_b64_tr_b16 a[102:103], v229 offset:24832\n"
    "v_accvgpr_write_b32 a20, v52\n"
    "v_accvgpr_write_b32 a21, v53\n"
    "v_mfma_f32_16x16x32_bf16 v[152:155], a[44:47], a[188:191], v[152:155]\n"
    "v_accvgpr_write_b32 a22, v54\n"
    "v_accvgpr_write_b32 a23, v55\n"
    "v_cvt_pk_bf16_f32 v56, v56, v57\n"
    "v_cvt_pk_bf16_f32 v57, v58, v59\n"
    "v_mfma_f32_16x16x32_bf16 v[156:159], a[40:43], a[192:195], v[156:159]\n"
    "ds_read_b128 a[104:107], v224 offset:0\n"
    "v_cvt_pk_bf16_f32 v58, v88, v89\n"
    "v_cvt_pk_bf16_f32 v59, v90, v91\n"
    "v_mfma_f32_16x16x32_bf16 v[156:159], a[44:47], a[196:199], v[156:159]\n"
    "s_add_u32 s75, s75, 0x80\n"
    "v_accvgpr_write_b32 a24, v56\n"
    "v_accvgpr_write_b32 a25, v57\n"
    "v_accvgpr_write_b32 a26, v58\n"
    "v_mfma_f32_16x16x32_bf16 v[160:163], a[136:139], a[48:51], v[160:163]\n"
    "ds_read_b128 a[108:111], v224 offset:2048\n"
    "s_add_u32 s76, s76, 0x80\n"
    "v_accvgpr_write_b32 a27, v59\n"
    "v_mfma_f32_16x16x32_bf16 v[160:163], a[140:143], a[52:55], v[160:163]\n"
    "s_add_u32 s82, s81, 0x0\n"
    "buffer_store_dwordx4 v[16:19], v244, s[44:47], s82 offen offset:0 sc1\n"
    "v_cvt_pk_bf16_f32 v60, v60, v61\n"
    "v_cvt_pk_bf16_f32 v61, v62, v63\n"
    "v_mfma_f32_16x16x32_bf16 v[164:167], a[144:147], a[48:51], v[164:167]\n"
    "ds_read_b128 a[112:115], v224 offset:4096\n"
    "v_cvt_pk_bf16_f32 v62, v92, v93\n"
    "v_cvt_pk_bf16_f32 v63, v94, v95\n"
    "v_mfma_f32_16x16x32_bf16 v[164:167], a[148:151], a[52:55], v[164:167]\n"
    "v_accvgpr_write_b32 a28, v60\n"
    "v_accvgpr_write_b32 a29, v61\n"
    "v_accvgpr_write_b32 a30, v62\n"
    "v_accvgpr_write_b32 a31, v63\n"
    "v_mfma_f32_16x16x32_bf16 v[168:171], a[152:155], a[48:51], v[168:171]\n"
    "ds_read_b128 a[116:119], v224 offset:6144\n"
    "v_cvt_pk_bf16_f32 v96, v96, v97\n"
    "v_cvt_pk_bf16_f32 v97, v98, v99\n"
    "v_mfma_f32_16x16x32_bf16 v[168:171], a[156:159], a[52:55], v[168:171]\n"
    "v_cvt_pk_bf16_f32 v98, v100, v101\n"
    "v_cvt_pk_bf16_f32 v99, v102, v103\n"
    "v_mfma_f32_16x16x32_bf16 v[172:175], a[160:163], a[48:51], v[172:175]\n"
    "ds_read_b128 a[120:123], v224 offset:8192\n"
    "v_accvgpr_write_b32 a32, v96\n"
    "v_accvgpr_write_b32 a33, v97\n"
    "v_mfma_f32_16x16x32_bf16 v[172:175], a[164:167], a[52:55], v[172:175]\n"
    "v_accvgpr_write_b32 a34, v98\n"
    "v_accvgpr_write_b32 a35, v99\n"
    "v_cvt_pk_bf16_f32 v104, v104, v105\n"
    "v_mfma_f32_16x16x32_bf16 v[176:179], a[48:51], a[168:171], v[176:179]\n"
    "ds_read_b128 a[124:127], v224 offset:10240\n"
    "v_cvt_pk_bf16_f32 v105, v106, v107\n"
    "v_mfma_f32_16x16x32_bf16 v[176:179], a[52:55], a[172:175], v[176:179]\n"
    "s_add_u32 s82, s81, 0x0\n"
    "buffer_store_dwordx4 v[20:23], v244, s[44:47], s82 offen offset:1024 sc1\n"
    "v_cvt_pk_bf16_f32 v106, v108, v109\n"
    "v_mfma_f32_16x16x32_bf16 v[180:183], a[48:51], a[176:179], v[180:183]\n"
    "ds_read_b128 a[128:131], v224 offset:12288\n"
    "v_cvt_pk_bf16_f32 v107, v110, v111\n"
    "v_accvgpr_write_b32 a36, v104\n"
    "v_mfma_f32_16x16x32_bf16 v[180:183], a[52:55], a[180:183], v[180:183]\n"
    "v_accvgpr_write_b32 a37, v105\n"
    "v_accvgpr_write_b32 a38, v106\n"
    "v_mfma_f32_16x16x32_bf16 v[184:187], a[48:51], a[184:187], v[184:187]\n"
    "ds_read_b128 a[132:135], v224 offset:14336\n"
    "v_accvgpr_write_b32 a39, v107\n"
    "v_cvt_pk_bf16_f32 v128, v128, v129\n"
    "v_mfma_f32_16x16x32_bf16 v[184:187], a[52:55], a[188:191], v[184:187]\n"
    "v_cvt_pk_bf16_f32 v129, v130, v131\n"
    "v_cvt_pk_bf16_f32 v130, v132, v133\n"
    "v_cvt_pk_bf16_f32 v131, v134, v135\n"
    "v_mfma_f32_16x16x32_bf16 v[188:191], a[48:51], a[192:195], v[188:191]\n"
    "v_accvgpr_write_b32 a40, v128\n"
    "v_accvgpr_write_b32 a41, v129\n"
    "v_mfma_f32_16x16x32_bf16 v[188:191], a[52:55], a[196:199], v[188:191]\n"
    "v_accvgpr_write_b32 a42, v130\n"
    "v_accvgpr_write_b32 a43, v131\n"
    "v_cvt_pk_bf16_f32 v136, v136, v137\n"
    "v_mfma_f32_16x16x32_bf16 v[192:195], a[136:139], a[64:67], v[192:195]\n"
    "v_cvt_pk_bf16_f32 v137, v138, v139\n"
    "v_cvt_pk_bf16_f32 v138, v140, v141\n"
    "v_cvt_pk_bf16_f32 v139, v142, v143\n"
    "v_mfma_f32_16x16x32_bf16 v[192:195], a[140:143], a[68:71], v[192:195]\n"
    "s_add_u32 s82, s81, 0x1000\n"
    "buffer_store_dwordx4 v[24:27], v244, s[44:47], s82 offen offset:0 sc1\n"
    "v_mfma_f32_16x16x32_bf16 v[196:199], a[144:147], a[64:67], v[196:199]\n"
    "v_accvgpr_write_b32 a44, v136\n"
    "v_accvgpr_write_b32 a45, v137\n"
    "v_accvgpr_write_b32 a46, v138\n"
    "v_mfma_f32_16x16x32_bf16 v[196:199], a[148:151], a[68:71], v[196:199]\n"
    "v_accvgpr_write_b32 a47, v139\n"
    "v_cvt_pk_bf16_f32 v112, v112, v113\n"
    "v_cvt_pk_bf16_f32 v113, v114, v115\n"
    "v_mfma_f32_16x16x32_bf16 v[200:203], a[152:155], a[64:67], v[200:203]\n"
    "v_cvt_pk_bf16_f32 v114, v144, v145\n"
    "v_cvt_pk_bf16_f32 v115, v146, v147\n"
    "v_accvgpr_write_b32 a56, v112\n"
    "v_mfma_f32_16x16x32_bf16 v[200:203], a[156:159], a[68:71], v[200:203]\n"
    "v_accvgpr_write_b32 a57, v113\n"
    "v_accvgpr_write_b32 a58, v114\n"
    "v_mfma_f32_16x16x32_bf16 v[204:207], a[160:163], a[64:67], v[204:207]\n"
    "v_accvgpr_write_b32 a59, v115\n"
    "v_cvt_pk_bf16_f32 v116, v116, v117\n"
    "v_cvt_pk_bf16_f32 v117, v118, v119\n"
    "v_mfma_f32_16x16x32_bf16 v[204:207], a[164:167], a[68:71], v[204:207]\n"
    "v_cvt_pk_bf16_f32 v118, v148, v149\n"
    "v_cvt_pk_bf16_f32 v119, v150, v151\n"
    "v_accvgpr_write_b32 a60, v116\n"
    "v_mfma_f32_16x16x32_bf16 v[208:211], a[64:67], a[168:171], v[208:211]\n"
    "v_accvgpr_write_b32 a61, v117\n"
    "v_accvgpr_write_b32 a62, v118\n"
    "v_mfma_f32_16x16x32_bf16 v[208:211], a[68:71], a[172:175], v[208:211]\n"
    "s_add_u32 s82, s81, 0x1000\n"
    "buffer_store_dwordx4 v[28:31], v244, s[44:47], s82 offen offset:1024 sc1\n"
    "v_accvgpr_write_b32 a63, v119\n"
    "v_mfma_f32_16x16x32_bf16 v[212:215], a[64:67], a[176:179], v[212:215]\n"
    "v_cvt_pk_bf16_f32 v120, v120, v121\n"
    "v_cvt_pk_bf16_f32 v121, v122, v123\n"
    "v_cvt_pk_bf16_f32 v122, v152, v153\n"
    "v_mfma_f32_16x16x32_bf16 v[212:215], a[68:71], a[180:183], v[212:215]\n"
    "v_cvt_pk_bf16_f32 v123, v154, v155\n"
    "v_accvgpr_write_b32 a248, v120\n"
    "v_mfma_f32_16x16x32_bf16 v[216:219], a[64:67], a[184:187], v[216:219]\n"
    "v_accvgpr_write_b32 a249, v121\n"
    "v_accvgpr_write_b32 a250, v122\n"
    "v_accvgpr_write_b32 a251, v123\n"
    "v_mfma_f32_16x16x32_bf16 v[216:219], a[68:71], a[188:191], v[216:219]\n"
    "v_cvt_pk_bf16_f32 v124, v124, v125\n"
    "v_cvt_pk_bf16_f32 v125, v126, v127\n"
    "v_cvt_pk_bf16_f32 v126, v156, v157\n"
    "v_mfma_f32_16x16x32_bf16 v[220:223], a[64:67], a[192:195], v[220:223]\n"
    "v_cvt_pk_bf16_f32 v127, v158, v159\n"
    "v_accvgpr_write_b32 a252, v124\n"
    "v_mfma_f32_16x16x32_bf16 v[220:223], a[68:71], a[196:199], v[220:223]\n"
    "v_accvgpr_write_b32 a253, v125\n"
    "v_accvgpr_write_b32 a254, v126\n"
    "v_accvgpr_write_b32 a255, v127\n"
    "s_waitcnt lgkmcnt(0)\n"
    "v_mfma_f32_16x16x32_bf16 a[200:203], a[72:75], a[104:107], 0\n"
    "v_mfma_f32_16x16x32_bf16 a[204:207], a[76:79], a[104:107], 0\n"
    "v_mfma_f32_16x16x32_bf16 a[208:211], a[80:83], a[104:107], 0\n"
    "v_mfma_f32_16x16x32_bf16 a[212:215], a[84:87], a[104:107], 0\n"
    "s_add_u32 m0, s80, 0x18000\n"
    "v_cvt_pk_bf16_f32 v160, v160, v161\n"
    "v_cvt_pk_bf16_f32 v161, v162, v163\n"
    "v_mfma_f32_16x16x32_bf16 v[16:19], a[88:91], a[104:107], 0\n"
    "buffer_load_dwordx4 v238, s[52:55], s77 offen offset:0 lds\n"
    "v_mfma_f32_16x16x32_bf16 v[20:23], a[92:95], a[104:107], 0\n"
    "ds_read_b64_tr_b16 a[136:137], v228 offset:4096\n"
    "v_cvt_pk_bf16_f32 v162, v164, v165\n"
    "v_cvt_pk_bf16_f32 v163, v166, v167\n"
    "v_mfma_f32_16x16x32_bf16 v[24:27], a[96:99], a[104:107], 0\n"
    "ds_read_b64_tr_b16 a[138:139], v228 offset:4352\n"
    "v_accvgpr_write_b32 a168, v160\n"
    "v_mfma_f32_16x16x32_bf16 v[28:31], a[100:103], a[104:107], 0\n"
    "v_accvgpr_write_b32 a169, v161\n"
    "v_accvgpr_write_b32 a170, v162\n"
    "v_accvgpr_write_b32 a171, v163\n"
    "v_mfma_f32_16x16x32_bf16 a[216:219], a[72:75], a[108:111], 0\n"
    "ds_read_b128 a[104:107], v225 offset:0\n"
    "v_cvt_pk_bf16_f32 v168, v168, v169\n"
    "v_mfma_f32_16x16x32_bf16 a[220:223], a[76:79], a[108:111], 0\n"
    "buffer_load_dwordx4 v239, s[52:55], s77 offen offset:1024 lds\n"
    "v_cvt_pk_bf16_f32 v169, v170, v171\n"
    "v_mfma_f32_16x16x32_bf16 a[224:227], a[80:83], a[108:111], 0\n"
    "ds_read_b64_tr_b16 a[140:141], v229 offset:4096\n"
    "v_cvt_pk_bf16_f32 v170, v172, v173\n"
    "v_cvt_pk_bf16_f32 v171, v174, v175\n"
    "v_mfma_f32_16x16x32_bf16 a[228:231], a[84:87], a[108:111], 0\n"
    "ds_read_b64_tr_b16 a[142:143], v229 offset:4352\n"
    "v_accvgpr_write_b32 a172, v168\n"
    "v_mfma_f32_16x16x32_bf16 v[32:35], a[88:91], a[108:111], 0\n"
    "v_accvgpr_write_b32 a173, v169\n"
    "v_accvgpr_write_b32 a174, v170\n"
    "v_accvgpr_write_b32 a175, v171\n"
    "v_mfma_f32_16x16x32_bf16 v[36:39], a[92:95], a[108:111], 0\n"
    "buffer_load_dwordx4 v240, s[52:55], s77 offen offset:2048 lds\n"
    "v_mfma_f32_16x16x32_bf16 v[40:43], a[96:99], a[108:111], 0\n"
    "ds_read_b64_tr_b16 a[144:145], v228 offset:12288\n"
    "v_cvt_pk_bf16_f32 v192, v192, v193\n"
    "v_cvt_pk_bf16_f32 v193, v194, v195\n"
    "v_mfma_f32_16x16x32_bf16 v[44:47], a[100:103], a[108:111], 0\n"
    "v_cvt_pk_bf16_f32 v194, v196, v197\n"
    "v_cvt_pk_bf16_f32 v195, v198, v199\n"
    "v_mfma_f32_16x16x32_bf16 a[232:235], a[72:75], a[112:115], 0\n"
    "ds_read_b128 a[108:111], v225 offset:2048\n"
    "v_accvgpr_write_b32 a176, v192\n"
    "v_accvgpr_write_b32 a177, v193\n"
    "v_mfma_f32_16x16x32_bf16 a[236:239], a[76:79], a[112:115], 0\n"
    "buffer_load_dwordx4 v241, s[52:55], s77 offen offset:3072 lds\n"
    "v_accvgpr_write_b32 a178, v194\n"
    "v_mfma_f32_16x16x32_bf16 a[240:243], a[80:83], a[112:115], 0\n"
    "ds_read_b64_tr_b16 a[146:147], v228 offset:12544\n"
    "v_accvgpr_write_b32 a179, v195\n"
    "v_mfma_f32_16x16x32_bf16 a[244:247], a[84:87], a[112:115], 0\n"
    "ds_read_b64_tr_b16 a[148:149], v229 offset:12288\n"
    "v_cvt_pk_bf16_f32 v200, v200, v201\n"
    "v_cvt_pk_bf16_f32 v201, v202, v203\n"
    "v_mfma_f32_16x16x32_bf16 v[48:51], a[88:91], a[112:115], 0\n"
    "v_cvt_pk_bf16_f32 v202, v204, v205\n"
    "v_cvt_pk_bf16_f32 v203, v206, v207\n"
    "v_mfma_f32_16x16x32_bf16 v[52:55], a[92:95], a[112:115], 0\n"
    "s_add_u32 m0, s80, 0x19000\n"
    "v_accvgpr_write_b32 a180, v200\n"
    "v_accvgpr_write_b32 a181, v201\n"
    "v_mfma_f32_16x16x32_bf16 v[56:59], a[96:99], a[112:115], 0\n"
    "buffer_load_dwordx4 v238, s[52:55], s78 offen offset:0 lds\n"
    "v_mfma_f32_16x16x32_bf16 v[60:63], a[100:103], a[112:115], 0\n"
    "ds_read_b64_tr_b16 a[150:151], v229 offset:12544\n"
    "v_accvgpr_write_b32 a182, v202\n"
    "v_accvgpr_write_b32 a183, v203\n"
    "v_mfma_f32_16x16x32_bf16 v[64:67], a[72:75], a[116:119], 0\n"
    "ds_read_b128 a[112:115], v225 offset:4096\n"
    "v_cvt_pk_bf16_f32 v176, v176, v177\n"
    "v_cvt_pk_bf16_f32 v177, v178, v179\n"
    "v_mfma_f32_16x16x32_bf16 v[68:71], a[76:79], a[116:119], 0\n"
    "ds_read_b64_tr_b16 a[152:153], v228 offset:20480\n"
    "v_cvt_pk_bf16_f32 v178, v208, v209\n"
    "v_mfma_f32_16x16x32_bf16 v[72:75], a[80:83], a[116:119], 0\n"
    "buffer_load_dwordx4 v239, s[52:55], s78 offen offset:1024 lds\n"
    "v_cvt_pk_bf16_f32 v179, v210, v211\n"
    "v_mfma_f32_16x16x32_bf16 v[76:79], a[84:87], a[116:119], 0\n"
    "ds_read_b64_tr_b16 a[154:155], v228 offset:20736\n"
    "v_accvgpr_write_b32 a184, v176\n"
    "v_mfma_f32_16x16x32_bf16 v[80:83], a[88:91], a[116:119], 0\n"
    "v_accvgpr_write_b32 a185, v177\n"
    "v_accvgpr_write_b32 a186, v178\n"
    "v_accvgpr_write_b32 a187, v179\n"
    "v_mfma_f32_16x16x32_bf16 v[84:87], a[92:95], a[116:119], 0\n"
    "ds_read_b64_tr_b16 a[156:157], v229 offset:20480\n"
    "v_cvt_pk_bf16_f32 v180, v180, v181\n"
    "v_mfma_f32_16x16x32_bf16 v[88:91], a[96:99], a[116:119], 0\n"
    "buffer_load_dwordx4 v240, s[52:55], s78 offen offset:2048 lds\n"
    "v_cvt_pk_bf16_f32 v181, v182, v183\n"
    "v_mfma_f32_16x16x32_bf16 v[92:95], a[100:103], a[116:119], 0\n"
    "ds_read_b64_tr_b16 a[158:159], v229 offset:20736\n"
    "v_cvt_pk_bf16_f32 v182, v212, v213\n"
    "v_cvt_pk_bf16_f32 v183, v214, v215\n"
    "v_mfma_f32_16x16x32_bf16 v[96:99], a[72:75], a[120:123], 0\n"
    "ds_read_b128 a[116:119], v225 offset:6144\n"
    "v_accvgpr_write_b32 a188, v180\n"
    "v_mfma_f32_16x16x32_bf16 v[100:103], a[76:79], a[120:123], 0\n"
    "v_accvgpr_write_b32 a189, v181\n"
    "v_accvgpr_write_b32 a190, v182\n"
    "v_accvgpr_write_b32 a191, v183\n"
    "v_mfma_f32_16x16x32_bf16 v[104:107], a[80:83], a[120:123], 0\n"
    "buffer_load_dwordx4 v241, s[52:55], s78 offen offset:3072 lds\n"
    "v_mfma_f32_16x16x32_bf16 v[108:111], a[84:87], a[120:123], 0\n"
    "ds_read_b64_tr_b16 a[160:161], v228 offset:28672\n"
    "v_cvt_pk_bf16_f32 v184, v184, v185\n"
    "v_cvt_pk_bf16_f32 v185, v186, v187\n"
    "v_mfma_f32_16x16x32_bf16 v[112:115], a[88:91], a[120:123], 0\n"
    "ds_read_b64_tr_b16 a[162:163], v228 offset:28928\n"
    "v_cvt_pk_bf16_f32 v186, v216, v217\n"
    "v_mfma_f32_16x16x32_bf16 v[116:119], a[92:95], a[120:123], 0\n"
    "v_cvt_pk_bf16_f32 v187, v218, v219\n"
    "v_accvgpr_write_b32 a192, v184\n"
    "v_accvgpr_write_b32 a193, v185\n"
    "v_mfma_f32_16x16x32_bf16 v[120:123], a[96:99], a[120:123], 0\n"
    "s_add_u32 s77, s77, s79\n"
    "v_accvgpr_write_b32 a194, v186\n"
    "v_accvgpr_write_b32 a195, v187\n"
    "v_mfma_f32_16x16x32_bf16 v[124:127], a[100:103], a[120:123], 0\n"
    "s_add_u32 s78, s78, s79\n"
    "v_cvt_pk_bf16_f32 v188, v188, v189\n"
    "v_mfma_f32_16x16x32_bf16 v[128:131], a[72:75], a[124:127], 0\n"
    "ds_read_b128 a[120:123], v225 offset:8192\n"
    "v_cvt_pk_bf16_f32 v189, v190, v191\n"
    "v_cvt_pk_bf16_f32 v190, v220, v221\n"
    "v_mfma_f32_16x16x32_bf16 v[132:135], a[76:79], a[124:127], 0\n"
    "ds_read_b64_tr_b16 a[164:165], v229 offset:28672\n"
    "v_cvt_pk_bf16_f32 v191, v222, v223\n"
    "v_mfma_f32_16x16x32_bf16 v[136:139], a[80:83], a[124:127], 0\n"
    "ds_read_b64_tr_b16 a[166:167], v229 offset:28928\n"
    "v_accvgpr_write_b32 a196, v188\n"
    "v_accvgpr_write_b32 a197, v189\n"
    "v_mfma_f32_16x16x32_bf16 v[140:143], a[84:87], a[124:127], 0\n"
    "s_add_u32 s82, s81, 0x2000\n"
    "buffer_store_dwordx4 a[0:3], v243, s[40:43], s82 offen offset:0 sc1\n"
    "v_mfma_f32_16x16x32_bf16 v[144:147], a[88:91], a[124:127], 0\n"
    "ds_read_b128 a[48:51], v225 offset:12288\n"
    "v_accvgpr_write_b32 a198, v190\n"
    "v_accvgpr_write_b32 a199, v191\n"
    "v_mfma_f32_16x16x32_bf16 v[148:151], a[92:95], a[124:127], 0\n"
    "v_mfma_f32_16x16x32_bf16 v[152:155], a[96:99], a[124:127], 0\n"
    "ds_read_b128 a[52:55], v225 offset:14336\n"
    "v_mfma_f32_16x16x32_bf16 v[156:159], a[100:103], a[124:127], 0\n"
    "s_add_u32 s82, s81, 0x2000\n"
    "buffer_store_dwordx4 a[4:7], v243, s[40:43], s82 offen offset:2048 sc1\n"
    "v_mfma_f32_16x16x32_bf16 v[160:163], a[72:75], a[128:131], 0\n"
    "ds_read_b128 a[124:127], v225 offset:10240\n"
    "v_mfma_f32_16x16x32_bf16 v[164:167], a[76:79], a[128:131], 0\n"
    "v_mfma_f32_16x16x32_bf16 v[168:171], a[80:83], a[128:131], 0\n"
    "v_mfma_f32_16x16x32_bf16 v[172:175], a[84:87], a[128:131], 0\n"
    "v_mfma_f32_16x16x32_bf16 v[176:179], a[88:91], a[128:131], 0\n"
    "v_mfma_f32_16x16x32_bf16 v[180:183], a[92:95], a[128:131], 0\n"
    "v_mfma_f32_16x16x32_bf16 v[184:187], a[96:99], a[128:131], 0\n"
    "v_mfma_f32_16x16x32_bf16 v[188:191], a[100:103], a[128:131], 0\n"
    "v_mfma_f32_16x16x32_bf16 v[192:195], a[72:75], a[132:135], 0\n"
    "v_mfma_f32_16x16x32_bf16 v[196:199], a[76:79], a[132:135], 0\n"
    "v_mfma_f32_16x16x32_bf16 v[200:203], a[80:83], a[132:135], 0\n"
    "v_mfma_f32_16x16x32_bf16 v[204:207], a[84:87], a[132:135], 0\n"
    "v_mfma_f32_16x16x32_bf16 v[208:211], a[88:91], a[132:135], 0\n"
    "v_mfma_f32_16x16x32_bf16 v[212:215], a[92:95], a[132:135], 0\n"
    "v_mfma_f32_16x16x32_bf16 v[216:219], a[96:99], a[132:135], 0\n"
    "v_mfma_f32_16x16x32_bf16 v[220:223], a[100:103], a[132:135], 0\n"
    "s_waitcnt lgkmcnt(0)\n"
    "v_mfma_f32_16x16x32_bf16 a[200:203], a[136:139], a[104:107], a[200:203]\n"
    "s_waitcnt vmcnt(14)\n"
    "s_barrier\n"
    "v_mfma_f32_16x16x32_bf16 a[204:207], a[140:143], a[104:107], a[204:207]\n"
    "v_add_u32 v228, 0x0, v226\n"
    "v_mfma_f32_16x16x32_bf16 a[208:211], a[144:147], a[104:107], a[208:211]\n"
    "v_add_u32 v229, 0x0, v227\n"
    "v_mfma_f32_16x16x32_bf16 a[212:215], a[148:151], a[104:107], a[212:215]\n"
    "s_add_u32 m0, s80, 0x0\n"
    "v_mfma_f32_16x16x32_bf16 v[16:19], a[152:155], a[104:107], v[16:19]\n"
    "buffer_load_dwordx4 v230, s[48:51], s75 offen offset:0 sc1 lds\n"
    "v_mfma_f32_16x16x32_bf16 v[20:23], a[156:159], a[104:107], v[20:23]\n"
    "ds_read_b64_tr_b16 a[72:73], v228 offset:0\n"
    "v_mfma_f32_16x16x32_bf16 v[24:27], a[160:163], a[104:107], v[24:27]\n"
    "ds_read_b64_tr_b16 a[74:75], v228 offset:256\n"
    "v_mfma_f32_16x16x32_bf16 v[28:31], a[164:167], a[104:107], v[28:31]\n"
    "v_mfma_f32_16x16x32_bf16 a[216:219], a[136:139], a[108:111], a[216:219]\n"
    "ds_read_b128 a[104:107], v224 offset:32768\n"
    "v_mfma_f32_16x16x32_bf16 a[220:223], a[140:143], a[108:111], a[220:223]\n"
    "buffer_load_dwordx4 v231, s[48:51], s75 offen offset:1024 sc1 lds\n"
    "v_mfma_f32_16x16x32_bf16 a[224:227], a[144:147], a[108:111], a[224:227]\n"
    "ds_read_b64_tr_b16 a[76:77], v229 offset:0\n"
    "v_mfma_f32_16x16x32_bf16 a[228:231], a[148:151], a[108:111], a[228:231]\n"
    "ds_read_b64_tr_b16 a[78:79], v229 offset:256\n"
    "v_mfma_f32_16x16x32_bf16 v[32:35], a[152:155], a[108:111], v[32:35]\n"
    "v_mfma_f32_16x16x32_bf16 v[36:39], a[156:159], a[108:111], v[36:39]\n"
    "buffer_load_dwordx4 v232, s[48:51], s75 offen offset:2048 sc1 lds\n"
    "v_mfma_f32_16x16x32_bf16 v[40:43], a[160:163], a[108:111], v[40:43]\n"
    "ds_read_b64_tr_b16 a[80:81], v228 offset:8192\n"
    "v_mfma_f32_16x16x32_bf16 v[44:47], a[164:167], a[108:111], v[44:47]\n"
    "v_mfma_f32_16x16x32_bf16 a[232:235], a[136:139], a[112:115], a[232:235]\n"
    "ds_read_b128 a[108:111], v224 offset:34816\n"
    "v_mfma_f32_16x16x32_bf16 a[236:239], a[140:143], a[112:115], a[236:239]\n"
    "buffer_load_dwordx4 v233, s[48:51], s75 offen offset:3072 sc1 lds\n"
    "v_mfma_f32_16x16x32_bf16 a[240:243], a[144:147], a[112:115], a[240:243]\n"
    "ds_read_b64_tr_b16 a[82:83], v228 offset:8448\n"
    "v_mfma_f32_16x16x32_bf16 a[244:247], a[148:151], a[112:115], a[244:247]\n"
    "ds_read_b64_tr_b16 a[84:85], v229 offset:8192\n"
    "v_mfma_f32_16x16x32_bf16 v[48:51], a[152:155], a[112:115], v[48:51]\n"
    "v_mfma_f32_16x16x32_bf16 v[52:55], a[156:159], a[112:115], v[52:55]\n"
    "s_add_u32 m0, s80, 0x1000\n"
    "v_mfma_f32_16x16x32_bf16 v[56:59], a[160:163], a[112:115], v[56:59]\n"
    "buffer_load_dwordx4 v230, s[48:51], s76 offen offset:0 sc1 lds\n"
    "v_mfma_f32_16x16x32_bf16 v[60:63], a[164:167], a[112:115], v[60:63]\n"
    "ds_read_b64_tr_b16 a[86:87], v229 offset:8448\n"
    "v_mfma_f32_16x16x32_bf16 v[64:67], a[136:139], a[116:119], v[64:67]\n"
    "ds_read_b128 a[112:115], v224 offset:36864\n"
    "v_mfma_f32_16x16x32_bf16 v[68:71], a[140:143], a[116:119], v[68:71]\n"
    "ds_read_b64_tr_b16 a[88:89], v228 offset:16384\n"
    "v_mfma_f32_16x16x32_bf16 v[72:75], a[144:147], a[116:119], v[72:75]\n"
    "buffer_load_dwordx4 v231, s[48:51], s76 offen offset:1024 sc1 lds\n"
    "v_mfma_f32_16x16x32_bf16 v[76:79], a[148:151], a[116:119], v[76:79]\n"
    "ds_read_b64_tr_b16 a[90:91], v228 offset:16640\n"
    "v_mfma_f32_16x16x32_bf16 v[80:83], a[152:155], a[116:119], v[80:83]\n"
    "v_mfma_f32_16x16x32_bf16 v[84:87], a[156:159], a[116:119], v[84:87]\n"
    "ds_read_b64_tr_b16 a[92:93], v229 offset:16384\n"
    "v_mfma_f32_16x16x32_bf16 v[88:91], a[160:163], a[116:119], v[88:91]\n"
    "buffer_load_dwordx4 v232, s[48:51], s76 offen offset:2048 sc1 lds\n"
    "v_mfma_f32_16x16x32_bf16 v[92:95], a[164:167], a[116:119], v[92:95]\n"
    "ds_read_b64_tr_b16 a[94:95], v229 offset:16640\n"
    "v_mfma_f32_16x16x32_bf16 v[96:99], a[136:139], a[120:123], v[96:99]\n"
    "ds_read_b128 a[116:119], v224 offset:38912\n"
    "v_mfma_f32_16x16x32_bf16 v[100:103], a[140:143], a[120:123], v[100:103]\n"
    "v_mfma_f32_16x16x32_bf16 v[104:107], a[144:147], a[120:123], v[104:107]\n"
    "buffer_load_dwordx4 v233, s[48:51], s76 offen offset:3072 sc1 lds\n"
    "v_mfma_f32_16x16x32_bf16 v[108:111], a[148:151], a[120:123], v[108:111]\n"
    "ds_read_b64_tr_b16 a[96:97], v228 offset:24576\n"
    "v_mfma_f32_16x16x32_bf16 v[112:115], a[152:155], a[120:123], v[112:115]\n"
    "ds_read_b64_tr_b16 a[98:99], v228 offset:24832\n"
    "v_mfma_f32_16x16x32_bf16 v[116:119], a[156:159], a[120:123], v[116:119]\n"
    "v_mfma_f32_16x16x32_bf16 v[120:123], a[160:163], a[120:123], v[120:123]\n"
    "s_add_u32 s75, s75, 0x80\n"
    "v_mfma_f32_16x16x32_bf16 v[124:127], a[164:167], a[120:123], v[124:127]\n"
    "s_add_u32 s76, s76, 0x80\n"
    "v_mfma_f32_16x16x32_bf16 v[128:131], a[136:139], a[124:127], v[128:131]\n"
    "ds_read_b128 a[120:123], v224 offset:40960\n"
    "v_mfma_f32_16x16x32_bf16 v[132:135], a[140:143], a[124:127], v[132:135]\n"
    "ds_read_b64_tr_b16 a[100:101], v229 offset:24576\n"
    "v_mfma_f32_16x16x32_bf16 v[136:139], a[144:147], a[124:127], v[136:139]\n"
    "ds_read_b64_tr_b16 a[102:103], v229 offset:24832\n"
    "v_mfma_f32_16x16x32_bf16 v[140:143], a[148:151], a[124:127], v[140:143]\n"
    "s_add_u32 s82, s81, 0x3000\n"
    "buffer_store_dwordx4 a[8:11], v243, s[40:43], s82 offen offset:0 sc1\n"
    "v_mfma_f32_16x16x32_bf16 v[144:147], a[152:155], a[124:127], v[144:147]\n"
    "ds_read_b128 a[128:131], v224 offset:45056\n"
    "v_mfma_f32_16x16x32_bf16 v[148:151], a[156:159], a[124:127], v[148:151]\n"
    "v_mfma_f32_16x16x32_bf16 v[152:155], a[160:163], a[124:127], v[152:155]\n"
    "ds_read_b128 a[132:135], v224 offset:47104\n"
    "v_mfma_f32_16x16x32_bf16 v[156:159], a[164:167], a[124:127], v[156:159]\n"
    "s_add_u32 s82, s81, 0x3000\n"
    "buffer_store_dwordx4 a[12:15], v243, s[40:43], s82 offen offset:2048 sc1\n"
    "v_mfma_f32_16x16x32_bf16 v[160:163], a[136:139], a[48:51], v[160:163]\n"
    "ds_read_b128 a[124:127], v224 offset:43008\n"
    "v_mfma_f32_16x16x32_bf16 v[164:167], a[140:143], a[48:51], v[164:167]\n"
    "v_mfma_f32_16x16x32_bf16 v[168:171], a[144:147], a[48:51], v[168:171]\n"
    "s_add_u32 s82, s81, 0x2000\n"
    "buffer_store_dwordx4 a[16:19], v244, s[44:47], s82 offen offset:0 sc1\n"
    "v_mfma_f32_16x16x32_bf16 v[172:175], a[148:151], a[48:51], v[172:175]\n"
    "v_mfma_f32_16x16x32_bf16 v[176:179], a[152:155], a[48:51], v[176:179]\n"
    "v_mfma_f32_16x16x32_bf16 v[180:183], a[156:159], a[48:51], v[180:183]\n"
    "v_mfma_f32_16x16x32_bf16 v[184:187], a[160:163], a[48:51], v[184:187]\n"
    "v_mfma_f32_16x16x32_bf16 v[188:191], a[164:167], a[48:51], v[188:191]\n"
    "v_mfma_f32_16x16x32_bf16 v[192:195], a[136:139], a[52:55], v[192:195]\n"
    "v_mfma_f32_16x16x32_bf16 v[196:199], a[140:143], a[52:55], v[196:199]\n"
    "v_mfma_f32_16x16x32_bf16 v[200:203], a[144:147], a[52:55], v[200:203]\n"
    "v_mfma_f32_16x16x32_bf16 v[204:207], a[148:151], a[52:55], v[204:207]\n"
    "v_mfma_f32_16x16x32_bf16 v[208:211], a[152:155], a[52:55], v[208:211]\n"
    "v_mfma_f32_16x16x32_bf16 v[212:215], a[156:159], a[52:55], v[212:215]\n"
    "v_mfma_f32_16x16x32_bf16 v[216:219], a[160:163], a[52:55], v[216:219]\n"
    "v_mfma_f32_16x16x32_bf16 v[220:223], a[164:167], a[52:55], v[220:223]\n"
    "s_waitcnt lgkmcnt(0)\n"
    "v_mfma_f32_16x16x32_bf16 a[200:203], a[72:75], a[104:107], a[200:203]\n"
    "v_mfma_f32_16x16x32_bf16 a[204:207], a[76:79], a[104:107], a[204:207]\n"
    "v_mfma_f32_16x16x32_bf16 a[208:211], a[80:83], a[104:107], a[208:211]\n"
    "v_mfma_f32_16x16x32_bf16 a[212:215], a[84:87], a[104:107], a[212:215]\n"
    "s_add_u32 m0, s80, 0x20000\n"
    "v_mfma_f32_16x16x32_bf16 v[16:19], a[88:91], a[104:107], v[16:19]\n"
    "buffer_load_dwordx4 v238, s[52:55], s77 offen offset:0 lds\n"
    "v_mfma_f32_16x16x32_bf16 v[20:23], a[92:95], a[104:107], v[20:23]\n"
    "ds_read_b64_tr_b16 a[136:137], v228 offset:4096\n"
    "v_mfma_f32_16x16x32_bf16 v[24:27], a[96:99], a[104:107], v[24:27]\n"
    "ds_read_b64_tr_b16 a[138:139], v228 offset:4352\n"
    "v_mfma_f32_16x16x32_bf16 v[28:31], a[100:103], a[104:107], v[28:31]\n"
    "v_mfma_f32_16x16x32_bf16 a[216:219], a[72:75], a[108:111], a[216:219]\n"
    "ds_read_b128 a[104:107], v225 offset:32768\n"
    "v_mfma_f32_16x16x32_bf16 a[220:223], a[76:79], a[108:111], a[220:223]\n"
    "buffer_load_dwordx4 v239, s[52:55], s77 offen offset:1024 lds\n"
    "v_mfma_f32_16x16x32_bf16 a[224:227], a[80:83], a[108:111], a[224:227]\n"
    "ds_read_b64_tr_b16 a[140:141], v229 offset:4096\n"
    "v_mfma_f32_16x16x32_bf16 a[228:231], a[84:87], a[108:111], a[228:231]\n"
    "ds_read_b64_tr_b16 a[142:143], v229 offset:4352\n"
    "v_mfma_f32_16x16x32_bf16 v[32:35], a[88:91], a[108:111], v[32:35]\n"
    "v_mfma_f32_16x16x32_bf16 v[36:39], a[92:95], a[108:111], v[36:39]\n"
    "buffer_load_dwordx4 v240, s[52:55], s77 offen offset:2048 lds\n"
    "v_mfma_f32_16x16x32_bf16 v[40:43], a[96:99], a[108:111], v[40:43]\n"
    "ds_read_b64_tr_b16 a[144:145], v228 offset:12288\n"
    "v_mfma_f32_16x16x32_bf16 v[44:47], a[100:103], a[108:111], v[44:47]\n"
    "v_mfma_f32_16x16x32_bf16 a[232:235], a[72:75], a[112:115], a[232:235]\n"
    "ds_read_b128 a[108:111], v225 offset:34816\n"
    "v_mfma_f32_16x16x32_bf16 a[236:239], a[76:79], a[112:115], a[236:239]\n"
    "buffer_load_dwordx4 v241, s[52:55], s77 offen offset:3072 lds\n"
    "v_mfma_f32_16x16x32_bf16 a[240:243], a[80:83], a[112:115], a[240:243]\n"
    "ds_read_b64_tr_b16 a[146:147], v228 offset:12544\n"
    "v_mfma_f32_16x16x32_bf16 a[244:247], a[84:87], a[112:115], a[244:247]\n"
    "ds_read_b64_tr_b16 a[148:149], v229 offset:12288\n"
    "v_mfma_f32_16x16x32_bf16 v[48:51], a[88:91], a[112:115], v[48:51]\n"
    "v_mfma_f32_16x16x32_bf16 v[52:55], a[92:95], a[112:115], v[52:55]\n"
    "s_add_u32 m0, s80, 0x21000\n"
    "v_mfma_f32_16x16x32_bf16 v[56:59], a[96:99], a[112:115], v[56:59]\n"
    "buffer_load_dwordx4 v238, s[52:55], s78 offen offset:0 lds\n"
    "v_mfma_f32_16x16x32_bf16 v[60:63], a[100:103], a[112:115], v[60:63]\n"
    "ds_read_b64_tr_b16 a[150:151], v229 offset:12544\n"
    "v_mfma_f32_16x16x32_bf16 v[64:67], a[72:75], a[116:119], v[64:67]\n"
    "ds_read_b128 a[112:115], v225 offset:36864\n"
    "v_mfma_f32_16x16x32_bf16 v[68:71], a[76:79], a[116:119], v[68:71]\n"
    "ds_read_b64_tr_b16 a[152:153], v228 offset:20480\n"
    "v_mfma_f32_16x16x32_bf16 v[72:75], a[80:83], a[116:119], v[72:75]\n"
    "buffer_load_dwordx4 v239, s[52:55], s78 offen offset:1024 lds\n"
    "v_mfma_f32_16x16x32_bf16 v[76:79], a[84:87], a[116:119], v[76:79]\n"
    "ds_read_b64_tr_b16 a[154:155], v228 offset:20736\n"
    "v_mfma_f32_16x16x32_bf16 v[80:83], a[88:91], a[116:119], v[80:83]\n"
    "v_mfma_f32_16x16x32_bf16 v[84:87], a[92:95], a[116:119], v[84:87]\n"
    "ds_read_b64_tr_b16 a[156:157], v229 offset:20480\n"
    "v_mfma_f32_16x16x32_bf16 v[88:91], a[96:99], a[116:119], v[88:91]\n"
    "buffer_load_dwordx4 v240, s[52:55], s78 offen offset:2048 lds\n"
    "v_mfma_f32_16x16x32_bf16 v[92:95], a[100:103], a[116:119], v[92:95]\n"
    "ds_read_b64_tr_b16 a[158:159], v229 offset:20736\n"
    "v_mfma_f32_16x16x32_bf16 v[96:99], a[72:75], a[120:123], v[96:99]\n"
    "ds_read_b128 a[116:119], v225 offset:38912\n"
    "v_mfma_f32_16x16x32_bf16 v[100:103], a[76:79], a[120:123], v[100:103]\n"
    "v_mfma_f32_16x16x32_bf16 v[104:107], a[80:83], a[120:123], v[104:107]\n"
    "buffer_load_dwordx4 v241, s[52:55], s78 offen offset:3072 lds\n"
    "v_mfma_f32_16x16x32_bf16 v[108:111], a[84:87], a[120:123], v[108:111]\n"
    "ds_read_b64_tr_b16 a[160:161], v228 offset:28672\n"
    "v_mfma_f32_16x16x32_bf16 v[112:115], a[88:91], a[120:123], v[112:115]\n"
    "ds_read_b64_tr_b16 a[162:163], v228 offset:28928\n"
    "v_mfma_f32_16x16x32_bf16 v[116:119], a[92:95], a[120:123], v[116:119]\n"
    "v_mfma_f32_16x16x32_bf16 v[120:123], a[96:99], a[120:123], v[120:123]\n"
    "s_add_u32 s77, s77, s79\n"
    "v_mfma_f32_16x16x32_bf16 v[124:127], a[100:103], a[120:123], v[124:127]\n"
    "s_add_u32 s78, s78, s79\n"
    "v_mfma_f32_16x16x32_bf16 v[128:131], a[72:75], a[124:127], v[128:131]\n"
    "ds_read_b128 a[120:123], v225 offset:40960\n"
    "v_mfma_f32_16x16x32_bf16 v[132:135], a[76:79], a[124:127], v[132:135]\n"
    "ds_read_b64_tr_b16 a[164:165], v229 offset:28672\n"
    "v_mfma_f32_16x16x32_bf16 v[136:139], a[80:83], a[124:127], v[136:139]\n"
    "ds_read_b64_tr_b16 a[166:167], v229 offset:28928\n"
    "v_mfma_f32_16x16x32_bf16 v[140:143], a[84:87], a[124:127], v[140:143]\n"
    "s_add_u32 s82, s81, 0x2000\n"
    "buffer_store_dwordx4 a[20:23], v244, s[44:47], s82 offen offset:1024 sc1\n"
    "v_mfma_f32_16x16x32_bf16 v[144:147], a[88:91], a[124:127], v[144:147]\n"
    "ds_read_b128 a[48:51], v225 offset:45056\n"
    "v_mfma_f32_16x16x32_bf16 v[148:151], a[92:95], a[124:127], v[148:151]\n"
    "v_mfma_f32_16x16x32_bf16 v[152:155], a[96:99], a[124:127], v[152:155]\n"
    "ds_read_b128 a[52:55], v225 offset:47104\n"
    "v_mfma_f32_16x16x32_bf16 v[156:159], a[100:103], a[124:127], v[156:159]\n"
    "s_add_u32 s82, s81, 0x3000\n"
    "buffer_store_dwordx4 a[24:27], v244, s[44:47], s82 offen offset:0 sc1\n"
    "v_mfma_f32_16x16x32_bf16 v[160:163], a[72:75], a[128:131], v[160:163]\n"
    "ds_read_b128 a[124:127], v225 offset:43008\n"
    "v_mfma_f32_16x16x32_bf16 v[164:167], a[76:79], a[128:131], v[164:167]\n"
    "v_mfma_f32_16x16x32_bf16 v[168:171], a[80:83], a[128:131], v[168:171]\n"
    "v_mfma_f32_16x16x32_bf16 v[172:175], a[84:87], a[128:131], v[172:175]\n"
    "v_mfma_f32_16x16x32_bf16 v[176:179], a[88:91], a[128:131], v[176:179]\n"
    "v_mfma_f32_16x16x32_bf16 v[180:183], a[92:95], a[128:131], v[180:183]\n"
    "v_mfma_f32_16x16x32_bf16 v[184:187], a[96:99], a[128:131], v[184:187]\n"
    "v_mfma_f32_16x16x32_bf16 v[188:191], a[100:103], a[128:131], v[188:191]\n"
    "v_mfma_f32_16x16x32_bf16 v[192:195], a[72:75], a[132:135], v[192:195]\n"
    "v_mfma_f32_16x16x32_bf16 v[196:199], a[76:79], a[132:135], v[196:199]\n"
    "v_mfma_f32_16x16x32_bf16 v[200:203], a[80:83], a[132:135], v[200:203]\n"
    "v_mfma_f32_16x16x32_bf16 v[204:207], a[84:87], a[132:135], v[204:207]\n"
    "v_mfma_f32_16x16x32_bf16 v[208:211], a[88:91], a[132:135], v[208:211]\n"
    "v_mfma_f32_16x16x32_bf16 v[212:215], a[92:95], a[132:135], v[212:215]\n"
    "v_mfma_f32_16x16x32_bf16 v[216:219], a[96:99], a[132:135], v[216:219]\n"
    "v_mfma_f32_16x16x32_bf16 v[220:223], a[100:103], a[132:135], v[220:223]\n"
    "s_waitcnt lgkmcnt(0)\n"
    "v_mfma_f32_16x16x32_bf16 a[200:203], a[136:139], a[104:107], a[200:203]\n"
    "s_waitcnt vmcnt(13)\n"
    "s_barrier\n"
    "v_mfma_f32_16x16x32_bf16 a[204:207], a[140:143], a[104:107], a[204:207]\n"
    "s_or_b32 s73, s66, s67\n"
    "s_or_b32 s73, s73, s68\n"
    "s_cbranch_scc1 .Lw10_%=\n"
    "v_lshlrev_b32 v5, 1, v8\n"
    "v_add_u32 v5, 0x1000, v5\n"
    "buffer_load_dwordx4 v[234:237], v5, s[56:59], 0 offen sc1\n"
    "buffer_load_dwordx4 v[252:255], v5, s[56:59], 0 offen offset:16 sc1\n"
    ".Lw10_%=:\n"
    "v_mfma_f32_16x16x32_bf16 a[208:211], a[144:147], a[104:107], a[208:211]\n"
    "v_add_u32 v228, 0x8000, v226\n"
    "v_mfma_f32_16x16x32_bf16 a[212:215], a[148:151], a[104:107], a[212:215]\n"
    "v_add_u32 v229, 0x8000, v227\n"
    "v_mfma_f32_16x16x32_bf16 v[16:19], a[152:155], a[104:107], v[16:19]\n"
    "s_add_u32 m0, s80, 0x8000\n"
    "v_mfma_f32_16x16x32_bf16 v[20:23], a[156:159], a[104:107], v[20:23]\n"
    "buffer_load_dwordx4 v230, s[48:51], s75 offen offset:0 sc1 lds\n"
    "v_mfma_f32_16x16x32_bf16 v[24:27], a[160:163], a[104:107], v[24:27]\n"
    "ds_read_b64_tr_b16 a[72:73], v228 offset:0\n"
    "v_mfma_f32_16x16x32_bf16 v[28:31], a[164:167], a[104:107], v[28:31]\n"
    "ds_read_b64_tr_b16 a[74:75], v228 offset:256\n"
    "v_mfma_f32_16x16x32_bf16 a[216:219], a[136:139], a[108:111], a[216:219]\n"
    "ds_read_b128 a[104:107], v224 offset:0\n"
    "v_mfma_f32_16x16x32_bf16 a[220:223], a[140:143], a[108:111], a[220:223]\n"
    "buffer_load_dwordx4 v231, s[48:51], s75 offen offset:1024 sc1 lds\n"
    "v_mfma_f32_16x16x32_bf16 a[224:227], a[144:147], a[108:111], a[224:227]\n"
    "ds_read_b64_tr_b16 a[76:77], v229 offset:0\n"
    "v_mfma_f32_16x16x32_bf16 a[228:231], a[148:151], a[108:111], a[228:231]\n"
    "ds_read_b64_tr_b16 a[78:79], v229 offset:256\n"
    "v_mfma_f32_16x16x32_bf16 v[32:35], a[152:155], a[108:111], v[32:35]\n"
    "v_mfma_f32_16x16x32_bf16 v[36:39], a[156:159], a[108:111], v[36:39]\n"
    "buffer_load_dwordx4 v232, s[48:51], s75 offen offset:2048 sc1 lds\n"
    "v_mfma_f32_16x16x32_bf16 v[40:43], a[160:163], a[108:111], v[40:43]\n"
    "ds_read_b64_tr_b16 a[80:81], v228 offset:8192\n"
    "v_mfma_f32_16x16x32_bf16 v[44:47], a[164:167], a[108:111], v[44:47]\n"
    "v_mfma_f32_16x16x32_bf16 a[232:235], a[136:139], a[112:115], a[232:235]\n"
    "ds_read_b128 a[108:111], v224 offset:2048\n"
    "v_mfma_f32_16x16x32_bf16 a[236:239], a[140:143], a[112:115], a[236:239]\n"
    "buffer_load_dwordx4 v233, s[48:51], s75 offen offset:3072 sc1 lds\n"
    "v_mfma_f32_16x16x32_bf16 a[240:243], a[144:147], a[112:115], a[240:243]\n"
    "ds_read_b64_tr_b16 a[82:83], v228 offset:8448\n"
    "v_mfma_f32_16x16x32_bf16 a[244:247], a[148:151], a[112:115], a[244:247]\n"
    "ds_read_b64_tr_b16 a[84:85], v229 offset:8192\n"
    "v_mfma_f32_16x16x32_bf16 v[48:51], a[152:155], a[112:115], v[48:51]\n"
    "v_mfma_f32_16x16x32_bf16 v[52:55], a[156:159], a[112:115], v[52:55]\n"
    "s_add_u32 m0, s80, 0x9000\n"
    "v_mfma_f32_16x16x32_bf16 v[56:59], a[160:163], a[112:115], v[56:59]\n"
    "buffer_load_dwordx4 v230, s[48:51], s76 offen offset:0 sc1 lds\n"
    "v_mfma_f32_16x16x32_bf16 v[60:63], a[164:167], a[112:115], v[60:63]\n"
    "ds_read_b64_tr_b16 a[86:87], v229 offset:8448\n"
    "v_mfma_f32_16x16x32_bf16 v[64:67], a[136:139], a[116:119], v[64:67]\n"
    "ds_read_b128 a[112:115], v224 offset:4096\n"
    "v_mfma_f32_16x16x32_bf16 v[68:71], a[140:143], a[116:119], v[68:71]\n"
    "ds_read_b64_tr_b16 a[88:89], v228 offset:16384\n"
    "v_mfma_f32_16x16x32_bf16 v[72:75], a[144:147], a[116:119], v[72:75]\n"
    "buffer_load_dwordx4 v231, s[48:51], s76 offen offset:1024 sc1 lds\n"
    "v_mfma_f32_16x16x32_bf16 v[76:79], a[148:151], a[116:119], v[76:79]\n"
    "ds_read_b64_tr_b16 a[90:91], v228 offset:16640\n"
    "v_mfma_f32_16x16x32_bf16 v[80:83], a[152:155], a[116:119], v[80:83]\n"
    "v_mfma_f32_16x16x32_bf16 v[84:87], a[156:159], a[116:119], v[84:87]\n"
    "ds_read_b64_tr_b16 a[92:93], v229 offset:16384\n"
    "v_mfma_f32_16x16x32_bf16 v[88:91], a[160:163], a[116:119], v[88:91]\n"
    "buffer_load_dwordx4 v232, s[48:51], s76 offen offset:2048 sc1 lds\n"
    "v_mfma_f32_16x16x32_bf16 v[92:95], a[164:167], a[116:119], v[92:95]\n"
    "ds_read_b64_tr_b16 a[94:95], v229 offset:16640\n"
    "v_mfma_f32_16x16x32_bf16 v[96:99], a[136:139], a[120:123], v[96:99]\n"
    "ds_read_b128 a[116:119], v224 offset:6144\n"
    "v_mfma_f32_16x16x32_bf16 v[100:103], a[140:143], a[120:123], v[100:103]\n"
    "v_mfma_f32_16x16x32_bf16 v[104:107], a[144:147], a[120:123], v[104:107]\n"
    "buffer_load_dwordx4 v233, s[48:51], s76 offen offset:3072 sc1 lds\n"
    "v_mfma_f32_16x16x32_bf16 v[108:111], a[148:151], a[120:123], v[108:111]\n"
    "ds_read_b64_tr_b16 a[96:97], v228 offset:24576\n"
    "v_mfma_f32_16x16x32_bf16 v[112:115], a[152:155], a[120:123], v[112:115]\n"
    "ds_read_b64_tr_b16 a[98:99], v228 offset:24832\n"
    "v_mfma_f32_16x16x32_bf16 v[116:119], a[156:159], a[120:123], v[116:119]\n"
    "v_mfma_f32_16x16x32_bf16 v[120:123], a[160:163], a[120:123], v[120:123]\n"
    "s_add_u32 s75, s75, 0x80\n"
    "v_mfma_f32_16x16x32_bf16 v[124:127], a[164:167], a[120:123], v[124:127]\n"
    "s_add_u32 s76, s76, 0x80\n"
    "v_mfma_f32_16x16x32_bf16 v[128:131], a[136:139], a[124:127], v[128:131]\n"
    "ds_read_b128 a[120:123], v224 offset:8192\n"
    "v_mfma_f32_16x16x32_bf16 v[132:135], a[140:143], a[124:127], v[132:135]\n"
    "ds_read_b64_tr_b16 a[100:101], v229 offset:24576\n"
    "v_mfma_f32_16x16x32_bf16 v[136:139], a[144:147], a[124:127], v[136:139]\n"
    "ds_read_b64_tr_b16 a[102:103], v229 offset:24832\n"
    "v_mfma_f32_16x16x32_bf16 v[140:143], a[148:151], a[124:127], v[140:143]\n"
    "s_add_u32 s82, s81, 0x3000\n"
    "buffer_store_dwordx4 a[28:31], v244, s[44:47], s82 offen offset:1024 sc1\n"
    "v_mfma_f32_16x16x32_bf16 v[144:147], a[152:155], a[124:127], v[144:147]\n"
    "ds_read_b128 a[128:131], v224 offset:12288\n"
    "v_mfma_f32_16x16x32_bf16 v[148:151], a[156:159], a[124:127], v[148:151]\n"
    "v_mfma_f32_16x16x32_bf16 v[152:155], a[160:163], a[124:127], v[152:155]\n"
    "ds_read_b128 a[132:135], v224 offset:14336\n"
    "v_mfma_f32_16x16x32_bf16 v[156:159], a[164:167], a[124:127], v[156:159]\n"
    "s_add_u32 s82, s81, 0x4000\n"
    "buffer_store_dwordx4 a[32:35], v243, s[40:43], s82 offen offset:0 sc1\n"
    "v_mfma_f32_16x16x32_bf16 v[160:163], a[136:139], a[48:51], v[160:163]\n"
    "ds_read_b128 a[124:127], v224 offset:10240\n"
    "v_mfma_f32_16x16x32_bf16 v[164:167], a[140:143], a[48:51], v[164:167]\n"
    "v_mfma_f32_16x16x32_bf16 v[168:171], a[144:147], a[48:51], v[168:171]\n"
    "s_add_u32 s82, s81, 0x4000\n"
    "buffer_store_dwordx4 a[36:39], v243, s[40:43], s82 offen offset:2048 sc1\n"
    "v_mfma_f32_16x16x32_bf16 v[172:175], a[148:151], a[48:51], v[172:175]\n"
    "v_mfma_f32_16x16x32_bf16 v[176:179], a[152:155], a[48:51], v[176:179]\n"
    "v_mfma_f32_16x16x32_bf16 v[180:183], a[156:159], a[48:51], v[180:183]\n"
    "v_mfma_f32_16x16x32_bf16 v[184:187], a[160:163], a[48:51], v[184:187]\n"
    "v_mfma_f32_16x16x32_bf16 v[188:191], a[164:167], a[48:51], v[188:191]\n"
    "v_mfma_f32_16x16x32_bf16 v[192:195], a[136:139], a[52:55], v[192:195]\n"
    "v_mfma_f32_16x16x32_bf16 v[196:199], a[140:143], a[52:55], v[196:199]\n"
    "v_mfma_f32_16x16x32_bf16 v[200:203], a[144:147], a[52:55], v[200:203]\n"
    "v_mfma_f32_16x16x32_bf16 v[204:207], a[148:151], a[52:55], v[204:207]\n"
    "v_mfma_f32_16x16x32_bf16 v[208:211], a[152:155], a[52:55], v[208:211]\n"
    "v_mfma_f32_16x16x32_bf16 v[212:215], a[156:159], a[52:55], v[212:215]\n"
    "v_mfma_f32_16x16x32_bf16 v[216:219], a[160:163], a[52:55], v[216:219]\n"
    "v_mfma_f32_16x16x32_bf16 v[220:223], a[164:167], a[52:55], v[220:223]\n"
    "s_waitcnt lgkmcnt(0)\n"
    "v_mfma_f32_16x16x32_bf16 a[200:203], a[72:75], a[104:107], a[200:203]\n"
    "v_mfma_f32_16x16x32_bf16 a[204:207], a[76:79], a[104:107], a[204:207]\n"
    "v_mfma_f32_16x16x32_bf16 a[208:211], a[80:83], a[104:107], a[208:211]\n"
    "v_mfma_f32_16x16x32_bf16 a[212:215], a[84:87], a[104:107], a[212:215]\n"
    "s_add_u32 m0, s80, 0x10000\n"
    "v_mfma_f32_16x16x32_bf16 v[16:19], a[88:91], a[104:107], v[16:19]\n"
    "buffer_load_dwordx4 v238, s[52:55], s77 offen offset:0 lds\n"
    "v_mfma_f32_16x16x32_bf16 v[20:23], a[92:95], a[104:107], v[20:23]\n"
    "ds_read_b64_tr_b16 a[136:137], v228 offset:4096\n"
    "v_mfma_f32_16x16x32_bf16 v[24:27], a[96:99], a[104:107], v[24:27]\n"
    "ds_read_b64_tr_b16 a[138:139], v228 offset:4352\n"
    "v_mfma_f32_16x16x32_bf16 v[28:31], a[100:103], a[104:107], v[28:31]\n"
    "v_mfma_f32_16x16x32_bf16 a[216:219], a[72:75], a[108:111], a[216:219]\n"
    "ds_read_b128 a[104:107], v225 offset:0\n"
    "v_mfma_f32_16x16x32_bf16 a[220:223], a[76:79], a[108:111], a[220:223]\n"
    "buffer_load_dwordx4 v239, s[52:55], s77 offen offset:1024 lds\n"
    "v_mfma_f32_16x16x32_bf16 a[224:227], a[80:83], a[108:111], a[224:227]\n"
    "ds_read_b64_tr_b16 a[140:141], v229 offset:4096\n"
    "v_mfma_f32_16x16x32_bf16 a[228:231], a[84:87], a[108:111], a[228:231]\n"
    "ds_read_b64_tr_b16 a[142:143], v229 offset:4352\n"
    "v_mfma_f32_16x16x32_bf16 v[32:35], a[88:91], a[108:111], v[32:35]\n"
    "v_mfma_f32_16x16x32_bf16 v[36:39], a[92:95], a[108:111], v[36:39]\n"
    "buffer_load_dwordx4 v240, s[52:55], s77 offen offset:2048 lds\n"
    "v_mfma_f32_16x16x32_bf16 v[40:43], a[96:99], a[108:111], v[40:43]\n"
    "ds_read_b64_tr_b16 a[144:145], v228 offset:12288\n"
    "v_mfma_f32_16x16x32_bf16 v[44:47], a[100:103], a[108:111], v[44:47]\n"
    "v_mfma_f32_16x16x32_bf16 a[232:235], a[72:75], a[112:115], a[232:235]\n"
    "ds_read_b128 a[108:111], v225 offset:2048\n"
    "v_mfma_f32_16x16x32_bf16 a[236:239], a[76:79], a[112:115], a[236:239]\n"
    "buffer_load_dwordx4 v241, s[52:55], s77 offen offset:3072 lds\n"
    "v_mfma_f32_16x16x32_bf16 a[240:243], a[80:83], a[112:115], a[240:243]\n"
    "ds_read_b64_tr_b16 a[146:147], v228 offset:12544\n"
    "v_mfma_f32_16x16x32_bf16 a[244:247], a[84:87], a[112:115], a[244:247]\n"
    "ds_read_b64_tr_b16 a[148:149], v229 offset:12288\n"
    "v_mfma_f32_16x16x32_bf16 v[48:51], a[88:91], a[112:115], v[48:51]\n"
    "v_mfma_f32_16x16x32_bf16 v[52:55], a[92:95], a[112:115], v[52:55]\n"
    "s_add_u32 m0, s80, 0x11000\n"
    "v_mfma_f32_16x16x32_bf16 v[56:59], a[96:99], a[112:115], v[56:59]\n"
    "buffer_load_dwordx4 v238, s[52:55], s78 offen offset:0 lds\n"
    "v_mfma_f32_16x16x32_bf16 v[60:63], a[100:103], a[112:115], v[60:63]\n"
    "ds_read_b64_tr_b16 a[150:151], v229 offset:12544\n"
    "v_mfma_f32_16x16x32_bf16 v[64:67], a[72:75], a[116:119], v[64:67]\n"
    "ds_read_b128 a[112:115], v225 offset:4096\n"
    "v_mfma_f32_16x16x32_bf16 v[68:71], a[76:79], a[116:119], v[68:71]\n"
    "ds_read_b64_tr_b16 a[152:153], v228 offset:20480\n"
    "v_mfma_f32_16x16x32_bf16 v[72:75], a[80:83], a[116:119], v[72:75]\n"
    "buffer_load_dwordx4 v239, s[52:55], s78 offen offset:1024 lds\n"
    "v_mfma_f32_16x16x32_bf16 v[76:79], a[84:87], a[116:119], v[76:79]\n"
    "ds_read_b64_tr_b16 a[154:155], v228 offset:20736\n"
    "v_mfma_f32_16x16x32_bf16 v[80:83], a[88:91], a[116:119], v[80:83]\n"
    "v_mfma_f32_16x16x32_bf16 v[84:87], a[92:95], a[116:119], v[84:87]\n"
    "ds_read_b64_tr_b16 a[156:157], v229 offset:20480\n"
    "v_mfma_f32_16x16x32_bf16 v[88:91], a[96:99], a[116:119], v[88:91]\n"
    "buffer_load_dwordx4 v240, s[52:55], s78 offen offset:2048 lds\n"
    "v_mfma_f32_16x16x32_bf16 v[92:95], a[100:103], a[116:119], v[92:95]\n"
    "ds_read_b64_tr_b16 a[158:159], v229 offset:20736\n"
    "v_mfma_f32_16x16x32_bf16 v[96:99], a[72:75], a[120:123], v[96:99]\n"
    "ds_read_b128 a[116:119], v225 offset:6144\n"
    "v_mfma_f32_16x16x32_bf16 v[100:103], a[76:79], a[120:123], v[100:103]\n"
    "v_mfma_f32_16x16x32_bf16 v[104:107], a[80:83], a[120:123], v[104:107]\n"
    "buffer_load_dwordx4 v241, s[52:55], s78 offen offset:3072 lds\n"
    "v_mfma_f32_16x16x32_bf16 v[108:111], a[84:87], a[120:123], v[108:111]\n"
    "ds_read_b64_tr_b16 a[160:161], v228 offset:28672\n"
    "v_mfma_f32_16x16x32_bf16 v[112:115], a[88:91], a[120:123], v[112:115]\n"
    "ds_read_b64_tr_b16 a[162:163], v228 offset:28928\n"
    "v_mfma_f32_16x16x32_bf16 v[116:119], a[92:95], a[120:123], v[116:119]\n"
    "v_mfma_f32_16x16x32_bf16 v[120:123], a[96:99], a[120:123], v[120:123]\n"
    "s_add_u32 s77, s77, s79\n"
    "v_mfma_f32_16x16x32_bf16 v[124:127], a[100:103], a[120:123], v[124:127]\n"
    "s_add_u32 s78, s78, s79\n"
    "v_mfma_f32_16x16x32_bf16 v[128:131], a[72:75], a[124:127], v[128:131]\n"
    "ds_read_b128 a[120:123], v225 offset:8192\n"
    "v_mfma_f32_16x16x32_bf16 v[132:135], a[76:79], a[124:127], v[132:135]\n"
    "ds_read_b64_tr_b16 a[164:165], v229 offset:28672\n"
    "v_mfma_f32_16x16x32_bf16 v[136:139], a[80:83], a[124:127], v[136:139]\n"
    "ds_read_b64_tr_b16 a[166:167], v229 offset:28928\n"
    "v_mfma_f32_16x16x32_bf16 v[140:143], a[84:87], a[124:127], v[140:143]\n"
    "s_add_u32 s82, s81, 0x5000\n"
    "buffer_store_dwordx4 a[40:43], v243, s[40:43], s82 offen offset:0 sc1\n"
    "v_mfma_f32_16x16x32_bf16 v[144:147], a[88:91], a[124:127], v[144:147]\n"
    "ds_read_b128 a[48:51], v225 offset:12288\n"
    "v_mfma_f32_16x16x32_bf16 v[148:151], a[92:95], a[124:127], v[148:151]\n"
    "v_mfma_f32_16x16x32_bf16 v[152:155], a[96:99], a[124:127], v[152:155]\n"
    "ds_read_b128 a[52:55], v225 offset:14336\n"
    "v_mfma_f32_16x16x32_bf16 v[156:159], a[100:103], a[124:127], v[156:159]\n"
    "s_add_u32 s82, s81, 0x5000\n"
    "buffer_store_dwordx4 a[44:47], v243, s[40:43], s82 offen offset:2048 sc1\n"
    "v_mfma_f32_16x16x32_bf16 v[160:163], a[72:75], a[128:131], v[160:163]\n"
    "ds_read_b128 a[124:127], v225 offset:10240\n"
    "v_mfma_f32_16x16x32_bf16 v[164:167], a[76:79], a[128:131], v[164:167]\n"
    "v_mfma_f32_16x16x32_bf16 v[168:171], a[80:83], a[128:131], v[168:171]\n"
    "v_mfma_f32_16x16x32_bf16 v[172:175], a[84:87], a[128:131], v[172:175]\n"
    "v_mfma_f32_16x16x32_bf16 v[176:179], a[88:91], a[128:131], v[176:179]\n"
    "v_mfma_f32_16x16x32_bf16 v[180:183], a[92:95], a[128:131], v[180:183]\n"
    "v_mfma_f32_16x16x32_bf16 v[184:187], a[96:99], a[128:131], v[184:187]\n"
    "v_mfma_f32_16x16x32_bf16 v[188:191], a[100:103], a[128:131], v[188:191]\n"
    "v_mfma_f32_16x16x32_bf16 v[192:195], a[72:75], a[132:135], v[192:195]\n"
    "v_mfma_f32_16x16x32_bf16 v[196:199], a[76:79], a[132:135], v[196:199]\n"
    "v_mfma_f32_16x16x32_bf16 v[200:203], a[80:83], a[132:135], v[200:203]\n"
    "v_mfma_f32_16x16x32_bf16 v[204:207], a[84:87], a[132:135], v[204:207]\n"
    "v_mfma_f32_16x16x32_bf16 v[208:211], a[88:91], a[132:135], v[208:211]\n"
    "v_mfma_f32_16x16x32_bf16 v[212:215], a[92:95], a[132:135], v[212:215]\n"
    "v_mfma_f32_16x16x32_bf16 v[216:219], a[96:99], a[132:135], v[216:219]\n"
    "v_mfma_f32_16x16x32_bf16 v[220:223], a[100:103], a[132:135], v[220:223]\n"
    "s_waitcnt lgkmcnt(0)\n"
    "v_mfma_f32_16x16x32_bf16 a[200:203], a[136:139], a[104:107], a[200:203]\n"
    "s_waitcnt vmcnt(13)\n"
    "s_barrier\n"
    "v_mfma_f32_16x16x32_bf16 a[204:207], a[140:143], a[104:107], a[204:207]\n"
    "v_add_u32 v228, 0x10000, v226\n"
    "v_mfma_f32_16x16x32_bf16 a[208:211], a[144:147], a[104:107], a[208:211]\n"
    "v_add_u32 v229, 0x10000, v227\n"
    "v_mfma_f32_16x16x32_bf16 a[212:215], a[148:151], a[104:107], a[212:215]\n"
    "s_add_u32 m0, s80, 0x0\n"
    "v_mfma_f32_16x16x32_bf16 v[16:19], a[152:155], a[104:107], v[16:19]\n"
    "buffer_load_dwordx4 v230, s[48:51], s75 offen offset:0 sc1 lds\n"
    "v_mfma_f32_16x16x32_bf16 v[20:23], a[156:159], a[104:107], v[20:23]\n"
    "ds_read_b64_tr_b16 a[72:73], v228 offset:0\n"
    "v_mfma_f32_16x16x32_bf16 v[24:27], a[160:163], a[104:107], v[24:27]\n"
    "ds_read_b64_tr_b16 a[74:75], v228 offset:256\n"
    "v_mfma_f32_16x16x32_bf16 v[28:31], a[164:167], a[104:107], v[28:31]\n"
    "v_mfma_f32_16x16x32_bf16 a[216:219], a[136:139], a[108:111], a[216:219]\n"
    "ds_read_b128 a[104:107], v224 offset:32768\n"
    "v_mfma_f32_16x16x32_bf16 a[220:223], a[140:143], a[108:111], a[220:223]\n"
    "buffer_load_dwordx4 v231, s[48:51], s75 offen offset:1024 sc1 lds\n"
    "v_mfma_f32_16x16x32_bf16 a[224:227], a[144:147], a[108:111], a[224:227]\n"
    "ds_read_b64_tr_b16 a[76:77], v229 offset:0\n"
    "v_mfma_f32_16x16x32_bf16 a[228:231], a[148:151], a[108:111], a[228:231]\n"
    "ds_read_b64_tr_b16 a[78:79], v229 offset:256\n"
    "v_mfma_f32_16x16x32_bf16 v[32:35], a[152:155], a[108:111], v[32:35]\n"
    "v_mfma_f32_16x16x32_bf16 v[36:39], a[156:159], a[108:111], v[36:39]\n"
    "buffer_load_dwordx4 v232, s[48:51], s75 offen offset:2048 sc1 lds\n"
    "v_mfma_f32_16x16x32_bf16 v[40:43], a[160:163], a[108:111], v[40:43]\n"
    "ds_read_b64_tr_b16 a[80:81], v228 offset:8192\n"
    "v_mfma_f32_16x16x32_bf16 v[44:47], a[164:167], a[108:111], v[44:47]\n"
    "v_mfma_f32_16x16x32_bf16 a[232:235], a[136:139], a[112:115], a[232:235]\n"
    "ds_read_b128 a[108:111], v224 offset:34816\n"
    "v_mfma_f32_16x16x32_bf16 a[236:239], a[140:143], a[112:115], a[236:239]\n"
    "buffer_load_dwordx4 v233, s[48:51], s75 offen offset:3072 sc1 lds\n"
    "v_mfma_f32_16x16x32_bf16 a[240:243], a[144:147], a[112:115], a[240:243]\n"
    "ds_read_b64_tr_b16 a[82:83], v228 offset:8448\n"
    "v_mfma_f32_16x16x32_bf16 a[244:247], a[148:151], a[112:115], a[244:247]\n"
    "ds_read_b64_tr_b16 a[84:85], v229 offset:8192\n"
    "v_mfma_f32_16x16x32_bf16 v[48:51], a[152:155], a[112:115], v[48:51]\n"
    "v_mfma_f32_16x16x32_bf16 v[52:55], a[156:159], a[112:115], v[52:55]\n"
    "s_add_u32 m0, s80, 0x1000\n"
    "v_mfma_f32_16x16x32_bf16 v[56:59], a[160:163], a[112:115], v[56:59]\n"
    "buffer_load_dwordx4 v230, s[48:51], s76 offen offset:0 sc1 lds\n"
    "v_mfma_f32_16x16x32_bf16 v[60:63], a[164:167], a[112:115], v[60:63]\n"
    "ds_read_b64_tr_b16 a[86:87], v229 offset:8448\n"
    "v_mfma_f32_16x16x32_bf16 v[64:67], a[136:139], a[116:119], v[64:67]\n"
    "ds_read_b128 a[112:115], v224 offset:36864\n"
    "v_mfma_f32_16x16x32_bf16 v[68:71], a[140:143], a[116:119], v[68:71]\n"
    "ds_read_b64_tr_b16 a[88:89], v228 offset:16384\n"
    "v_mfma_f32_16x16x32_bf16 v[72:75], a[144:147], a[116:119], v[72:75]\n"
    "buffer_load_dwordx4 v231, s[48:51], s76 offen offset:1024 sc1 lds\n"
    "v_mfma_f32_16x16x32_bf16 v[76:79], a[148:151], a[116:119], v[76:79]\n"
    "ds_read_b64_tr_b16 a[90:91], v228 offset:16640\n"
    "v_mfma_f32_16x16x32_bf16 v[80:83], a[152:155], a[116:119], v[80:83]\n"
    "v_mfma_f32_16x16x32_bf16 v[84:87], a[156:159], a[116:119], v[84:87]\n"
    "ds_read_b64_tr_b16 a[92:93], v229 offset:16384\n"
    "v_mfma_f32_16x16x32_bf16 v[88:91], a[160:163], a[116:119], v[88:91]\n"
    "buffer_load_dwordx4 v232, s[48:51], s76 offen offset:2048 sc1 lds\n"
    "v_mfma_f32_16x16x32_bf16 v[92:95], a[164:167], a[116:119], v[92:95]\n"
    "ds_read_b64_tr_b16 a[94:95], v229 offset:16640\n"
    "v_mfma_f32_16x16x32_bf16 v[96:99], a[136:139], a[120:123], v[96:99]\n"
    "ds_read_b128 a[116:119], v224 offset:38912\n"
    "v_mfma_f32_16x16x32_bf16 v[100:103], a[140:143], a[120:123], v[100:103]\n"
    "v_mfma_f32_16x16x32_bf16 v[104:107], a[144:147], a[120:123], v[104:107]\n"
    "buffer_load_dwordx4 v233, s[48:51], s76 offen offset:3072 sc1 lds\n"
    "v_mfma_f32_16x16x32_bf16 v[108:111], a[148:151], a[120:123], v[108:111]\n"
    "ds_read_b64_tr_b16 a[96:97], v228 offset:24576\n"
    "v_mfma_f32_16x16x32_bf16 v[112:115], a[152:155], a[120:123], v[112:115]\n"
    "ds_read_b64_tr_b16 a[98:99], v228 offset:24832\n"
    "v_mfma_f32_16x16x32_bf16 v[116:119], a[156:159], a[120:123], v[116:119]\n"
    "v_mfma_f32_16x16x32_bf16 v[120:123], a[160:163], a[120:123], v[120:123]\n"
    "s_add_u32 s75, s75, 0x80\n"
    "v_mfma_f32_16x16x32_bf16 v[124:127], a[164:167], a[120:123], v[124:127]\n"
    "s_add_u32 s76, s76, 0x80\n"
    "v_mfma_f32_16x16x32_bf16 v[128:131], a[136:139], a[124:127], v[128:131]\n"
    "ds_read_b128 a[120:123], v224 offset:40960\n"
    "v_mfma_f32_16x16x32_bf16 v[132:135], a[140:143], a[124:127], v[132:135]\n"
    "ds_read_b64_tr_b16 a[100:101], v229 offset:24576\n"
    "v_mfma_f32_16x16x32_bf16 v[136:139], a[144:147], a[124:127], v[136:139]\n"
    "ds_read_b64_tr_b16 a[102:103], v229 offset:24832\n"
    "v_mfma_f32_16x16x32_bf16 v[140:143], a[148:151], a[124:127], v[140:143]\n"
    "s_add_u32 s82, s81, 0x4000\n"
    "buffer_store_dwordx4 a[56:59], v244, s[44:47], s82 offen offset:0 sc1\n"
    "v_mfma_f32_16x16x32_bf16 v[144:147], a[152:155], a[124:127], v[144:147]\n"
    "ds_read_b128 a[128:131], v224 offset:45056\n"
    "v_mfma_f32_16x16x32_bf16 v[148:151], a[156:159], a[124:127], v[148:151]\n"
    "v_mfma_f32_16x16x32_bf16 v[152:155], a[160:163], a[124:127], v[152:155]\n"
    "ds_read_b128 a[132:135], v224 offset:47104\n"
    "v_mfma_f32_16x16x32_bf16 v[156:159], a[164:167], a[124:127], v[156:159]\n"
    "s_add_u32 s82, s81, 0x4000\n"
    "buffer_store_dwordx4 a[60:63], v244, s[44:47], s82 offen offset:1024 sc1\n"
    "v_mfma_f32_16x16x32_bf16 v[160:163], a[136:139], a[48:51], v[160:163]\n"
    "ds_read_b128 a[124:127], v224 offset:43008\n"
    "v_mfma_f32_16x16x32_bf16 v[164:167], a[140:143], a[48:51], v[164:167]\n"
    "v_mfma_f32_16x16x32_bf16 v[168:171], a[144:147], a[48:51], v[168:171]\n"
    "s_add_u32 s82, s81, 0x5000\n"
    "buffer_store_dwordx4 a[248:251], v244, s[44:47], s82 offen offset:0 sc1\n"
    "v_mfma_f32_16x16x32_bf16 v[172:175], a[148:151], a[48:51], v[172:175]\n"
    "v_mfma_f32_16x16x32_bf16 v[176:179], a[152:155], a[48:51], v[176:179]\n"
    "v_mfma_f32_16x16x32_bf16 v[180:183], a[156:159], a[48:51], v[180:183]\n"
    "v_mfma_f32_16x16x32_bf16 v[184:187], a[160:163], a[48:51], v[184:187]\n"
    "v_mfma_f32_16x16x32_bf16 v[188:191], a[164:167], a[48:51], v[188:191]\n"
    "v_mfma_f32_16x16x32_bf16 v[192:195], a[136:139], a[52:55], v[192:195]\n"
    "v_mfma_f32_16x16x32_bf16 v[196:199], a[140:143], a[52:55], v[196:199]\n"
    "v_mfma_f32_16x16x32_bf16 v[200:203], a[144:147], a[52:55], v[200:203]\n"
    "v_mfma_f32_16x16x32_bf16 v[204:207], a[148:151], a[52:55], v[204:207]\n"
    "v_mfma_f32_16x16x32_bf16 v[208:211], a[152:155], a[52:55], v[208:211]\n"
    "s_or_b32 s73, s66, s67\n"
    "s_or_b32 s73, s73, s68\n"
    "s_cbranch_scc1 .Lw11_%=\n"
    "s_mov_b32 s74, 0x40000\n"
    ".Lep_%=:\n"
    "v_cmp_ne_u64 vcc, s[64:65], v[234:235]\n"
    "v_cmp_ne_u64 s[96:97], s[64:65], v[236:237]\n"
    "s_nop 4\n"
    "s_or_b64 s[36:37], vcc, s[96:97]\n"
    "v_cmp_ne_u64 vcc, s[64:65], v[252:253]\n"
    "v_cmp_ne_u64 s[96:97], s[64:65], v[254:255]\n"
    "s_nop 4\n"
    "s_or_b64 s[36:37], s[36:37], vcc\n"
    "s_or_b64 s[36:37], s[36:37], s[96:97]\n"
    "s_cbranch_scc0 .Lqep_%=\n"
    "s_sleep 2\n"
    "buffer_load_dwordx4 v[234:237], v5, s[56:59], 0 offen sc1\n"
    "buffer_load_dwordx4 v[252:255], v5, s[56:59], 0 offen offset:16 sc1\n"
    "s_waitcnt vmcnt(0)\n"
    "s_sub_u32 s74, s74, 1\n"
    "s_cbranch_scc0 .Lep_%=\n"
    ".Lqep_%=:\n"
    "v_mov_b32 v5, 0\n"
    "v_mov_b32 v6, s64\n"
    "v_mov_b32 v7, s65\n"
    "s_mov_b64 exec, 1\n"
    "buffer_store_dwordx2 v[6:7], v5, s[56:59], 0 offen sc1\n"
    "s_mov_b64 exec, -1\n"
    ".Lw11_%=:\n"
    "v_mfma_f32_16x16x32_bf16 v[212:215], a[156:159], a[52:55], v[212:215]\n"
    "v_mfma_f32_16x16x32_bf16 v[216:219], a[160:163], a[52:55], v[216:219]\n"
    "v_mfma_f32_16x16x32_bf16 v[220:223], a[164:167], a[52:55], v[220:223]\n"
    "s_waitcnt lgkmcnt(0)\n"
    "v_mfma_f32_16x16x32_bf16 a[200:203], a[72:75], a[104:107], a[200:203]\n"
    "v_mfma_f32_16x16x32_bf16 a[204:207], a[76:79], a[104:107], a[204:207]\n"
    "v_mfma_f32_16x16x32_bf16 a[208:211], a[80:83], a[104:107], a[208:211]\n"
    "v_mfma_f32_16x16x32_bf16 a[212:215], a[84:87], a[104:107], a[212:215]\n"
    "s_add_u32 m0, s80, 0x18000\n"
    "v_mfma_f32_16x16x32_bf16 v[16:19], a[88:91], a[104:107], v[16:19]\n"
    "buffer_load_dwordx4 v238, s[52:55], s77 offen offset:0 lds\n"
    "v_mfma_f32_16x16x32_bf16 v[20:23], a[92:95], a[104:107], v[20:23]\n"
    "ds_read_b64_tr_b16 a[136:137], v228 offset:4096\n"
    "v_mfma_f32_16x16x32_bf16 v[24:27], a[96:99], a[104:107], v[24:27]\n"
    "ds_read_b64_tr_b16 a[138:139], v228 offset:4352\n"
    "v_mfma_f32_16x16x32_bf16 v[28:31], a[100:103], a[104:107], v[28:31]\n"
    "v_mfma_f32_16x16x32_bf16 a[216:219], a[72:75], a[108:111], a[216:219]\n"
    "ds_read_b128 a[104:107], v225 offset:32768\n"
    "v_mfma_f32_16x16x32_bf16 a[220:223], a[76:79], a[108:111], a[220:223]\n"
    "buffer_load_dwordx4 v239, s[52:55], s77 offen offset:1024 lds\n"
    "v_mfma_f32_16x16x32_bf16 a[224:227], a[80:83], a[108:111], a[224:227]\n"
    "ds_read_b64_tr_b16 a[140:141], v229 offset:4096\n"
    "v_mfma_f32_16x16x32_bf16 a[228:231], a[84:87], a[108:111], a[228:231]\n"
    "ds_read_b64_tr_b16 a[142:143], v229 offset:4352\n"
    "v_mfma_f32_16x16x32_bf16 v[32:35], a[88:91], a[108:111], v[32:35]\n"
    "v_mfma_f32_16x16x32_bf16 v[36:39], a[92:95], a[108:111], v[36:39]\n"
    "buffer_load_dwordx4 v240, s[52:55], s77 offen offset:2048 lds\n"
    "v_mfma_f32_16x16x32_bf16 v[40:43], a[96:99], a[108:111], v[40:43]\n"
    "ds_read_b64_tr_b16 a[144:145], v228 offset:12288\n"
    "v_mfma_f32_16x16x32_bf16 v[44:47], a[100:103], a[108:111], v[44:47]\n"
    "v_mfma_f32_16x16x32_bf16 a[232:235], a[72:75], a[112:115], a[232:235]\n"
    "ds_read_b128 a[108:111], v225 offset:34816\n"
    "v_mfma_f32_16x16x32_bf16 a[236:239], a[76:79], a[112:115], a[236:239]\n"
    "buffer_load_dwordx4 v241, s[52:55], s77 offen offset:3072 lds\n"
    "v_mfma_f32_16x16x32_bf16 a[240:243], a[80:83], a[112:115], a[240:243]\n"
    "ds_read_b64_tr_b16 a[146:147], v228 offset:12544\n"
    "v_mfma_f32_16x16x32_bf16 a[244:247], a[84:87], a[112:115], a[244:247]\n"
    "ds_read_b64_tr_b16 a[148:149], v229 offset:12288\n"
    "v_mfma_f32_16x16x32_bf16 v[48:51], a[88:91], a[112:115], v[48:51]\n"
    "v_mfma_f32_16x16x32_bf16 v[52:55], a[92:95], a[112:115], v[52:55]\n"
    "s_add_u32 m0, s80, 0x19000\n"
    "v_mfma_f32_16x16x32_bf16 v[56:59], a[96:99], a[112:115], v[56:59]\n"
    "buffer_load_dwordx4 v238, s[52:55], s78 offen offset:0 lds\n"
    "v_mfma_f32_16x16x32_bf16 v[60:63], a[100:103], a[112:115], v[60:63]\n"
    "ds_read_b64_tr_b16 a[150:151], v229 offset:12544\n"
    "v_mfma_f32_16x16x32_bf16 v[64:67], a[72:75], a[116:119], v[64:67]\n"
    "ds_read_b128 a[112:115], v225 offset:36864\n"
    "v_mfma_f32_16x16x32_bf16 v[68:71], a[76:79], a[116:119], v[68:71]\n"
    "ds_read_b64_tr_b16 a[152:153], v228 offset:20480\n"
    "v_mfma_f32_16x16x32_bf16 v[72:75], a[80:83], a[116:119], v[72:75]\n"
    "buffer_load_dwordx4 v239, s[52:55], s78 offen offset:1024 lds\n"
    "v_mfma_f32_16x16x32_bf16 v[76:79], a[84:87], a[116:119], v[76:79]\n"
    "ds_read_b64_tr_b16 a[154:155], v228 offset:20736\n"
    "v_mfma_f32_16x16x32_bf16 v[80:83], a[88:91], a[116:119], v[80:83]\n"
    "v_mfma_f32_16x16x32_bf16 v[84:87], a[92:95], a[116:119], v[84:87]\n"
    "ds_read_b64_tr_b16 a[156:157], v229 offset:20480\n"
    "v_mfma_f32_16x16x32_bf16 v[88:91], a[96:99], a[116:119], v[88:91]\n"
    "buffer_load_dwordx4 v240, s[52:55], s78 offen offset:2048 lds\n"
    "v_mfma_f32_16x16x32_bf16 v[92:95], a[100:103], a[116:119], v[92:95]\n"
    "ds_read_b64_tr_b16 a[158:159], v229 offset:20736\n"
    "v_mfma_f32_16x16x32_bf16 v[96:99], a[72:75], a[120:123], v[96:99]\n"
    "ds_read_b128 a[116:119], v225 offset:38912\n"
    "v_mfma_f32_16x16x32_bf16 v[100:103], a[76:79], a[120:123], v[100:103]\n"
    "v_mfma_f32_16x16x32_bf16 v[104:107], a[80:83], a[120:123], v[104:107]\n"
    "buffer_load_dwordx4 v241, s[52:55], s78 offen offset:3072 lds\n"
    "v_mfma_f32_16x16x32_bf16 v[108:111], a[84:87], a[120:123], v[108:111]\n"
    "ds_read_b64_tr_b16 a[160:161], v228 offset:28672\n"
    "v_mfma_f32_16x16x32_bf16 v[112:115], a[88:91], a[120:123], v[112:115]\n"
    "ds_read_b64_tr_b16 a[162:163], v228 offset:28928\n"
    "v_mfma_f32_16x16x32_bf16 v[116:119], a[92:95], a[120:123], v[116:119]\n"
    "v_mfma_f32_16x16x32_bf16 v[120:123], a[96:99], a[120:123], v[120:123]\n"
    "s_add_u32 s77, s77, s79\n"
    "v_mfma_f32_16x16x32_bf16 v[124:127], a[100:103], a[120:123], v[124:127]\n"
    "s_add_u32 s78, s78, s79\n"
    "v_mfma_f32_16x16x32_bf16 v[128:131], a[72:75], a[124:127], v[128:131]\n"
    "ds_read_b128 a[120:123], v225 offset:40960\n"
    "v_mfma_f32_16x16x32_bf16 v[132:135], a[76:79], a[124:127], v[132:135]\n"
    "ds_read_b64_tr_b16 a[164:165], v229 offset:28672\n"
    "v_mfma_f32_16x16x32_bf16 v[136:139], a[80:83], a[124:127], v[136:139]\n"
    "ds_read_b64_tr_b16 a[166:167], v229 offset:28928\n"
    "v_mfma_f32_16x16x32_bf16 v[140:143], a[84:87], a[124:127], v[140:143]\n"
    "s_add_u32 s82, s81, 0x5000\n"
    "buffer_store_dwordx4 a[252:255], v244, s[44:47], s82 offen offset:1024 sc1\n"
    "v_mfma_f32_16x16x32_bf16 v[144:147], a[88:91], a[124:127], v[144:147]\n"
    "ds_read_b128 a[48:51], v225 offset:45056\n"
    "v_mfma_f32_16x16x32_bf16 v[148:151], a[92:95], a[124:127], v[148:151]\n"
    "v_mfma_f32_16x16x32_bf16 v[152:155], a[96:99], a[124:127], v[152:155]\n"
    "ds_read_b128 a[52:55], v225 offset:47104\n"
    "v_mfma_f32_16x16x32_bf16 v[156:159], a[100:103], a[124:127], v[156:159]\n"
    "s_add_u32 s82, s81, 0x6000\n"
    "buffer_store_dwordx4 a[168:171], v243, s[40:43], s82 offen offset:0 sc1\n"
    "v_mfma_f32_16x16x32_bf16 v[160:163], a[72:75], a[128:131], v[160:163]\n"
    "ds_read_b128 a[124:127], v225 offset:43008\n"
    "v_mfma_f32_16x16x32_bf16 v[164:167], a[76:79], a[128:131], v[164:167]\n"
    "v_mfma_f32_16x16x32_bf16 v[168:171], a[80:83], a[128:131], v[168:171]\n"
    "v_mfma_f32_16x16x32_bf16 v[172:175], a[84:87], a[128:131], v[172:175]\n"
    "v_mfma_f32_16x16x32_bf16 v[176:179], a[88:91], a[128:131], v[176:179]\n"
    "v_mfma_f32_16x16x32_bf16 v[180:183], a[92:95], a[128:131], v[180:183]\n"
    "v_mfma_f32_16x16x32_bf16 v[184:187], a[96:99], a[128:131], v[184:187]\n"
    "v_mfma_f32_16x16x32_bf16 v[188:191], a[100:103], a[128:131], v[188:191]\n"
    "v_mfma_f32_16x16x32_bf16 v[192:195], a[72:75], a[132:135], v[192:195]\n"
    "v_mfma_f32_16x16x32_bf16 v[196:199], a[76:79], a[132:135], v[196:199]\n"
    "v_mfma_f32_16x16x32_bf16 v[200:203], a[80:83], a[132:135], v[200:203]\n"
    "v_mfma_f32_16x16x32_bf16 v[204:207], a[84:87], a[132:135], v[204:207]\n"
    "v_mfma_f32_16x16x32_bf16 v[208:211], a[88:91], a[132:135], v[208:211]\n"
    "v_mfma_f32_16x16x32_bf16 v[212:215], a[92:95], a[132:135], v[212:215]\n"
    "v_mfma_f32_16x16x32_bf16 v[216:219], a[96:99], a[132:135], v[216:219]\n"
    "v_mfma_f32_16x16x32_bf16 v[220:223], a[100:103], a[132:135], v[220:223]\n"
    "s_waitcnt lgkmcnt(0)\n"
    "v_mfma_f32_16x16x32_bf16 a[200:203], a[136:139], a[104:107], a[200:203]\n"
    "s_waitcnt vmcnt(13)\n"
    "s_barrier\n"
    "v_mfma_f32_16x16x32_bf16 a[204:207], a[140:143], a[104:107], a[204:207]\n"
    "v_add_u32 v228, 0x0, v226\n"
    "v_mfma_f32_16x16x32_bf16 a[208:211], a[144:147], a[104:107], a[208:211]\n"
    "v_add_u32 v229, 0x0, v227\n"
    "v_mfma_f32_16x16x32_bf16 a[212:215], a[148:151], a[104:107], a[212:215]\n"
    "s_add_u32 m0, s80, 0x8000\n"
    "v_mfma_f32_16x16x32_bf16 v[16:19], a[152:155], a[104:107], v[16:19]\n"
    "buffer_load_dwordx4 v230, s[48:51], s75 offen offset:0 sc1 lds\n"
    "v_mfma_f32_16x16x32_bf16 v[20:23], a[156:159], a[104:107], v[20:23]\n"
    "ds_read_b64_tr_b16 a[72:73], v228 offset:0\n"
    "v_mfma_f32_16x16x32_bf16 v[24:27], a[160:163], a[104:107], v[24:27]\n"
    "ds_read_b64_tr_b16 a[74:75], v228 offset:256\n"
    "v_mfma_f32_16x16x32_bf16 v[28:31], a[164:167], a[104:107], v[28:31]\n"
    "v_mfma_f32_16x16x32_bf16 a[216:219], a[136:139], a[108:111], a[216:219]\n"
    "ds_read_b128 a[104:107], v224 offset:0\n"
    "v_mfma_f32_16x16x32_bf16 a[220:223], a[140:143], a[108:111], a[220:223]\n"
    "buffer_load_dwordx4 v231, s[48:51], s75 offen offset:1024 sc1 lds\n"
    "v_mfma_f32_16x16x32_bf16 a[224:227], a[144:147], a[108:111], a[224:227]\n"
    "ds_read_b64_tr_b16 a[76:77], v229 offset:0\n"
    "v_mfma_f32_16x16x32_bf16 a[228:231], a[148:151], a[108:111], a[228:231]\n"
    "ds_read_b64_tr_b16 a[78:79], v229 offset:256\n"
    "v_mfma_f32_16x16x32_bf16 v[32:35], a[152:155], a[108:111], v[32:35]\n"
    "v_mfma_f32_16x16x32_bf16 v[36:39], a[156:159], a[108:111], v[36:39]\n"
    "buffer_load_dwordx4 v232, s[48:51], s75 offen offset:2048 sc1 lds\n"
    "v_mfma_f32_16x16x32_bf16 v[40:43], a[160:163], a[108:111], v[40:43]\n"
    "ds_read_b64_tr_b16 a[80:81], v228 offset:8192\n"
    "v_mfma_f32_16x16x32_bf16 v[44:47], a[164:167], a[108:111], v[44:47]\n"
    "v_mfma_f32_16x16x32_bf16 a[232:235], a[136:139], a[112:115], a[232:235]\n"
    "ds_read_b128 a[108:111], v224 offset:2048\n"
    "v_mfma_f32_16x16x32_bf16 a[236:239], a[140:143], a[112:115], a[236:239]\n"
    "buffer_load_dwordx4 v233, s[48:51], s75 offen offset:3072 sc1 lds\n"
    "v_mfma_f32_16x16x32_bf16 a[240:243], a[144:147], a[112:115], a[240:243]\n"
    "ds_read_b64_tr_b16 a[82:83], v228 offset:8448\n"
    "v_mfma_f32_16x16x32_bf16 a[244:247], a[148:151], a[112:115], a[244:247]\n"
    "ds_read_b64_tr_b16 a[84:85], v229 offset:8192\n"
    "v_mfma_f32_16x16x32_bf16 v[48:51], a[152:155], a[112:115], v[48:51]\n"
    "v_mfma_f32_16x16x32_bf16 v[52:55], a[156:159], a[112:115], v[52:55]\n"
    "s_add_u32 m0, s80, 0x9000\n"
    "v_mfma_f32_16x16x32_bf16 v[56:59], a[160:163], a[112:115], v[56:59]\n"
    "buffer_load_dwordx4 v230, s[48:51], s76 offen offset:0 sc1 lds\n"
    "v_mfma_f32_16x16x32_bf16 v[60:63], a[164:167], a[112:115], v[60:63]\n"
    "ds_read_b64_tr_b16 a[86:87], v229 offset:8448\n"
    "v_mfma_f32_16x16x32_bf16 v[64:67], a[136:139], a[116:119], v[64:67]\n"
    "ds_read_b128 a[112:115], v224 offset:4096\n"
    "v_mfma_f32_16x16x32_bf16 v[68:71], a[140:143], a[116:119], v[68:71]\n"
    "ds_read_b64_tr_b16 a[88:89], v228 offset:16384\n"
    "v_mfma_f32_16x16x32_bf16 v[72:75], a[144:147], a[116:119], v[72:75]\n"
    "buffer_load_dwordx4 v231, s[48:51], s76 offen offset:1024 sc1 lds\n"
    "v_mfma_f32_16x16x32_bf16 v[76:79], a[148:151], a[116:119], v[76:79]\n"
    "ds_read_b64_tr_b16 a[90:91], v228 offset:16640\n"
    "v_mfma_f32_16x16x32_bf16 v[80:83], a[152:155], a[116:119], v[80:83]\n"
    "v_mfma_f32_16x16x32_bf16 v[84:87], a[156:159], a[116:119], v[84:87]\n"
    "ds_read_b64_tr_b16 a[92:93], v229 offset:16384\n"
    "v_mfma_f32_16x16x32_bf16 v[88:91], a[160:163], a[116:119], v[88:91]\n"
    "buffer_load_dwordx4 v232, s[48:51], s76 offen offset:2048 sc1 lds\n"
    "v_mfma_f32_16x16x32_bf16 v[92:95], a[164:167], a[116:119], v[92:95]\n"
    "ds_read_b64_tr_b16 a[94:95], v229 offset:16640\n"
    "v_mfma_f32_16x16x32_bf16 v[96:99], a[136:139], a[120:123], v[96:99]\n"
    "ds_read_b128 a[116:119], v224 offset:6144\n"
    "v_mfma_f32_16x16x32_bf16 v[100:103], a[140:143], a[120:123], v[100:103]\n"
    "v_mfma_f32_16x16x32_bf16 v[104:107], a[144:147], a[120:123], v[104:107]\n"
    "buffer_load_dwordx4 v233, s[48:51], s76 offen offset:3072 sc1 lds\n"
    "v_mfma_f32_16x16x32_bf16 v[108:111], a[148:151], a[120:123], v[108:111]\n"
    "ds_read_b64_tr_b16 a[96:97], v228 offset:24576\n"
    "v_mfma_f32_16x16x32_bf16 v[112:115], a[152:155], a[120:123], v[112:115]\n"
    "ds_read_b64_tr_b16 a[98:99], v228 offset:24832\n"
    "v_mfma_f32_16x16x32_bf16 v[116:119], a[156:159], a[120:123], v[116:119]\n"
    "v_mfma_f32_16x16x32_bf16 v[120:123], a[160:163], a[120:123], v[120:123]\n"
    "s_add_u32 s75, s75, 0x80\n"
    "v_mfma_f32_16x16x32_bf16 v[124:127], a[164:167], a[120:123], v[124:127]\n"
    "s_add_u32 s76, s76, 0x80\n"
    "v_mfma_f32_16x16x32_bf16 v[128:131], a[136:139], a[124:127], v[128:131]\n"
    "ds_read_b128 a[120:123], v224 offset:8192\n"
    "v_mfma_f32_16x16x32_bf16 v[132:135], a[140:143], a[124:127], v[132:135]\n"
    "ds_read_b64_tr_b16 a[100:101], v229 offset:24576\n"
    "v_mfma_f32_16x16x32_bf16 v[136:139], a[144:147], a[124:127], v[136:139]\n"
    "ds_read_b64_tr_b16 a[102:103], v229 offset:24832\n"
    "v_mfma_f32_16x16x32_bf16 v[140:143], a[148:151], a[124:127], v[140:143]\n"
    "s_add_u32 s82, s81, 0x6000\n"
    "buffer_store_dwordx4 a[172:175], v243, s[40:43], s82 offen offset:2048 sc1\n"
    "v_mfma_f32_16x16x32_bf16 v[144:147], a[152:155], a[124:127], v[144:147]\n"
    "ds_read_b128 a[128:131], v224 offset:12288\n"
    "v_mfma_f32_16x16x32_bf16 v[148:151], a[156:159], a[124:127], v[148:151]\n"
    "v_mfma_f32_16x16x32_bf16 v[152:155], a[160:163], a[124:127], v[152:155]\n"
    "ds_read_b128 a[132:135], v224 offset:14336\n"
    "v_mfma_f32_16x16x32_bf16 v[156:159], a[164:167], a[124:127], v[156:159]\n"
    "s_add_u32 s82, s81, 0x7000\n"
    "buffer_store_dwordx4 a[176:179], v243, s[40:43], s82 offen offset:0 sc1\n"
    "v_mfma_f32_16x16x32_bf16 v[160:163], a[136:139], a[48:51], v[160:163]\n"
    "ds_read_b128 a[124:127], v224 offset:10240\n"
    "v_mfma_f32_16x16x32_bf16 v[164:167], a[140:143], a[48:51], v[164:167]\n"
    "v_mfma_f32_16x16x32_bf16 v[168:171], a[144:147], a[48:51], v[168:171]\n"
    "s_add_u32 s82, s81, 0x7000\n"
    "buffer_store_dwordx4 a[180:183], v243, s[40:43], s82 offen offset:2048 sc1\n"
    "v_mfma_f32_16x16x32_bf16 v[172:175], a[148:151], a[48:51], v[172:175]\n"
    "v_mfma_f32_16x16x32_bf16 v[176:179], a[152:155], a[48:51], v[176:179]\n"
    "v_mfma_f32_16x16x32_bf16 v[180:183], a[156:159], a[48:51], v[180:183]\n"
    "v_mfma_f32_16x16x32_bf16 v[184:187], a[160:163], a[48:51], v[184:187]\n"
    "v_mfma_f32_16x16x32_bf16 v[188:191], a[164:167], a[48:51], v[188:191]\n"
    "v_mfma_f32_16x16x32_bf16 v[192:195], a[136:139], a[52:55], v[192:195]\n"
    "v_mfma_f32_16x16x32_bf16 v[196:199], a[140:143], a[52:55], v[196:199]\n"
    "v_mfma_f32_16x16x32_bf16 v[200:203], a[144:147], a[52:55], v[200:203]\n"
    "v_mfma_f32_16x16x32_bf16 v[204:207], a[148:151], a[52:55], v[204:207]\n"
    "v_mfma_f32_16x16x32_bf16 v[208:211], a[152:155], a[52:55], v[208:211]\n"
    "v_mfma_f32_16x16x32_bf16 v[212:215], a[156:159], a[52:55], v[212:215]\n"
    "v_mfma_f32_16x16x32_bf16 v[216:219], a[160:163], a[52:55], v[216:219]\n"
    "v_mfma_f32_16x16x32_bf16 v[220:223], a[164:167], a[52:55], v[220:223]\n"
    "s_waitcnt lgkmcnt(0)\n"
    "v_mfma_f32_16x16x32_bf16 a[200:203], a[72:75], a[104:107], a[200:203]\n"
    "v_mfma_f32_16x16x32_bf16 a[204:207], a[76:79], a[104:107], a[204:207]\n"
    "v_mfma_f32_16x16x32_bf16 a[208:211], a[80:83], a[104:107], a[208:211]\n"
    "v_mfma_f32_16x16x32_bf16 a[212:215], a[84:87], a[104:107], a[212:215]\n"
    "s_add_u32 m0, s80, 0x20000\n"
    "v_mfma_f32_16x16x32_bf16 v[16:19], a[88:91], a[104:107], v[16:19]\n"
    "buffer_load_dwordx4 v238, s[52:55], s77 offen offset:0 lds\n"
    "v_mfma_f32_16x16x32_bf16 v[20:23], a[92:95], a[104:107], v[20:23]\n"
    "ds_read_b64_tr_b16 a[136:137], v228 offset:4096\n"
    "v_mfma_f32_16x16x32_bf16 v[24:27], a[96:99], a[104:107], v[24:27]\n"
    "ds_read_b64_tr_b16 a[138:139], v228 offset:4352\n"
    "v_mfma_f32_16x16x32_bf16 v[28:31], a[100:103], a[104:107], v[28:31]\n"
    "v_mfma_f32_16x16x32_bf16 a[216:219], a[72:75], a[108:111], a[216:219]\n"
    "ds_read_b128 a[104:107], v225 offset:0\n"
    "v_mfma_f32_16x16x32_bf16 a[220:223], a[76:79], a[108:111], a[220:223]\n"
    "buffer_load_dwordx4 v239, s[52:55], s77 offen offset:1024 lds\n"
    "v_mfma_f32_16x16x32_bf16 a[224:227], a[80:83], a[108:111], a[224:227]\n"
    "ds_read_b64_tr_b16 a[140:141], v229 offset:4096\n"
    "v_mfma_f32_16x16x32_bf16 a[228:231], a[84:87], a[108:111], a[228:231]\n"
    "ds_read_b64_tr_b16 a[142:143], v229 offset:4352\n"
    "v_mfma_f32_16x16x32_bf16 v[32:35], a[88:91], a[108:111], v[32:35]\n"
    "v_mfma_f32_16x16x32_bf16 v[36:39], a[92:95], a[108:111], v[36:39]\n"
    "buffer_load_dwordx4 v240, s[52:55], s77 offen offset:2048 lds\n"
    "v_mfma_f32_16x16x32_bf16 v[40:43], a[96:99], a[108:111], v[40:43]\n"
    "ds_read_b64_tr_b16 a[144:145], v228 offset:12288\n"
    "v_mfma_f32_16x16x32_bf16 v[44:47], a[100:103], a[108:111], v[44:47]\n"
    "v_mfma_f32_16x16x32_bf16 a[232:235], a[72:75], a[112:115], a[232:235]\n"
    "ds_read_b128 a[108:111], v225 offset:2048\n"
    "v_mfma_f32_16x16x32_bf16 a[236:239], a[76:79], a[112:115], a[236:239]\n"
    "buffer_load_dwordx4 v241, s[52:55], s77 offen offset:3072 lds\n"
    "v_mfma_f32_16x16x32_bf16 a[240:243], a[80:83], a[112:115], a[240:243]\n"
    "ds_read_b64_tr_b16 a[146:147], v228 offset:12544\n"
    "v_mfma_f32_16x16x32_bf16 a[244:247], a[84:87], a[112:115], a[244:247]\n"
    "ds_read_b64_tr_b16 a[148:149], v229 offset:12288\n"
    "v_mfma_f32_16x16x32_bf16 v[48:51], a[88:91], a[112:115], v[48:51]\n"
    "v_mfma_f32_16x16x32_bf16 v[52:55], a[92:95], a[112:115], v[52:55]\n"
    "s_add_u32 m0, s80, 0x21000\n"
    "v_mfma_f32_16x16x32_bf16 v[56:59], a[96:99], a[112:115], v[56:59]\n"
    "buffer_load_dwordx4 v238, s[52:55], s78 offen offset:0 lds\n"
    "v_mfma_f32_16x16x32_bf16 v[60:63], a[100:103], a[112:115], v[60:63]\n"
    "ds_read_b64_tr_b16 a[150:151], v229 offset:12544\n"
    "v_mfma_f32_16x16x32_bf16 v[64:67], a[72:75], a[116:119], v[64:67]\n"
    "ds_read_b128 a[112:115], v225 offset:4096\n"
    "v_mfma_f32_16x16x32_bf16 v[68:71], a[76:79], a[116:119], v[68:71]\n"
    "ds_read_b64_tr_b16 a[152:153], v228 offset:20480\n"
    "v_mfma_f32_16x16x32_bf16 v[72:75], a[80:83], a[116:119], v[72:75]\n"
    "buffer_load_dwordx4 v239, s[52:55], s78 offen offset:1024 lds\n"
    "v_mfma_f32_16x16x32_bf16 v[76:79], a[84:87], a[116:119], v[76:79]\n"
    "ds_read_b64_tr_b16 a[154:155], v228 offset:20736\n"
    "v_mfma_f32_16x16x32_bf16 v[80:83], a[88:91], a[116:119], v[80:83]\n"
    "v_mfma_f32_16x16x32_bf16 v[84:87], a[92:95], a[116:119], v[84:87]\n"
    "ds_read_b64_tr_b16 a[156:157], v229 offset:20480\n"
    "v_mfma_f32_16x16x32_bf16 v[88:91], a[96:99], a[116:119], v[88:91]\n"
    "buffer_load_dwordx4 v240, s[52:55], s78 offen offset:2048 lds\n"
    "v_mfma_f32_16x16x32_bf16 v[92:95], a[100:103], a[116:119], v[92:95]\n"
    "ds_read_b64_tr_b16 a[158:159], v229 offset:20736\n"
    "v_mfma_f32_16x16x32_bf16 v[96:99], a[72:75], a[120:123], v[96:99]\n"
    "ds_read_b128 a[116:119], v225 offset:6144\n"
    "v_mfma_f32_16x16x32_bf16 v[100:103], a[76:79], a[120:123], v[100:103]\n"
    "v_mfma_f32_16x16x32_bf16 v[104:107], a[80:83], a[120:123], v[104:107]\n"
    "buffer_load_dwordx4 v241, s[52:55], s78 offen offset:3072 lds\n"
    "v_mfma_f32_16x16x32_bf16 v[108:111], a[84:87], a[120:123], v[108:111]\n"
    "ds_read_b64_tr_b16 a[160:161], v228 offset:28672\n"
    "v_mfma_f32_16x16x32_bf16 v[112:115], a[88:91], a[120:123], v[112:115]\n"
    "ds_read_b64_tr_b16 a[162:163], v228 offset:28928\n"
    "v_mfma_f32_16x16x32_bf16 v[116:119], a[92:95], a[120:123], v[116:119]\n"
    "v_mfma_f32_16x16x32_bf16 v[120:123], a[96:99], a[120:123], v[120:123]\n"
    "s_add_u32 s77, s77, s79\n"
    "v_mfma_f32_16x16x32_bf16 v[124:127], a[100:103], a[120:123], v[124:127]\n"
    "s_add_u32 s78, s78, s79\n"
    "v_mfma_f32_16x16x32_bf16 v[128:131], a[72:75], a[124:127], v[128:131]\n"
    "ds_read_b128 a[120:123], v225 offset:8192\n"
    "v_mfma_f32_16x16x32_bf16 v[132:135], a[76:79], a[124:127], v[132:135]\n"
    "ds_read_b64_tr_b16 a[164:165], v229 offset:28672\n"
    "v_mfma_f32_16x16x32_bf16 v[136:139], a[80:83], a[124:127], v[136:139]\n"
    "ds_read_b64_tr_b16 a[166:167], v229 offset:28928\n"
    "v_mfma_f32_16x16x32_bf16 v[140:143], a[84:87], a[124:127], v[140:143]\n"
    "s_add_u32 s82, s81, 0x6000\n"
    "buffer_store_dwordx4 a[184:187], v244, s[44:47], s82 offen offset:0 sc1\n"
    "v_mfma_f32_16x16x32_bf16 v[144:147], a[88:91], a[124:127], v[144:147]\n"
    "ds_read_b128 a[48:51], v225 offset:12288\n"
    "v_mfma_f32_16x16x32_bf16 v[148:151], a[92:95], a[124:127], v[148:151]\n"
    "v_mfma_f32_16x16x32_bf16 v[152:155], a[96:99], a[124:127], v[152:155]\n"
    "ds_read_b128 a[52:55], v225 offset:14336\n"
    "v_mfma_f32_16x16x32_bf16 v[156:159], a[100:103], a[124:127], v[156:159]\n"
    "s_add_u32 s82, s81, 0x6000\n"
    "buffer_store_dwordx4 a[188:191], v244, s[44:47], s82 offen offset:1024 sc1\n"
    "v_mfma_f32_16x16x32_bf16 v[160:163], a[72:75], a[128:131], v[160:163]\n"
    "ds_read_b128 a[124:127], v225 offset:10240\n"
    "v_mfma_f32_16x16x32_bf16 v[164:167], a[76:79], a[128:131], v[164:167]\n"
    "v_mfma_f32_16x16x32_bf16 v[168:171], a[80:83], a[128:131], v[168:171]\n"
    "v_mfma_f32_16x16x32_bf16 v[172:175], a[84:87], a[128:131], v[172:175]\n"
    "v_mfma_f32_16x16x32_bf16 v[176:179], a[88:91], a[128:131], v[176:179]\n"
    "v_mfma_f32_16x16x32_bf16 v[180:183], a[92:95], a[128:131], v[180:183]\n"
    "v_mfma_f32_16x16x32_bf16 v[184:187], a[96:99], a[128:131], v[184:187]\n"
    "v_mfma_f32_16x16x32_bf16 v[188:191], a[100:103], a[128:131], v[188:191]\n"
    "v_mfma_f32_16x16x32_bf16 v[192:195], a[72:75], a[132:135], v[192:195]\n"
    "v_mfma_f32_16x16x32_bf16 v[196:199], a[76:79], a[132:135], v[196:199]\n"
    "v_mfma_f32_16x16x32_bf16 v[200:203], a[80:83], a[132:135], v[200:203]\n"
    "v_mfma_f32_16x16x32_bf16 v[204:207], a[84:87], a[132:135], v[204:207]\n"
    "v_mfma_f32_16x16x32_bf16 v[208:211], a[88:91], a[132:135], v[208:211]\n"
    "v_mfma_f32_16x16x32_bf16 v[212:215], a[92:95], a[132:135], v[212:215]\n"
    "v_mfma_f32_16x16x32_bf16 v[216:219], a[96:99], a[132:135], v[216:219]\n"
    "v_mfma_f32_16x16x32_bf16 v[220:223], a[100:103], a[132:135], v[220:223]\n"
    "s_waitcnt lgkmcnt(0)\n"
    "v_mfma_f32_16x16x32_bf16 a[200:203], a[136:139], a[104:107], a[200:203]\n"
    "s_waitcnt vmcnt(13)\n"
    "s_barrier\n"
    "v_mfma_f32_16x16x32_bf16 a[204:207], a[140:143], a[104:107], a[204:207]\n"
    "v_add_u32 v228, 0x8000, v226\n"
    "v_mfma_f32_16x16x32_bf16 a[208:211], a[144:147], a[104:107], a[208:211]\n"
    "v_add_u32 v229, 0x8000, v227\n"
    "v_mfma_f32_16x16x32_bf16 a[212:215], a[148:151], a[104:107], a[212:215]\n"
    "s_add_u32 m0, s80, 0x0\n"
    "v_mfma_f32_16x16x32_bf16 v[16:19], a[152:155], a[104:107], v[16:19]\n"
    "buffer_load_dwordx4 v230, s[48:51], s75 offen offset:0 sc1 lds\n"
    "v_mfma_f32_16x16x32_bf16 v[20:23], a[156:159], a[104:107], v[20:23]\n"
    "ds_read_b64_tr_b16 a[72:73], v228 offset:0\n"
    "v_mfma_f32_16x16x32_bf16 v[24:27], a[160:163], a[104:107], v[24:27]\n"
    "ds_read_b64_tr_b16 a[74:75], v228 offset:256\n"
    "v_mfma_f32_16x16x32_bf16 v[28:31], a[164:167], a[104:107], v[28:31]\n"
    "v_lshrrev_b32 v245, 4, v8\n"
    "v_mfma_f32_16x16x32_bf16 a[216:219], a[136:139], a[108:111], a[216:219]\n"
    "ds_read_b128 a[104:107], v224 offset:32768\n"
    "v_mfma_f32_16x16x32_bf16 a[220:223], a[140:143], a[108:111], a[220:223]\n"
    "buffer_load_dwordx4 v231, s[48:51], s75 offen offset:1024 sc1 lds\n"
    "v_mfma_f32_16x16x32_bf16 a[224:227], a[144:147], a[108:111], a[224:227]\n"
    "ds_read_b64_tr_b16 a[76:77], v229 offset:0\n"
    "v_mfma_f32_16x16x32_bf16 a[228:231], a[148:151], a[108:111], a[228:231]\n"
    "ds_read_b64_tr_b16 a[78:79], v229 offset:256\n"
    "v_mfma_f32_16x16x32_bf16 v[32:35], a[152:155], a[108:111], v[32:35]\n"
    "v_and_b32 v246, 15, v245\n"
    "v_mfma_f32_16x16x32_bf16 v[36:39], a[156:159], a[108:111], v[36:39]\n"
    "buffer_load_dwordx4 v232, s[48:51], s75 offen offset:2048 sc1 lds\n"
    "v_mfma_f32_16x16x32_bf16 v[40:43], a[160:163], a[108:111], v[40:43]\n"
    "ds_read_b64_tr_b16 a[80:81], v228 offset:8192\n"
    "v_mfma_f32_16x16x32_bf16 v[44:47], a[164:167], a[108:111], v[44:47]\n"
    "v_lshrrev_b32 v245, 4, v245\n"
    "v_mfma_f32_16x16x32_bf16 a[232:235], a[136:139], a[112:115], a[232:235]\n"
    "ds_read_b128 a[108:111], v224 offset:34816\n"
    "v_mfma_f32_16x16x32_bf16 a[236:239], a[140:143], a[112:115], a[236:239]\n"
    "buffer_load_dwordx4 v233, s[48:51], s75 offen offset:3072 sc1 lds\n"
    "v_mfma_f32_16x16x32_bf16 a[240:243], a[144:147], a[112:115], a[240:243]\n"
    "ds_read_b64_tr_b16 a[82:83], v228 offset:8448\n"
    "v_mfma_f32_16x16x32_bf16 a[244:247], a[148:151], a[112:115], a[244:247]\n"
    "ds_read_b64_tr_b16 a[84:85], v229 offset:8192\n"
    "v_mfma_f32_16x16x32_bf16 v[48:51], a[152:155], a[112:115], v[48:51]\n"
    "v_lshlrev_b32 v245, 2, v245\n"
    "v_mfma_f32_16x16x32_bf16 v[52:55], a[156:159], a[112:115], v[52:55]\n"
    "s_add_u32 m0, s80, 0x1000\n"
    "v_mfma_f32_16x16x32_bf16 v[56:59], a[160:163], a[112:115], v[56:59]\n"
    "buffer_load_dwordx4 v230, s[48:51], s76 offen offset:0 sc1 lds\n"
    "v_mfma_f32_16x16x32_bf16 v[60:63], a[164:167], a[112:115], v[60:63]\n"
    "ds_read_b64_tr_b16 a[86:87], v229 offset:8448\n"
    "v_mfma_f32_16x16x32_bf16 v[64:67], a[136:139], a[116:119], v[64:67]\n"
    "ds_read_b128 a[112:115], v224 offset:36864\n"
    "v_mfma_f32_16x16x32_bf16 v[68:71], a[140:143], a[116:119], v[68:71]\n"
    "ds_read_b64_tr_b16 a[88:89], v228 offset:16384\n"
    "v_mfma_f32_16x16x32_bf16 v[72:75], a[144:147], a[116:119], v[72:75]\n"
    "buffer_load_dwordx4 v231, s[48:51], s76 offen offset:1024 sc1 lds\n"
    "v_mfma_f32_16x16x32_bf16 v[76:79], a[148:151], a[116:119], v[76:79]\n"
    "ds_read_b64_tr_b16 a[90:91], v228 offset:16640\n"
    "v_mfma_f32_16x16x32_bf16 v[80:83], a[152:155], a[116:119], v[80:83]\n"
    "v_sub_u32 v246, v246, v245\n"
    "v_mfma_f32_16x16x32_bf16 v[84:87], a[156:159], a[116:119], v[84:87]\n"
    "ds_read_b64_tr_b16 a[92:93], v229 offset:16384\n"
    "v_mfma_f32_16x16x32_bf16 v[88:91], a[160:163], a[116:119], v[88:91]\n"
    "buffer_load_dwordx4 v232, s[48:51], s76 offen offset:2048 sc1 lds\n"
    "v_mfma_f32_16x16x32_bf16 v[92:95], a[164:167], a[116:119], v[92:95]\n"
    "ds_read_b64_tr_b16 a[94:95], v229 offset:16640\n"
    "v_mfma_f32_16x16x32_bf16 v[96:99], a[136:139], a[120:123], v[96:99]\n"
    "ds_read_b128 a[116:119], v224 offset:38912\n"
    "v_mfma_f32_16x16x32_bf16 v[100:103], a[140:143], a[120:123], v[100:103]\n"
    "v_mov_b32 v247, 0xff800000\n"
    "v_mfma_f32_16x16x32_bf16 v[104:107], a[144:147], a[120:123], v[104:107]\n"
    "buffer_load_dwordx4 v233, s[48:51], s76 offen offset:3072 sc1 lds\n"
    "v_mfma_f32_16x16x32_bf16 v[108:111], a[148:151], a[120:123], v[108:111]\n"
    "ds_read_b64_tr_b16 a[96:97], v228 offset:24576\n"
    "v_mfma_f32_16x16x32_bf16 v[112:115], a[152:155], a[120:123], v[112:115]\n"
    "ds_read_b64_tr_b16 a[98:99], v228 offset:24832\n"
    "v_mfma_f32_16x16x32_bf16 v[116:119], a[156:159], a[120:123], v[116:119]\n"
    "s_add_u32 s73, s72, s71\n"
    "s_sub_u32 s73, 1, s73\n"
    "s_lshl_b32 s73, s73, 5\n"
    "v_add_u32 v245, s73, v246\n"
    "v_mfma_f32_16x16x32_bf16 v[120:123], a[160:163], a[120:123], v[120:123]\n"
    "s_add_u32 s75, s75, 0x80\n"
    "v_mfma_f32_16x16x32_bf16 v[124:127], a[164:167], a[120:123], v[124:127]\n"
    "s_add_u32 s76, s76, 0x80\n"
    "v_mfma_f32_16x16x32_bf16 v[128:131], a[136:139], a[124:127], v[128:131]\n"
    "ds_read_b128 a[120:123], v224 offset:40960\n"
    "v_mfma_f32_16x16x32_bf16 v[132:135], a[140:143], a[124:127], v[132:135]\n"
    "ds_read_b64_tr_b16 a[100:101], v229 offset:24576\n"
    "v_mfma_f32_16x16x32_bf16 v[136:139], a[144:147], a[124:127], v[136:139]\n"
    "ds_read_b64_tr_b16 a[102:103], v229 offset:24832\n"
    "v_mfma_f32_16x16x32_bf16 v[140:143], a[148:151], a[124:127], v[140:143]\n"
    "s_add_u32 s82, s81, 0x7000\n"
    "buffer_store_dwordx4 a[192:195], v244, s[44:47], s82 offen offset:0 sc1\n"
    "v_mfma_f32_16x16x32_bf16 v[144:147], a[152:155], a[124:127], v[144:147]\n"
    "ds_read_b128 a[128:131], v224 offset:45056\n"
    "v_mfma_f32_16x16x32_bf16 v[148:151], a[156:159], a[124:127], v[148:151]\n"
    "s_sub_u32 s73, s72, s71\n"
    "s_lshl_b32 s73, s73, 5\n"
    "v_add_u32 v246, s73, v246\n"
    "v_mfma_f32_16x16x32_bf16 v[152:155], a[160:163], a[124:127], v[152:155]\n"
    "ds_read_b128 a[132:135], v224 offset:47104\n"
    "v_mfma_f32_16x16x32_bf16 v[156:159], a[164:167], a[124:127], v[156:159]\n"
    "s_add_u32 s82, s81, 0x7000\n"
    "buffer_store_dwordx4 a[196:199], v244, s[44:47], s82 offen offset:1024 sc1\n"
    "v_mfma_f32_16x16x32_bf16 v[160:163], a[136:139], a[48:51], v[160:163]\n"
    "ds_read_b128 a[124:127], v224 offset:43008\n"
    "v_mfma_f32_16x16x32_bf16 v[164:167], a[140:143], a[48:51], v[164:167]\n"
    "v_cmp_gt_i32 vcc, 0, v245\n"
    "v_cmp_gt_i32 s[96:97], 0, v246\n"
    "v_mfma_f32_16x16x32_bf16 v[168:171], a[144:147], a[48:51], v[168:171]\n"
    "v_cndmask_b32 v248, 0, v247, vcc\n"
    "v_cndmask_b32 v249, 0, v247, s[96:97]\n"
    "v_mfma_f32_16x16x32_bf16 v[172:175], a[148:151], a[48:51], v[172:175]\n"
    "v_accvgpr_write_b32 a0, v248\n"
    "v_accvgpr_write_b32 a16, v249\n"
    "v_mfma_f32_16x16x32_bf16 v[176:179], a[152:155], a[48:51], v[176:179]\n"
    "v_cmp_gt_i32 vcc, 1, v245\n"
    "v_cmp_gt_i32 s[96:97], 1, v246\n"
    "v_mfma_f32_16x16x32_bf16 v[180:183], a[156:159], a[48:51], v[180:183]\n"
    "v_cndmask_b32 v248, 0, v247, vcc\n"
    "v_cndmask_b32 v249, 0, v247, s[96:97]\n"
    "v_mfma_f32_16x16x32_bf16 v[184:187], a[160:163], a[48:51], v[184:187]\n"
    "v_accvgpr_write_b32 a1, v248\n"
    "v_accvgpr_write_b32 a17, v249\n"
    "v_mfma_f32_16x16x32_bf16 v[188:191], a[164:167], a[48:51], v[188:191]\n"
    "v_cmp_gt_i32 vcc, 2, v245\n"
    "v_cmp_gt_i32 s[96:97], 2, v246\n"
    "v_mfma_f32_16x16x32_bf16 v[192:195], a[136:139], a[52:55], v[192:195]\n"
    "v_cndmask_b32 v248, 0, v247, vcc\n"
    "v_cndmask_b32 v249, 0, v247, s[96:97]\n"
    "v_mfma_f32_16x16x32_bf16 v[196:199], a[140:143], a[52:55], v[196:199]\n"
    "v_accvgpr_write_b32 a2, v248\n"
    "v_accvgpr_write_b32 a18, v249\n"
    "v_mfma_f32_16x16x32_bf16 v[200:203], a[144:147], a[52:55], v[200:203]\n"
    "v_mfma_f32_16x16x32_bf16 v[204:207], a[148:151], a[52:55], v[204:207]\n"
    "v_mfma_f32_16x16x32_bf16 v[208:211], a[152:155], a[52:55], v[208:211]\n"
    "v_mfma_f32_16x16x32_bf16 v[212:215], a[156:159], a[52:55], v[212:215]\n"
    "v_mfma_f32_16x16x32_bf16 v[216:219], a[160:163], a[52:55], v[216:219]\n"
    "v_mfma_f32_16x16x32_bf16 v[220:223], a[164:167], a[52:55], v[220:223]\n"
    "s_waitcnt lgkmcnt(0)\n"
    "v_mfma_f32_16x16x32_bf16 a[200:203], a[72:75], a[104:107], a[200:203]\n"
    "v_mfma_f32_16x16x32_bf16 a[204:207], a[76:79], a[104:107], a[204:207]\n"
    "v_mfma_f32_16x16x32_bf16 a[208:211], a[80:83], a[104:107], a[208:211]\n"
    "v_cmp_gt_i32 vcc, 3, v245\n"
    "v_cmp_gt_i32 s[96:97], 3, v246\n"
    "v_mfma_f32_16x16x32_bf16 a[212:215], a[84:87], a[104:107], a[212:215]\n"
    "s_add_u32 m0, s80, 0x10000\n"
    "v_mfma_f32_16x16x32_bf16 v[16:19], a[88:91], a[104:107], v[16:19]\n"
    "buffer_load_dwordx4 v238, s[52:55], s77 offen offset:0 lds\n"
    "v_mfma_f32_16x16x32_bf16 v[20:23], a[92:95], a[104:107], v[20:23]\n"
    "ds_read_b64_tr_b16 a[136:137], v228 offset:4096\n"
    "v_mfma_f32_16x16x32_bf16 v[24:27], a[96:99], a[104:107], v[24:27]\n"
    "ds_read_b64_tr_b16 a[138:139], v228 offset:4352\n"
    "v_mfma_f32_16x16x32_bf16 v[28:31], a[100:103], a[104:107], v[28:31]\n"
    "v_cndmask_b32 v248, 0, v247, vcc\n"
    "v_cndmask_b32 v249, 0, v247, s[96:97]\n"
    "v_mfma_f32_16x16x32_bf16 a[216:219], a[72:75], a[108:111], a[216:219]\n"
    "ds_read_b128 a[104:107], v225 offset:32768\n"
    "v_mfma_f32_16x16x32_bf16 a[220:223], a[76:79], a[108:111], a[220:223]\n"
    "buffer_load_dwordx4 v239, s[52:55], s77 offen offset:1024 lds\n"
    "v_mfma_f32_16x16x32_bf16 a[224:227], a[80:83], a[108:111], a[224:227]\n"
    "ds_read_b64_tr_b16 a[140:141], v229 offset:4096\n"
    "v_mfma_f32_16x16x32_bf16 a[228:231], a[84:87], a[108:111], a[228:231]\n"
    "ds_read_b64_tr_b16 a[142:143], v229 offset:4352\n"
    "v_mfma_f32_16x16x32_bf16 v[32:35], a[88:91], a[108:111], v[32:35]\n"
    "v_accvgpr_write_b32 a3, v248\n"
    "v_accvgpr_write_b32 a19, v249\n"
    "v_mfma_f32_16x16x32_bf16 v[36:39], a[92:95], a[108:111], v[36:39]\n"
    "buffer_load_dwordx4 v240, s[52:55], s77 offen offset:2048 lds\n"
    "v_mfma_f32_16x16x32_bf16 v[40:43], a[96:99], a[108:111], v[40:43]\n"
    "ds_read_b64_tr_b16 a[144:145], v228 offset:12288\n"
    "v_mfma_f32_16x16x32_bf16 v[44:47], a[100:103], a[108:111], v[44:47]\n"
    "v_cmp_gt_i32 vcc, 16, v245\n"
    "v_cmp_gt_i32 s[96:97], 16, v246\n"
    "v_mfma_f32_16x16x32_bf16 a[232:235], a[72:75], a[112:115], a[232:235]\n"
    "ds_read_b128 a[108:111], v225 offset:34816\n"
    "v_mfma_f32_16x16x32_bf16 a[236:239], a[76:79], a[112:115], a[236:239]\n"
    "buffer_load_dwordx4 v241, s[52:55], s77 offen offset:3072 lds\n"
    "v_mfma_f32_16x16x32_bf16 a[240:243], a[80:83], a[112:115], a[240:243]\n"
    "ds_read_b64_tr_b16 a[146:147], v228 offset:12544\n"
    "v_mfma_f32_16x16x32_bf16 a[244:247], a[84:87], a[112:115], a[244:247]\n"
    "ds_read_b64_tr_b16 a[148:149], v229 offset:12288\n"
    "v_mfma_f32_16x16x32_bf16 v[48:51], a[88:91], a[112:115], v[48:51]\n"
    "v_cndmask_b32 v248, 0, v247, vcc\n"
    "v_cndmask_b32 v249, 0, v247, s[96:97]\n"
    "v_mfma_f32_16x16x32_bf16 v[52:55], a[92:95], a[112:115], v[52:55]\n"
    "s_add_u32 m0, s80, 0x11000\n"
    "v_mfma_f32_16x16x32_bf16 v[56:59], a[96:99], a[112:115], v[56:59]\n"
    "buffer_load_dwordx4 v238, s[52:55], s78 offen offset:0 lds\n"
    "v_mfma_f32_16x16x32_bf16 v[60:63], a[100:103], a[112:115], v[60:63]\n"
    "ds_read_b64_tr_b16 a[150:151], v229 offset:12544\n"
    "v_mfma_f32_16x16x32_bf16 v[64:67], a[72:75], a[116:119], v[64:67]\n"
    "ds_read_b128 a[112:115], v225 offset:36864\n"
    "v_mfma_f32_16x16x32_bf16 v[68:71], a[76:79], a[116:119], v[68:71]\n"
    "ds_read_b64_tr_b16 a[152:153], v228 offset:20480\n"
    "v_mfma_f32_16x16x32_bf16 v[72:75], a[80:83], a[116:119], v[72:75]\n"
    "buffer_load_dwordx4 v239, s[52:55], s78 offen offset:1024 lds\n"
    "v_mfma_f32_16x16x32_bf16 v[76:79], a[84:87], a[116:119], v[76:79]\n"
    "ds_read_b64_tr_b16 a[154:155], v228 offset:20736\n"
    "v_mfma_f32_16x16x32_bf16 v[80:83], a[88:91], a[116:119], v[80:83]\n"
    "v_accvgpr_write_b32 a4, v248\n"
    "v_accvgpr_write_b32 a20, v249\n"
    "v_mfma_f32_16x16x32_bf16 v[84:87], a[92:95], a[116:119], v[84:87]\n"
    "ds_read_b64_tr_b16 a[156:157], v229 offset:20480\n"
    "v_mfma_f32_16x16x32_bf16 v[88:91], a[96:99], a[116:119], v[88:91]\n"
    "buffer_load_dwordx4 v240, s[52:55], s78 offen offset:2048 lds\n"
    "v_mfma_f32_16x16x32_bf16 v[92:95], a[100:103], a[116:119], v[92:95]\n"
    "ds_read_b64_tr_b16 a[158:159], v229 offset:20736\n"
    "v_mfma_f32_16x16x32_bf16 v[96:99], a[72:75], a[120:123], v[96:99]\n"
    "ds_read_b128 a[116:119], v225 offset:38912\n"
    "v_mfma_f32_16x16x32_bf16 v[100:103], a[76:79], a[120:123], v[100:103]\n"
    "v_cmp_gt_i32 vcc, 17, v245\n"
    "v_cmp_gt_i32 s[96:97], 17, v246\n"
    "v_mfma_f32_16x16x32_bf16 v[104:107], a[80:83], a[120:123], v[104:107]\n"
    "buffer_load_dwordx4 v241, s[52:55], s78 offen offset:3072 lds\n"
    "v_mfma_f32_16x16x32_bf16 v[108:111], a[84:87], a[120:123], v[108:111]\n"
    "ds_read_b64_tr_b16 a[160:161], v228 offset:28672\n"
    "v_mfma_f32_16x16x32_bf16 v[112:115], a[88:91], a[120:123], v[112:115]\n"
    "ds_read_b64_tr_b16 a[162:163], v228 offset:28928\n"
    "v_mfma_f32_16x16x32_bf16 v[116:119], a[92:95], a[120:123], v[116:119]\n"
    "v_cndmask_b32 v248, 0, v247, vcc\n"
    "v_cndmask_b32 v249, 0, v247, s[96:97]\n"
    "v_mfma_f32_16x16x32_bf16 v[120:123], a[96:99], a[120:123], v[120:123]\n"
    "s_add_u32 s77, s77, s79\n"
    "v_mfma_f32_16x16x32_bf16 v[124:127], a[100:103], a[120:123], v[124:127]\n"
    "s_add_u32 s78, s78, s79\n"
    "v_mfma_f32_16x16x32_bf16 v[128:131], a[72:75], a[124:127], v[128:131]\n"
    "ds_read_b128 a[120:123], v225 offset:40960\n"
    "v_mfma_f32_16x16x32_bf16 v[132:135], a[76:79], a[124:127], v[132:135]\n"
    "ds_read_b64_tr_b16 a[164:165], v229 offset:28672\n"
    "v_mfma_f32_16x16x32_bf16 v[136:139], a[80:83], a[124:127], v[136:139]\n"
    "ds_read_b64_tr_b16 a[166:167], v229 offset:28928\n"
    "v_mfma_f32_16x16x32_bf16 v[140:143], a[84:87], a[124:127], v[140:143]\n"
    "v_accvgpr_write_b32 a5, v248\n"
    "v_accvgpr_write_b32 a21, v249\n"
    "v_mfma_f32_16x16x32_bf16 v[144:147], a[88:91], a[124:127], v[144:147]\n"
    "ds_read_b128 a[48:51], v225 offset:45056\n"
    "v_mfma_f32_16x16x32_bf16 v[148:151], a[92:95], a[124:127], v[148:151]\n"
    "v_cmp_gt_i32 vcc, 18, v245\n"
    "v_cmp_gt_i32 s[96:97], 18, v246\n"
    "v_mfma_f32_16x16x32_bf16 v[152:155], a[96:99], a[124:127], v[152:155]\n"
    "ds_read_b128 a[52:55], v225 offset:47104\n"
    "v_mfma_f32_16x16x32_bf16 v[156:159], a[100:103], a[124:127], v[156:159]\n"
    "v_cndmask_b32 v248, 0, v247, vcc\n"
    "v_cndmask_b32 v249, 0, v247, s[96:97]\n"
    "v_mfma_f32_16x16x32_bf16 v[160:163], a[72:75], a[128:131], v[160:163]\n"
    "ds_read_b128 a[124:127], v225 offset:43008\n"
    "v_mfma_f32_16x16x32_bf16 v[164:167], a[76:79], a[128:131], v[164:167]\n"
    "v_accvgpr_write_b32 a6, v248\n"
    "v_accvgpr_write_b32 a22, v249\n"
    "v_mfma_f32_16x16x32_bf16 v[168:171], a[80:83], a[128:131], v[168:171]\n"
    "v_cmp_gt_i32 vcc, 19, v245\n"
    "v_cmp_gt_i32 s[96:97], 19, v246\n"
    "v_mfma_f32_16x16x32_bf16 v[172:175], a[84:87], a[128:131], v[172:175]\n"
    "v_cndmask_b32 v248, 0, v247, vcc\n"
    "v_cndmask_b32 v249, 0, v247, s[96:97]\n"
    "v_mfma_f32_16x16x32_bf16 v[176:179], a[88:91], a[128:131], v[176:179]\n"
    "v_accvgpr_write_b32 a7, v248\n"
    "v_accvgpr_write_b32 a23, v249\n"
    "v_mfma_f32_16x16x32_bf16 v[180:183], a[92:95], a[128:131], v[180:183]\n"
    "v_cmp_gt_i32 vcc, -16, v245\n"
    "v_cmp_gt_i32 s[96:97], -16, v246\n"
    "v_mfma_f32_16x16x32_bf16 v[184:187], a[96:99], a[128:131], v[184:187]\n"
    "v_cndmask_b32 v248, 0, v247, vcc\n"
    "v_cndmask_b32 v249, 0, v247, s[96:97]\n"
    "v_mfma_f32_16x16x32_bf16 v[188:191], a[100:103], a[128:131], v[188:191]\n"
    "v_accvgpr_write_b32 a8, v248\n"
    "v_accvgpr_write_b32 a24, v249\n"
    "v_mfma_f32_16x16x32_bf16 v[192:195], a[72:75], a[132:135], v[192:195]\n"
    "v_cmp_gt_i32 vcc, -15, v245\n"
    "v_cmp_gt_i32 s[96:97], -15, v246\n"
    "v_mfma_f32_16x16x32_bf16 v[196:199], a[76:79], a[132:135], v[196:199]\n"
    "v_cndmask_b32 v248, 0, v247, vcc\n"
    "v_cndmask_b32 v249, 0, v247, s[96:97]\n"
    "v_mfma_f32_16x16x32_bf16 v[200:203], a[80:83], a[132:135], v[200:203]\n"
    "v_accvgpr_write_b32 a9, v248\n"
    "v_accvgpr_write_b32 a25, v249\n"
    "v_mfma_f32_16x16x32_bf16 v[204:207], a[84:87], a[132:135], v[204:207]\n"
    "v_mfma_f32_16x16x32_bf16 v[208:211], a[88:91], a[132:135], v[208:211]\n"
    "v_mfma_f32_16x16x32_bf16 v[212:215], a[92:95], a[132:135], v[212:215]\n"
    "v_mfma_f32_16x16x32_bf16 v[216:219], a[96:99], a[132:135], v[216:219]\n"
    "v_mfma_f32_16x16x32_bf16 v[220:223], a[100:103], a[132:135], v[220:223]\n"
    "s_waitcnt lgkmcnt(0)\n"
    "v_mfma_f32_16x16x32_bf16 a[200:203], a[136:139], a[104:107], a[200:203]\n"
    "s_waitcnt vmcnt(8)\n"
    "s_barrier\n"
    "v_mfma_f32_16x16x32_bf16 a[204:207], a[140:143], a[104:107], a[204:207]\n"
    "s_cmp_lg_u32 s68, 0\n"
    "s_cbranch_scc1 .Lw12_%=\n"
    "s_lshl_b32 s73, s66, 3\n"
    "s_add_u32 s73, s73, s67\n"
    "s_lshl_b32 s73, s73, 3\n"
    "s_add_u32 s73, s73, 0x2000\n"
    "v_mov_b32 v5, s73\n"
    "v_mov_b32 v6, s64\n"
    "v_mov_b32 v7, s65\n"
    "s_mov_b64 exec, 1\n"
    "buffer_store_dwordx2 v[6:7], v5, s[56:59], 0 offen sc1\n"
    "s_mov_b64 exec, -1\n"
    ".Lw12_%=:\n"
    "v_mfma_f32_16x16x32_bf16 a[208:211], a[144:147], a[104:107], a[208:211]\n"
    "v_add_u32 v228, 0x10000, v226\n"
    "v_mfma_f32_16x16x32_bf16 a[212:215], a[148:151], a[104:107], a[212:215]\n"
    "v_add_u32 v229, 0x10000, v227\n"
    "v_mfma_f32_16x16x32_bf16 v[16:19], a[152:155], a[104:107], v[16:19]\n"
    "s_add_u32 m0, s80, 0x8000\n"
    "v_mfma_f32_16x16x32_bf16 v[20:23], a[156:159], a[104:107], v[20:23]\n"
    "buffer_load_dwordx4 v230, s[48:51], s75 offen offset:0 sc1 lds\n"
    "v_mfma_f32_16x16x32_bf16 v[24:27], a[160:163], a[104:107], v[24:27]\n"
    "ds_read_b64_tr_b16 a[72:73], v228 offset:0\n"
    "v_mfma_f32_16x16x32_bf16 v[28:31], a[164:167], a[104:107], v[28:31]\n"
    "ds_read_b64_tr_b16 a[74:75], v228 offset:256\n"
    "v_mfma_f32_16x16x32_bf16 a[216:219], a[136:139], a[108:111], a[216:219]\n"
    "ds_read_b128 a[104:107], v224 offset:0\n"
    "v_mfma_f32_16x16x32_bf16 a[220:223], a[140:143], a[108:111], a[220:223]\n"
    "buffer_load_dwordx4 v231, s[48:51], s75 offen offset:1024 sc1 lds\n"
    "v_mfma_f32_16x16x32_bf16 a[224:227], a[144:147], a[108:111], a[224:227]\n"
    "ds_read_b64_tr_b16 a[76:77], v229 offset:0\n"
    "v_mfma_f32_16x16x32_bf16 a[228:231], a[148:151], a[108:111], a[228:231]\n"
    "ds_read_b64_tr_b16 a[78:79], v229 offset:256\n"
    "v_mfma_f32_16x16x32_bf16 v[32:35], a[152:155], a[108:111], v[32:35]\n"
    "v_cmp_gt_i32 vcc, -14, v245\n"
    "v_cmp_gt_i32 s[96:97], -14, v246\n"
    "v_mfma_f32_16x16x32_bf16 v[36:39], a[156:159], a[108:111], v[36:39]\n"
    "buffer_load_dwordx4 v232, s[48:51], s75 offen offset:2048 sc1 lds\n"
    "v_mfma_f32_16x16x32_bf16 v[40:43], a[160:163], a[108:111], v[40:43]\n"
    "ds_read_b64_tr_b16 a[80:81], v228 offset:8192\n"
    "v_mfma_f32_16x16x32_bf16 v[44:47], a[164:167], a[108:111], v[44:47]\n"
    "v_cndmask_b32 v248, 0, v247, vcc\n"
    "v_cndmask_b32 v249, 0, v247, s[96:97]\n"
    "v_mfma_f32_16x16x32_bf16 a[232:235], a[136:139], a[112:115], a[232:235]\n"
    "ds_read_b128 a[108:111], v224 offset:2048\n"
    "v_mfma_f32_16x16x32_bf16 a[236:239], a[140:143], a[112:115], a[236:239]\n"
    "buffer_load_dwordx4 v233, s[48:51], s75 offen offset:3072 sc1 lds\n"
    "v_mfma_f32_16x16x32_bf16 a[240:243], a[144:147], a[112:115], a[240:243]\n"
    "ds_read_b64_tr_b16 a[82:83], v228 offset:8448\n"
    "v_mfma_f32_16x16x32_bf16 a[244:247], a[148:151], a[112:115], a[244:247]\n"
    "ds_read_b64_tr_b16 a[84:85], v229 offset:8192\n"
    "v_mfma_f32_16x16x32_bf16 v[48:51], a[152:155], a[112:115], v[48:51]\n"
    "v_accvgpr_write_b32 a10, v248\n"
    "v_accvgpr_write_b32 a26, v249\n"
    "v_mfma_f32_16x16x32_bf16 v[52:55], a[156:159], a[112:115], v[52:55]\n"
    "s_add_u32 m0, s80, 0x9000\n"
    "v_mfma_f32_16x16x32_bf16 v[56:59], a[160:163], a[112:115], v[56:59]\n"
    "buffer_load_dwordx4 v230, s[48:51], s76 offen offset:0 sc1 lds\n"
    "v_mfma_f32_16x16x32_bf16 v[60:63], a[164:167], a[112:115], v[60:63]\n"
    "ds_read_b64_tr_b16 a[86:87], v229 offset:8448\n"
    "v_mfma_f32_16x16x32_bf16 v[64:67], a[136:139], a[116:119], v[64:67]\n"
    "ds_read_b128 a[112:115], v224 offset:4096\n"
    "v_mfma_f32_16x16x32_bf16 v[68:71], a[140:143], a[116:119], v[68:71]\n"
    "ds_read_b64_tr_b16 a[88:89], v228 offset:16384\n"
    "v_mfma_f32_16x16x32_bf16 v[72:75], a[144:147], a[116:119], v[72:75]\n"
    "buffer_load_dwordx4 v231, s[48:51], s76 offen offset:1024 sc1 lds\n"
    "v_mfma_f32_16x16x32_bf16 v[76:79], a[148:151], a[116:119], v[76:79]\n"
    "ds_read_b64_tr_b16 a[90:91], v228 offset:16640\n"
    "v_mfma_f32_16x16x32_bf16 v[80:83], a[152:155], a[116:119], v[80:83]\n"
    "v_cmp_gt_i32 vcc, -13, v245\n"
    "v_cmp_gt_i32 s[96:97], -13, v246\n"
    "v_mfma_f32_16x16x32_bf16 v[84:87], a[156:159], a[116:119], v[84:87]\n"
    "ds_read_b64_tr_b16 a[92:93], v229 offset:16384\n"
    "v_mfma_f32_16x16x32_bf16 v[88:91], a[160:163], a[116:119], v[88:91]\n"
    "buffer_load_dwordx4 v232, s[48:51], s76 offen offset:2048 sc1 lds\n"
    "v_mfma_f32_16x16x32_bf16 v[92:95], a[164:167], a[116:119], v[92:95]\n"
    "ds_read_b64_tr_b16 a[94:95], v229 offset:16640\n"
    "v_mfma_f32_16x16x32_bf16 v[96:99], a[136:139], a[120:123], v[96:99]\n"
    "ds_read_b128 a[116:119], v224 offset:6144\n"
    "v_mfma_f32_16x16x32_bf16 v[100:103], a[140:143], a[120:123], v[100:103]\n"
    "v_cndmask_b32 v248, 0, v247, vcc\n"
    "v_cndmask_b32 v249, 0, v247, s[96:97]\n"
    "v_mfma_f32_16x16x32_bf16 v[104:107], a[144:147], a[120:123], v[104:107]\n"
    "buffer_load_dwordx4 v233, s[48:51], s76 offen offset:3072 sc1 lds\n"
    "v_mfma_f32_16x16x32_bf16 v[108:111], a[148:151], a[120:123], v[108:111]\n"
    "ds_read_b64_tr_b16 a[96:97], v228 offset:24576\n"
    "v_mfma_f32_16x16x32_bf16 v[112:115], a[152:155], a[120:123], v[112:115]\n"
    "ds_read_b64_tr_b16 a[98:99], v228 offset:24832\n"
    "v_mfma_f32_16x16x32_bf16 v[116:119], a[156:159], a[120:123], v[116:119]\n"
    "v_accvgpr_write_b32 a11, v248\n"
    "v_accvgpr_write_b32 a27, v249\n"
    "v_mfma_f32_16x16x32_bf16 v[120:123], a[160:163], a[120:123], v[120:123]\n"
    "s_add_u32 s75, s75, 0x80\n"
    "v_mfma_f32_16x16x32_bf16 v[124:127], a[164:167], a[120:123], v[124:127]\n"
    "s_add_u32 s76, s76, 0x80\n"
    "v_mfma_f32_16x16x32_bf16 v[128:131], a[136:139], a[124:127], v[128:131]\n"
    "ds_read_b128 a[120:123], v224 offset:8192\n"
    "v_mfma_f32_16x16x32_bf16 v[132:135], a[140:143], a[124:127], v[132:135]\n"
    "ds_read_b64_tr_b16 a[100:101], v229 offset:24576\n"
    "v_mfma_f32_16x16x32_bf16 v[136:139], a[144:147], a[124:127], v[136:139]\n"
    "ds_read_b64_tr_b16 a[102:103], v229 offset:24832\n"
    "v_mfma_f32_16x16x32_bf16 v[140:143], a[148:151], a[124:127], v[140:143]\n"
    "v_cmp_gt_i32 vcc, 0, v245\n"
    "v_cmp_gt_i32 s[96:97], 0, v246\n"
    "v_mfma_f32_16x16x32_bf16 v[144:147], a[152:155], a[124:127], v[144:147]\n"
    "ds_read_b128 a[128:131], v224 offset:12288\n"
    "v_mfma_f32_16x16x32_bf16 v[148:151], a[156:159], a[124:127], v[148:151]\n"
    "v_cndmask_b32 v248, 0, v247, vcc\n"
    "v_cndmask_b32 v249, 0, v247, s[96:97]\n"
    "v_mfma_f32_16x16x32_bf16 v[152:155], a[160:163], a[124:127], v[152:155]\n"
    "ds_read_b128 a[132:135], v224 offset:14336\n"
    "v_mfma_f32_16x16x32_bf16 v[156:159], a[164:167], a[124:127], v[156:159]\n"
    "v_accvgpr_write_b32 a12, v248\n"
    "v_accvgpr_write_b32 a28, v249\n"
    "v_mfma_f32_16x16x32_bf16 v[160:163], a[136:139], a[48:51], v[160:163]\n"
    "ds_read_b128 a[124:127], v224 offset:10240\n"
    "v_mfma_f32_16x16x32_bf16 v[164:167], a[140:143], a[48:51], v[164:167]\n"
    "v_cmp_gt_i32 vcc, 1, v245\n"
    "v_cmp_gt_i32 s[96:97], 1, v246\n"
    "v_mfma_f32_16x16x32_bf16 v[168:171], a[144:147], a[48:51], v[168:171]\n"
    "v_cndmask_b32 v248, 0, v247, vcc\n"
    "v_cndmask_b32 v249, 0, v247, s[96:97]\n"
    "v_mfma_f32_16x16x32_bf16 v[172:175], a[148:151], a[48:51], v[172:175]\n"
    "v_accvgpr_write_b32 a13, v248\n"
    "v_accvgpr_write_b32 a29, v249\n"
    "v_mfma_f32_16x16x32_bf16 v[176:179], a[152:155], a[48:51], v[176:179]\n"
    "v_cmp_gt_i32 vcc, 2, v245\n"
    "v_cmp_gt_i32 s[96:97], 2, v246\n"
    "v_mfma_f32_16x16x32_bf16 v[180:183], a[156:159], a[48:51], v[180:183]\n"
    "v_cndmask_b32 v248, 0, v247, vcc\n"
    "v_cndmask_b32 v249, 0, v247, s[96:97]\n"
    "v_mfma_f32_16x16x32_bf16 v[184:187], a[160:163], a[48:51], v[184:187]\n"
    "v_accvgpr_write_b32 a14, v248\n"
    "v_accvgpr_write_b32 a30, v249\n"
    "v_mfma_f32_16x16x32_bf16 v[188:191], a[164:167], a[48:51], v[188:191]\n"
    "v_cmp_gt_i32 vcc, 3, v245\n"
    "v_cmp_gt_i32 s[96:97], 3, v246\n"
    "v_mfma_f32_16x16x32_bf16 v[192:195], a[136:139], a[52:55], v[192:195]\n"
    "v_cndmask_b32 v248, 0, v247, vcc\n"
    "v_cndmask_b32 v249, 0, v247, s[96:97]\n"
    "v_mfma_f32_16x16x32_bf16 v[196:199], a[140:143], a[52:55], v[196:199]\n"
    "v_accvgpr_write_b32 a15, v248\n"
    "v_accvgpr_write_b32 a31, v249\n"
    "v_mfma_f32_16x16x32_bf16 v[200:203], a[144:147], a[52:55], v[200:203]\n"
    "v_mfma_f32_16x16x32_bf16 v[204:207], a[148:151], a[52:55], v[204:207]\n"
    "v_mfma_f32_16x16x32_bf16 v[208:211], a[152:155], a[52:55], v[208:211]\n"
    "v_mfma_f32_16x16x32_bf16 v[212:215], a[156:159], a[52:55], v[212:215]\n"
    "v_mfma_f32_16x16x32_bf16 v[216:219], a[160:163], a[52:55], v[216:219]\n"
    "v_mfma_f32_16x16x32_bf16 v[220:223], a[164:167], a[52:55], v[220:223]\n"
    "s_waitcnt lgkmcnt(0)\n"
    "v_mfma_f32_16x16x32_bf16 a[200:203], a[72:75], a[104:107], a[200:203]\n"
    "v_mfma_f32_16x16x32_bf16 a[204:207], a[76:79], a[104:107], a[204:207]\n"
    "v_mfma_f32_16x16x32_bf16 a[208:211], a[80:83], a[104:107], a[208:211]\n"
    "s_sub_u32 s96, 15, s67\n"
    "v_mfma_f32_16x16x32_bf16 a[212:215], a[84:87], a[104:107], a[212:215]\n"
    "s_and_b32 s97, s66, 4\n"
    "v_mfma_f32_16x16x32_bf16 v[16:19], a[88:91], a[104:107], v[16:19]\n"
    "ds_read_b64_tr_b16 a[136:137], v228 offset:4096\n"
    "s_cmp_lg_u32 s97, 0\n"
    "s_cselect_b32 s78, s96, s67\n"
    "s_cselect_b32 s77, 0, 1\n"
    "v_mfma_f32_16x16x32_bf16 v[20:23], a[92:95], a[104:107], v[20:23]\n"
    "s_and_b32 s79, s66, 3\n"
    "v_mfma_f32_16x16x32_bf16 v[24:27], a[96:99], a[104:107], v[24:27]\n"
    "ds_read_b64_tr_b16 a[138:139], v228 offset:4352\n"
    "s_lshr_b32 s83, s66, 3\n"
    "v_mfma_f32_16x16x32_bf16 v[28:31], a[100:103], a[104:107], v[28:31]\n"
    "s_lshr_b32 s96, s66, 4\n"
    "v_mfma_f32_16x16x32_bf16 a[216:219], a[72:75], a[108:111], a[216:219]\n"
    "ds_read_b128 a[104:107], v225 offset:0\n"
    "s_xor_b32 s96, s96, s68\n"
    "v_mfma_f32_16x16x32_bf16 a[220:223], a[76:79], a[108:111], a[220:223]\n"
    "ds_read_b64_tr_b16 a[140:141], v229 offset:4096\n"
    "s_lshl_b32 s80, s78, 2\n"
    "v_mfma_f32_16x16x32_bf16 a[224:227], a[80:83], a[108:111], a[224:227]\n"
    "s_add_u32 s80, s80, s96\n"
    "v_mfma_f32_16x16x32_bf16 a[228:231], a[84:87], a[108:111], a[228:231]\n"
    "ds_read_b64_tr_b16 a[142:143], v229 offset:4352\n"
    "v_lshrrev_b32 v0, 1, v8\n"
    "v_mfma_f32_16x16x32_bf16 v[32:35], a[88:91], a[108:111], v[32:35]\n"
    "v_min_u32 v0, 0x68, v0\n"
    "v_mfma_f32_16x16x32_bf16 v[36:39], a[92:95], a[108:111], v[36:39]\n"
    "s_lshl_b32 s96, s78, 9\n"
    "v_mfma_f32_16x16x32_bf16 v[40:43], a[96:99], a[108:111], v[40:43]\n"
    "ds_read_b64_tr_b16 a[144:145], v228 offset:12288\n"
    "s_mul_i32 s97, s83, 0x70\n"
    "v_mfma_f32_16x16x32_bf16 v[44:47], a[100:103], a[108:111], v[44:47]\n"
    "s_add_u32 s96, s96, s97\n"
    "v_mfma_f32_16x16x32_bf16 a[232:235], a[72:75], a[112:115], a[232:235]\n"
    "ds_read_b128 a[108:111], v225 offset:2048\n"
    "s_add_u32 s96, s96, 0x3000\n"
    "v_mfma_f32_16x16x32_bf16 a[236:239], a[76:79], a[112:115], a[236:239]\n"
    "ds_read_b64_tr_b16 a[146:147], v228 offset:12544\n"
    "v_add_u32 v0, s96, v0\n"
    "v_mfma_f32_16x16x32_bf16 a[240:243], a[80:83], a[112:115], a[240:243]\n"
    "v_lshlrev_b32 v4, 3, v8\n"
    "v_mfma_f32_16x16x32_bf16 a[244:247], a[84:87], a[112:115], a[244:247]\n"
    "ds_read_b64_tr_b16 a[148:149], v229 offset:12288\n"
    "v_min_u32 v2, 0xd80, v4\n"
    "v_mfma_f32_16x16x32_bf16 v[48:51], a[88:91], a[112:115], v[48:51]\n"
    "v_min_u32 v3, 0x1b80, v4\n"
    "v_mfma_f32_16x16x32_bf16 v[52:55], a[92:95], a[112:115], v[52:55]\n"
    "ds_read_b64_tr_b16 a[150:151], v229 offset:12544\n"
    "s_lshl_b32 s97, s79, 3\n"
    "v_mfma_f32_16x16x32_bf16 v[56:59], a[96:99], a[112:115], v[56:59]\n"
    "s_add_u32 s97, s97, s67\n"
    "v_mfma_f32_16x16x32_bf16 v[60:63], a[100:103], a[112:115], v[60:63]\n"
    "s_lshl_b32 s97, s97, 2\n"
    "v_mfma_f32_16x16x32_bf16 v[64:67], a[72:75], a[116:119], v[64:67]\n"
    "ds_read_b128 a[112:115], v225 offset:4096\n"
    "s_add_u32 s97, s97, s68\n"
    "v_mfma_f32_16x16x32_bf16 v[68:71], a[76:79], a[116:119], v[68:71]\n"
    "ds_read_b64_tr_b16 a[152:153], v228 offset:20480\n"
    "s_lshl_b32 s96, s97, 13\n"
    "v_mfma_f32_16x16x32_bf16 v[72:75], a[80:83], a[116:119], v[72:75]\n"
    "ds_read_b64_tr_b16 a[154:155], v228 offset:20736\n"
    "s_mul_i32 s75, s97, 0xe00\n"
    "v_mfma_f32_16x16x32_bf16 v[76:79], a[84:87], a[116:119], v[76:79]\n"
    "s_lshl_b32 s97, s83, 20\n"
    "v_mfma_f32_16x16x32_bf16 v[80:83], a[88:91], a[116:119], v[80:83]\n"
    "s_add_u32 s96, s96, s97\n"
    "v_mfma_f32_16x16x32_bf16 v[84:87], a[92:95], a[116:119], v[84:87]\n"
    "ds_read_b64_tr_b16 a[156:157], v229 offset:20480\n"
    "v_add_u32 v1, s96, v4\n"
    "v_mfma_f32_16x16x32_bf16 v[88:91], a[96:99], a[116:119], v[88:91]\n"
    "s_mul_i32 s97, s83, 0x70000\n"
    "v_mfma_f32_16x16x32_bf16 v[92:95], a[100:103], a[116:119], v[92:95]\n"
    "ds_read_b64_tr_b16 a[158:159], v229 offset:20736\n"
    "s_add_u32 s96, s75, s97\n"
    "v_mfma_f32_16x16x32_bf16 v[96:99], a[72:75], a[120:123], v[96:99]\n"
    "ds_read_b128 a[116:119], v225 offset:6144\n"
    "v_add_u32 v2, s96, v2\n"
    "v_mfma_f32_16x16x32_bf16 v[100:103], a[76:79], a[120:123], v[100:103]\n"
    "s_lshl_b32 s96, s78, 2\n"
    "v_mfma_f32_16x16x32_bf16 v[104:107], a[80:83], a[120:123], v[104:107]\n"
    "ds_read_b64_tr_b16 a[160:161], v228 offset:28672\n"
    "s_add_u32 s96, s96, s79\n"
    "v_mfma_f32_16x16x32_bf16 v[108:111], a[84:87], a[120:123], v[108:111]\n"
    "s_mul_i32 s96, s96, 0x1c000\n"
    "v_mfma_f32_16x16x32_bf16 v[112:115], a[88:91], a[120:123], v[112:115]\n"
    "ds_read_b64_tr_b16 a[162:163], v228 offset:28928\n"
    "s_mul_i32 s97, s83, 0x7000\n"
    "v_mfma_f32_16x16x32_bf16 v[116:119], a[92:95], a[120:123], v[116:119]\n"
    "s_add_u32 s96, s96, s97\n"
    "v_mfma_f32_16x16x32_bf16 v[120:123], a[96:99], a[120:123], v[120:123]\n"
    "s_mul_i32 s97, s68, 0x1c00\n"
    "v_mfma_f32_16x16x32_bf16 v[124:127], a[100:103], a[120:123], v[124:127]\n"
    "ds_read_b64_tr_b16 a[164:165], v229 offset:28672\n"
    "s_add_u32 s96, s96, s97\n"
    "v_mfma_f32_16x16x32_bf16 v[128:131], a[72:75], a[124:127], v[128:131]\n"
    "ds_read_b128 a[120:123], v225 offset:8192\n"
    "s_cmp_lg_u32 s68, 0\n"
    "s_cbranch_scc1 .Lw14_%=\n"
    "v_lshrrev_b32 v5, 1, v8\n"
    "v_and_b32 v5, 0x3f, v5\n"
    "s_lshl_b32 s73, s66, 6\n"
    "s_add_u32 s73, s73, 0x2000\n"
    "v_add_u32 v5, s73, v5\n"
    "buffer_load_dwordx2 v[6:7], v5, s[56:59], 0 offen sc1\n"
    ".Lw14_%=:\n"
    "v_mfma_f32_16x16x32_bf16 v[132:135], a[76:79], a[124:127], v[132:135]\n"
    "ds_read_b64_tr_b16 a[166:167], v229 offset:28928\n"
    "s_add_u32 s96, s96, 0x4000000\n"
    "v_mfma_f32_16x16x32_bf16 v[136:139], a[80:83], a[124:127], v[136:139]\n"
    "v_add_u32 v3, s96, v3\n"
    "v_mfma_f32_16x16x32_bf16 v[140:143], a[84:87], a[124:127], v[140:143]\n"
    "s_getpc_b64 s[36:37]\n"
    ".Lpc13_%=:\n"
    "s_add_u32 s36, s36, .Lcodeq_%=-.Lpc13_%=\n"
    "s_addc_u32 s37, s37, 0\n"
    "v_mfma_f32_16x16x32_bf16 v[144:147], a[88:91], a[124:127], v[144:147]\n"
    "ds_read_b128 a[48:51], v225 offset:12288\n"
    "s_lshl_b32 s96, s68, 13\n"
    "v_mfma_f32_16x16x32_bf16 v[148:151], a[92:95], a[124:127], v[148:151]\n"
    "v_add_u32 v4, s96, v4\n"
    "v_mfma_f32_16x16x32_bf16 v[152:155], a[96:99], a[124:127], v[152:155]\n"
    "ds_read_b128 a[52:55], v225 offset:14336\n"
    "v_min_u32 v4, .Lcode1_%=-.Lcodeq_%=-4, v4\n"
    "v_mfma_f32_16x16x32_bf16 v[156:159], a[100:103], a[124:127], v[156:159]\n"
    "v_mfma_f32_16x16x32_bf16 v[160:163], a[72:75], a[128:131], v[160:163]\n"
    "ds_read_b128 a[124:127], v225 offset:10240\n"
    "v_mfma_f32_16x16x32_bf16 v[164:167], a[76:79], a[128:131], v[164:167]\n"
    "v_mfma_f32_16x16x32_bf16 v[168:171], a[80:83], a[128:131], v[168:171]\n"
    "v_mfma_f32_16x16x32_bf16 v[172:175], a[84:87], a[128:131], v[172:175]\n"
    "v_mfma_f32_16x16x32_bf16 v[176:179], a[88:91], a[128:131], v[176:179]\n"
    "v_mfma_f32_16x16x32_bf16 v[180:183], a[92:95], a[128:131], v[180:183]\n"
    "v_mfma_f32_16x16x32_bf16 v[184:187], a[96:99], a[128:131], v[184:187]\n"
    "v_mfma_f32_16x16x32_bf16 v[188:191], a[100:103], a[128:131], v[188:191]\n"
    "v_mfma_f32_16x16x32_bf16 v[192:195], a[72:75], a[132:135], v[192:195]\n"
    "v_mfma_f32_16x16x32_bf16 v[196:199], a[76:79], a[132:135], v[196:199]\n"
    "v_mfma_f32_16x16x32_bf16 v[200:203], a[80:83], a[132:135], v[200:203]\n"
    "v_mfma_f32_16x16x32_bf16 v[204:207], a[84:87], a[132:135], v[204:207]\n"
    "v_mfma_f32_16x16x32_bf16 v[208:211], a[88:91], a[132:135], v[208:211]\n"
    "v_mfma_f32_16x16x32_bf16 v[212:215], a[92:95], a[132:135], v[212:215]\n"
    "v_mfma_f32_16x16x32_bf16 v[216:219], a[96:99], a[132:135], v[216:219]\n"
    "v_mfma_f32_16x16x32_bf16 v[220:223], a[100:103], a[132:135], v[220:223]\n"
    "s_waitcnt lgkmcnt(0)\n"
    "v_mfma_f32_16x16x32_bf16 a[200:203], a[136:139], a[104:107], a[200:203]\n"
    "s_waitcnt vmcnt(0)\n"
    "s_barrier\n"
    "v_mfma_f32_16x16x32_bf16 a[204:207], a[140:143], a[104:107], a[204:207]\n"
    "v_add_u32 v228, 0x0, v226\n"
    "v_mfma_f32_16x16x32_bf16 a[208:211], a[144:147], a[104:107], a[208:211]\n"
    "v_add_u32 v229, 0x0, v227\n"
    "v_mfma_f32_16x16x32_bf16 a[212:215], a[148:151], a[104:107], a[212:215]\n"
    "s_mov_b64 s[60:61], %9\n"
    "s_add_u32 s60, s60, 0x4000000\n"
    "s_addc_u32 s61, s61, 0\n"
    "s_mov_b32 s62, 0x700000\n"
    "s_mov_b32 s63, 0x20000\n"
    "s_cmp_lt_u32 s66, 28\n"
    "s_cselect_b32 s62, 0x700000, 0\n"
    "v_mfma_f32_16x16x32_bf16 v[16:19], a[152:155], a[104:107], v[16:19]\n"
    "ds_read_b64_tr_b16 a[72:73], v228 offset:0\n"
    "v_mfma_f32_16x16x32_bf16 v[20:23], a[156:159], a[104:107], v[20:23]\n"
    "s_cmp_eq_u32 s71, 0\n"
    "s_cselect_b32 s73, s38, s39\n"
    "s_lshl_b32 s85, s73, 5\n"
    "s_add_u32 s85, s85, s66\n"
    "s_lshl_b32 s85, s85, 1\n"
    "s_add_u32 s85, s85, s72\n"
    "s_lshl_b32 s85, s85, 3\n"
    "s_add_u32 s85, s85, 0x3000\n"
    "s_mul_i32 s84, s73, 0x70000\n"
    "s_lshl_b32 s73, s66, 12\n"
    "s_add_u32 s84, s84, s73\n"
    "v_mfma_f32_16x16x32_bf16 v[24:27], a[160:163], a[104:107], v[24:27]\n"
    "ds_read_b64_tr_b16 a[74:75], v228 offset:256\n"
    "v_mfma_f32_16x16x32_bf16 v[28:31], a[164:167], a[104:107], v[28:31]\n"
    "s_lshl_b32 s73, s71, 15\n"
    "s_sub_u32 s73, s69, s73\n"
    "s_add_u32 s73, s73, 0x20000\n"
    "v_add_u32 v249, s73, v243\n"
    "v_mfma_f32_16x16x32_bf16 a[216:219], a[136:139], a[108:111], a[216:219]\n"
    "ds_read_b128 a[104:107], v224 offset:32768\n"
    "v_mfma_f32_16x16x32_bf16 a[220:223], a[140:143], a[108:111], a[220:223]\n"
    "ds_read_b64_tr_b16 a[76:77], v229 offset:0\n"
    "v_mfma_f32_16x16x32_bf16 a[224:227], a[144:147], a[108:111], a[224:227]\n"
    "s_lshl_b32 s73, s72, 13\n"
    "s_add_u32 s73, s73, s69\n"
    "s_add_u32 s73, s73, 0x18000\n"
    "v_add_u32 v250, s73, v8\n"
    "v_xor_b32 v251, 0x6000, v250\n"
    "v_mfma_f32_16x16x32_bf16 a[228:231], a[148:151], a[108:111], a[228:231]\n"
    "ds_read_b64_tr_b16 a[78:79], v229 offset:256\n"
    "v_mfma_f32_16x16x32_bf16 v[32:35], a[152:155], a[108:111], v[32:35]\n"
    "v_mov_b32 v230, 0x3e0293ee\n"
    "v_mfma_f32_16x16x32_bf16 v[36:39], a[156:159], a[108:111], v[36:39]\n"
    "v_mov_b32 v231, 0x3e0293ee\n"
    "v_mfma_f32_16x16x32_bf16 v[40:43], a[160:163], a[108:111], v[40:43]\n"
    "ds_read_b64_tr_b16 a[80:81], v228 offset:8192\n"
    "v_mfma_f32_16x16x32_bf16 v[44:47], a[164:167], a[108:111], v[44:47]\n"
    "v_mov_b32 v232, 0xc13504f3\n"
    "v_mfma_f32_16x16x32_bf16 a[232:235], a[136:139], a[112:115], a[232:235]\n"
    "ds_read_b128 a[108:111], v224 offset:34816\n"
    "v_mfma_f32_16x16x32_bf16 a[236:239], a[140:143], a[112:115], a[236:239]\n"
    "ds_read_b64_tr_b16 a[82:83], v228 offset:8448\n"
    "v_mfma_f32_16x16x32_bf16 a[240:243], a[144:147], a[112:115], a[240:243]\n"
    "v_mov_b32 v233, 0xc13504f3\n"
    "v_mfma_f32_16x16x32_bf16 a[244:247], a[148:151], a[112:115], a[244:247]\n"
    "ds_read_b64_tr_b16 a[84:85], v229 offset:8192\n"
    "v_mfma_f32_16x16x32_bf16 v[48:51], a[152:155], a[112:115], v[48:51]\n"
    "s_mov_b64 s[48:49], %9\n"
    "s_add_u32 s48, s48, 0x3000000\n"
    "s_addc_u32 s49, s49, 0\n"
    "s_mov_b32 s50, 0x1000000\n"
    "s_mov_b32 s51, 0x20000\n"
    "v_mfma_f32_16x16x32_bf16 v[52:55], a[156:159], a[112:115], v[52:55]\n"
    "ds_read_b64_tr_b16 a[86:87], v229 offset:8448\n"
    "v_mfma_f32_16x16x32_bf16 v[56:59], a[160:163], a[112:115], v[56:59]\n"
    "v_add_u32 v10, s86, v9\n"
    "v_mfma_f32_16x16x32_bf16 v[60:63], a[164:167], a[112:115], v[60:63]\n"
    "v_mfma_f32_16x16x32_bf16 v[64:67], a[136:139], a[116:119], v[64:67]\n"
    "ds_read_b128 a[112:115], v224 offset:36864\n"
    "v_mfma_f32_16x16x32_bf16 v[68:71], a[140:143], a[116:119], v[68:71]\n"
    "ds_read_b64_tr_b16 a[88:89], v228 offset:16384\n"
    "v_mfma_f32_16x16x32_bf16 v[72:75], a[144:147], a[116:119], v[72:75]\n"
    "ds_read_b64_tr_b16 a[90:91], v228 offset:16640\n"
    "v_mfma_f32_16x16x32_bf16 v[76:79], a[148:151], a[116:119], v[76:79]\n"
    "v_mfma_f32_16x16x32_bf16 v[80:83], a[152:155], a[116:119], v[80:83]\n"
    "v_mfma_f32_16x16x32_bf16 v[84:87], a[156:159], a[116:119], v[84:87]\n"
    "ds_read_b64_tr_b16 a[92:93], v229 offset:16384\n"
    "v_mfma_f32_16x16x32_bf16 v[88:91], a[160:163], a[116:119], v[88:91]\n"
    "s_cmp_lg_u32 s68, 0\n"
    "s_cbranch_scc1 .Lw15_%=\n"
    "s_waitcnt vmcnt(0)\n"
    ".Lw15_%=:\n"
    "v_mfma_f32_16x16x32_bf16 v[92:95], a[164:167], a[116:119], v[92:95]\n"
    "s_cmp_lg_u32 s68, 0\n"
    "s_cbranch_scc1 .Lw17_%=\n"
    "s_mov_b32 s73, 0x40000\n"
    ".Lp16_%=:\n"
    "v_cmp_ne_u64 vcc, s[64:65], v[6:7]\n"
    "s_nop 4\n"
    "s_cbranch_vccz .Lq16_%=\n"
    "s_sleep 2\n"
    "buffer_load_dwordx2 v[6:7], v5, s[56:59], 0 offen sc1\n"
    "s_waitcnt vmcnt(0)\n"
    "s_sub_u32 s73, s73, 1\n"
    "s_cbranch_scc0 .Lp16_%=\n"
    ".Lq16_%=:\n"
    ".Lw17_%=:\n"
    "v_mfma_f32_16x16x32_bf16 v[96:99], a[136:139], a[120:123], v[96:99]\n"
    "ds_read_b128 a[116:119], v224 offset:38912\n"
    "v_mfma_f32_16x16x32_bf16 v[100:103], a[140:143], a[120:123], v[100:103]\n"
    "ds_read_b64_tr_b16 a[94:95], v229 offset:16640\n"
    "v_mfma_f32_16x16x32_bf16 v[104:107], a[144:147], a[120:123], v[104:107]\n"
    "ds_read_b64_tr_b16 a[96:97], v228 offset:24576\n"
    "v_mfma_f32_16x16x32_bf16 v[108:111], a[148:151], a[120:123], v[108:111]\n"
    "v_mfma_f32_16x16x32_bf16 v[112:115], a[152:155], a[120:123], v[112:115]\n"
    "ds_read_b64_tr_b16 a[98:99], v228 offset:24832\n"
    "v_mfma_f32_16x16x32_bf16 v[116:119], a[156:159], a[120:123], v[116:119]\n"
    "v_mfma_f32_16x16x32_bf16 v[120:123], a[160:163], a[120:123], v[120:123]\n"
    "v_mfma_f32_16x16x32_bf16 v[124:127], a[164:167], a[120:123], v[124:127]\n"
    "ds_read_b64_tr_b16 a[100:101], v229 offset:24576\n"
    "v_mfma_f32_16x16x32_bf16 v[128:131], a[136:139], a[124:127], v[128:131]\n"
    "ds_read_b128 a[120:123], v224 offset:40960\n"
    "v_mfma_f32_16x16x32_bf16 v[132:135], a[140:143], a[124:127], v[132:135]\n"
    "ds_read_b64_tr_b16 a[102:103], v229 offset:24832\n"
    "v_mfma_f32_16x16x32_bf16 v[136:139], a[144:147], a[124:127], v[136:139]\n"
    "v_mfma_f32_16x16x32_bf16 v[140:143], a[148:151], a[124:127], v[140:143]\n"
    "v_mfma_f32_16x16x32_bf16 v[144:147], a[152:155], a[124:127], v[144:147]\n"
    "ds_read_b128 a[128:131], v224 offset:45056\n"
    "v_mfma_f32_16x16x32_bf16 v[148:151], a[156:159], a[124:127], v[148:151]\n"
    "v_mfma_f32_16x16x32_bf16 v[152:155], a[160:163], a[124:127], v[152:155]\n"
    "ds_read_b128 a[132:135], v224 offset:47104\n"
    "v_mfma_f32_16x16x32_bf16 v[156:159], a[164:167], a[124:127], v[156:159]\n"
    "v_mfma_f32_16x16x32_bf16 v[160:163], a[136:139], a[48:51], v[160:163]\n"
    "ds_read_b128 a[124:127], v224 offset:43008\n"
    "v_mfma_f32_16x16x32_bf16 v[164:167], a[140:143], a[48:51], v[164:167]\n"
    "v_mfma_f32_16x16x32_bf16 v[168:171], a[144:147], a[48:51], v[168:171]\n"
    "v_mfma_f32_16x16x32_bf16 v[172:175], a[148:151], a[48:51], v[172:175]\n"
    "v_mfma_f32_16x16x32_bf16 v[176:179], a[152:155], a[48:51], v[176:179]\n"
    "v_mfma_f32_16x16x32_bf16 v[180:183], a[156:159], a[48:51], v[180:183]\n"
    "v_mfma_f32_16x16x32_bf16 v[184:187], a[160:163], a[48:51], v[184:187]\n"
    "v_mfma_f32_16x16x32_bf16 v[188:191], a[164:167], a[48:51], v[188:191]\n"
    "v_mfma_f32_16x16x32_bf16 v[192:195], a[136:139], a[52:55], v[192:195]\n"
    "v_mfma_f32_16x16x32_bf16 v[196:199], a[140:143], a[52:55], v[196:199]\n"
    "v_mfma_f32_16x16x32_bf16 v[200:203], a[144:147], a[52:55], v[200:203]\n"
    "v_mfma_f32_16x16x32_bf16 v[204:207], a[148:151], a[52:55], v[204:207]\n"
    "v_mfma_f32_16x16x32_bf16 v[208:211], a[152:155], a[52:55], v[208:211]\n"
    "v_mfma_f32_16x16x32_bf16 v[212:215], a[156:159], a[52:55], v[212:215]\n"
    "v_mfma_f32_16x16x32_bf16 v[216:219], a[160:163], a[52:55], v[216:219]\n"
    "v_mfma_f32_16x16x32_bf16 v[220:223], a[164:167], a[52:55], v[220:223]\n"
    "s_waitcnt lgkmcnt(0)\n"
    "v_mfma_f32_16x16x32_bf16 a[200:203], a[72:75], a[104:107], a[200:203]\n"
    "s_barrier\n"
    "v_mfma_f32_16x16x32_bf16 a[204:207], a[76:79], a[104:107], a[204:207]\n"
    "ds_read_b64_tr_b16 a[136:137], v228 offset:4096\n"
    "s_mov_b32 s81, 0x80000\n"
    "v_mfma_f32_16x16x32_bf16 a[208:211], a[80:83], a[104:107], a[208:211]\n"
    "ds_read_b64_tr_b16 a[138:139], v228 offset:4352\n"
    "v_mfma_f32_16x16x32_bf16 a[212:215], a[84:87], a[104:107], a[212:215]\n"
    "ds_read_b64_tr_b16 a[140:141], v229 offset:4096\n"
    "v_mfma_f32_16x16x32_bf16 a[216:219], a[72:75], a[108:111], a[216:219]\n"
    "ds_read_b64_tr_b16 a[142:143], v229 offset:4352\n"
    "s_mov_b32 s82, 0x80000\n"
    "v_mfma_f32_16x16x32_bf16 a[220:223], a[76:79], a[108:111], a[220:223]\n"
    "ds_read_b64_tr_b16 a[144:145], v228 offset:12288\n"
    "v_mfma_f32_16x16x32_bf16 a[224:227], a[80:83], a[108:111], a[224:227]\n"
    "ds_read_b64_tr_b16 a[146:147], v228 offset:12544\n"
    "v_mfma_f32_16x16x32_bf16 a[228:231], a[84:87], a[108:111], a[228:231]\n"
    "ds_read_b64_tr_b16 a[148:149], v229 offset:12288\n"
    "v_mfma_f32_16x16x32_bf16 a[232:235], a[72:75], a[112:115], a[232:235]\n"
    "ds_read_b64_tr_b16 a[150:151], v229 offset:12544\n"
    "s_add_u32 m0, s86, s88\n"
    "s_nop 0\n"
    "v_mfma_f32_16x16x32_bf16 a[236:239], a[76:79], a[112:115], a[236:239]\n"
    "ds_read_b128 a[168:171], v225 offset:32768\n"
    "v_mfma_f32_16x16x32_bf16 a[240:243], a[80:83], a[112:115], a[240:243]\n"
    "ds_read_b128 a[172:175], v225 offset:34816\n"
    "v_mfma_f32_16x16x32_bf16 a[244:247], a[84:87], a[112:115], a[244:247]\n"
    "ds_read_b128 a[176:179], v225 offset:36864\n"
    "v_mfma_f32_16x16x32_bf16 v[64:67], a[72:75], a[116:119], v[64:67]\n"
    "ds_read_b128 a[180:183], v225 offset:38912\n"
    "buffer_load_dwordx4 v12, s[40:43], s89 offen offset:0 sc1 lds\n"
    "v_mfma_f32_16x16x32_bf16 v[68:71], a[76:79], a[116:119], v[68:71]\n"
    "ds_read_b128 a[184:187], v225 offset:40960\n"
    "v_mfma_f32_16x16x32_bf16 v[72:75], a[80:83], a[116:119], v[72:75]\n"
    "ds_read_b128 a[188:191], v225 offset:43008\n"
    "v_mfma_f32_16x16x32_bf16 v[76:79], a[84:87], a[116:119], v[76:79]\n"
    "ds_read_b128 a[48:51], v225 offset:45056\n"
    "v_mfma_f32_16x16x32_bf16 v[96:99], a[72:75], a[120:123], v[96:99]\n"
    "ds_read_b128 a[52:55], v225 offset:47104\n"
    "buffer_load_dwordx4 v12, s[40:43], s89 offen offset:1024 sc1 lds\n"
    "v_mfma_f32_16x16x32_bf16 v[100:103], a[76:79], a[120:123], v[100:103]\n"
    "ds_read_b64_tr_b16 a[152:153], v228 offset:20480\n"
    "v_mfma_f32_16x16x32_bf16 v[104:107], a[80:83], a[120:123], v[104:107]\n"
    "ds_read_b64_tr_b16 a[154:155], v228 offset:20736\n"
    "v_mfma_f32_16x16x32_bf16 v[108:111], a[84:87], a[120:123], v[108:111]\n"
    "ds_read_b64_tr_b16 a[156:157], v229 offset:20480\n"
    "v_mfma_f32_16x16x32_bf16 v[128:131], a[72:75], a[124:127], v[128:131]\n"
    "ds_read_b64_tr_b16 a[158:159], v229 offset:20736\n"
    "buffer_load_dwordx4 v12, s[40:43], s89 offen offset:2048 sc1 lds\n"
    "v_mfma_f32_16x16x32_bf16 v[132:135], a[76:79], a[124:127], v[132:135]\n"
    "ds_read_b64_tr_b16 a[160:161], v228 offset:28672\n"
    "v_mfma_f32_16x16x32_bf16 v[136:139], a[80:83], a[124:127], v[136:139]\n"
    "ds_read_b64_tr_b16 a[162:163], v228 offset:28928\n"
    "v_mfma_f32_16x16x32_bf16 v[140:143], a[84:87], a[124:127], v[140:143]\n"
    "ds_read_b64_tr_b16 a[164:165], v229 offset:28672\n"
    "v_mfma_f32_16x16x32_bf16 v[160:163], a[72:75], a[128:131], v[160:163]\n"
    "ds_read_b64_tr_b16 a[166:167], v229 offset:28928\n"
    "buffer_load_dwordx4 v12, s[40:43], s89 offen offset:3072 sc1 lds\n"
    "v_mfma_f32_16x16x32_bf16 v[164:167], a[76:79], a[128:131], v[164:167]\n"
    "v_mfma_f32_16x16x32_bf16 v[168:171], a[80:83], a[128:131], v[168:171]\n"
    "v_mfma_f32_16x16x32_bf16 v[172:175], a[84:87], a[128:131], v[172:175]\n"
    "v_mfma_f32_16x16x32_bf16 v[192:195], a[72:75], a[132:135], v[192:195]\n"
    "s_sub_u32 s89, s89, 0x4000\n"
    "s_cselect_b32 s89, s87, s89\n"
    "s_cselect_b32 s42, s81, s42\n"
    "s_cselect_b32 s81, 0, s81\n"
    "v_mfma_f32_16x16x32_bf16 v[196:199], a[76:79], a[132:135], v[196:199]\n"
    "v_mfma_f32_16x16x32_bf16 v[200:203], a[80:83], a[132:135], v[200:203]\n"
    "v_mfma_f32_16x16x32_bf16 v[204:207], a[84:87], a[132:135], v[204:207]\n"
    "s_waitcnt lgkmcnt(0)\n"
    "v_mfma_f32_16x16x32_bf16 a[200:203], a[136:139], a[168:171], a[200:203]\n"
    "s_barrier\n"
    "v_mfma_f32_16x16x32_bf16 a[204:207], a[140:143], a[168:171], a[204:207]\n"
    "s_add_u32 s73, s86, 0x8000\n"
    "s_and_b32 s73, s73, 0x1ffff\n"
    "s_add_u32 m0, s73, s88\n"
    "s_nop 0\n"
    "v_mfma_f32_16x16x32_bf16 a[208:211], a[144:147], a[168:171], a[208:211]\n"
    "buffer_load_dwordx4 v12, s[40:43], s89 offen offset:0 sc1 lds\n"
    "v_mfma_f32_16x16x32_bf16 a[212:215], a[148:151], a[168:171], a[212:215]\n"
    "v_mfma_f32_16x16x32_bf16 a[216:219], a[136:139], a[172:175], a[216:219]\n"
    "v_mfma_f32_16x16x32_bf16 a[220:223], a[140:143], a[172:175], a[220:223]\n"
    "v_mfma_f32_16x16x32_bf16 a[224:227], a[144:147], a[172:175], a[224:227]\n"
    "v_mfma_f32_16x16x32_bf16 a[228:231], a[148:151], a[172:175], a[228:231]\n"
    "v_mfma_f32_16x16x32_bf16 a[232:235], a[136:139], a[176:179], a[232:235]\n"
    "v_accvgpr_read_b32 v245, a200\n"
    "v_accvgpr_read_b32 v246, a201\n"
    "v_mfma_f32_16x16x32_bf16 a[236:239], a[140:143], a[176:179], a[236:239]\n"
    "v_cvt_pk_bf16_f32 v252, v245, v246\n"
    "v_accvgpr_read_b32 v247, a202\n"
    "v_mfma_f32_16x16x32_bf16 a[240:243], a[144:147], a[176:179], a[240:243]\n"
    "v_accvgpr_read_b32 v248, a203\n"
    "v_cvt_pk_bf16_f32 v253, v247, v248\n"
    "v_mfma_f32_16x16x32_bf16 a[244:247], a[148:151], a[176:179], a[244:247]\n"
    "buffer_load_dwordx4 v12, s[40:43], s89 offen offset:1024 sc1 lds\n"
    "v_mfma_f32_16x16x32_bf16 v[64:67], a[136:139], a[180:183], v[64:67]\n"
    "v_accvgpr_read_b32 v245, a204\n"
    "v_accvgpr_read_b32 v246, a205\n"
    "v_mfma_f32_16x16x32_bf16 v[68:71], a[140:143], a[180:183], v[68:71]\n"
    "v_cvt_pk_bf16_f32 v254, v245, v246\n"
    "v_accvgpr_read_b32 v247, a206\n"
    "v_accvgpr_read_b32 v248, a207\n"
    "v_mfma_f32_16x16x32_bf16 v[72:75], a[144:147], a[180:183], v[72:75]\n"
    "v_cvt_pk_bf16_f32 v255, v247, v248\n"
    "ds_write_b128 v249, v[252:255] offset:0\n"
    "v_mfma_f32_16x16x32_bf16 v[76:79], a[148:151], a[180:183], v[76:79]\n"
    "v_accvgpr_read_b32 v245, a208\n"
    "v_accvgpr_read_b32 v246, a209\n"
    "v_mfma_f32_16x16x32_bf16 v[96:99], a[136:139], a[184:187], v[96:99]\n"
    "v_cvt_pk_bf16_f32 v234, v245, v246\n"
    "v_accvgpr_read_b32 v247, a210\n"
    "v_mfma_f32_16x16x32_bf16 v[100:103], a[140:143], a[184:187], v[100:103]\n"
    "v_accvgpr_read_b32 v248, a211\n"
    "v_cvt_pk_bf16_f32 v235, v247, v248\n"
    "v_mfma_f32_16x16x32_bf16 v[104:107], a[144:147], a[184:187], v[104:107]\n"
    "v_accvgpr_read_b32 v245, a212\n"
    "v_accvgpr_read_b32 v246, a213\n"
    "v_mfma_f32_16x16x32_bf16 v[108:111], a[148:151], a[184:187], v[108:111]\n"
    "v_cvt_pk_bf16_f32 v236, v245, v246\n"
    "v_accvgpr_read_b32 v247, a214\n"
    "v_mfma_f32_16x16x32_bf16 v[128:131], a[136:139], a[188:191], v[128:131]\n"
    "buffer_load_dwordx4 v12, s[40:43], s89 offen offset:2048 sc1 lds\n"
    "v_mfma_f32_16x16x32_bf16 v[132:135], a[140:143], a[188:191], v[132:135]\n"
    "v_accvgpr_read_b32 v248, a215\n"
    "v_cvt_pk_bf16_f32 v237, v247, v248\n"
    "v_mfma_f32_16x16x32_bf16 v[136:139], a[144:147], a[188:191], v[136:139]\n"
    "ds_write_b128 v249, v[234:237] offset:2048\n"
    "v_accvgpr_read_b32 v245, a216\n"
    "v_mfma_f32_16x16x32_bf16 v[140:143], a[148:151], a[188:191], v[140:143]\n"
    "v_accvgpr_read_b32 v246, a217\n"
    "v_cvt_pk_bf16_f32 v252, v245, v246\n"
    "v_accvgpr_read_b32 v247, a218\n"
    "v_mfma_f32_16x16x32_bf16 v[160:163], a[136:139], a[48:51], v[160:163]\n"
    "v_accvgpr_read_b32 v248, a219\n"
    "v_cvt_pk_bf16_f32 v253, v247, v248\n"
    "v_mfma_f32_16x16x32_bf16 v[164:167], a[140:143], a[48:51], v[164:167]\n"
    "v_accvgpr_read_b32 v245, a220\n"
    "v_accvgpr_read_b32 v246, a221\n"
    "v_mfma_f32_16x16x32_bf16 v[168:171], a[144:147], a[48:51], v[168:171]\n"
    "v_cvt_pk_bf16_f32 v254, v245, v246\n"
    "v_accvgpr_read_b32 v247, a222\n"
    "v_mfma_f32_16x16x32_bf16 v[172:175], a[148:151], a[48:51], v[172:175]\n"
    "v_accvgpr_read_b32 v248, a223\n"
    "v_cvt_pk_bf16_f32 v255, v247, v248\n"
    "v_mfma_f32_16x16x32_bf16 v[192:195], a[136:139], a[52:55], v[192:195]\n"
    "ds_write_b128 v249, v[252:255] offset:4096\n"
    "v_accvgpr_read_b32 v245, a224\n"
    "v_mfma_f32_16x16x32_bf16 v[196:199], a[140:143], a[52:55], v[196:199]\n"
    "buffer_load_dwordx4 v12, s[40:43], s89 offen offset:3072 sc1 lds\n"
    "v_mfma_f32_16x16x32_bf16 v[200:203], a[144:147], a[52:55], v[200:203]\n"
    "v_accvgpr_read_b32 v246, a225\n"
    "v_cvt_pk_bf16_f32 v234, v245, v246\n"
    "v_mfma_f32_16x16x32_bf16 v[204:207], a[148:151], a[52:55], v[204:207]\n"
    "v_accvgpr_read_b32 v247, a226\n"
    "v_accvgpr_read_b32 v248, a227\n"
    "v_mfma_f32_16x16x32_bf16 v[16:19], a[88:91], a[104:107], v[16:19]\n"
    "v_cvt_pk_bf16_f32 v235, v247, v248\n"
    "v_accvgpr_read_b32 v245, a228\n"
    "v_mfma_f32_16x16x32_bf16 v[16:19], a[152:155], a[168:171], v[16:19]\n"
    "v_accvgpr_read_b32 v246, a229\n"
    "v_cvt_pk_bf16_f32 v236, v245, v246\n"
    "v_mfma_f32_16x16x32_bf16 v[20:23], a[92:95], a[104:107], v[20:23]\n"
    "v_accvgpr_read_b32 v247, a230\n"
    "v_accvgpr_read_b32 v248, a231\n"
    "v_cvt_pk_bf16_f32 v237, v247, v248\n"
    "v_mfma_f32_16x16x32_bf16 v[20:23], a[156:159], a[168:171], v[20:23]\n"
    "ds_write_b128 v249, v[234:237] offset:6144\n"
    "v_accvgpr_read_b32 v245, a232\n"
    "v_mfma_f32_16x16x32_bf16 v[24:27], a[96:99], a[104:107], v[24:27]\n"
    "v_accvgpr_read_b32 v246, a233\n"
    "v_cvt_pk_bf16_f32 v252, v245, v246\n"
    "v_mfma_f32_16x16x32_bf16 v[24:27], a[160:163], a[168:171], v[24:27]\n"
    "v_accvgpr_read_b32 v247, a234\n"
    "v_accvgpr_read_b32 v248, a235\n"
    "v_mfma_f32_16x16x32_bf16 v[28:31], a[100:103], a[104:107], v[28:31]\n"
    "s_sub_u32 s89, s89, 0x4000\n"
    "s_cselect_b32 s89, s87, s89\n"
    "s_cselect_b32 s42, s81, s42\n"
    "s_cselect_b32 s81, 0, s81\n"
    "v_mfma_f32_16x16x32_bf16 v[28:31], a[164:167], a[168:171], v[28:31]\n"
    "s_add_u32 s73, s86, 0x4000\n"
    "s_add_u32 m0, s73, s88\n"
    "s_nop 0\n"
    "v_mfma_f32_16x16x32_bf16 v[32:35], a[88:91], a[108:111], v[32:35]\n"
    "buffer_load_dwordx4 v12, s[44:47], s90 offen offset:0 sc1 lds\n"
    "v_mfma_f32_16x16x32_bf16 v[32:35], a[152:155], a[172:175], v[32:35]\n"
    "v_mfma_f32_16x16x32_bf16 v[36:39], a[92:95], a[108:111], v[36:39]\n"
    "v_cvt_pk_bf16_f32 v253, v247, v248\n"
    "v_mfma_f32_16x16x32_bf16 v[36:39], a[156:159], a[172:175], v[36:39]\n"
    "v_accvgpr_read_b32 v245, a236\n"
    "v_accvgpr_read_b32 v246, a237\n"
    "v_mfma_f32_16x16x32_bf16 v[40:43], a[96:99], a[108:111], v[40:43]\n"
    "v_cvt_pk_bf16_f32 v254, v245, v246\n"
    "v_accvgpr_read_b32 v247, a238\n"
    "v_accvgpr_read_b32 v248, a239\n"
    "v_mfma_f32_16x16x32_bf16 v[40:43], a[160:163], a[172:175], v[40:43]\n"
    "v_cvt_pk_bf16_f32 v255, v247, v248\n"
    "ds_write_b128 v249, v[252:255] offset:8192\n"
    "v_mfma_f32_16x16x32_bf16 v[44:47], a[100:103], a[108:111], v[44:47]\n"
    "v_accvgpr_read_b32 v245, a240\n"
    "v_accvgpr_read_b32 v246, a241\n"
    "v_mfma_f32_16x16x32_bf16 v[44:47], a[164:167], a[172:175], v[44:47]\n"
    "v_cvt_pk_bf16_f32 v234, v245, v246\n"
    "v_accvgpr_read_b32 v247, a242\n"
    "v_mfma_f32_16x16x32_bf16 v[48:51], a[88:91], a[112:115], v[48:51]\n"
    "v_accvgpr_read_b32 v248, a243\n"
    "v_cvt_pk_bf16_f32 v235, v247, v248\n"
    "v_mfma_f32_16x16x32_bf16 v[48:51], a[152:155], a[176:179], v[48:51]\n"
    "buffer_load_dwordx4 v12, s[44:47], s90 offen offset:1024 sc1 lds\n"
    "v_mfma_f32_16x16x32_bf16 v[52:55], a[92:95], a[112:115], v[52:55]\n"
    "v_accvgpr_read_b32 v245, a244\n"
    "v_accvgpr_read_b32 v246, a245\n"
    "v_mfma_f32_16x16x32_bf16 v[52:55], a[156:159], a[176:179], v[52:55]\n"
    "v_cvt_pk_bf16_f32 v236, v245, v246\n"
    "v_accvgpr_read_b32 v247, a246\n"
    "v_mfma_f32_16x16x32_bf16 v[56:59], a[96:99], a[112:115], v[56:59]\n"
    "v_accvgpr_read_b32 v248, a247\n"
    "v_cvt_pk_bf16_f32 v237, v247, v248\n"
    "v_mfma_f32_16x16x32_bf16 v[56:59], a[160:163], a[176:179], v[56:59]\n"
    "ds_write_b128 v249, v[234:237] offset:10240\n"
    "v_cvt_pk_bf16_f32 v64, v64, v65\n"
    "v_mfma_f32_16x16x32_bf16 v[60:63], a[100:103], a[112:115], v[60:63]\n"
    "v_cvt_pk_bf16_f32 v65, v66, v67\n"
    "v_cvt_pk_bf16_f32 v66, v68, v69\n"
    "v_cvt_pk_bf16_f32 v67, v70, v71\n"
    "v_mfma_f32_16x16x32_bf16 v[60:63], a[164:167], a[176:179], v[60:63]\n"
    "ds_write_b128 v249, v[64:67] offset:12288\n"
    "v_cvt_pk_bf16_f32 v72, v72, v73\n"
    "v_mfma_f32_16x16x32_bf16 v[80:83], a[88:91], a[116:119], v[80:83]\n"
    "v_cvt_pk_bf16_f32 v73, v74, v75\n"
    "v_cvt_pk_bf16_f32 v74, v76, v77\n"
    "v_mfma_f32_16x16x32_bf16 v[80:83], a[152:155], a[180:183], v[80:83]\n"
    "v_cvt_pk_bf16_f32 v75, v78, v79\n"
    "ds_write_b128 v249, v[72:75] offset:14336\n"
    "v_mfma_f32_16x16x32_bf16 v[84:87], a[92:95], a[116:119], v[84:87]\n"
    "buffer_load_dwordx4 v12, s[44:47], s90 offen offset:2048 sc1 lds\n"
    "v_mfma_f32_16x16x32_bf16 v[84:87], a[156:159], a[180:183], v[84:87]\n"
    "v_cvt_pk_bf16_f32 v96, v96, v97\n"
    "v_cvt_pk_bf16_f32 v97, v98, v99\n"
    "v_mfma_f32_16x16x32_bf16 v[88:91], a[96:99], a[116:119], v[88:91]\n"
    "v_cvt_pk_bf16_f32 v98, v100, v101\n"
    "v_cvt_pk_bf16_f32 v99, v102, v103\n"
    "v_mfma_f32_16x16x32_bf16 v[88:91], a[160:163], a[180:183], v[88:91]\n"
    "ds_write_b128 v249, v[96:99] offset:16384\n"
    "v_cvt_pk_bf16_f32 v104, v104, v105\n"
    "v_mfma_f32_16x16x32_bf16 v[92:95], a[100:103], a[116:119], v[92:95]\n"
    "v_cvt_pk_bf16_f32 v105, v106, v107\n"
    "v_cvt_pk_bf16_f32 v106, v108, v109\n"
    "v_mfma_f32_16x16x32_bf16 v[92:95], a[164:167], a[180:183], v[92:95]\n"
    "v_cvt_pk_bf16_f32 v107, v110, v111\n"
    "ds_write_b128 v249, v[104:107] offset:18432\n"
    "v_mfma_f32_16x16x32_bf16 v[112:115], a[88:91], a[120:123], v[112:115]\n"
    "v_cvt_pk_bf16_f32 v128, v128, v129\n"
    "v_cvt_pk_bf16_f32 v129, v130, v131\n"
    "v_mfma_f32_16x16x32_bf16 v[112:115], a[152:155], a[184:187], v[112:115]\n"
    "v_cvt_pk_bf16_f32 v130, v132, v133\n"
    "v_cvt_pk_bf16_f32 v131, v134, v135\n"
    "ds_write_b128 v249, v[128:131] offset:20480\n"
    "v_mfma_f32_16x16x32_bf16 v[116:119], a[92:95], a[120:123], v[116:119]\n"
    "v_cvt_pk_bf16_f32 v136, v136, v137\n"
    "v_cvt_pk_bf16_f32 v137, v138, v139\n"
    "v_mfma_f32_16x16x32_bf16 v[116:119], a[156:159], a[184:187], v[116:119]\n"
    "buffer_load_dwordx4 v12, s[44:47], s90 offen offset:3072 sc1 lds\n"
    "v_mfma_f32_16x16x32_bf16 v[120:123], a[96:99], a[120:123], v[120:123]\n"
    "v_cvt_pk_bf16_f32 v138, v140, v141\n"
    "v_cvt_pk_bf16_f32 v139, v142, v143\n"
    "v_mfma_f32_16x16x32_bf16 v[120:123], a[160:163], a[184:187], v[120:123]\n"
    "ds_write_b128 v249, v[136:139] offset:22528\n"
    "v_cvt_pk_bf16_f32 v160, v160, v161\n"
    "v_mfma_f32_16x16x32_bf16 v[124:127], a[100:103], a[120:123], v[124:127]\n"
    "v_cvt_pk_bf16_f32 v161, v162, v163\n"
    "v_cvt_pk_bf16_f32 v162, v164, v165\n"
    "v_mfma_f32_16x16x32_bf16 v[124:127], a[164:167], a[184:187], v[124:127]\n"
    "v_cvt_pk_bf16_f32 v163, v166, v167\n"
    "ds_write_b128 v249, v[160:163] offset:24576\n"
    "v_mfma_f32_16x16x32_bf16 v[144:147], a[88:91], a[124:127], v[144:147]\n"
    "v_cvt_pk_bf16_f32 v168, v168, v169\n"
    "v_cvt_pk_bf16_f32 v169, v170, v171\n"
    "v_mfma_f32_16x16x32_bf16 v[144:147], a[152:155], a[188:191], v[144:147]\n"
    "v_cvt_pk_bf16_f32 v170, v172, v173\n"
    "v_cvt_pk_bf16_f32 v171, v174, v175\n"
    "v_mfma_f32_16x16x32_bf16 v[148:151], a[92:95], a[124:127], v[148:151]\n"
    "ds_write_b128 v249, v[168:171] offset:26624\n"
    "v_cvt_pk_bf16_f32 v192, v192, v193\n"
    "v_mfma_f32_16x16x32_bf16 v[148:151], a[156:159], a[188:191], v[148:151]\n"
    "v_cvt_pk_bf16_f32 v193, v194, v195\n"
    "v_cvt_pk_bf16_f32 v194, v196, v197\n"
    "v_cvt_pk_bf16_f32 v195, v198, v199\n"
    "v_mfma_f32_16x16x32_bf16 v[152:155], a[96:99], a[124:127], v[152:155]\n"
    "s_sub_u32 s90, s90, 0x4000\n"
    "s_cselect_b32 s90, s87, s90\n"
    "s_cselect_b32 s46, s82, s46\n"
    "s_cselect_b32 s82, 0, s82\n"
    "v_mfma_f32_16x16x32_bf16 v[152:155], a[160:163], a[188:191], v[152:155]\n"
    "v_mfma_f32_16x16x32_bf16 v[156:159], a[100:103], a[124:127], v[156:159]\n"
    "ds_write_b128 v249, v[192:195] offset:28672\n"
    "v_cvt_pk_bf16_f32 v200, v200, v201\n"
    "v_mfma_f32_16x16x32_bf16 v[156:159], a[164:167], a[188:191], v[156:159]\n"
    "v_cvt_pk_bf16_f32 v201, v202, v203\n"
    "v_cvt_pk_bf16_f32 v202, v204, v205\n"
    "v_mfma_f32_16x16x32_bf16 v[176:179], a[88:91], a[128:131], v[176:179]\n"
    "v_cvt_pk_bf16_f32 v203, v206, v207\n"
    "ds_write_b128 v249, v[200:203] offset:30720\n"
    "v_mfma_f32_16x16x32_bf16 v[176:179], a[152:155], a[48:51], v[176:179]\n"
    "v_mfma_f32_16x16x32_bf16 v[180:183], a[92:95], a[128:131], v[180:183]\n"
    "v_mfma_f32_16x16x32_bf16 v[180:183], a[156:159], a[48:51], v[180:183]\n"
    "v_mfma_f32_16x16x32_bf16 v[184:187], a[96:99], a[128:131], v[184:187]\n"
    "v_mfma_f32_16x16x32_bf16 v[184:187], a[160:163], a[48:51], v[184:187]\n"
    "v_mfma_f32_16x16x32_bf16 v[188:191], a[100:103], a[128:131], v[188:191]\n"
    "v_mfma_f32_16x16x32_bf16 v[188:191], a[164:167], a[48:51], v[188:191]\n"
    "v_mfma_f32_16x16x32_bf16 v[208:211], a[88:91], a[132:135], v[208:211]\n"
    "v_mfma_f32_16x16x32_bf16 v[208:211], a[152:155], a[52:55], v[208:211]\n"
    "v_mfma_f32_16x16x32_bf16 v[212:215], a[92:95], a[132:135], v[212:215]\n"
    "v_mfma_f32_16x16x32_bf16 v[212:215], a[156:159], a[52:55], v[212:215]\n"
    "v_mfma_f32_16x16x32_bf16 v[216:219], a[96:99], a[132:135], v[216:219]\n"
    "v_mfma_f32_16x16x32_bf16 v[216:219], a[160:163], a[52:55], v[216:219]\n"
    "v_mfma_f32_16x16x32_bf16 v[220:223], a[100:103], a[132:135], v[220:223]\n"
    "v_mfma_f32_16x16x32_bf16 v[220:223], a[164:167], a[52:55], v[220:223]\n"
    "s_waitcnt vmcnt(8) lgkmcnt(0)\n"
    "s_barrier\n"
    "v_pk_mul_f32 v[16:17], v[16:17], v[232:233]\n"
    "v_pk_mul_f32 v[18:19], v[18:19], v[232:233]\n"
    "v_pk_mul_f32 v[20:21], v[20:21], v[232:233]\n"
    "v_pk_mul_f32 v[22:23], v[22:23], v[232:233]\n"
    "s_add_u32 s73, s86, 0x10000\n"
    "s_and_b32 s73, s73, 0x1ffff\n"
    "s_add_u32 m0, s73, s88\n"
    "s_nop 0\n"
    "v_exp_f32 v16, v16\n"
    "v_exp_f32 v17, v17\n"
    "v_exp_f32 v18, v18\n"
    "ds_read_b128 a[160:163], v251 offset:32768\n"
    "v_exp_f32 v19, v19\n"
    "v_exp_f32 v20, v20\n"
    "v_exp_f32 v21, v21\n"
    "v_exp_f32 v22, v22\n"
    "v_exp_f32 v23, v23\n"
    "v_pk_fma_f32 v[16:17], v[16:17], v[230:231], v[230:231]\n"
    "v_pk_fma_f32 v[18:19], v[18:19], v[230:231], v[230:231]\n"
    "v_pk_fma_f32 v[20:21], v[20:21], v[230:231], v[230:231]\n"
    "buffer_load_dwordx4 v12, s[40:43], s89 offen offset:0 sc1 lds\n"
    "v_pk_fma_f32 v[22:23], v[22:23], v[230:231], v[230:231]\n"
    "v_rcp_f32 v16, v16\n"
    "ds_read_b128 a[164:167], v251 offset:33792\n"
    "v_rcp_f32 v17, v17\n"
    "v_rcp_f32 v18, v18\n"
    "v_rcp_f32 v19, v19\n"
    "v_rcp_f32 v20, v20\n"
    "v_rcp_f32 v21, v21\n"
    "v_rcp_f32 v22, v22\n"
    "v_rcp_f32 v23, v23\n"
    "v_pk_mul_f32 v[16:17], v[16:17], v[24:25]\n"
    "v_pk_mul_f32 v[18:19], v[18:19], v[26:27]\n"
    "buffer_load_dwordx4 v12, s[40:43], s89 offen offset:1024 sc1 lds\n"
    "v_pk_mul_f32 v[20:21], v[20:21], v[28:29]\n"
    "ds_read_b128 a[168:171], v251 offset:34816\n"
    "v_pk_mul_f32 v[22:23], v[22:23], v[30:31]\n"
    "v_cvt_pk_bf16_f32 v24, v16, v17\n"
    "v_cvt_pk_bf16_f32 v25, v18, v19\n"
    "v_cvt_pk_bf16_f32 v26, v20, v21\n"
    "v_cvt_pk_bf16_f32 v27, v22, v23\n"
    "buffer_store_dwordx4 v[24:27], v244, s[60:63], s84 offen offset:0 sc1\n"
    "v_pk_mul_f32 v[32:33], v[32:33], v[232:233]\n"
    "v_pk_mul_f32 v[34:35], v[34:35], v[232:233]\n"
    "v_pk_mul_f32 v[36:37], v[36:37], v[232:233]\n"
    "v_pk_mul_f32 v[38:39], v[38:39], v[232:233]\n"
    "buffer_load_dwordx4 v12, s[40:43], s89 offen offset:2048 sc1 lds\n"
    "v_exp_f32 v32, v32\n"
    "v_exp_f32 v33, v33\n"
    "v_exp_f32 v34, v34\n"
    "ds_read_b128 a[172:175], v251 offset:35840\n"
    "v_exp_f32 v35, v35\n"
    "v_exp_f32 v36, v36\n"
    "v_exp_f32 v37, v37\n"
    "v_exp_f32 v38, v38\n"
    "v_exp_f32 v39, v39\n"
    "v_pk_fma_f32 v[32:33], v[32:33], v[230:231], v[230:231]\n"
    "v_pk_fma_f32 v[34:35], v[34:35], v[230:231], v[230:231]\n"
    "v_pk_fma_f32 v[36:37], v[36:37], v[230:231], v[230:231]\n"
    "buffer_load_dwordx4 v12, s[40:43], s89 offen offset:3072 sc1 lds\n"
    "v_pk_fma_f32 v[38:39], v[38:39], v[230:231], v[230:231]\n"
    "v_rcp_f32 v32, v32\n"
    "ds_read_b128 a[176:179], v251 offset:36864\n"
    "v_rcp_f32 v33, v33\n"
    "v_rcp_f32 v34, v34\n"
    "v_rcp_f32 v35, v35\n"
    "v_rcp_f32 v36, v36\n"
    "v_rcp_f32 v37, v37\n"
    "v_rcp_f32 v38, v38\n"
    "v_rcp_f32 v39, v39\n"
    "v_pk_mul_f32 v[32:33], v[32:33], v[40:41]\n"
    "v_pk_mul_f32 v[34:35], v[34:35], v[42:43]\n"
    "s_sub_u32 s89, s89, 0x4000\n"
    "s_cselect_b32 s89, s87, s89\n"
    "s_cselect_b32 s42, s81, s42\n"
    "s_cselect_b32 s81, 0, s81\n"
    "v_pk_mul_f32 v[36:37], v[36:37], v[44:45]\n"
    "ds_read_b128 a[180:183], v251 offset:37888\n"
    "v_pk_mul_f32 v[38:39], v[38:39], v[46:47]\n"
    "v_cvt_pk_bf16_f32 v40, v32, v33\n"
    "v_cvt_pk_bf16_f32 v41, v34, v35\n"
    "v_cvt_pk_bf16_f32 v42, v36, v37\n"
    "v_cvt_pk_bf16_f32 v43, v38, v39\n"
    "buffer_store_dwordx4 v[40:43], v244, s[60:63], s84 offen offset:1024 sc1\n"
    "s_add_u32 s84, s84, 0x1c000\n"
    "v_pk_mul_f32 v[48:49], v[48:49], v[232:233]\n"
    "v_pk_mul_f32 v[50:51], v[50:51], v[232:233]\n"
    "v_pk_mul_f32 v[52:53], v[52:53], v[232:233]\n"
    "v_pk_mul_f32 v[54:55], v[54:55], v[232:233]\n"
    "s_add_u32 s73, s86, 0x8000\n"
    "s_and_b32 s73, s73, 0x1ffff\n"
    "s_add_u32 s73, s73, 0x4000\n"
    "s_add_u32 m0, s73, s88\n"
    "s_nop 0\n"
    "v_exp_f32 v48, v48\n"
    "v_exp_f32 v49, v49\n"
    "v_exp_f32 v50, v50\n"
    "ds_read_b128 a[184:187], v251 offset:38912\n"
    "v_exp_f32 v51, v51\n"
    "v_exp_f32 v52, v52\n"
    "v_exp_f32 v53, v53\n"
    "v_exp_f32 v54, v54\n"
    "v_exp_f32 v55, v55\n"
    "v_pk_fma_f32 v[48:49], v[48:49], v[230:231], v[230:231]\n"
    "v_pk_fma_f32 v[50:51], v[50:51], v[230:231], v[230:231]\n"
    "v_pk_fma_f32 v[52:53], v[52:53], v[230:231], v[230:231]\n"
    "buffer_load_dwordx4 v12, s[44:47], s90 offen offset:0 sc1 lds\n"
    "v_pk_fma_f32 v[54:55], v[54:55], v[230:231], v[230:231]\n"
    "v_rcp_f32 v48, v48\n"
    "ds_read_b128 a[188:191], v251 offset:39936\n"
    "v_rcp_f32 v49, v49\n"
    "v_rcp_f32 v50, v50\n"
    "v_rcp_f32 v51, v51\n"
    "v_rcp_f32 v52, v52\n"
    "v_rcp_f32 v53, v53\n"
    "v_rcp_f32 v54, v54\n"
    "v_rcp_f32 v55, v55\n"
    "v_pk_mul_f32 v[48:49], v[48:49], v[56:57]\n"
    "v_pk_mul_f32 v[50:51], v[50:51], v[58:59]\n"
    "buffer_load_dwordx4 v12, s[44:47], s90 offen offset:1024 sc1 lds\n"
    "v_pk_mul_f32 v[52:53], v[52:53], v[60:61]\n"
    "ds_read_b128 a[64:67], v10 offset:0\n"
    "v_pk_mul_f32 v[54:55], v[54:55], v[62:63]\n"
    "v_cvt_pk_bf16_f32 v56, v48, v49\n"
    "v_cvt_pk_bf16_f32 v57, v50, v51\n"
    "v_cvt_pk_bf16_f32 v58, v52, v53\n"
    "v_cvt_pk_bf16_f32 v59, v54, v55\n"
    "buffer_store_dwordx4 v[56:59], v244, s[60:63], s84 offen offset:0 sc1\n"
    "v_pk_mul_f32 v[80:81], v[80:81], v[232:233]\n"
    "v_pk_mul_f32 v[82:83], v[82:83], v[232:233]\n"
    "v_pk_mul_f32 v[84:85], v[84:85], v[232:233]\n"
    "v_pk_mul_f32 v[86:87], v[86:87], v[232:233]\n"
    "buffer_load_dwordx4 v12, s[44:47], s90 offen offset:2048 sc1 lds\n"
    "v_exp_f32 v80, v80\n"
    "v_accvgpr_read_b32 v48, a0\n"
    "v_exp_f32 v81, v81\n"
    "ds_read_b128 a[68:71], v10 offset:1024\n"
    "v_accvgpr_read_b32 v49, a1\n"
    "v_exp_f32 v82, v82\n"
    "v_accvgpr_read_b32 v50, a2\n"
    "v_exp_f32 v83, v83\n"
    "v_accvgpr_read_b32 v51, a3\n"
    "v_exp_f32 v84, v84\n"
    "v_accvgpr_read_b32 v52, a4\n"
    "v_exp_f32 v85, v85\n"
    "buffer_load_dwordx4 v12, s[44:47], s90 offen offset:3072 sc1 lds\n"
    "v_accvgpr_read_b32 v53, a5\n"
    "v_exp_f32 v86, v86\n"
    "ds_read_b128 a[72:75], v10 offset:2048\n"
    "v_accvgpr_read_b32 v54, a6\n"
    "v_exp_f32 v87, v87\n"
    "v_accvgpr_read_b32 v55, a7\n"
    "v_pk_fma_f32 v[80:81], v[80:81], v[230:231], v[230:231]\n"
    "v_pk_fma_f32 v[82:83], v[82:83], v[230:231], v[230:231]\n"
    "v_pk_fma_f32 v[84:85], v[84:85], v[230:231], v[230:231]\n"
    "v_pk_fma_f32 v[86:87], v[86:87], v[230:231], v[230:231]\n"
    "v_rcp_f32 v80, v80\n"
    "v_rcp_f32 v81, v81\n"
    "s_sub_u32 s90, s90, 0x4000\n"
    "s_cselect_b32 s90, s87, s90\n"
    "s_cselect_b32 s46, s82, s46\n"
    "s_cselect_b32 s82, 0, s82\n"
    "v_rcp_f32 v82, v82\n"
    "ds_read_b128 a[76:79], v10 offset:3072\n"
    "v_rcp_f32 v83, v83\n"
    "v_rcp_f32 v84, v84\n"
    "v_rcp_f32 v85, v85\n"
    "v_rcp_f32 v86, v86\n"
    "v_rcp_f32 v87, v87\n"
    "v_pk_mul_f32 v[80:81], v[80:81], v[88:89]\n"
    "v_pk_mul_f32 v[82:83], v[82:83], v[90:91]\n"
    "v_pk_mul_f32 v[84:85], v[84:85], v[92:93]\n"
    "v_pk_mul_f32 v[86:87], v[86:87], v[94:95]\n"
    "v_cvt_pk_bf16_f32 v88, v80, v81\n"
    "ds_read_b128 a[80:83], v10 offset:4096\n"
    "v_cvt_pk_bf16_f32 v89, v82, v83\n"
    "v_cvt_pk_bf16_f32 v90, v84, v85\n"
    "v_cvt_pk_bf16_f32 v91, v86, v87\n"
    "buffer_store_dwordx4 v[88:91], v244, s[60:63], s84 offen offset:1024 sc1\n"
    "s_add_u32 s84, s84, 0x1c000\n"
    "v_pk_mul_f32 v[112:113], v[112:113], v[232:233]\n"
    "v_pk_mul_f32 v[114:115], v[114:115], v[232:233]\n"
    "v_pk_mul_f32 v[116:117], v[116:117], v[232:233]\n"
    "v_pk_mul_f32 v[118:119], v[118:119], v[232:233]\n"
    "v_exp_f32 v112, v112\n"
    "v_accvgpr_read_b32 v56, a8\n"
    "v_exp_f32 v113, v113\n"
    "ds_read_b128 a[84:87], v10 offset:5120\n"
    "v_accvgpr_read_b32 v57, a9\n"
    "v_exp_f32 v114, v114\n"
    "v_accvgpr_read_b32 v58, a10\n"
    "v_exp_f32 v115, v115\n"
    "v_accvgpr_read_b32 v59, a11\n"
    "v_exp_f32 v116, v116\n"
    "v_accvgpr_read_b32 v60, a12\n"
    "v_exp_f32 v117, v117\n"
    "v_accvgpr_read_b32 v61, a13\n"
    "v_exp_f32 v118, v118\n"
    "ds_read_b128 a[88:91], v10 offset:6144\n"
    "v_accvgpr_read_b32 v62, a14\n"
    "v_exp_f32 v119, v119\n"
    "v_accvgpr_read_b32 v63, a15\n"
    "v_pk_fma_f32 v[112:113], v[112:113], v[230:231], v[230:231]\n"
    "v_pk_fma_f32 v[114:115], v[114:115], v[230:231], v[230:231]\n"
    "v_pk_fma_f32 v[116:117], v[116:117], v[230:231], v[230:231]\n"
    "v_pk_fma_f32 v[118:119], v[118:119], v[230:231], v[230:231]\n"
    "v_rcp_f32 v112, v112\n"
    "v_rcp_f32 v113, v113\n"
    "v_rcp_f32 v114, v114\n"
    "ds_read_b128 a[92:95], v10 offset:7168\n"
    "v_rcp_f32 v115, v115\n"
    "v_rcp_f32 v116, v116\n"
    "v_rcp_f32 v117, v117\n"
    "v_rcp_f32 v118, v118\n"
    "v_rcp_f32 v119, v119\n"
    "v_pk_mul_f32 v[112:113], v[112:113], v[120:121]\n"
    "v_pk_mul_f32 v[114:115], v[114:115], v[122:123]\n"
    "v_pk_mul_f32 v[116:117], v[116:117], v[124:125]\n"
    "v_pk_mul_f32 v[118:119], v[118:119], v[126:127]\n"
    "v_cvt_pk_bf16_f32 v120, v112, v113\n"
    "v_cvt_pk_bf16_f32 v121, v114, v115\n"
    "v_cvt_pk_bf16_f32 v122, v116, v117\n"
    "v_cvt_pk_bf16_f32 v123, v118, v119\n"
    "buffer_store_dwordx4 v[120:123], v244, s[60:63], s84 offen offset:0 sc1\n"
    "v_pk_mul_f32 v[144:145], v[144:145], v[232:233]\n"
    "v_pk_mul_f32 v[146:147], v[146:147], v[232:233]\n"
    "v_pk_mul_f32 v[148:149], v[148:149], v[232:233]\n"
    "v_pk_mul_f32 v[150:151], v[150:151], v[232:233]\n"
    "v_exp_f32 v144, v144\n"
    "v_accvgpr_read_b32 v16, a16\n"
    "v_exp_f32 v145, v145\n"
    "v_accvgpr_read_b32 v17, a17\n"
    "v_exp_f32 v146, v146\n"
    "v_accvgpr_read_b32 v18, a18\n"
    "v_exp_f32 v147, v147\n"
    "v_accvgpr_read_b32 v19, a19\n"
    "v_exp_f32 v148, v148\n"
    "v_accvgpr_read_b32 v20, a20\n"
    "v_exp_f32 v149, v149\n"
    "v_accvgpr_read_b32 v21, a21\n"
    "v_exp_f32 v150, v150\n"
    "v_accvgpr_read_b32 v22, a22\n"
    "v_exp_f32 v151, v151\n"
    "v_accvgpr_read_b32 v23, a23\n"
    "v_pk_fma_f32 v[144:145], v[144:145], v[230:231], v[230:231]\n"
    "v_pk_fma_f32 v[146:147], v[146:147], v[230:231], v[230:231]\n"
    "v_pk_fma_f32 v[148:149], v[148:149], v[230:231], v[230:231]\n"
    "v_pk_fma_f32 v[150:151], v[150:151], v[230:231], v[230:231]\n"
    "v_rcp_f32 v144, v144\n"
    "v_rcp_f32 v145, v145\n"
    "v_rcp_f32 v146, v146\n"
    "v_rcp_f32 v147, v147\n"
    "v_rcp_f32 v148, v148\n"
    "v_rcp_f32 v149, v149\n"
    "v_rcp_f32 v150, v150\n"
    "v_rcp_f32 v151, v151\n"
    "v_pk_mul_f32 v[144:145], v[144:145], v[152:153]\n"
    "v_pk_mul_f32 v[146:147], v[146:147], v[154:155]\n"
    "v_pk_mul_f32 v[148:149], v[148:149], v[156:157]\n"
    "v_pk_mul_f32 v[150:151], v[150:151], v[158:159]\n"
    "v_cvt_pk_bf16_f32 v152, v144, v145\n"
    "v_cvt_pk_bf16_f32 v153, v146, v147\n"
    "v_cvt_pk_bf16_f32 v154, v148, v149\n"
    "v_cvt_pk_bf16_f32 v155, v150, v151\n"
    "buffer_store_dwordx4 v[152:155], v244, s[60:63], s84 offen offset:1024 sc1\n"
    "s_add_u32 s84, s84, 0x1c000\n"
    "v_pk_mul_f32 v[176:177], v[176:177], v[232:233]\n"
    "v_pk_mul_f32 v[178:179], v[178:179], v[232:233]\n"
    "v_pk_mul_f32 v[180:181], v[180:181], v[232:233]\n"
    "v_pk_mul_f32 v[182:183], v[182:183], v[232:233]\n"
    "v_exp_f32 v176, v176\n"
    "v_accvgpr_read_b32 v24, a24\n"
    "v_exp_f32 v177, v177\n"
    "v_accvgpr_read_b32 v25, a25\n"
    "v_exp_f32 v178, v178\n"
    "v_accvgpr_read_b32 v26, a26\n"
    "v_exp_f32 v179, v179\n"
    "v_accvgpr_read_b32 v27, a27\n"
    "v_exp_f32 v180, v180\n"
    "v_accvgpr_read_b32 v28, a28\n"
    "v_exp_f32 v181, v181\n"
    "v_accvgpr_read_b32 v29, a29\n"
    "v_exp_f32 v182, v182\n"
    "v_accvgpr_read_b32 v30, a30\n"
    "v_exp_f32 v183, v183\n"
    "v_accvgpr_read_b32 v31, a31\n"
    "v_pk_fma_f32 v[176:177], v[176:177], v[230:231], v[230:231]\n"
    "v_pk_fma_f32 v[178:179], v[178:179], v[230:231], v[230:231]\n"
    "v_pk_fma_f32 v[180:181], v[180:181], v[230:231], v[230:231]\n"
    "v_pk_fma_f32 v[182:183], v[182:183], v[230:231], v[230:231]\n"
    "v_rcp_f32 v176, v176\n"
    "v_rcp_f32 v177, v177\n"
    "v_rcp_f32 v178, v178\n"
    "v_rcp_f32 v179, v179\n"
    "v_rcp_f32 v180, v180\n"
    "v_rcp_f32 v181, v181\n"
    "v_rcp_f32 v182, v182\n"
    "v_rcp_f32 v183, v183\n"
    "v_pk_mul_f32 v[176:177], v[176:177], v[184:185]\n"
    "v_pk_mul_f32 v[178:179], v[178:179], v[186:187]\n"
    "v_pk_mul_f32 v[180:181], v[180:181], v[188:189]\n"
    "v_pk_mul_f32 v[182:183], v[182:183], v[190:191]\n"
    "v_cvt_pk_bf16_f32 v184, v176, v177\n"
    "v_cvt_pk_bf16_f32 v185, v178, v179\n"
    "v_cvt_pk_bf16_f32 v186, v180, v181\n"
    "v_cvt_pk_bf16_f32 v187, v182, v183\n"
    "buffer_store_dwordx4 v[184:187], v244, s[60:63], s84 offen offset:0 sc1\n"
    "v_pk_mul_f32 v[208:209], v[208:209], v[232:233]\n"
    "v_pk_mul_f32 v[210:211], v[210:211], v[232:233]\n"
    "v_pk_mul_f32 v[212:213], v[212:213], v[232:233]\n"
    "v_pk_mul_f32 v[214:215], v[214:215], v[232:233]\n"
    "v_exp_f32 v208, v208\n"
    "v_exp_f32 v209, v209\n"
    "v_exp_f32 v210, v210\n"
    "v_exp_f32 v211, v211\n"
    "v_exp_f32 v212, v212\n"
    "v_exp_f32 v213, v213\n"
    "v_exp_f32 v214, v214\n"
    "v_exp_f32 v215, v215\n"
    "v_pk_fma_f32 v[208:209], v[208:209], v[230:231], v[230:231]\n"
    "v_pk_fma_f32 v[210:211], v[210:211], v[230:231], v[230:231]\n"
    "v_pk_fma_f32 v[212:213], v[212:213], v[230:231], v[230:231]\n"
    "v_pk_fma_f32 v[214:215], v[214:215], v[230:231], v[230:231]\n"
    "v_rcp_f32 v208, v208\n"
    "v_rcp_f32 v209, v209\n"
    "v_rcp_f32 v210, v210\n"
    "v_rcp_f32 v211, v211\n"
    "v_rcp_f32 v212, v212\n"
    "v_rcp_f32 v213, v213\n"
    "v_rcp_f32 v214, v214\n"
    "v_rcp_f32 v215, v215\n"
    "v_pk_mul_f32 v[208:209], v[208:209], v[216:217]\n"
    "v_pk_mul_f32 v[210:211], v[210:211], v[218:219]\n"
    "v_pk_mul_f32 v[212:213], v[212:213], v[220:221]\n"
    "v_pk_mul_f32 v[214:215], v[214:215], v[222:223]\n"
    "v_cvt_pk_bf16_f32 v216, v208, v209\n"
    "v_cvt_pk_bf16_f32 v217, v210, v211\n"
    "v_cvt_pk_bf16_f32 v218, v212, v213\n"
    "v_cvt_pk_bf16_f32 v219, v214, v215\n"
    "buffer_store_dwordx4 v[216:219], v244, s[60:63], s84 offen offset:1024 sc1\n"
    "s_add_u32 s84, s84, 0x1c000\n"
    "v_mov_b32 v112, 0x3f803f80\n"
    "v_accvgpr_write_b32 a32, v112\n"
    "v_accvgpr_write_b32 a33, v112\n"
    "v_accvgpr_write_b32 a34, v112\n"
    "v_accvgpr_write_b32 a35, v112\n"
    "s_mov_b32 s84, 0\n"
    "v_add_u32 v120, 0x1000, v8\n"
    "v_mov_b32 v125, 0xff800000\n"
    "s_lshl_b32 s73, s68, 8\n"
    "s_add_u32 s73, s73, s69\n"
    "s_add_u32 s73, s73, 0x24000\n"
    "v_lshrrev_b32 v122, 2, v8\n"
    "v_add_u32 v122, s73, v122\n"
    "v_xor_b32 v123, 0x200, v122\n"
    "s_waitcnt vmcnt(20) lgkmcnt(0)\n"
    "s_barrier\n"
    "v_add_u32 v10, s86, v9\n"
    "s_add_u32 s73, s86, 0x8000\n"
    "s_and_b32 s73, s73, 0x1ffff\n"
    "v_add_u32 v11, s73, v9\n"
    "v_mfma_f32_16x16x32_bf16 v[48:51], a[64:67], a[160:163], v[48:51]\n"
    "ds_read_b128 a[128:131], v250 offset:32768\n"
    "v_mfma_f32_16x16x32_bf16 v[48:51], a[68:71], a[164:167], v[48:51]\n"
    "ds_read_b128 a[132:135], v250 offset:33792\n"
    "v_mfma_f32_16x16x32_bf16 v[48:51], a[72:75], a[168:171], v[48:51]\n"
    "ds_read_b128 a[136:139], v250 offset:34816\n"
    "v_mfma_f32_16x16x32_bf16 v[48:51], a[76:79], a[172:175], v[48:51]\n"
    "ds_read_b128 a[140:143], v250 offset:35840\n"
    "v_mfma_f32_16x16x32_bf16 v[56:59], a[64:67], a[176:179], v[56:59]\n"
    "ds_read_b128 a[64:67], v11 offset:0\n"
    "v_mfma_f32_16x16x32_bf16 v[56:59], a[68:71], a[180:183], v[56:59]\n"
    "ds_read_b128 a[68:71], v11 offset:1024\n"
    "v_mfma_f32_16x16x32_bf16 v[56:59], a[72:75], a[184:187], v[56:59]\n"
    "ds_read_b128 a[72:75], v11 offset:2048\n"
    "v_mfma_f32_16x16x32_bf16 v[56:59], a[76:79], a[188:191], v[56:59]\n"
    "ds_read_b128 a[76:79], v11 offset:3072\n"
    "v_mfma_f32_16x16x32_bf16 v[52:55], a[80:83], a[160:163], v[52:55]\n"
    "ds_read_b128 a[144:147], v250 offset:36864\n"
    "v_mfma_f32_16x16x32_bf16 v[52:55], a[84:87], a[164:167], v[52:55]\n"
    "ds_read_b128 a[148:151], v250 offset:37888\n"
    "v_mfma_f32_16x16x32_bf16 v[52:55], a[88:91], a[168:171], v[52:55]\n"
    "ds_read_b128 a[152:155], v250 offset:38912\n"
    "v_mfma_f32_16x16x32_bf16 v[52:55], a[92:95], a[172:175], v[52:55]\n"
    "ds_read_b128 a[156:159], v250 offset:39936\n"
    "v_mfma_f32_16x16x32_bf16 v[60:63], a[80:83], a[176:179], v[60:63]\n"
    "ds_read_b128 a[80:83], v11 offset:4096\n"
    "v_mfma_f32_16x16x32_bf16 v[60:63], a[84:87], a[180:183], v[60:63]\n"
    "ds_read_b128 a[84:87], v11 offset:5120\n"
    "v_mfma_f32_16x16x32_bf16 v[60:63], a[88:91], a[184:187], v[60:63]\n"
    "ds_read_b128 a[88:91], v11 offset:6144\n"
    "v_mfma_f32_16x16x32_bf16 v[60:63], a[92:95], a[188:191], v[60:63]\n"
    "ds_read_b128 a[92:95], v11 offset:7168\n"
    "s_waitcnt lgkmcnt(11)\n"
    "v_mfma_f32_16x16x32_bf16 v[16:19], a[64:67], a[128:131], v[16:19]\n"
    "ds_read_b128 a[224:227], v251 offset:0\n"
    "s_waitcnt lgkmcnt(11)\n"
    "v_mfma_f32_16x16x32_bf16 v[16:19], a[68:71], a[132:135], v[16:19]\n"
    "ds_read_b128 a[228:231], v251 offset:1024\n"
    "s_waitcnt lgkmcnt(11)\n"
    "v_mfma_f32_16x16x32_bf16 v[16:19], a[72:75], a[136:139], v[16:19]\n"
    "ds_read_b128 a[232:235], v251 offset:2048\n"
    "s_waitcnt lgkmcnt(11)\n"
    "v_mfma_f32_16x16x32_bf16 v[16:19], a[76:79], a[140:143], v[16:19]\n"
    "ds_read_b128 a[236:239], v251 offset:3072\n"
    "s_waitcnt lgkmcnt(11)\n"
    "v_mfma_f32_16x16x32_bf16 v[24:27], a[64:67], a[144:147], v[24:27]\n"
    "ds_read_b128 a[240:243], v251 offset:4096\n"
    "s_nop 3\n"
    "s_waitcnt lgkmcnt(11)\n"
    "v_mfma_f32_16x16x32_bf16 v[24:27], a[68:71], a[148:151], v[24:27]\n"
    "ds_read_b128 a[244:247], v251 offset:5120\n"
    "s_waitcnt lgkmcnt(11)\n"
    "v_mfma_f32_16x16x32_bf16 v[24:27], a[72:75], a[152:155], v[24:27]\n"
    "ds_read_b128 a[248:251], v251 offset:6144\n"
    "s_waitcnt lgkmcnt(11)\n"
    "v_mfma_f32_16x16x32_bf16 v[24:27], a[76:79], a[156:159], v[24:27]\n"
    "ds_read_b128 a[252:255], v251 offset:7168\n"
    "s_waitcnt lgkmcnt(11)\n"
    "v_mfma_f32_16x16x32_bf16 v[20:23], a[80:83], a[128:131], v[20:23]\n"
    "ds_read_b128 a[192:195], v250 offset:0\n"
    "v_max3_f32 v113, v48, v49, v50\n"
    "s_waitcnt lgkmcnt(11)\n"
    "v_mfma_f32_16x16x32_bf16 v[20:23], a[84:87], a[132:135], v[20:23]\n"
    "ds_read_b128 a[196:199], v250 offset:1024\n"
    "v_max3_f32 v113, v113, v51, v52\n"
    "s_waitcnt lgkmcnt(11)\n"
    "v_mfma_f32_16x16x32_bf16 v[20:23], a[88:91], a[136:139], v[20:23]\n"
    "ds_read_b128 a[200:203], v250 offset:2048\n"
    "v_max3_f32 v113, v113, v53, v54\n"
    "s_waitcnt lgkmcnt(11)\n"
    "v_mfma_f32_16x16x32_bf16 v[20:23], a[92:95], a[140:143], v[20:23]\n"
    "ds_read_b128 a[204:207], v250 offset:3072\n"
    "v_max_f32 v113, v113, v55\n"
    "v_mfma_f32_16x16x32_bf16 v[28:31], a[80:83], a[144:147], v[28:31]\n"
    "ds_read_b128 a[208:211], v250 offset:4096\n"
    "v_max3_f32 v14, v56, v57, v58\n"
    "v_mfma_f32_16x16x32_bf16 v[28:31], a[84:87], a[148:151], v[28:31]\n"
    "ds_read_b128 a[212:215], v250 offset:5120\n"
    "v_max3_f32 v14, v14, v59, v60\n"
    "v_mfma_f32_16x16x32_bf16 v[28:31], a[88:91], a[152:155], v[28:31]\n"
    "ds_read_b128 a[216:219], v250 offset:6144\n"
    "v_max3_f32 v14, v14, v61, v62\n"
    "v_mfma_f32_16x16x32_bf16 v[28:31], a[92:95], a[156:159], v[28:31]\n"
    "ds_read_b128 a[220:223], v250 offset:7168\n"
    "v_max_f32 v14, v14, v63\n"
    "v_mfma_f32_16x16x32_bf16 v[32:35], a[64:67], a[160:163], 0\n"
    "v_mfma_f32_16x16x32_bf16 v[32:35], a[68:71], a[164:167], v[32:35]\n"
    "v_mfma_f32_16x16x32_bf16 v[32:35], a[72:75], a[168:171], v[32:35]\n"
    "v_mfma_f32_16x16x32_bf16 v[32:35], a[76:79], a[172:175], v[32:35]\n"
    "s_nop 3\n"
    "v_mfma_f32_16x16x32_bf16 v[36:39], a[80:83], a[160:163], 0\n"
    "v_max3_f32 v124, v16, v17, v18\n"
    "v_max3_f32 v124, v124, v19, v20\n"
    "v_mfma_f32_16x16x32_bf16 v[36:39], a[84:87], a[164:167], v[36:39]\n"
    "v_max3_f32 v124, v124, v21, v22\n"
    "v_max_f32 v124, v124, v23\n"
    "v_mfma_f32_16x16x32_bf16 v[36:39], a[88:91], a[168:171], v[36:39]\n"
    "v_max3_f32 v13, v24, v25, v26\n"
    "v_max3_f32 v13, v13, v27, v28\n"
    "v_mfma_f32_16x16x32_bf16 v[36:39], a[92:95], a[172:175], v[36:39]\n"
    "v_max3_f32 v13, v13, v29, v30\n"
    "v_max_f32 v13, v13, v31\n"
    "v_mfma_f32_16x16x32_bf16 v[40:43], a[64:67], a[176:179], 0\n"
    "s_nop 1\n"
    "v_permlane32_swap_b32 v124, v113\n"
    "v_permlane32_swap_b32 v13, v14\n"
    "v_mfma_f32_16x16x32_bf16 v[40:43], a[68:71], a[180:183], v[40:43]\n"
    "v_max_f32 v124, v124, v113\n"
    "v_max_f32 v13, v13, v14\n"
    "v_mfma_f32_16x16x32_bf16 v[40:43], a[72:75], a[184:187], v[40:43]\n"
    "s_nop 1\n"
    "v_permlane16_swap_b32 v124, v13\n"
    "v_mfma_f32_16x16x32_bf16 v[40:43], a[76:79], a[188:191], v[40:43]\n"
    "v_max3_f32 v124, v124, v13, v125\n"
    "ds_write_b32 v122, v124 offset:1024\n"
    "v_mfma_f32_16x16x32_bf16 v[44:47], a[80:83], a[176:179], 0\n"
    "v_mfma_f32_16x16x32_bf16 v[44:47], a[84:87], a[180:183], v[44:47]\n"
    "s_waitcnt vmcnt(5) lgkmcnt(0)\n"
    "s_barrier\n"
    "ds_read_b32 v113, v123 offset:1024\n"
    "v_mfma_f32_16x16x32_bf16 v[44:47], a[88:91], a[184:187], v[44:47]\n"
    "v_mfma_f32_16x16x32_bf16 v[44:47], a[92:95], a[188:191], v[44:47]\n"
    "ds_read_b128 a[96:99], v10 offset:16384\n"
    "ds_read_b128 a[100:103], v10 offset:17408\n"
    "ds_read_b128 a[104:107], v10 offset:18432\n"
    "ds_read_b128 a[108:111], v10 offset:19456\n"
    "ds_read_b128 a[112:115], v10 offset:20480\n"
    "ds_read_b128 a[116:119], v10 offset:21504\n"
    "ds_read_b128 a[120:123], v10 offset:22528\n"
    "ds_read_b128 a[124:127], v10 offset:23552\n"
    "s_add_u32 s73, s86, 0x18000\n"
    "s_and_b32 s73, s73, 0x1ffff\n"
    "s_add_u32 m0, s73, s88\n"
    "s_nop 0\n"
    "buffer_load_dwordx4 v12, s[40:43], s89 offen offset:0 sc1 lds\n"
    "buffer_load_dwordx4 v12, s[40:43], s89 offen offset:1024 sc1 lds\n"
    "buffer_load_dwordx4 v12, s[40:43], s89 offen offset:2048 sc1 lds\n"
    "buffer_load_dwordx4 v12, s[40:43], s89 offen offset:3072 sc1 lds\n"
    "s_sub_u32 s89, s89, 0x4000\n"
    "s_cselect_b32 s89, s87, s89\n"
    "s_cselect_b32 s42, s81, s42\n"
    "s_cselect_b32 s81, 0, s81\n"
    "s_waitcnt lgkmcnt(8)\n"
    "v_max_f32 v124, v124, v113\n"
    "v_mov_b32 v13, v124\n"
    "s_nop 1\n"
    "v_permlane16_swap_b32 v124, v13\n"
    "v_mov_b32 v113, v124\n"
    "v_mov_b32 v14, v13\n"
    "s_nop 0\n"
    "v_permlane32_swap_b32 v124, v113\n"
    "v_permlane32_swap_b32 v13, v14\n"
    "s_add_u32 s73, s86, 0x10000\n"
    "s_and_b32 s73, s73, 0x1ffff\n"
    "v_add_u32 v10, s73, v9\n"
    "v_sub_f32 v48, v48, v113\n"
    "ds_read_b128 a[64:67], v10 offset:0\n"
    "v_sub_f32 v49, v49, v113\n"
    "v_sub_f32 v50, v50, v113\n"
    "ds_read_b128 a[68:71], v10 offset:1024\n"
    "v_sub_f32 v51, v51, v113\n"
    "v_sub_f32 v52, v52, v113\n"
    "ds_read_b128 a[72:75], v10 offset:2048\n"
    "v_sub_f32 v53, v53, v113\n"
    "v_sub_f32 v54, v54, v113\n"
    "ds_read_b128 a[76:79], v10 offset:3072\n"
    "v_sub_f32 v55, v55, v113\n"
    "v_exp_f32 v48, v48\n"
    "ds_read_b128 a[80:83], v10 offset:4096\n"
    "v_exp_f32 v49, v49\n"
    "v_exp_f32 v50, v50\n"
    "ds_read_b128 a[84:87], v10 offset:5120\n"
    "v_exp_f32 v51, v51\n"
    "v_exp_f32 v52, v52\n"
    "ds_read_b128 a[88:91], v10 offset:6144\n"
    "v_exp_f32 v53, v53\n"
    "v_exp_f32 v54, v54\n"
    "ds_read_b128 a[92:95], v10 offset:7168\n"
    "v_exp_f32 v55, v55\n"
    "v_cvt_pk_bf16_f32 v104, v48, v49\n"
    "v_cvt_pk_bf16_f32 v105, v50, v51\n"
    "v_cvt_pk_bf16_f32 v106, v52, v53\n"
    "v_cvt_pk_bf16_f32 v107, v54, v55\n"
    "s_nop 1\n"
    ".Lat_prob0_%=:\n"
    "s_waitcnt lgkmcnt(8)\n"
    "v_mfma_f32_16x16x32_bf16 v[192:195], a[96:99], v[104:107], 0\n"
    "s_add_u32 s73, s86, 0x10000\n"
    "s_and_b32 s73, s73, 0x1ffff\n"
    "s_add_u32 s73, s73, 0x4000\n"
    "s_add_u32 m0, s73, s88\n"
    "s_nop 0\n"
    "v_sub_f32 v56, v56, v14\n"
    "v_sub_f32 v57, v57, v14\n"
    "v_sub_f32 v58, v58, v14\n"
    "v_mfma_f32_16x16x32_bf16 v[196:199], a[100:103], v[104:107], 0\n"
    "buffer_load_dwordx4 v12, s[44:47], s90 offen offset:0 sc1 lds\n"
    "v_sub_f32 v59, v59, v14\n"
    "v_sub_f32 v60, v60, v14\n"
    "v_sub_f32 v61, v61, v14\n"
    "v_mfma_f32_16x16x32_bf16 v[200:203], a[104:107], v[104:107], 0\n"
    "v_sub_f32 v62, v62, v14\n"
    "v_sub_f32 v63, v63, v14\n"
    "v_exp_f32 v56, v56\n"
    "v_mfma_f32_16x16x32_bf16 v[204:207], a[108:111], v[104:107], 0\n"
    "buffer_load_dwordx4 v12, s[44:47], s90 offen offset:1024 sc1 lds\n"
    "v_exp_f32 v57, v57\n"
    "v_exp_f32 v58, v58\n"
    "v_exp_f32 v59, v59\n"
    "v_mfma_f32_16x16x32_bf16 v[208:211], a[112:115], v[104:107], 0\n"
    "buffer_load_dwordx4 v12, s[44:47], s90 offen offset:2048 sc1 lds\n"
    "v_exp_f32 v60, v60\n"
    "v_exp_f32 v61, v61\n"
    "v_exp_f32 v62, v62\n"
    "v_mfma_f32_16x16x32_bf16 v[212:215], a[116:119], v[104:107], 0\n"
    "v_exp_f32 v63, v63\n"
    "v_cvt_pk_bf16_f32 v108, v56, v57\n"
    "v_cvt_pk_bf16_f32 v109, v58, v59\n"
    "v_mfma_f32_16x16x32_bf16 v[216:219], a[120:123], v[104:107], 0\n"
    "buffer_load_dwordx4 v12, s[44:47], s90 offen offset:3072 sc1 lds\n"
    "v_cvt_pk_bf16_f32 v110, v60, v61\n"
    "v_cvt_pk_bf16_f32 v111, v62, v63\n"
    "v_mfma_f32_16x16x32_bf16 v[220:223], a[124:127], v[104:107], 0\n"
    "s_sub_u32 s90, s90, 0x4000\n"
    "s_cselect_b32 s90, s87, s90\n"
    "s_cselect_b32 s46, s82, s46\n"
    "s_cselect_b32 s82, 0, s82\n"
    "v_sub_f32 v16, v16, v124\n"
    "v_sub_f32 v17, v17, v124\n"
    "v_sub_f32 v18, v18, v124\n"
    "v_mfma_f32_16x16x32_bf16 v[88:91], a[32:35], v[104:107], 0\n"
    "v_sub_f32 v19, v19, v124\n"
    "v_sub_f32 v20, v20, v124\n"
    "v_mfma_f32_16x16x32_bf16 v[224:227], a[96:99], v[108:111], 0\n"
    "ds_read_b128 a[96:99], v11 offset:16384\n"
    "v_sub_f32 v21, v21, v124\n"
    "v_sub_f32 v22, v22, v124\n"
    "v_mfma_f32_16x16x32_bf16 v[228:231], a[100:103], v[108:111], 0\n"
    "ds_read_b128 a[100:103], v11 offset:17408\n"
    "v_sub_f32 v23, v23, v124\n"
    "v_exp_f32 v16, v16\n"
    "v_mfma_f32_16x16x32_bf16 v[232:235], a[104:107], v[108:111], 0\n"
    "ds_read_b128 a[104:107], v11 offset:18432\n"
    "v_exp_f32 v17, v17\n"
    "v_exp_f32 v18, v18\n"
    "v_exp_f32 v19, v19\n"
    "v_mfma_f32_16x16x32_bf16 v[236:239], a[108:111], v[108:111], 0\n"
    "ds_read_b128 a[108:111], v11 offset:19456\n"
    "v_exp_f32 v20, v20\n"
    "v_exp_f32 v21, v21\n"
    "v_mfma_f32_16x16x32_bf16 v[240:243], a[112:115], v[108:111], 0\n"
    "ds_read_b128 a[112:115], v11 offset:20480\n"
    "v_exp_f32 v22, v22\n"
    "v_exp_f32 v23, v23\n"
    "v_mfma_f32_16x16x32_bf16 v[244:247], a[116:119], v[108:111], 0\n"
    "ds_read_b128 a[116:119], v11 offset:21504\n"
    "v_cvt_pk_bf16_f32 v96, v16, v17\n"
    "v_cvt_pk_bf16_f32 v97, v18, v19\n"
    "v_mfma_f32_16x16x32_bf16 v[248:251], a[120:123], v[108:111], 0\n"
    "ds_read_b128 a[120:123], v11 offset:22528\n"
    "v_cvt_pk_bf16_f32 v98, v20, v21\n"
    "v_cvt_pk_bf16_f32 v99, v22, v23\n"
    "v_mfma_f32_16x16x32_bf16 v[252:255], a[124:127], v[108:111], 0\n"
    "ds_read_b128 a[124:127], v11 offset:23552\n"
    "v_sub_f32 v24, v24, v13\n"
    "v_sub_f32 v25, v25, v13\n"
    "v_sub_f32 v26, v26, v13\n"
    "v_mfma_f32_16x16x32_bf16 v[92:95], a[32:35], v[108:111], 0\n"
    "v_sub_f32 v27, v27, v13\n"
    "v_sub_f32 v28, v28, v13\n"
    "s_waitcnt lgkmcnt(7)\n"
    "v_mfma_f32_16x16x32_bf16 v[128:131], a[96:99], v[96:99], 0\n"
    "s_add_u32 m0, s86, s88\n"
    "s_nop 0\n"
    "v_sub_f32 v29, v29, v13\n"
    "v_sub_f32 v30, v30, v13\n"
    "s_waitcnt lgkmcnt(6)\n"
    "v_mfma_f32_16x16x32_bf16 v[132:135], a[100:103], v[96:99], 0\n"
    "buffer_load_dwordx4 v12, s[40:43], s89 offen offset:0 sc1 lds\n"
    "v_sub_f32 v31, v31, v13\n"
    "v_exp_f32 v24, v24\n"
    "s_waitcnt lgkmcnt(5)\n"
    "v_mfma_f32_16x16x32_bf16 v[136:139], a[104:107], v[96:99], 0\n"
    "buffer_load_dwordx4 v12, s[40:43], s89 offen offset:1024 sc1 lds\n"
    "v_exp_f32 v25, v25\n"
    "v_exp_f32 v26, v26\n"
    "v_exp_f32 v27, v27\n"
    "s_waitcnt lgkmcnt(4)\n"
    "v_mfma_f32_16x16x32_bf16 v[140:143], a[108:111], v[96:99], 0\n"
    "buffer_load_dwordx4 v12, s[40:43], s89 offen offset:2048 sc1 lds\n"
    "v_exp_f32 v28, v28\n"
    "v_exp_f32 v29, v29\n"
    "s_waitcnt lgkmcnt(3)\n"
    "v_mfma_f32_16x16x32_bf16 v[144:147], a[112:115], v[96:99], 0\n"
    "buffer_load_dwordx4 v12, s[40:43], s89 offen offset:3072 sc1 lds\n"
    "v_exp_f32 v30, v30\n"
    "v_exp_f32 v31, v31\n"
    "s_waitcnt lgkmcnt(2)\n"
    "v_mfma_f32_16x16x32_bf16 v[148:151], a[116:119], v[96:99], 0\n"
    "s_sub_u32 s89, s89, 0x4000\n"
    "s_cselect_b32 s89, s87, s89\n"
    "s_cselect_b32 s42, s81, s42\n"
    "s_cselect_b32 s81, 0, s81\n"
    "v_cvt_pk_bf16_f32 v100, v24, v25\n"
    "v_cvt_pk_bf16_f32 v101, v26, v27\n"
    "s_waitcnt lgkmcnt(1)\n"
    "v_mfma_f32_16x16x32_bf16 v[152:155], a[120:123], v[96:99], 0\n"
    "s_add_u32 s73, s86, 0x18000\n"
    "s_and_b32 s73, s73, 0x1ffff\n"
    "s_add_u32 s73, s73, 0x4000\n"
    "s_add_u32 m0, s73, s88\n"
    "s_nop 0\n"
    "v_cvt_pk_bf16_f32 v102, v28, v29\n"
    "v_cvt_pk_bf16_f32 v103, v30, v31\n"
    "s_waitcnt lgkmcnt(0)\n"
    "v_mfma_f32_16x16x32_bf16 v[156:159], a[124:127], v[96:99], 0\n"
    "buffer_load_dwordx4 v12, s[44:47], s90 offen offset:0 sc1 lds\n"
    "v_sub_f32 v64, 0, v124\n"
    "v_sub_f32 v65, 0, v124\n"
    "v_sub_f32 v66, 0, v124\n"
    "v_sub_f32 v67, 0, v124\n"
    "v_sub_f32 v68, 0, v13\n"
    "v_mfma_f32_16x16x32_bf16 v[80:83], a[32:35], v[96:99], 0\n"
    "buffer_load_dwordx4 v12, s[44:47], s90 offen offset:1024 sc1 lds\n"
    "v_sub_f32 v69, 0, v13\n"
    "v_sub_f32 v70, 0, v13\n"
    "v_sub_f32 v71, 0, v13\n"
    "v_sub_f32 v72, 0, v113\n"
    "v_mfma_f32_16x16x32_bf16 v[160:163], a[96:99], v[100:103], 0\n"
    "buffer_load_dwordx4 v12, s[44:47], s90 offen offset:2048 sc1 lds\n"
    "v_sub_f32 v73, 0, v113\n"
    "v_sub_f32 v74, 0, v113\n"
    "v_sub_f32 v75, 0, v113\n"
    "v_sub_f32 v76, 0, v14\n"
    "v_sub_f32 v77, 0, v14\n"
    "v_mfma_f32_16x16x32_bf16 v[164:167], a[100:103], v[100:103], 0\n"
    "buffer_load_dwordx4 v12, s[44:47], s90 offen offset:3072 sc1 lds\n"
    "v_sub_f32 v78, 0, v14\n"
    "v_sub_f32 v79, 0, v14\n"
    "v_sub_f32 v32, v32, v113\n"
    "v_sub_f32 v33, v33, v113\n"
    "v_mfma_f32_16x16x32_bf16 v[168:171], a[104:107], v[100:103], 0\n"
    "s_sub_u32 s90, s90, 0x4000\n"
    "s_cselect_b32 s90, s87, s90\n"
    "s_cselect_b32 s46, s82, s46\n"
    "s_cselect_b32 s82, 0, s82\n"
    "v_sub_f32 v34, v34, v113\n"
    "v_sub_f32 v35, v35, v113\n"
    "v_sub_f32 v36, v36, v113\n"
    "v_sub_f32 v37, v37, v113\n"
    "v_sub_f32 v38, v38, v113\n"
    "v_mfma_f32_16x16x32_bf16 v[172:175], a[108:111], v[100:103], 0\n"
    "s_add_u32 s73, s86, 0x8000\n"
    "s_and_b32 s73, s73, 0x1ffff\n"
    "s_add_u32 m0, s73, s88\n"
    "s_nop 0\n"
    "v_sub_f32 v39, v39, v113\n"
    "v_sub_f32 v40, v40, v14\n"
    "v_sub_f32 v41, v41, v14\n"
    "v_sub_f32 v42, v42, v14\n"
    "v_mfma_f32_16x16x32_bf16 v[176:179], a[112:115], v[100:103], 0\n"
    "buffer_load_dwordx4 v12, s[40:43], s89 offen offset:0 sc1 lds\n"
    "v_sub_f32 v43, v43, v14\n"
    "v_sub_f32 v44, v44, v14\n"
    "v_sub_f32 v45, v45, v14\n"
    "v_sub_f32 v46, v46, v14\n"
    "v_sub_f32 v47, v47, v14\n"
    "v_mfma_f32_16x16x32_bf16 v[180:183], a[116:119], v[100:103], 0\n"
    "buffer_load_dwordx4 v12, s[40:43], s89 offen offset:1024 sc1 lds\n"
    "v_exp_f32 v32, v32\n"
    "v_exp_f32 v33, v33\n"
    "v_exp_f32 v34, v34\n"
    "v_exp_f32 v35, v35\n"
    "v_mfma_f32_16x16x32_bf16 v[184:187], a[120:123], v[100:103], 0\n"
    "buffer_load_dwordx4 v12, s[40:43], s89 offen offset:2048 sc1 lds\n"
    "v_exp_f32 v36, v36\n"
    "v_exp_f32 v37, v37\n"
    "v_exp_f32 v38, v38\n"
    "v_exp_f32 v39, v39\n"
    "v_exp_f32 v40, v40\n"
    "v_mfma_f32_16x16x32_bf16 v[188:191], a[124:127], v[100:103], 0\n"
    "buffer_load_dwordx4 v12, s[40:43], s89 offen offset:3072 sc1 lds\n"
    "v_exp_f32 v41, v41\n"
    "v_exp_f32 v42, v42\n"
    "v_exp_f32 v43, v43\n"
    "v_cvt_pk_bf16_f32 v104, v32, v33\n"
    "v_mfma_f32_16x16x32_bf16 v[84:87], a[32:35], v[100:103], 0\n"
    "v_cvt_pk_bf16_f32 v105, v34, v35\n"
    "v_cvt_pk_bf16_f32 v106, v36, v37\n"
    "v_cvt_pk_bf16_f32 v107, v38, v39\n"
    "v_cvt_pk_bf16_f32 v108, v40, v41\n"
    "s_waitcnt vmcnt(16)\n"
    "v_mov_b32 v112, s85\n"
    "v_mov_b32 v116, s64\n"
    "v_mov_b32 v117, s65\n"
    "s_mov_b64 exec, 1\n"
    "buffer_store_dwordx2 v[116:117], v112, s[56:59], 0 offen sc1\n"
    "s_mov_b64 exec, -1\n"
    "s_xor_b32 s86, s86, 0x10000\n"
    "s_lshl_b32 s91, s38, 1\n"
    "s_sub_u32 s91, s91, 1\n"
    "s_cbranch_scc1 .Lat_drain0_%=\n"
    "s_waitcnt lgkmcnt(0)\n"
    ".p2align 6\n"
    ".Lat_loop0_%=:\n"
    "v_mfma_f32_16x16x32_bf16 v[16:19], a[64:67], a[128:131], v[64:67]\n"
    "v_cvt_pk_bf16_f32 v109, v42, v43\n"
    "s_xor_b32 s73, s86, 0x14000\n"
    "v_mfma_f32_16x16x32_bf16 v[16:19], a[68:71], a[132:135], v[16:19]\n"
    "s_add_u32 m0, s73, s88\n"
    "v_exp_f32 v44, v44\n"
    "v_mfma_f32_16x16x32_bf16 v[16:19], a[72:75], a[136:139], v[16:19]\n"
    "buffer_load_dwordx4 v12, s[44:47], s90 offen offset:0 sc1 lds\n"
    "v_mfma_f32_16x16x32_bf16 v[16:19], a[76:79], a[140:143], v[16:19]\n"
    "s_sub_u32 s89, s89, 0x4000\n"
    "v_exp_f32 v45, v45\n"
    "v_mfma_f32_16x16x32_bf16 v[20:23], a[80:83], a[128:131], v[64:67]\n"
    "v_cvt_pk_bf16_f32 v110, v44, v45\n"
    "v_mfma_f32_16x16x32_bf16 v[20:23], a[84:87], a[132:135], v[20:23]\n"
    "s_cselect_b32 s89, s87, s89\n"
    "v_exp_f32 v46, v46\n"
    "v_mfma_f32_16x16x32_bf16 v[20:23], a[88:91], a[136:139], v[20:23]\n"
    "buffer_load_dwordx4 v12, s[44:47], s90 offen offset:1024 sc1 lds\n"
    "v_mfma_f32_16x16x32_bf16 v[20:23], a[92:95], a[140:143], v[20:23]\n"
    "s_cselect_b32 s42, s81, s42\n"
    "v_exp_f32 v47, v47\n"
    "v_mfma_f32_16x16x32_bf16 v[24:27], a[64:67], a[144:147], v[68:71]\n"
    "v_cvt_pk_bf16_f32 v111, v46, v47\n"
    "v_mfma_f32_16x16x32_bf16 v[24:27], a[68:71], a[148:151], v[24:27]\n"
    "s_cselect_b32 s81, 0, s81\n"
    "v_exp_f32 v16, v16\n"
    "v_mfma_f32_16x16x32_bf16 v[24:27], a[72:75], a[152:155], v[24:27]\n"
    "buffer_load_dwordx4 v12, s[44:47], s90 offen offset:2048 sc1 lds\n"
    "v_mfma_f32_16x16x32_bf16 v[24:27], a[76:79], a[156:159], v[24:27]\n"
    "s_add_u32 s93, s86, 0x8000\n"
    "v_exp_f32 v17, v17\n"
    "v_mfma_f32_16x16x32_bf16 v[28:31], a[80:83], a[144:147], v[68:71]\n"
    "v_cvt_pk_bf16_f32 v96, v16, v17\n"
    "v_mfma_f32_16x16x32_bf16 v[28:31], a[84:87], a[148:151], v[28:31]\n"
    "s_and_b32 s93, s93, 0x1ffff\n"
    "v_exp_f32 v18, v18\n"
    "v_mfma_f32_16x16x32_bf16 v[28:31], a[88:91], a[152:155], v[28:31]\n"
    "buffer_load_dwordx4 v12, s[44:47], s90 offen offset:3072 sc1 lds\n"
    "v_mfma_f32_16x16x32_bf16 v[28:31], a[92:95], a[156:159], v[28:31]\n"
    "s_sub_u32 s90, s90, 0x4000\n"
    "v_exp_f32 v19, v19\n"
    "v_mfma_f32_16x16x32_bf16 v[192:195], a[96:99], v[104:107], v[192:195]\n"
    "v_cvt_pk_bf16_f32 v97, v18, v19\n"
    "s_cselect_b32 s90, s87, s90\n"
    "v_mfma_f32_16x16x32_bf16 v[196:199], a[100:103], v[104:107], v[196:199]\n"
    "s_cselect_b32 s46, s82, s46\n"
    "v_exp_f32 v20, v20\n"
    "v_mfma_f32_16x16x32_bf16 v[200:203], a[104:107], v[104:107], v[200:203]\n"
    "s_cselect_b32 s82, 0, s82\n"
    "v_mfma_f32_16x16x32_bf16 v[204:207], a[108:111], v[104:107], v[204:207]\n"
    "v_exp_f32 v21, v21\n"
    "v_mfma_f32_16x16x32_bf16 v[208:211], a[112:115], v[104:107], v[208:211]\n"
    "v_cvt_pk_bf16_f32 v98, v20, v21\n"
    "v_mfma_f32_16x16x32_bf16 v[212:215], a[116:119], v[104:107], v[212:215]\n"
    "s_sub_u32 s95, s95, 1\n"
    "v_exp_f32 v22, v22\n"
    "v_mfma_f32_16x16x32_bf16 v[216:219], a[120:123], v[104:107], v[216:219]\n"
    "s_cbranch_scc1 .Lat_side0_%=\n"
    ".Lat_back0_%=:\n"
    "v_mfma_f32_16x16x32_bf16 v[220:223], a[124:127], v[104:107], v[220:223]\n"
    "v_exp_f32 v23, v23\n"
    "v_mfma_f32_16x16x32_bf16 v[88:91], a[32:35], v[104:107], v[88:91]\n"
    "v_cvt_pk_bf16_f32 v99, v22, v23\n"
    "v_mfma_f32_16x16x32_bf16 v[224:227], a[96:99], v[108:111], v[224:227]\n"
    "s_waitcnt vmcnt(16)\n"
    "s_barrier\n"
    "v_mfma_f32_16x16x32_bf16 v[228:231], a[100:103], v[108:111], v[228:231]\n"
    "ds_read_b128 a[96:99], v10 offset:16384\n"
    "v_mfma_f32_16x16x32_bf16 v[232:235], a[104:107], v[108:111], v[232:235]\n"
    "v_exp_f32 v24, v24\n"
    "v_mfma_f32_16x16x32_bf16 v[236:239], a[108:111], v[108:111], v[236:239]\n"
    "ds_read_b128 a[100:103], v10 offset:17408\n"
    "v_mfma_f32_16x16x32_bf16 v[240:243], a[112:115], v[108:111], v[240:243]\n"
    "v_exp_f32 v25, v25\n"
    "v_mfma_f32_16x16x32_bf16 v[244:247], a[116:119], v[108:111], v[244:247]\n"
    "ds_read_b128 a[104:107], v10 offset:18432\n"
    "v_cvt_pk_bf16_f32 v100, v24, v25\n"
    "v_mfma_f32_16x16x32_bf16 v[248:251], a[120:123], v[108:111], v[248:251]\n"
    "v_exp_f32 v26, v26\n"
    "v_mfma_f32_16x16x32_bf16 v[252:255], a[124:127], v[108:111], v[252:255]\n"
    "ds_read_b128 a[108:111], v10 offset:19456\n"
    "v_mfma_f32_16x16x32_bf16 v[92:95], a[32:35], v[108:111], v[92:95]\n"
    "v_exp_f32 v27, v27\n"
    "v_mfma_f32_16x16x32_bf16 v[32:35], a[64:67], a[160:163], v[72:75]\n"
    "ds_read_b128 a[112:115], v10 offset:20480\n"
    "v_cvt_pk_bf16_f32 v101, v26, v27\n"
    "v_mfma_f32_16x16x32_bf16 v[32:35], a[68:71], a[164:167], v[32:35]\n"
    "v_exp_f32 v28, v28\n"
    "v_mfma_f32_16x16x32_bf16 v[32:35], a[72:75], a[168:171], v[32:35]\n"
    "ds_read_b128 a[116:119], v10 offset:21504\n"
    "v_mfma_f32_16x16x32_bf16 v[32:35], a[76:79], a[172:175], v[32:35]\n"
    "v_exp_f32 v29, v29\n"
    "v_mfma_f32_16x16x32_bf16 v[36:39], a[80:83], a[160:163], v[72:75]\n"
    "ds_read_b128 a[120:123], v10 offset:22528\n"
    "v_cvt_pk_bf16_f32 v102, v28, v29\n"
    "v_mfma_f32_16x16x32_bf16 v[36:39], a[84:87], a[164:167], v[36:39]\n"
    "v_exp_f32 v30, v30\n"
    "v_mfma_f32_16x16x32_bf16 v[36:39], a[88:91], a[168:171], v[36:39]\n"
    "ds_read_b128 a[124:127], v10 offset:23552\n"
    "v_mfma_f32_16x16x32_bf16 v[36:39], a[92:95], a[172:175], v[36:39]\n"
    "v_exp_f32 v31, v31\n"
    "v_mfma_f32_16x16x32_bf16 v[40:43], a[64:67], a[176:179], v[76:79]\n"
    "v_cvt_pk_bf16_f32 v103, v30, v31\n"
    "v_add_u32 v10, s93, v9\n"
    "v_mfma_f32_16x16x32_bf16 v[40:43], a[68:71], a[180:183], v[40:43]\n"
    "v_exp_f32 v32, v32\n"
    "v_mfma_f32_16x16x32_bf16 v[40:43], a[72:75], a[184:187], v[40:43]\n"
    "ds_read_b128 a[64:67], v10 offset:0\n"
    "v_mfma_f32_16x16x32_bf16 v[40:43], a[76:79], a[188:191], v[40:43]\n"
    "v_exp_f32 v33, v33\n"
    "v_mfma_f32_16x16x32_bf16 v[44:47], a[80:83], a[176:179], v[76:79]\n"
    "ds_read_b128 a[68:71], v10 offset:1024\n"
    "v_cvt_pk_bf16_f32 v104, v32, v33\n"
    "v_mfma_f32_16x16x32_bf16 v[44:47], a[84:87], a[180:183], v[44:47]\n"
    "v_exp_f32 v34, v34\n"
    "v_mfma_f32_16x16x32_bf16 v[44:47], a[88:91], a[184:187], v[44:47]\n"
    "ds_read_b128 a[72:75], v10 offset:2048\n"
    "v_mfma_f32_16x16x32_bf16 v[44:47], a[92:95], a[188:191], v[44:47]\n"
    "s_waitcnt lgkmcnt(3)\n"
    "v_exp_f32 v35, v35\n"
    "v_mfma_f32_16x16x32_bf16 v[128:131], a[96:99], v[96:99], v[128:131]\n"
    "ds_read_b128 a[76:79], v10 offset:3072\n"
    "v_cvt_pk_bf16_f32 v105, v34, v35\n"
    "v_mfma_f32_16x16x32_bf16 v[132:135], a[100:103], v[96:99], v[132:135]\n"
    "v_exp_f32 v36, v36\n"
    "v_mfma_f32_16x16x32_bf16 v[136:139], a[104:107], v[96:99], v[136:139]\n"
    "ds_read_b128 a[80:83], v10 offset:4096\n"
    "v_mfma_f32_16x16x32_bf16 v[140:143], a[108:111], v[96:99], v[140:143]\n"
    "v_exp_f32 v37, v37\n"
    "v_mfma_f32_16x16x32_bf16 v[144:147], a[112:115], v[96:99], v[144:147]\n"
    "ds_read_b128 a[84:87], v10 offset:5120\n"
    "v_cvt_pk_bf16_f32 v106, v36, v37\n"
    "v_mfma_f32_16x16x32_bf16 v[148:151], a[116:119], v[96:99], v[148:151]\n"
    "v_exp_f32 v38, v38\n"
    "v_mfma_f32_16x16x32_bf16 v[152:155], a[120:123], v[96:99], v[152:155]\n"
    "ds_read_b128 a[88:91], v10 offset:6144\n"
    "v_mfma_f32_16x16x32_bf16 v[156:159], a[124:127], v[96:99], v[156:159]\n"
    "s_add_u32 m0, s86, s88\n"
    "v_exp_f32 v39, v39\n"
    "v_mfma_f32_16x16x32_bf16 v[80:83], a[32:35], v[96:99], v[80:83]\n"
    "ds_read_b128 a[92:95], v10 offset:7168\n"
    "v_cvt_pk_bf16_f32 v107, v38, v39\n"
    "v_mfma_f32_16x16x32_bf16 v[160:163], a[96:99], v[100:103], v[160:163]\n"
    "v_mfma_f32_16x16x32_bf16 v[164:167], a[100:103], v[100:103], v[164:167]\n"
    "buffer_load_dwordx4 v12, s[40:43], s89 offen offset:0 sc1 lds\n"
    "v_mfma_f32_16x16x32_bf16 v[168:171], a[104:107], v[100:103], v[168:171]\n"
    "s_mov_b32 s86, s93\n"
    "v_exp_f32 v40, v40\n"
    "v_mfma_f32_16x16x32_bf16 v[172:175], a[108:111], v[100:103], v[172:175]\n"
    "buffer_load_dwordx4 v12, s[40:43], s89 offen offset:1024 sc1 lds\n"
    "v_mfma_f32_16x16x32_bf16 v[176:179], a[112:115], v[100:103], v[176:179]\n"
    "v_exp_f32 v41, v41\n"
    "v_mfma_f32_16x16x32_bf16 v[180:183], a[116:119], v[100:103], v[180:183]\n"
    "buffer_load_dwordx4 v12, s[40:43], s89 offen offset:2048 sc1 lds\n"
    "v_cvt_pk_bf16_f32 v108, v40, v41\n"
    "v_mfma_f32_16x16x32_bf16 v[184:187], a[120:123], v[100:103], v[184:187]\n"
    "s_sub_u32 s91, s91, 1\n"
    "v_exp_f32 v42, v42\n"
    "v_mfma_f32_16x16x32_bf16 v[188:191], a[124:127], v[100:103], v[188:191]\n"
    "buffer_load_dwordx4 v12, s[40:43], s89 offen offset:3072 sc1 lds\n"
    "v_mfma_f32_16x16x32_bf16 v[84:87], a[32:35], v[100:103], v[84:87]\n"
    "s_waitcnt lgkmcnt(0)\n"
    "v_exp_f32 v43, v43\n"
    "s_cbranch_scc0 .Lat_loop0_%=\n"
    ".Lat_drain0_%=:\n"
    "s_sub_u32 s89, s89, 0x4000\n"
    "s_cselect_b32 s89, s87, s89\n"
    "s_cselect_b32 s42, s81, s42\n"
    "s_cselect_b32 s81, 0, s81\n"
    "s_lshl_b32 s73, s68, 15\n"
    "s_add_u32 s73, s73, 0x4000\n"
    "s_cmp_eq_u32 s68, 1\n"
    "s_cselect_b32 s73, 0, s73\n"
    "s_xor_b32 s73, s73, s86\n"
    "s_cmp_eq_u32 s68, 0\n"
    "s_cselect_b32 s73, 0x20000, s73\n"
    "s_add_u32 s73, s73, s69\n"
    "v_add_u32 v114, s73, v8\n"
    "s_waitcnt vmcnt(12) lgkmcnt(0)\n"
    "s_barrier\n"
    "s_cmp_lg_u32 s71, 0\n"
    "s_cbranch_scc1 .Lat_epi0_%=\n"
    "v_mfma_f32_16x16x32_bf16 v[192:195], a[96:99], v[104:107], v[192:195]\n"
    "v_exp_f32 v44, v44\n"
    "v_exp_f32 v45, v45\n"
    "v_mfma_f32_16x16x32_bf16 v[196:199], a[100:103], v[104:107], v[196:199]\n"
    "v_exp_f32 v46, v46\n"
    "v_mfma_f32_16x16x32_bf16 v[200:203], a[104:107], v[104:107], v[200:203]\n"
    "v_exp_f32 v47, v47\n"
    "v_mfma_f32_16x16x32_bf16 v[204:207], a[108:111], v[104:107], v[204:207]\n"
    "v_cvt_pk_bf16_f32 v109, v42, v43\n"
    "v_mfma_f32_16x16x32_bf16 v[208:211], a[112:115], v[104:107], v[208:211]\n"
    "v_cvt_pk_bf16_f32 v110, v44, v45\n"
    "v_mfma_f32_16x16x32_bf16 v[212:215], a[116:119], v[104:107], v[212:215]\n"
    "v_cvt_pk_bf16_f32 v111, v46, v47\n"
    "v_mfma_f32_16x16x32_bf16 v[216:219], a[120:123], v[104:107], v[216:219]\n"
    "s_xor_b32 s93, s68, 2\n"
    "s_lshl_b32 s73, s93, 15\n"
    "s_add_u32 s73, s73, 0x4000\n"
    "s_cmp_eq_u32 s93, 1\n"
    "s_cselect_b32 s73, 0, s73\n"
    "v_mfma_f32_16x16x32_bf16 v[220:223], a[124:127], v[104:107], v[220:223]\n"
    "s_xor_b32 s73, s73, s86\n"
    "s_cmp_eq_u32 s93, 0\n"
    "s_cselect_b32 s73, 0x20000, s73\n"
    "s_add_u32 s73, s73, s69\n"
    "v_mfma_f32_16x16x32_bf16 v[88:91], a[32:35], v[104:107], v[88:91]\n"
    "v_add_u32 v115, s73, v8\n"
    "s_sub_u32 s73, 3, s72\n"
    "s_cmp_eq_u32 s71, 0\n"
    "s_cselect_b32 s73, s72, s73\n"
    "v_mfma_f32_16x16x32_bf16 v[224:227], a[96:99], v[108:111], v[224:227]\n"
    "s_lshl_b32 s74, s38, 2\n"
    "s_add_u32 s73, s73, s74\n"
    "s_lshl_b32 s73, s73, 18\n"
    "s_lshl_b32 s74, s66, 13\n"
    "v_mfma_f32_16x16x32_bf16 v[228:231], a[100:103], v[108:111], v[228:231]\n"
    "s_add_u32 s92, s73, s74\n"
    "v_accvgpr_read_b32 v48, a0\n"
    "v_accvgpr_read_b32 v49, a1\n"
    "v_accvgpr_read_b32 v50, a2\n"
    "v_mfma_f32_16x16x32_bf16 v[232:235], a[104:107], v[108:111], v[232:235]\n"
    "v_accvgpr_read_b32 v51, a3\n"
    "v_accvgpr_read_b32 v52, a4\n"
    "v_accvgpr_read_b32 v53, a5\n"
    "v_accvgpr_read_b32 v54, a6\n"
    "v_mfma_f32_16x16x32_bf16 v[236:239], a[108:111], v[108:111], v[236:239]\n"
    "v_accvgpr_read_b32 v55, a7\n"
    "v_accvgpr_read_b32 v56, a8\n"
    "v_accvgpr_read_b32 v57, a9\n"
    "v_accvgpr_read_b32 v58, a10\n"
    "v_accvgpr_read_b32 v59, a11\n"
    "v_mfma_f32_16x16x32_bf16 v[240:243], a[112:115], v[108:111], v[240:243]\n"
    "v_accvgpr_read_b32 v60, a12\n"
    "v_accvgpr_read_b32 v61, a13\n"
    "v_accvgpr_read_b32 v62, a14\n"
    "v_accvgpr_read_b32 v63, a15\n"
    "v_mfma_f32_16x16x32_bf16 v[244:247], a[116:119], v[108:111], v[244:247]\n"
    "v_accvgpr_read_b32 v16, a16\n"
    "v_accvgpr_read_b32 v17, a17\n"
    "v_accvgpr_read_b32 v18, a18\n"
    "v_accvgpr_read_b32 v19, a19\n"
    "v_mfma_f32_16x16x32_bf16 v[248:251], a[120:123], v[108:111], v[248:251]\n"
    "v_accvgpr_read_b32 v20, a20\n"
    "v_accvgpr_read_b32 v21, a21\n"
    "v_accvgpr_read_b32 v22, a22\n"
    "v_accvgpr_read_b32 v23, a23\n"
    "v_mfma_f32_16x16x32_bf16 v[252:255], a[124:127], v[108:111], v[252:255]\n"
    "v_accvgpr_read_b32 v24, a24\n"
    "v_accvgpr_read_b32 v25, a25\n"
    "v_accvgpr_read_b32 v26, a26\n"
    "v_accvgpr_read_b32 v27, a27\n"
    "v_mfma_f32_16x16x32_bf16 v[92:95], a[32:35], v[108:111], v[92:95]\n"
    "v_accvgpr_read_b32 v28, a28\n"
    "v_accvgpr_read_b32 v29, a29\n"
    "v_accvgpr_read_b32 v30, a30\n"
    "v_accvgpr_read_b32 v31, a31\n"
    "v_add_u32 v10, s86, v9\n"
    "s_add_u32 s73, s86, 0x8000\n"
    "s_and_b32 s73, s73, 0x1ffff\n"
    "v_add_u32 v11, s73, v9\n"
    "v_mfma_f32_16x16x32_bf16 v[48:51], a[64:67], a[224:227], v[48:51]\n"
    "v_mov_b32 v116, -1\n"
    "v_mfma_f32_16x16x32_bf16 v[48:51], a[68:71], a[228:231], v[48:51]\n"
    "v_mov_b32 v117, -1\n"
    "v_mfma_f32_16x16x32_bf16 v[48:51], a[72:75], a[232:235], v[48:51]\n"
    "v_mov_b32 v118, -1\n"
    "v_mfma_f32_16x16x32_bf16 v[48:51], a[76:79], a[236:239], v[48:51]\n"
    "v_mov_b32 v119, -1\n"
    "v_mfma_f32_16x16x32_bf16 v[56:59], a[64:67], a[240:243], v[56:59]\n"
    "ds_read_b128 a[64:67], v11 offset:0\n"
    "s_lshl_b32 s75, s78, 2\n"
    "v_mfma_f32_16x16x32_bf16 v[56:59], a[68:71], a[244:247], v[56:59]\n"
    "ds_read_b128 a[68:71], v11 offset:1024\n"
    "s_add_u32 s75, s75, s79\n"
    "v_mfma_f32_16x16x32_bf16 v[56:59], a[72:75], a[248:251], v[56:59]\n"
    "ds_read_b128 a[72:75], v11 offset:2048\n"
    "s_lshl_b32 s75, s75, 18\n"
    "v_mfma_f32_16x16x32_bf16 v[56:59], a[76:79], a[252:255], v[56:59]\n"
    "ds_read_b128 a[76:79], v11 offset:3072\n"
    "s_lshl_b32 s73, s68, 11\n"
    "v_mfma_f32_16x16x32_bf16 v[52:55], a[80:83], a[224:227], v[52:55]\n"
    "s_add_u32 s75, s75, s73\n"
    "v_mfma_f32_16x16x32_bf16 v[52:55], a[84:87], a[228:231], v[52:55]\n"
    "s_lshl_b32 s73, s83, 14\n"
    "v_mfma_f32_16x16x32_bf16 v[52:55], a[88:91], a[232:235], v[52:55]\n"
    "s_add_u32 s75, s75, s73\n"
    "v_mfma_f32_16x16x32_bf16 v[52:55], a[92:95], a[236:239], v[52:55]\n"
    "v_mfma_f32_16x16x32_bf16 v[60:63], a[80:83], a[240:243], v[60:63]\n"
    "ds_read_b128 a[80:83], v11 offset:4096\n"
    "s_xor_b32 s73, s83, 1\n"
    "v_mfma_f32_16x16x32_bf16 v[60:63], a[84:87], a[244:247], v[60:63]\n"
    "ds_read_b128 a[84:87], v11 offset:5120\n"
    "s_lshl_b32 s73, s73, 16\n"
    "v_mfma_f32_16x16x32_bf16 v[60:63], a[88:91], a[248:251], v[60:63]\n"
    "ds_read_b128 a[88:91], v11 offset:6144\n"
    "s_add_u32 s73, s73, s75\n"
    "v_mfma_f32_16x16x32_bf16 v[60:63], a[92:95], a[252:255], v[60:63]\n"
    "ds_read_b128 a[92:95], v11 offset:7168\n"
    "s_add_u32 s73, s73, 0x2000\n"
    "s_waitcnt lgkmcnt(7)\n"
    "v_mfma_f32_16x16x32_bf16 v[16:19], a[64:67], a[192:195], v[16:19]\n"
    "ds_write_b128 v114, v[192:195] offset:0\n"
    "v_add_u32 v112, s73, v8\n"
    "s_waitcnt lgkmcnt(7)\n"
    "v_mfma_f32_16x16x32_bf16 v[16:19], a[68:71], a[196:199], v[16:19]\n"
    "ds_write_b128 v114, v[196:199] offset:1024\n"
    "global_store_dwordx4 v112, v[116:119], %9 offset:0 sc1\n"
    "s_waitcnt lgkmcnt(7)\n"
    "v_mfma_f32_16x16x32_bf16 v[16:19], a[72:75], a[200:203], v[16:19]\n"
    "ds_write_b128 v114, v[200:203] offset:2048\n"
    "global_store_dwordx4 v112, v[116:119], %9 offset:1024 sc1\n"
    "s_waitcnt lgkmcnt(7)\n"
    "v_mfma_f32_16x16x32_bf16 v[16:19], a[76:79], a[204:207], v[16:19]\n"
    "s_xor_b32 s73, s83, 2\n"
    "v_mfma_f32_16x16x32_bf16 v[24:27], a[64:67], a[208:211], v[24:27]\n"
    "ds_read_b128 a[96:99], v10 offset:16384\n"
    "ds_write_b128 v114, v[204:207] offset:3072\n"
    "s_lshl_b32 s73, s73, 16\n"
    "v_mfma_f32_16x16x32_bf16 v[24:27], a[68:71], a[212:215], v[24:27]\n"
    "ds_write_b128 v114, v[208:211] offset:4096\n"
    "s_add_u32 s73, s73, s75\n"
    "v_mfma_f32_16x16x32_bf16 v[24:27], a[72:75], a[216:219], v[24:27]\n"
    "ds_read_b128 a[100:103], v10 offset:17408\n"
    "s_add_u32 s73, s73, 0x2000\n"
    "v_mfma_f32_16x16x32_bf16 v[24:27], a[76:79], a[220:223], v[24:27]\n"
    "ds_write_b128 v114, v[212:215] offset:5120\n"
    "s_waitcnt lgkmcnt(11)\n"
    "v_mfma_f32_16x16x32_bf16 v[20:23], a[80:83], a[192:195], v[20:23]\n"
    "ds_read_b128 a[104:107], v10 offset:18432\n"
    "ds_write_b128 v114, v[216:219] offset:6144\n"
    "v_add_u32 v112, s73, v8\n"
    "v_max3_f32 v113, v48, v49, v50\n"
    "s_waitcnt lgkmcnt(12)\n"
    "v_mfma_f32_16x16x32_bf16 v[20:23], a[84:87], a[196:199], v[20:23]\n"
    "global_store_dwordx4 v112, v[116:119], %9 offset:0 sc1\n"
    "v_max3_f32 v113, v113, v51, v52\n"
    "s_waitcnt lgkmcnt(11)\n"
    "v_mfma_f32_16x16x32_bf16 v[20:23], a[88:91], a[200:203], v[20:23]\n"
    "ds_read_b128 a[108:111], v10 offset:19456\n"
    "ds_write_b128 v114, v[220:223] offset:7168\n"
    "global_store_dwordx4 v112, v[116:119], %9 offset:1024 sc1\n"
    "v_max3_f32 v113, v113, v53, v54\n"
    "s_waitcnt lgkmcnt(12)\n"
    "v_mfma_f32_16x16x32_bf16 v[20:23], a[92:95], a[204:207], v[20:23]\n"
    "ds_write_b128 v114, v[224:227] offset:8192\n"
    "s_xor_b32 s73, s83, 3\n"
    "v_max_f32 v113, v113, v55\n"
    "v_mfma_f32_16x16x32_bf16 v[28:31], a[80:83], a[208:211], v[28:31]\n"
    "ds_read_b128 a[112:115], v10 offset:20480\n"
    "s_lshl_b32 s73, s73, 16\n"
    "v_max3_f32 v14, v56, v57, v58\n"
    "v_mfma_f32_16x16x32_bf16 v[28:31], a[84:87], a[212:215], v[28:31]\n"
    "ds_write_b128 v114, v[228:231] offset:9216\n"
    "s_add_u32 s73, s73, s75\n"
    "v_max3_f32 v14, v14, v59, v60\n"
    "v_mfma_f32_16x16x32_bf16 v[28:31], a[88:91], a[216:219], v[28:31]\n"
    "ds_read_b128 a[116:119], v10 offset:21504\n"
    "ds_write_b128 v114, v[232:235] offset:10240\n"
    "s_add_u32 s73, s73, 0x2000\n"
    "v_max3_f32 v14, v14, v61, v62\n"
    "v_mfma_f32_16x16x32_bf16 v[28:31], a[92:95], a[220:223], v[28:31]\n"
    "v_add_u32 v112, s73, v8\n"
    "v_max_f32 v14, v14, v63\n"
    "v_mfma_f32_16x16x32_bf16 v[32:35], a[64:67], a[224:227], 0\n"
    "ds_read_b128 a[120:123], v10 offset:22528\n"
    "ds_write_b128 v114, v[236:239] offset:11264\n"
    "global_store_dwordx4 v112, v[116:119], %9 offset:0 sc1\n"
    "v_mfma_f32_16x16x32_bf16 v[32:35], a[68:71], a[228:231], v[32:35]\n"
    "ds_write_b128 v114, v[240:243] offset:12288\n"
    "global_store_dwordx4 v112, v[116:119], %9 offset:1024 sc1\n"
    "v_mfma_f32_16x16x32_bf16 v[32:35], a[72:75], a[232:235], v[32:35]\n"
    "ds_read_b128 a[124:127], v10 offset:23552\n"
    "v_mfma_f32_16x16x32_bf16 v[32:35], a[76:79], a[236:239], v[32:35]\n"
    "ds_write_b128 v114, v[244:247] offset:13312\n"
    "s_nop 3\n"
    "v_mfma_f32_16x16x32_bf16 v[36:39], a[80:83], a[224:227], 0\n"
    "ds_write_b128 v114, v[248:251] offset:14336\n"
    "v_max3_f32 v124, v16, v17, v18\n"
    "v_max3_f32 v124, v124, v19, v20\n"
    "v_mfma_f32_16x16x32_bf16 v[36:39], a[84:87], a[228:231], v[36:39]\n"
    "v_max3_f32 v124, v124, v21, v22\n"
    "v_max_f32 v124, v124, v23\n"
    "v_mfma_f32_16x16x32_bf16 v[36:39], a[88:91], a[232:235], v[36:39]\n"
    "ds_write_b128 v114, v[252:255] offset:15360\n"
    "v_max3_f32 v13, v24, v25, v26\n"
    "v_max3_f32 v13, v13, v27, v28\n"
    "v_mfma_f32_16x16x32_bf16 v[36:39], a[92:95], a[236:239], v[36:39]\n"
    "ds_write2st64_b32 v122, v88, v92 offset0:0 offset1:12\n"
    "v_max3_f32 v13, v13, v29, v30\n"
    "v_max_f32 v13, v13, v31\n"
    "v_mfma_f32_16x16x32_bf16 v[40:43], a[64:67], a[240:243], 0\n"
    "s_nop 1\n"
    "v_permlane32_swap_b32 v124, v113\n"
    "v_permlane32_swap_b32 v13, v14\n"
    "v_mfma_f32_16x16x32_bf16 v[40:43], a[68:71], a[244:247], v[40:43]\n"
    "v_max_f32 v124, v124, v113\n"
    "v_max_f32 v13, v13, v14\n"
    "v_mfma_f32_16x16x32_bf16 v[40:43], a[72:75], a[248:251], v[40:43]\n"
    "s_nop 1\n"
    "v_permlane16_swap_b32 v124, v13\n"
    "v_mfma_f32_16x16x32_bf16 v[40:43], a[76:79], a[252:255], v[40:43]\n"
    "v_max_f32 v124, v124, v13\n"
    "ds_write_b32 v122, v124 offset:1024\n"
    "v_mfma_f32_16x16x32_bf16 v[44:47], a[80:83], a[240:243], 0\n"
    "v_mfma_f32_16x16x32_bf16 v[44:47], a[84:87], a[244:247], v[44:47]\n"
    "s_waitcnt vmcnt(10) lgkmcnt(0)\n"
    "s_barrier\n"
    "ds_read_b32 v113, v123 offset:1024\n"
    "v_mfma_f32_16x16x32_bf16 v[44:47], a[88:91], a[248:251], v[44:47]\n"
    "v_mfma_f32_16x16x32_bf16 v[44:47], a[92:95], a[252:255], v[44:47]\n"
    "ds_read2st64_b32 v[126:127], v123 offset0:0 offset1:12\n"
    "ds_read_b128 v[192:195], v115 offset:0\n"
    "ds_read_b128 v[196:199], v115 offset:1024\n"
    "ds_read_b128 v[200:203], v115 offset:2048\n"
    "ds_read_b128 v[204:207], v115 offset:3072\n"
    "ds_read_b128 v[208:211], v115 offset:4096\n"
    "ds_read_b128 v[212:215], v115 offset:5120\n"
    "s_waitcnt lgkmcnt(6)\n"
    "v_add_f32 v126, v80, v126\n"
    "v_add_f32 v127, v84, v127\n"
    "v_rcp_f32 v116, v126\n"
    "v_rcp_f32 v117, v126\n"
    "v_rcp_f32 v64, v127\n"
    "v_rcp_f32 v65, v127\n"
    "ds_read_b128 v[216:219], v115 offset:6144\n"
    "ds_read_b128 v[220:223], v115 offset:7168\n"
    "s_waitcnt lgkmcnt(7)\n"
    "v_pk_add_f32 v[128:129], v[128:129], v[192:193]\n"
    "v_pk_add_f32 v[130:131], v[130:131], v[194:195]\n"
    "s_waitcnt lgkmcnt(6)\n"
    "v_pk_add_f32 v[132:133], v[132:133], v[196:197]\n"
    "v_pk_add_f32 v[134:135], v[134:135], v[198:199]\n"
    "v_pk_mul_f32 v[128:129], v[128:129], v[116:117]\n"
    "v_pk_mul_f32 v[130:131], v[130:131], v[116:117]\n"
    "v_pk_mul_f32 v[132:133], v[132:133], v[116:117]\n"
    "v_pk_mul_f32 v[134:135], v[134:135], v[116:117]\n"
    "v_cvt_pk_bf16_f32 v80, v128, v129\n"
    "v_cvt_pk_bf16_f32 v81, v130, v131\n"
    "v_cvt_pk_bf16_f32 v82, v132, v133\n"
    "v_cvt_pk_bf16_f32 v83, v134, v135\n"
    "buffer_store_dwordx4 v[80:83], v8, s[48:51], s92 offen offset:0 sc1\n"
    "ds_read_b128 v[224:227], v115 offset:8192\n"
    "ds_read_b128 v[228:231], v115 offset:9216\n"
    "s_waitcnt lgkmcnt(7)\n"
    "v_pk_add_f32 v[136:137], v[136:137], v[200:201]\n"
    "v_pk_add_f32 v[138:139], v[138:139], v[202:203]\n"
    "s_waitcnt lgkmcnt(6)\n"
    "v_pk_add_f32 v[140:141], v[140:141], v[204:205]\n"
    "v_pk_add_f32 v[142:143], v[142:143], v[206:207]\n"
    "v_pk_mul_f32 v[136:137], v[136:137], v[116:117]\n"
    "v_pk_mul_f32 v[138:139], v[138:139], v[116:117]\n"
    "v_pk_mul_f32 v[140:141], v[140:141], v[116:117]\n"
    "v_pk_mul_f32 v[142:143], v[142:143], v[116:117]\n"
    "v_cvt_pk_bf16_f32 v84, v136, v137\n"
    "v_cvt_pk_bf16_f32 v85, v138, v139\n"
    "v_cvt_pk_bf16_f32 v86, v140, v141\n"
    "v_cvt_pk_bf16_f32 v87, v142, v143\n"
    "buffer_store_dwordx4 v[84:87], v8, s[48:51], s92 offen offset:2048 sc1\n"
    "ds_read_b128 v[232:235], v115 offset:10240\n"
    "ds_read_b128 v[236:239], v115 offset:11264\n"
    "s_waitcnt lgkmcnt(7)\n"
    "v_pk_add_f32 v[144:145], v[144:145], v[208:209]\n"
    "v_pk_add_f32 v[146:147], v[146:147], v[210:211]\n"
    "s_waitcnt lgkmcnt(6)\n"
    "v_pk_add_f32 v[148:149], v[148:149], v[212:213]\n"
    "v_pk_add_f32 v[150:151], v[150:151], v[214:215]\n"
    "v_pk_mul_f32 v[144:145], v[144:145], v[116:117]\n"
    "v_pk_mul_f32 v[146:147], v[146:147], v[116:117]\n"
    "v_pk_mul_f32 v[148:149], v[148:149], v[116:117]\n"
    "v_pk_mul_f32 v[150:151], v[150:151], v[116:117]\n"
    "v_cvt_pk_bf16_f32 v88, v144, v145\n"
    "v_cvt_pk_bf16_f32 v89, v146, v147\n"
    "v_cvt_pk_bf16_f32 v90, v148, v149\n"
    "v_cvt_pk_bf16_f32 v91, v150, v151\n"
    "buffer_store_dwordx4 v[88:91], v120, s[48:51], s92 offen offset:0 sc1\n"
    "ds_read_b128 v[240:243], v115 offset:12288\n"
    "ds_read_b128 v[244:247], v115 offset:13312\n"
    "s_waitcnt lgkmcnt(7)\n"
    "v_pk_add_f32 v[152:153], v[152:153], v[216:217]\n"
    "v_pk_add_f32 v[154:155], v[154:155], v[218:219]\n"
    "s_waitcnt lgkmcnt(6)\n"
    "v_pk_add_f32 v[156:157], v[156:157], v[220:221]\n"
    "v_pk_add_f32 v[158:159], v[158:159], v[222:223]\n"
    "v_pk_mul_f32 v[152:153], v[152:153], v[116:117]\n"
    "v_pk_mul_f32 v[154:155], v[154:155], v[116:117]\n"
    "v_pk_mul_f32 v[156:157], v[156:157], v[116:117]\n"
    "v_pk_mul_f32 v[158:159], v[158:159], v[116:117]\n"
    "v_cvt_pk_bf16_f32 v92, v152, v153\n"
    "v_cvt_pk_bf16_f32 v93, v154, v155\n"
    "v_cvt_pk_bf16_f32 v94, v156, v157\n"
    "v_cvt_pk_bf16_f32 v95, v158, v159\n"
    "buffer_store_dwordx4 v[92:95], v120, s[48:51], s92 offen offset:2048 sc1\n"
    "ds_read_b128 v[248:251], v115 offset:14336\n"
    "ds_read_b128 v[252:255], v115 offset:15360\n"
    "s_waitcnt lgkmcnt(7)\n"
    "v_pk_add_f32 v[160:161], v[160:161], v[224:225]\n"
    "v_pk_add_f32 v[162:163], v[162:163], v[226:227]\n"
    "s_waitcnt lgkmcnt(6)\n"
    "v_pk_add_f32 v[164:165], v[164:165], v[228:229]\n"
    "v_pk_add_f32 v[166:167], v[166:167], v[230:231]\n"
    "v_pk_mul_f32 v[160:161], v[160:161], v[64:65]\n"
    "v_pk_mul_f32 v[162:163], v[162:163], v[64:65]\n"
    "v_pk_mul_f32 v[164:165], v[164:165], v[64:65]\n"
    "v_pk_mul_f32 v[166:167], v[166:167], v[64:65]\n"
    "v_cvt_pk_bf16_f32 v96, v160, v161\n"
    "v_cvt_pk_bf16_f32 v97, v162, v163\n"
    "v_cvt_pk_bf16_f32 v98, v164, v165\n"
    "v_cvt_pk_bf16_f32 v99, v166, v167\n"
    "buffer_store_dwordx4 v[96:99], v8, s[48:51], s92 offen offset:1024 sc1\n"
    "s_waitcnt lgkmcnt(5)\n"
    "v_pk_add_f32 v[168:169], v[168:169], v[232:233]\n"
    "v_pk_add_f32 v[170:171], v[170:171], v[234:235]\n"
    "s_waitcnt lgkmcnt(4)\n"
    "v_pk_add_f32 v[172:173], v[172:173], v[236:237]\n"
    "v_pk_add_f32 v[174:175], v[174:175], v[238:239]\n"
    "v_pk_mul_f32 v[168:169], v[168:169], v[64:65]\n"
    "v_pk_mul_f32 v[170:171], v[170:171], v[64:65]\n"
    "v_pk_mul_f32 v[172:173], v[172:173], v[64:65]\n"
    "v_pk_mul_f32 v[174:175], v[174:175], v[64:65]\n"
    "v_cvt_pk_bf16_f32 v100, v168, v169\n"
    "v_cvt_pk_bf16_f32 v101, v170, v171\n"
    "v_cvt_pk_bf16_f32 v102, v172, v173\n"
    "v_cvt_pk_bf16_f32 v103, v174, v175\n"
    "buffer_store_dwordx4 v[100:103], v8, s[48:51], s92 offen offset:3072 sc1\n"
    "s_waitcnt lgkmcnt(3)\n"
    "v_pk_add_f32 v[176:177], v[176:177], v[240:241]\n"
    "v_pk_add_f32 v[178:179], v[178:179], v[242:243]\n"
    "s_waitcnt lgkmcnt(2)\n"
    "v_pk_add_f32 v[180:181], v[180:181], v[244:245]\n"
    "v_pk_add_f32 v[182:183], v[182:183], v[246:247]\n"
    "v_pk_mul_f32 v[176:177], v[176:177], v[64:65]\n"
    "v_pk_mul_f32 v[178:179], v[178:179], v[64:65]\n"
    "v_pk_mul_f32 v[180:181], v[180:181], v[64:65]\n"
    "v_pk_mul_f32 v[182:183], v[182:183], v[64:65]\n"
    "v_cvt_pk_bf16_f32 v104, v176, v177\n"
    "v_cvt_pk_bf16_f32 v105, v178, v179\n"
    "v_cvt_pk_bf16_f32 v106, v180, v181\n"
    "v_cvt_pk_bf16_f32 v107, v182, v183\n"
    "buffer_store_dwordx4 v[104:107], v120, s[48:51], s92 offen offset:1024 sc1\n"
    "s_waitcnt lgkmcnt(1)\n"
    "v_pk_add_f32 v[184:185], v[184:185], v[248:249]\n"
    "v_pk_add_f32 v[186:187], v[186:187], v[250:251]\n"
    "s_waitcnt lgkmcnt(0)\n"
    "v_pk_add_f32 v[188:189], v[188:189], v[252:253]\n"
    "v_pk_add_f32 v[190:191], v[190:191], v[254:255]\n"
    "v_pk_mul_f32 v[184:185], v[184:185], v[64:65]\n"
    "v_pk_mul_f32 v[186:187], v[186:187], v[64:65]\n"
    "v_pk_mul_f32 v[188:189], v[188:189], v[64:65]\n"
    "v_pk_mul_f32 v[190:191], v[190:191], v[64:65]\n"
    "v_cvt_pk_bf16_f32 v108, v184, v185\n"
    "v_cvt_pk_bf16_f32 v109, v186, v187\n"
    "v_cvt_pk_bf16_f32 v110, v188, v189\n"
    "v_cvt_pk_bf16_f32 v111, v190, v191\n"
    "buffer_store_dwordx4 v[108:111], v120, s[48:51], s92 offen offset:3072 sc1\n"
    "v_add_f32 v126, v126, v127\n"
    "v_and_b32 v127, 0xfffffcff, v122\n"
    "v_mfma_f32_16x16x32_bf16 v[68:71], a[32:35], v[104:107], 0\n"
    "v_mfma_f32_16x16x32_bf16 v[68:71], a[32:35], v[108:111], v[68:71]\n"
    "v_mfma_f32_16x16x32_bf16 v[68:71], a[32:35], v[80:83], v[68:71]\n"
    "v_mfma_f32_16x16x32_bf16 v[68:71], a[32:35], v[84:87], v[68:71]\n"
    "v_mfma_f32_16x16x32_bf16 v[68:71], a[32:35], v[88:91], v[68:71]\n"
    "v_mfma_f32_16x16x32_bf16 v[68:71], a[32:35], v[92:95], v[68:71]\n"
    "v_mfma_f32_16x16x32_bf16 v[68:71], a[32:35], v[96:99], v[68:71]\n"
    "v_mfma_f32_16x16x32_bf16 v[68:71], a[32:35], v[100:103], v[68:71]\n"
    "v_max_f32 v124, v124, v113\n"
    "v_mov_b32 v13, v124\n"
    "s_nop 1\n"
    "v_permlane16_swap_b32 v124, v13\n"
    "v_mov_b32 v113, v124\n"
    "v_mov_b32 v14, v13\n"
    "s_nop 0\n"
    "v_permlane32_swap_b32 v124, v113\n"
    "v_permlane32_swap_b32 v13, v14\n"
    "s_add_u32 s73, s86, 0x10000\n"
    "s_and_b32 s73, s73, 0x1ffff\n"
    "v_add_u32 v10, s73, v9\n"
    "v_sub_f32 v48, v48, v113\n"
    "ds_read_b128 a[64:67], v10 offset:0\n"
    "v_sub_f32 v49, v49, v113\n"
    "v_sub_f32 v50, v50, v113\n"
    "ds_read_b128 a[68:71], v10 offset:1024\n"
    "v_sub_f32 v51, v51, v113\n"
    "v_sub_f32 v52, v52, v113\n"
    "ds_read_b128 a[72:75], v10 offset:2048\n"
    "v_sub_f32 v53, v53, v113\n"
    "v_sub_f32 v54, v54, v113\n"
    "ds_read_b128 a[76:79], v10 offset:3072\n"
    "v_sub_f32 v55, v55, v113\n"
    "v_exp_f32 v48, v48\n"
    "ds_read_b128 a[80:83], v10 offset:4096\n"
    "v_exp_f32 v49, v49\n"
    "v_exp_f32 v50, v50\n"
    "ds_read_b128 a[84:87], v10 offset:5120\n"
    "v_exp_f32 v51, v51\n"
    "v_exp_f32 v52, v52\n"
    "ds_read_b128 a[88:91], v10 offset:6144\n"
    "v_exp_f32 v53, v53\n"
    "v_exp_f32 v54, v54\n"
    "ds_read_b128 a[92:95], v10 offset:7168\n"
    "v_exp_f32 v55, v55\n"
    "v_cvt_pk_bf16_f32 v104, v48, v49\n"
    "v_cvt_pk_bf16_f32 v105, v50, v51\n"
    "v_cvt_pk_bf16_f32 v106, v52, v53\n"
    "v_cvt_pk_bf16_f32 v107, v54, v55\n"
    "v_fma_f32 v126, v68, 0, v126\n"
    "ds_write_b32 v122, v126 offset:2048\n"
    "s_branch .Lat_v0_%=\n"
    ".Lat_side0_%=:\n"
    "s_add_u32 s84, s84, 1\n"
    "s_cmp_lg_u32 s84, 1\n"
    "s_cbranch_scc1 .Lat_sj0_0_%=\n"
    "buffer_load_dwordx2 v[6:7], v0, s[56:59], 0 offen sc1\n"
    "global_load_dword a40, v1, %5\n"
    "s_mov_b32 s95, 1\n"
    "s_branch .Lat_back0_%=\n"
    ".Lat_sj0_0_%=:\n"
    "s_cmp_lg_u32 s84, 2\n"
    "s_cbranch_scc1 .Lat_sj0_1_%=\n"
    "global_load_dword a41, v2, %8\n"
    "global_load_dword a42, v4, s[36:37]\n"
    "s_mov_b32 s95, 1\n"
    "s_branch .Lat_back0_%=\n"
    ".Lat_sj0_1_%=:\n"
    "s_waitcnt vmcnt(16)\n"
    "s_mov_b32 s73, 0x40000\n"
    ".Lp18_%=:\n"
    "v_cmp_ne_u64 vcc, s[64:65], v[6:7]\n"
    "s_nop 4\n"
    "s_cbranch_vccz .Lq18_%=\n"
    "s_sleep 1\n"
    "buffer_load_dwordx2 v[6:7], v0, s[56:59], 0 offen sc1\n"
    "s_waitcnt vmcnt(0)\n"
    "s_sub_u32 s73, s73, 1\n"
    "s_cbranch_scc0 .Lp18_%=\n"
    ".Lq18_%=:\n"
    "global_load_dword a43, v3, %9 sc1\n"
    "s_branch .Lat_back0_%=\n"
    ".Lat_side1_%=:\n"
    "s_add_u32 s84, s84, 1\n"
    "s_cmp_lg_u32 s84, 1\n"
    "s_cbranch_scc1 .Lat_sj1_0_%=\n"
    "buffer_load_dwordx2 v[6:7], v0, s[56:59], 0 offen sc1\n"
    "global_load_dword a40, v1, %5\n"
    "s_mov_b32 s95, 1\n"
    "s_branch .Lat_back1_%=\n"
    ".Lat_sj1_0_%=:\n"
    "s_cmp_lg_u32 s84, 2\n"
    "s_cbranch_scc1 .Lat_sj1_1_%=\n"
    "global_load_dword a41, v2, %8\n"
    "global_load_dword a42, v4, s[36:37]\n"
    "s_mov_b32 s95, 1\n"
    "s_branch .Lat_back1_%=\n"
    ".Lat_sj1_1_%=:\n"
    "s_waitcnt vmcnt(16)\n"
    "s_mov_b32 s73, 0x40000\n"
    ".Lp19_%=:\n"
    "v_cmp_ne_u64 vcc, s[64:65], v[6:7]\n"
    "s_nop 4\n"
    "s_cbranch_vccz .Lq19_%=\n"
    "s_sleep 1\n"
    "buffer_load_dwordx2 v[6:7], v0, s[56:59], 0 offen sc1\n"
    "s_waitcnt vmcnt(0)\n"
    "s_sub_u32 s73, s73, 1\n"
    "s_cbranch_scc0 .Lp19_%=\n"
    ".Lq19_%=:\n"
    "global_load_dword a43, v3, %9 sc1\n"
    "s_branch .Lat_back1_%=\n"
    ".Lat_epi0_%=:\n"
    "v_mfma_f32_16x16x32_bf16 v[192:195], a[96:99], v[104:107], v[192:195]\n"
    "v_exp_f32 v44, v44\n"
    "v_exp_f32 v45, v45\n"
    "v_mfma_f32_16x16x32_bf16 v[196:199], a[100:103], v[104:107], v[196:199]\n"
    "ds_write_b128 v114, v[128:131] offset:0\n"
    "v_exp_f32 v46, v46\n"
    "v_mfma_f32_16x16x32_bf16 v[200:203], a[104:107], v[104:107], v[200:203]\n"
    "ds_write_b128 v114, v[132:135] offset:1024\n"
    "v_exp_f32 v47, v47\n"
    "v_mfma_f32_16x16x32_bf16 v[204:207], a[108:111], v[104:107], v[204:207]\n"
    "ds_write_b128 v114, v[136:139] offset:2048\n"
    "v_cvt_pk_bf16_f32 v109, v42, v43\n"
    "v_mfma_f32_16x16x32_bf16 v[208:211], a[112:115], v[104:107], v[208:211]\n"
    "ds_write_b128 v114, v[140:143] offset:3072\n"
    "v_cvt_pk_bf16_f32 v110, v44, v45\n"
    "v_mfma_f32_16x16x32_bf16 v[212:215], a[116:119], v[104:107], v[212:215]\n"
    "ds_write_b128 v114, v[144:147] offset:4096\n"
    "v_cvt_pk_bf16_f32 v111, v46, v47\n"
    "v_mfma_f32_16x16x32_bf16 v[216:219], a[120:123], v[104:107], v[216:219]\n"
    "ds_write_b128 v114, v[148:151] offset:5120\n"
    "s_xor_b32 s93, s68, 2\n"
    "s_lshl_b32 s73, s93, 15\n"
    "s_add_u32 s73, s73, 0x4000\n"
    "s_cmp_eq_u32 s93, 1\n"
    "s_cselect_b32 s73, 0, s73\n"
    "v_mfma_f32_16x16x32_bf16 v[220:223], a[124:127], v[104:107], v[220:223]\n"
    "ds_write_b128 v114, v[152:155] offset:6144\n"
    "s_xor_b32 s73, s73, s86\n"
    "s_cmp_eq_u32 s93, 0\n"
    "s_cselect_b32 s73, 0x20000, s73\n"
    "s_add_u32 s73, s73, s69\n"
    "v_mfma_f32_16x16x32_bf16 v[88:91], a[32:35], v[104:107], v[88:91]\n"
    "ds_write_b128 v114, v[156:159] offset:7168\n"
    "v_add_u32 v115, s73, v8\n"
    "s_sub_u32 s73, 3, s72\n"
    "s_cmp_eq_u32 s71, 0\n"
    "s_cselect_b32 s73, s72, s73\n"
    "v_mfma_f32_16x16x32_bf16 v[224:227], a[96:99], v[108:111], v[224:227]\n"
    "ds_write_b128 v114, v[160:163] offset:8192\n"
    "s_lshl_b32 s74, s38, 2\n"
    "s_add_u32 s73, s73, s74\n"
    "s_lshl_b32 s73, s73, 18\n"
    "s_lshl_b32 s74, s66, 13\n"
    "v_mfma_f32_16x16x32_bf16 v[228:231], a[100:103], v[108:111], v[228:231]\n"
    "ds_write_b128 v114, v[164:167] offset:9216\n"
    "s_add_u32 s92, s73, s74\n"
    "v_accvgpr_read_b32 v48, a0\n"
    "v_accvgpr_read_b32 v49, a1\n"
    "v_accvgpr_read_b32 v50, a2\n"
    "v_mfma_f32_16x16x32_bf16 v[232:235], a[104:107], v[108:111], v[232:235]\n"
    "ds_write_b128 v114, v[168:171] offset:10240\n"
    "v_accvgpr_read_b32 v51, a3\n"
    "v_accvgpr_read_b32 v52, a4\n"
    "v_accvgpr_read_b32 v53, a5\n"
    "v_accvgpr_read_b32 v54, a6\n"
    "v_mfma_f32_16x16x32_bf16 v[236:239], a[108:111], v[108:111], v[236:239]\n"
    "ds_write_b128 v114, v[172:175] offset:11264\n"
    "v_accvgpr_read_b32 v55, a7\n"
    "v_accvgpr_read_b32 v56, a8\n"
    "v_accvgpr_read_b32 v57, a9\n"
    "v_accvgpr_read_b32 v58, a10\n"
    "v_accvgpr_read_b32 v59, a11\n"
    "v_mfma_f32_16x16x32_bf16 v[240:243], a[112:115], v[108:111], v[240:243]\n"
    "ds_write_b128 v114, v[176:179] offset:12288\n"
    "v_accvgpr_read_b32 v60, a12\n"
    "v_accvgpr_read_b32 v61, a13\n"
    "v_accvgpr_read_b32 v62, a14\n"
    "v_accvgpr_read_b32 v63, a15\n"
    "v_mfma_f32_16x16x32_bf16 v[244:247], a[116:119], v[108:111], v[244:247]\n"
    "ds_write_b128 v114, v[180:183] offset:13312\n"
    "v_accvgpr_read_b32 v16, a16\n"
    "v_accvgpr_read_b32 v17, a17\n"
    "v_accvgpr_read_b32 v18, a18\n"
    "v_accvgpr_read_b32 v19, a19\n"
    "v_mfma_f32_16x16x32_bf16 v[248:251], a[120:123], v[108:111], v[248:251]\n"
    "ds_write_b128 v114, v[184:187] offset:14336\n"
    "v_accvgpr_read_b32 v20, a20\n"
    "v_accvgpr_read_b32 v21, a21\n"
    "v_accvgpr_read_b32 v22, a22\n"
    "v_accvgpr_read_b32 v23, a23\n"
    "v_mfma_f32_16x16x32_bf16 v[252:255], a[124:127], v[108:111], v[252:255]\n"
    "ds_write_b128 v114, v[188:191] offset:15360\n"
    "v_accvgpr_read_b32 v24, a24\n"
    "v_accvgpr_read_b32 v25, a25\n"
    "v_accvgpr_read_b32 v26, a26\n"
    "v_accvgpr_read_b32 v27, a27\n"
    "v_mfma_f32_16x16x32_bf16 v[92:95], a[32:35], v[108:111], v[92:95]\n"
    "ds_write2st64_b32 v122, v80, v84 offset0:0 offset1:12\n"
    "v_accvgpr_read_b32 v28, a28\n"
    "v_accvgpr_read_b32 v29, a29\n"
    "v_accvgpr_read_b32 v30, a30\n"
    "v_accvgpr_read_b32 v31, a31\n"
    "v_add_u32 v10, s86, v9\n"
    "s_add_u32 s73, s86, 0x8000\n"
    "s_and_b32 s73, s73, 0x1ffff\n"
    "v_add_u32 v11, s73, v9\n"
    "v_mfma_f32_16x16x32_bf16 v[48:51], a[64:67], a[224:227], v[48:51]\n"
    "v_mov_b32 v116, -1\n"
    "v_mfma_f32_16x16x32_bf16 v[48:51], a[68:71], a[228:231], v[48:51]\n"
    "v_mov_b32 v117, -1\n"
    "v_mfma_f32_16x16x32_bf16 v[48:51], a[72:75], a[232:235], v[48:51]\n"
    "v_mov_b32 v118, -1\n"
    "v_mfma_f32_16x16x32_bf16 v[48:51], a[76:79], a[236:239], v[48:51]\n"
    "v_mov_b32 v119, -1\n"
    "v_mfma_f32_16x16x32_bf16 v[56:59], a[64:67], a[240:243], v[56:59]\n"
    "ds_read_b128 a[64:67], v11 offset:0\n"
    "s_lshl_b32 s75, s78, 2\n"
    "v_mfma_f32_16x16x32_bf16 v[56:59], a[68:71], a[244:247], v[56:59]\n"
    "ds_read_b128 a[68:71], v11 offset:1024\n"
    "s_add_u32 s75, s75, s79\n"
    "v_mfma_f32_16x16x32_bf16 v[56:59], a[72:75], a[248:251], v[56:59]\n"
    "ds_read_b128 a[72:75], v11 offset:2048\n"
    "s_lshl_b32 s75, s75, 18\n"
    "v_mfma_f32_16x16x32_bf16 v[56:59], a[76:79], a[252:255], v[56:59]\n"
    "ds_read_b128 a[76:79], v11 offset:3072\n"
    "s_lshl_b32 s73, s68, 11\n"
    "v_mfma_f32_16x16x32_bf16 v[52:55], a[80:83], a[224:227], v[52:55]\n"
    "s_add_u32 s75, s75, s73\n"
    "v_mfma_f32_16x16x32_bf16 v[52:55], a[84:87], a[228:231], v[52:55]\n"
    "s_lshl_b32 s73, s83, 14\n"
    "v_mfma_f32_16x16x32_bf16 v[52:55], a[88:91], a[232:235], v[52:55]\n"
    "s_add_u32 s75, s75, s73\n"
    "v_mfma_f32_16x16x32_bf16 v[52:55], a[92:95], a[236:239], v[52:55]\n"
    "v_mfma_f32_16x16x32_bf16 v[60:63], a[80:83], a[240:243], v[60:63]\n"
    "ds_read_b128 a[80:83], v11 offset:4096\n"
    "s_xor_b32 s73, s83, 1\n"
    "v_mfma_f32_16x16x32_bf16 v[60:63], a[84:87], a[244:247], v[60:63]\n"
    "ds_read_b128 a[84:87], v11 offset:5120\n"
    "s_lshl_b32 s73, s73, 16\n"
    "v_mfma_f32_16x16x32_bf16 v[60:63], a[88:91], a[248:251], v[60:63]\n"
    "ds_read_b128 a[88:91], v11 offset:6144\n"
    "s_add_u32 s73, s73, s75\n"
    "v_mfma_f32_16x16x32_bf16 v[60:63], a[92:95], a[252:255], v[60:63]\n"
    "ds_read_b128 a[92:95], v11 offset:7168\n"
    "s_add_u32 s73, s73, 0x2000\n"
    "s_waitcnt lgkmcnt(7)\n"
    "v_mfma_f32_16x16x32_bf16 v[16:19], a[64:67], a[192:195], v[16:19]\n"
    "v_add_u32 v112, s73, v8\n"
    "s_waitcnt lgkmcnt(6)\n"
    "v_mfma_f32_16x16x32_bf16 v[16:19], a[68:71], a[196:199], v[16:19]\n"
    "global_store_dwordx4 v112, v[116:119], %9 offset:0 sc1\n"
    "s_waitcnt lgkmcnt(5)\n"
    "v_mfma_f32_16x16x32_bf16 v[16:19], a[72:75], a[200:203], v[16:19]\n"
    "global_store_dwordx4 v112, v[116:119], %9 offset:1024 sc1\n"
    "s_waitcnt lgkmcnt(4)\n"
    "v_mfma_f32_16x16x32_bf16 v[16:19], a[76:79], a[204:207], v[16:19]\n"
    "s_xor_b32 s73, s83, 2\n"
    "v_mfma_f32_16x16x32_bf16 v[24:27], a[64:67], a[208:211], v[24:27]\n"
    "ds_read_b128 a[96:99], v10 offset:16384\n"
    "s_lshl_b32 s73, s73, 16\n"
    "v_mfma_f32_16x16x32_bf16 v[24:27], a[68:71], a[212:215], v[24:27]\n"
    "s_add_u32 s73, s73, s75\n"
    "v_mfma_f32_16x16x32_bf16 v[24:27], a[72:75], a[216:219], v[24:27]\n"
    "ds_read_b128 a[100:103], v10 offset:17408\n"
    "s_add_u32 s73, s73, 0x2000\n"
    "v_mfma_f32_16x16x32_bf16 v[24:27], a[76:79], a[220:223], v[24:27]\n"
    "s_waitcnt lgkmcnt(5)\n"
    "v_mfma_f32_16x16x32_bf16 v[20:23], a[80:83], a[192:195], v[20:23]\n"
    "ds_read_b128 a[104:107], v10 offset:18432\n"
    "v_add_u32 v112, s73, v8\n"
    "v_max3_f32 v113, v48, v49, v50\n"
    "s_waitcnt lgkmcnt(5)\n"
    "v_mfma_f32_16x16x32_bf16 v[20:23], a[84:87], a[196:199], v[20:23]\n"
    "global_store_dwordx4 v112, v[116:119], %9 offset:0 sc1\n"
    "v_max3_f32 v113, v113, v51, v52\n"
    "s_waitcnt lgkmcnt(4)\n"
    "v_mfma_f32_16x16x32_bf16 v[20:23], a[88:91], a[200:203], v[20:23]\n"
    "ds_read_b128 a[108:111], v10 offset:19456\n"
    "global_store_dwordx4 v112, v[116:119], %9 offset:1024 sc1\n"
    "v_max3_f32 v113, v113, v53, v54\n"
    "s_waitcnt lgkmcnt(4)\n"
    "v_mfma_f32_16x16x32_bf16 v[20:23], a[92:95], a[204:207], v[20:23]\n"
    "s_xor_b32 s73, s83, 3\n"
    "v_max_f32 v113, v113, v55\n"
    "v_mfma_f32_16x16x32_bf16 v[28:31], a[80:83], a[208:211], v[28:31]\n"
    "ds_read_b128 a[112:115], v10 offset:20480\n"
    "s_lshl_b32 s73, s73, 16\n"
    "v_max3_f32 v14, v56, v57, v58\n"
    "v_mfma_f32_16x16x32_bf16 v[28:31], a[84:87], a[212:215], v[28:31]\n"
    "s_add_u32 s73, s73, s75\n"
    "v_max3_f32 v14, v14, v59, v60\n"
    "v_mfma_f32_16x16x32_bf16 v[28:31], a[88:91], a[216:219], v[28:31]\n"
    "ds_read_b128 a[116:119], v10 offset:21504\n"
    "s_add_u32 s73, s73, 0x2000\n"
    "v_max3_f32 v14, v14, v61, v62\n"
    "v_mfma_f32_16x16x32_bf16 v[28:31], a[92:95], a[220:223], v[28:31]\n"
    "v_add_u32 v112, s73, v8\n"
    "v_max_f32 v14, v14, v63\n"
    "v_mfma_f32_16x16x32_bf16 v[32:35], a[64:67], a[224:227], 0\n"
    "ds_read_b128 a[120:123], v10 offset:22528\n"
    "global_store_dwordx4 v112, v[116:119], %9 offset:0 sc1\n"
    "v_mfma_f32_16x16x32_bf16 v[32:35], a[68:71], a[228:231], v[32:35]\n"
    "global_store_dwordx4 v112, v[116:119], %9 offset:1024 sc1\n"
    "v_mfma_f32_16x16x32_bf16 v[32:35], a[72:75], a[232:235], v[32:35]\n"
    "ds_read_b128 a[124:127], v10 offset:23552\n"
    "v_mfma_f32_16x16x32_bf16 v[32:35], a[76:79], a[236:239], v[32:35]\n"
    "s_nop 3\n"
    "v_mfma_f32_16x16x32_bf16 v[36:39], a[80:83], a[224:227], 0\n"
    "v_max3_f32 v124, v16, v17, v18\n"
    "v_max3_f32 v124, v124, v19, v20\n"
    "v_mfma_f32_16x16x32_bf16 v[36:39], a[84:87], a[228:231], v[36:39]\n"
    "v_max3_f32 v124, v124, v21, v22\n"
    "v_max_f32 v124, v124, v23\n"
    "v_mfma_f32_16x16x32_bf16 v[36:39], a[88:91], a[232:235], v[36:39]\n"
    "v_max3_f32 v13, v24, v25, v26\n"
    "v_max3_f32 v13, v13, v27, v28\n"
    "v_mfma_f32_16x16x32_bf16 v[36:39], a[92:95], a[236:239], v[36:39]\n"
    "v_max3_f32 v13, v13, v29, v30\n"
    "v_max_f32 v13, v13, v31\n"
    "v_mfma_f32_16x16x32_bf16 v[40:43], a[64:67], a[240:243], 0\n"
    "s_nop 1\n"
    "v_permlane32_swap_b32 v124, v113\n"
    "v_permlane32_swap_b32 v13, v14\n"
    "v_mfma_f32_16x16x32_bf16 v[40:43], a[68:71], a[244:247], v[40:43]\n"
    "v_max_f32 v124, v124, v113\n"
    "v_max_f32 v13, v13, v14\n"
    "v_mfma_f32_16x16x32_bf16 v[40:43], a[72:75], a[248:251], v[40:43]\n"
    "s_nop 1\n"
    "v_permlane16_swap_b32 v124, v13\n"
    "v_mfma_f32_16x16x32_bf16 v[40:43], a[76:79], a[252:255], v[40:43]\n"
    "v_max_f32 v124, v124, v13\n"
    "ds_write_b32 v122, v124 offset:1024\n"
    "v_mfma_f32_16x16x32_bf16 v[44:47], a[80:83], a[240:243], 0\n"
    "v_mfma_f32_16x16x32_bf16 v[44:47], a[84:87], a[244:247], v[44:47]\n"
    "s_waitcnt vmcnt(10) lgkmcnt(0)\n"
    "s_barrier\n"
    "ds_read_b32 v113, v123 offset:1024\n"
    "v_mfma_f32_16x16x32_bf16 v[44:47], a[88:91], a[248:251], v[44:47]\n"
    "v_mfma_f32_16x16x32_bf16 v[44:47], a[92:95], a[252:255], v[44:47]\n"
    "ds_read2st64_b32 v[126:127], v123 offset0:0 offset1:12\n"
    "ds_read_b128 v[128:131], v115 offset:0\n"
    "ds_read_b128 v[132:135], v115 offset:1024\n"
    "ds_read_b128 v[136:139], v115 offset:2048\n"
    "ds_read_b128 v[140:143], v115 offset:3072\n"
    "ds_read_b128 v[144:147], v115 offset:4096\n"
    "ds_read_b128 v[148:151], v115 offset:5120\n"
    "s_waitcnt lgkmcnt(6)\n"
    "v_add_f32 v126, v88, v126\n"
    "v_add_f32 v127, v92, v127\n"
    "v_rcp_f32 v116, v126\n"
    "v_rcp_f32 v117, v126\n"
    "v_rcp_f32 v64, v127\n"
    "v_rcp_f32 v65, v127\n"
    "ds_read_b128 v[152:155], v115 offset:6144\n"
    "ds_read_b128 v[156:159], v115 offset:7168\n"
    "s_waitcnt lgkmcnt(7)\n"
    "v_pk_add_f32 v[192:193], v[192:193], v[128:129]\n"
    "v_pk_add_f32 v[194:195], v[194:195], v[130:131]\n"
    "s_waitcnt lgkmcnt(6)\n"
    "v_pk_add_f32 v[196:197], v[196:197], v[132:133]\n"
    "v_pk_add_f32 v[198:199], v[198:199], v[134:135]\n"
    "v_pk_mul_f32 v[192:193], v[192:193], v[116:117]\n"
    "v_pk_mul_f32 v[194:195], v[194:195], v[116:117]\n"
    "v_pk_mul_f32 v[196:197], v[196:197], v[116:117]\n"
    "v_pk_mul_f32 v[198:199], v[198:199], v[116:117]\n"
    "v_cvt_pk_bf16_f32 v80, v192, v193\n"
    "v_cvt_pk_bf16_f32 v81, v194, v195\n"
    "v_cvt_pk_bf16_f32 v82, v196, v197\n"
    "v_cvt_pk_bf16_f32 v83, v198, v199\n"
    "buffer_store_dwordx4 v[80:83], v8, s[48:51], s92 offen offset:0 sc1\n"
    "ds_read_b128 v[160:163], v115 offset:8192\n"
    "ds_read_b128 v[164:167], v115 offset:9216\n"
    "s_waitcnt lgkmcnt(7)\n"
    "v_pk_add_f32 v[200:201], v[200:201], v[136:137]\n"
    "v_pk_add_f32 v[202:203], v[202:203], v[138:139]\n"
    "s_waitcnt lgkmcnt(6)\n"
    "v_pk_add_f32 v[204:205], v[204:205], v[140:141]\n"
    "v_pk_add_f32 v[206:207], v[206:207], v[142:143]\n"
    "v_pk_mul_f32 v[200:201], v[200:201], v[116:117]\n"
    "v_pk_mul_f32 v[202:203], v[202:203], v[116:117]\n"
    "v_pk_mul_f32 v[204:205], v[204:205], v[116:117]\n"
    "v_pk_mul_f32 v[206:207], v[206:207], v[116:117]\n"
    "v_cvt_pk_bf16_f32 v84, v200, v201\n"
    "v_cvt_pk_bf16_f32 v85, v202, v203\n"
    "v_cvt_pk_bf16_f32 v86, v204, v205\n"
    "v_cvt_pk_bf16_f32 v87, v206, v207\n"
    "buffer_store_dwordx4 v[84:87], v8, s[48:51], s92 offen offset:2048 sc1\n"
    "ds_read_b128 v[168:171], v115 offset:10240\n"
    "ds_read_b128 v[172:175], v115 offset:11264\n"
    "s_waitcnt lgkmcnt(7)\n"
    "v_pk_add_f32 v[208:209], v[208:209], v[144:145]\n"
    "v_pk_add_f32 v[210:211], v[210:211], v[146:147]\n"
    "s_waitcnt lgkmcnt(6)\n"
    "v_pk_add_f32 v[212:213], v[212:213], v[148:149]\n"
    "v_pk_add_f32 v[214:215], v[214:215], v[150:151]\n"
    "v_pk_mul_f32 v[208:209], v[208:209], v[116:117]\n"
    "v_pk_mul_f32 v[210:211], v[210:211], v[116:117]\n"
    "v_pk_mul_f32 v[212:213], v[212:213], v[116:117]\n"
    "v_pk_mul_f32 v[214:215], v[214:215], v[116:117]\n"
    "v_cvt_pk_bf16_f32 v88, v208, v209\n"
    "v_cvt_pk_bf16_f32 v89, v210, v211\n"
    "v_cvt_pk_bf16_f32 v90, v212, v213\n"
    "v_cvt_pk_bf16_f32 v91, v214, v215\n"
    "buffer_store_dwordx4 v[88:91], v120, s[48:51], s92 offen offset:0 sc1\n"
    "ds_read_b128 v[176:179], v115 offset:12288\n"
    "ds_read_b128 v[180:183], v115 offset:13312\n"
    "s_waitcnt lgkmcnt(7)\n"
    "v_pk_add_f32 v[216:217], v[216:217], v[152:153]\n"
    "v_pk_add_f32 v[218:219], v[218:219], v[154:155]\n"
    "s_waitcnt lgkmcnt(6)\n"
    "v_pk_add_f32 v[220:221], v[220:221], v[156:157]\n"
    "v_pk_add_f32 v[222:223], v[222:223], v[158:159]\n"
    "v_pk_mul_f32 v[216:217], v[216:217], v[116:117]\n"
    "v_pk_mul_f32 v[218:219], v[218:219], v[116:117]\n"
    "v_pk_mul_f32 v[220:221], v[220:221], v[116:117]\n"
    "v_pk_mul_f32 v[222:223], v[222:223], v[116:117]\n"
    "v_cvt_pk_bf16_f32 v92, v216, v217\n"
    "v_cvt_pk_bf16_f32 v93, v218, v219\n"
    "v_cvt_pk_bf16_f32 v94, v220, v221\n"
    "v_cvt_pk_bf16_f32 v95, v222, v223\n"
    "buffer_store_dwordx4 v[92:95], v120, s[48:51], s92 offen offset:2048 sc1\n"
    "ds_read_b128 v[184:187], v115 offset:14336\n"
    "ds_read_b128 v[188:191], v115 offset:15360\n"
    "s_waitcnt lgkmcnt(7)\n"
    "v_pk_add_f32 v[224:225], v[224:225], v[160:161]\n"
    "v_pk_add_f32 v[226:227], v[226:227], v[162:163]\n"
    "s_waitcnt lgkmcnt(6)\n"
    "v_pk_add_f32 v[228:229], v[228:229], v[164:165]\n"
    "v_pk_add_f32 v[230:231], v[230:231], v[166:167]\n"
    "v_pk_mul_f32 v[224:225], v[224:225], v[64:65]\n"
    "v_pk_mul_f32 v[226:227], v[226:227], v[64:65]\n"
    "v_pk_mul_f32 v[228:229], v[228:229], v[64:65]\n"
    "v_pk_mul_f32 v[230:231], v[230:231], v[64:65]\n"
    "v_cvt_pk_bf16_f32 v96, v224, v225\n"
    "v_cvt_pk_bf16_f32 v97, v226, v227\n"
    "v_cvt_pk_bf16_f32 v98, v228, v229\n"
    "v_cvt_pk_bf16_f32 v99, v230, v231\n"
    "buffer_store_dwordx4 v[96:99], v8, s[48:51], s92 offen offset:1024 sc1\n"
    "s_waitcnt lgkmcnt(5)\n"
    "v_pk_add_f32 v[232:233], v[232:233], v[168:169]\n"
    "v_pk_add_f32 v[234:235], v[234:235], v[170:171]\n"
    "s_waitcnt lgkmcnt(4)\n"
    "v_pk_add_f32 v[236:237], v[236:237], v[172:173]\n"
    "v_pk_add_f32 v[238:239], v[238:239], v[174:175]\n"
    "v_pk_mul_f32 v[232:233], v[232:233], v[64:65]\n"
    "v_pk_mul_f32 v[234:235], v[234:235], v[64:65]\n"
    "v_pk_mul_f32 v[236:237], v[236:237], v[64:65]\n"
    "v_pk_mul_f32 v[238:239], v[238:239], v[64:65]\n"
    "v_cvt_pk_bf16_f32 v100, v232, v233\n"
    "v_cvt_pk_bf16_f32 v101, v234, v235\n"
    "v_cvt_pk_bf16_f32 v102, v236, v237\n"
    "v_cvt_pk_bf16_f32 v103, v238, v239\n"
    "buffer_store_dwordx4 v[100:103], v8, s[48:51], s92 offen offset:3072 sc1\n"
    "s_waitcnt lgkmcnt(3)\n"
    "v_pk_add_f32 v[240:241], v[240:241], v[176:177]\n"
    "v_pk_add_f32 v[242:243], v[242:243], v[178:179]\n"
    "s_waitcnt lgkmcnt(2)\n"
    "v_pk_add_f32 v[244:245], v[244:245], v[180:181]\n"
    "v_pk_add_f32 v[246:247], v[246:247], v[182:183]\n"
    "v_pk_mul_f32 v[240:241], v[240:241], v[64:65]\n"
    "v_pk_mul_f32 v[242:243], v[242:243], v[64:65]\n"
    "v_pk_mul_f32 v[244:245], v[244:245], v[64:65]\n"
    "v_pk_mul_f32 v[246:247], v[246:247], v[64:65]\n"
    "v_cvt_pk_bf16_f32 v104, v240, v241\n"
    "v_cvt_pk_bf16_f32 v105, v242, v243\n"
    "v_cvt_pk_bf16_f32 v106, v244, v245\n"
    "v_cvt_pk_bf16_f32 v107, v246, v247\n"
    "buffer_store_dwordx4 v[104:107], v120, s[48:51], s92 offen offset:1024 sc1\n"
    "s_waitcnt lgkmcnt(1)\n"
    "v_pk_add_f32 v[248:249], v[248:249], v[184:185]\n"
    "v_pk_add_f32 v[250:251], v[250:251], v[186:187]\n"
    "s_waitcnt lgkmcnt(0)\n"
    "v_pk_add_f32 v[252:253], v[252:253], v[188:189]\n"
    "v_pk_add_f32 v[254:255], v[254:255], v[190:191]\n"
    "v_pk_mul_f32 v[248:249], v[248:249], v[64:65]\n"
    "v_pk_mul_f32 v[250:251], v[250:251], v[64:65]\n"
    "v_pk_mul_f32 v[252:253], v[252:253], v[64:65]\n"
    "v_pk_mul_f32 v[254:255], v[254:255], v[64:65]\n"
    "v_cvt_pk_bf16_f32 v108, v248, v249\n"
    "v_cvt_pk_bf16_f32 v109, v250, v251\n"
    "v_cvt_pk_bf16_f32 v110, v252, v253\n"
    "v_cvt_pk_bf16_f32 v111, v254, v255\n"
    "buffer_store_dwordx4 v[108:111], v120, s[48:51], s92 offen offset:3072 sc1\n"
    "v_add_f32 v126, v126, v127\n"
    "v_and_b32 v127, 0xfffffcff, v122\n"
    "v_mfma_f32_16x16x32_bf16 v[68:71], a[32:35], v[104:107], 0\n"
    "v_mfma_f32_16x16x32_bf16 v[68:71], a[32:35], v[108:111], v[68:71]\n"
    "v_mfma_f32_16x16x32_bf16 v[68:71], a[32:35], v[80:83], v[68:71]\n"
    "v_mfma_f32_16x16x32_bf16 v[68:71], a[32:35], v[84:87], v[68:71]\n"
    "v_mfma_f32_16x16x32_bf16 v[68:71], a[32:35], v[88:91], v[68:71]\n"
    "v_mfma_f32_16x16x32_bf16 v[68:71], a[32:35], v[92:95], v[68:71]\n"
    "v_mfma_f32_16x16x32_bf16 v[68:71], a[32:35], v[96:99], v[68:71]\n"
    "v_mfma_f32_16x16x32_bf16 v[68:71], a[32:35], v[100:103], v[68:71]\n"
    "v_max_f32 v124, v124, v113\n"
    "v_mov_b32 v13, v124\n"
    "s_nop 1\n"
    "v_permlane16_swap_b32 v124, v13\n"
    "v_mov_b32 v113, v124\n"
    "v_mov_b32 v14, v13\n"
    "s_nop 0\n"
    "v_permlane32_swap_b32 v124, v113\n"
    "v_permlane32_swap_b32 v13, v14\n"
    "s_add_u32 s73, s86, 0x10000\n"
    "s_and_b32 s73, s73, 0x1ffff\n"
    "v_add_u32 v10, s73, v9\n"
    "v_sub_f32 v48, v48, v113\n"
    "ds_read_b128 a[64:67], v10 offset:0\n"
    "v_sub_f32 v49, v49, v113\n"
    "v_sub_f32 v50, v50, v113\n"
    "ds_read_b128 a[68:71], v10 offset:1024\n"
    "v_sub_f32 v51, v51, v113\n"
    "v_sub_f32 v52, v52, v113\n"
    "ds_read_b128 a[72:75], v10 offset:2048\n"
    "v_sub_f32 v53, v53, v113\n"
    "v_sub_f32 v54, v54, v113\n"
    "ds_read_b128 a[76:79], v10 offset:3072\n"
    "v_sub_f32 v55, v55, v113\n"
    "v_exp_f32 v48, v48\n"
    "ds_read_b128 a[80:83], v10 offset:4096\n"
    "v_exp_f32 v49, v49\n"
    "v_exp_f32 v50, v50\n"
    "ds_read_b128 a[84:87], v10 offset:5120\n"
    "v_exp_f32 v51, v51\n"
    "v_exp_f32 v52, v52\n"
    "ds_read_b128 a[88:91], v10 offset:6144\n"
    "v_exp_f32 v53, v53\n"
    "v_exp_f32 v54, v54\n"
    "ds_read_b128 a[92:95], v10 offset:7168\n"
    "v_exp_f32 v55, v55\n"
    "v_cvt_pk_bf16_f32 v104, v48, v49\n"
    "v_cvt_pk_bf16_f32 v105, v50, v51\n"
    "v_cvt_pk_bf16_f32 v106, v52, v53\n"
    "v_cvt_pk_bf16_f32 v107, v54, v55\n"
    "v_fma_f32 v126, v68, 0, v126\n"
    "ds_write_b32 v122, v126 offset:2048\n"
    ".Lat_v0_%=:\n"
    ".Lat_prob1_%=:\n"
    "s_waitcnt lgkmcnt(8)\n"
    "v_mfma_f32_16x16x32_bf16 v[192:195], a[96:99], v[104:107], 0\n"
    "v_sub_f32 v56, v56, v14\n"
    "v_sub_f32 v57, v57, v14\n"
    "v_sub_f32 v58, v58, v14\n"
    "v_mfma_f32_16x16x32_bf16 v[196:199], a[100:103], v[104:107], 0\n"
    "s_waitcnt lgkmcnt(0)\n"
    "s_barrier\n"
    "ds_read2st64_b32 v[116:117], v127 offset0:8 offset1:9\n"
    "ds_read2st64_b32 v[118:119], v127 offset0:10 offset1:11\n"
    "v_sub_f32 v59, v59, v14\n"
    "v_sub_f32 v60, v60, v14\n"
    "v_sub_f32 v61, v61, v14\n"
    "v_mfma_f32_16x16x32_bf16 v[200:203], a[104:107], v[104:107], 0\n"
    "s_add_u32 s73, s86, 0x10000\n"
    "s_and_b32 s73, s73, 0x1ffff\n"
    "s_add_u32 s73, s73, 0x4000\n"
    "s_add_u32 m0, s73, s88\n"
    "s_nop 0\n"
    "v_sub_f32 v62, v62, v14\n"
    "v_sub_f32 v63, v63, v14\n"
    "v_exp_f32 v56, v56\n"
    "v_mfma_f32_16x16x32_bf16 v[204:207], a[108:111], v[104:107], 0\n"
    "buffer_load_dwordx4 v12, s[44:47], s90 offen offset:0 sc1 lds\n"
    "v_exp_f32 v57, v57\n"
    "v_exp_f32 v58, v58\n"
    "v_exp_f32 v59, v59\n"
    "v_mfma_f32_16x16x32_bf16 v[208:211], a[112:115], v[104:107], 0\n"
    "buffer_load_dwordx4 v12, s[44:47], s90 offen offset:1024 sc1 lds\n"
    "v_exp_f32 v60, v60\n"
    "v_exp_f32 v61, v61\n"
    "v_exp_f32 v62, v62\n"
    "v_mfma_f32_16x16x32_bf16 v[212:215], a[116:119], v[104:107], 0\n"
    "s_waitcnt lgkmcnt(0)\n"
    "v_add_f32 v118, v118, v116\n"
    "v_add_f32 v119, v119, v117\n"
    "v_add_f32 v126, v118, v119\n"
    "v_cmp_ngt_f32 vcc, 0x67800000, v126\n"
    "buffer_load_dwordx4 v12, s[44:47], s90 offen offset:2048 sc1 lds\n"
    "v_exp_f32 v63, v63\n"
    "v_cvt_pk_bf16_f32 v108, v56, v57\n"
    "v_cvt_pk_bf16_f32 v109, v58, v59\n"
    "v_mfma_f32_16x16x32_bf16 v[216:219], a[120:123], v[104:107], 0\n"
    "buffer_load_dwordx4 v12, s[44:47], s90 offen offset:3072 sc1 lds\n"
    "v_cvt_pk_bf16_f32 v110, v60, v61\n"
    "v_cvt_pk_bf16_f32 v111, v62, v63\n"
    "v_mfma_f32_16x16x32_bf16 v[220:223], a[124:127], v[104:107], 0\n"
    "s_cbranch_vccnz .Lat_redo0_%=\n"
    ".Lat_ok0_%=:\n"
    "s_lshr_b32 s85, s92, 10\n"
    "s_add_u32 s85, s85, 0x5000\n"
    "s_sub_u32 s90, s90, 0x4000\n"
    "s_cselect_b32 s90, s87, s90\n"
    "s_cselect_b32 s46, s82, s46\n"
    "s_cselect_b32 s82, 0, s82\n"
    "v_sub_f32 v16, v16, v124\n"
    "v_sub_f32 v17, v17, v124\n"
    "v_sub_f32 v18, v18, v124\n"
    "v_mfma_f32_16x16x32_bf16 v[88:91], a[32:35], v[104:107], 0\n"
    "v_sub_f32 v19, v19, v124\n"
    "v_sub_f32 v20, v20, v124\n"
    "v_mfma_f32_16x16x32_bf16 v[224:227], a[96:99], v[108:111], 0\n"
    "ds_read_b128 a[96:99], v11 offset:16384\n"
    "v_sub_f32 v21, v21, v124\n"
    "v_sub_f32 v22, v22, v124\n"
    "v_mfma_f32_16x16x32_bf16 v[228:231], a[100:103], v[108:111], 0\n"
    "ds_read_b128 a[100:103], v11 offset:17408\n"
    "v_sub_f32 v23, v23, v124\n"
    "v_exp_f32 v16, v16\n"
    "v_mfma_f32_16x16x32_bf16 v[232:235], a[104:107], v[108:111], 0\n"
    "ds_read_b128 a[104:107], v11 offset:18432\n"
    "v_exp_f32 v17, v17\n"
    "v_exp_f32 v18, v18\n"
    "v_exp_f32 v19, v19\n"
    "v_mfma_f32_16x16x32_bf16 v[236:239], a[108:111], v[108:111], 0\n"
    "ds_read_b128 a[108:111], v11 offset:19456\n"
    "v_exp_f32 v20, v20\n"
    "v_exp_f32 v21, v21\n"
    "v_mfma_f32_16x16x32_bf16 v[240:243], a[112:115], v[108:111], 0\n"
    "ds_read_b128 a[112:115], v11 offset:20480\n"
    "v_exp_f32 v22, v22\n"
    "v_exp_f32 v23, v23\n"
    "v_mfma_f32_16x16x32_bf16 v[244:247], a[116:119], v[108:111], 0\n"
    "ds_read_b128 a[116:119], v11 offset:21504\n"
    "v_cvt_pk_bf16_f32 v96, v16, v17\n"
    "v_cvt_pk_bf16_f32 v97, v18, v19\n"
    "v_mfma_f32_16x16x32_bf16 v[248:251], a[120:123], v[108:111], 0\n"
    "ds_read_b128 a[120:123], v11 offset:22528\n"
    "v_cvt_pk_bf16_f32 v98, v20, v21\n"
    "v_cvt_pk_bf16_f32 v99, v22, v23\n"
    "v_mfma_f32_16x16x32_bf16 v[252:255], a[124:127], v[108:111], 0\n"
    "ds_read_b128 a[124:127], v11 offset:23552\n"
    "v_sub_f32 v24, v24, v13\n"
    "v_sub_f32 v25, v25, v13\n"
    "v_sub_f32 v26, v26, v13\n"
    "v_mfma_f32_16x16x32_bf16 v[92:95], a[32:35], v[108:111], 0\n"
    "v_sub_f32 v27, v27, v13\n"
    "v_sub_f32 v28, v28, v13\n"
    "s_waitcnt lgkmcnt(7)\n"
    "v_mfma_f32_16x16x32_bf16 v[128:131], a[96:99], v[96:99], 0\n"
    "s_add_u32 m0, s86, s88\n"
    "s_nop 0\n"
    "v_sub_f32 v29, v29, v13\n"
    "v_sub_f32 v30, v30, v13\n"
    "s_waitcnt lgkmcnt(6)\n"
    "v_mfma_f32_16x16x32_bf16 v[132:135], a[100:103], v[96:99], 0\n"
    "buffer_load_dwordx4 v12, s[40:43], s89 offen offset:0 sc1 lds\n"
    "v_sub_f32 v31, v31, v13\n"
    "v_exp_f32 v24, v24\n"
    "s_waitcnt lgkmcnt(5)\n"
    "v_mfma_f32_16x16x32_bf16 v[136:139], a[104:107], v[96:99], 0\n"
    "buffer_load_dwordx4 v12, s[40:43], s89 offen offset:1024 sc1 lds\n"
    "v_exp_f32 v25, v25\n"
    "v_exp_f32 v26, v26\n"
    "v_exp_f32 v27, v27\n"
    "s_waitcnt lgkmcnt(4)\n"
    "v_mfma_f32_16x16x32_bf16 v[140:143], a[108:111], v[96:99], 0\n"
    "buffer_load_dwordx4 v12, s[40:43], s89 offen offset:2048 sc1 lds\n"
    "v_exp_f32 v28, v28\n"
    "v_exp_f32 v29, v29\n"
    "s_waitcnt lgkmcnt(3)\n"
    "v_mfma_f32_16x16x32_bf16 v[144:147], a[112:115], v[96:99], 0\n"
    "buffer_load_dwordx4 v12, s[40:43], s89 offen offset:3072 sc1 lds\n"
    "v_exp_f32 v30, v30\n"
    "v_exp_f32 v31, v31\n"
    "s_waitcnt lgkmcnt(2)\n"
    "v_mfma_f32_16x16x32_bf16 v[148:151], a[116:119], v[96:99], 0\n"
    "s_sub_u32 s89, s89, 0x4000\n"
    "s_cselect_b32 s89, s87, s89\n"
    "s_cselect_b32 s42, s81, s42\n"
    "s_cselect_b32 s81, 0, s81\n"
    "v_cvt_pk_bf16_f32 v100, v24, v25\n"
    "v_cvt_pk_bf16_f32 v101, v26, v27\n"
    "s_waitcnt lgkmcnt(1)\n"
    "v_mfma_f32_16x16x32_bf16 v[152:155], a[120:123], v[96:99], 0\n"
    "s_add_u32 s73, s86, 0x18000\n"
    "s_and_b32 s73, s73, 0x1ffff\n"
    "s_add_u32 s73, s73, 0x4000\n"
    "s_add_u32 m0, s73, s88\n"
    "s_nop 0\n"
    "v_cvt_pk_bf16_f32 v102, v28, v29\n"
    "v_cvt_pk_bf16_f32 v103, v30, v31\n"
    "s_waitcnt lgkmcnt(0)\n"
    "v_mfma_f32_16x16x32_bf16 v[156:159], a[124:127], v[96:99], 0\n"
    "buffer_load_dwordx4 v12, s[44:47], s90 offen offset:0 sc1 lds\n"
    "v_sub_f32 v64, 0, v124\n"
    "v_sub_f32 v65, 0, v124\n"
    "v_sub_f32 v66, 0, v124\n"
    "v_sub_f32 v67, 0, v124\n"
    "v_sub_f32 v68, 0, v13\n"
    "v_mfma_f32_16x16x32_bf16 v[80:83], a[32:35], v[96:99], 0\n"
    "buffer_load_dwordx4 v12, s[44:47], s90 offen offset:1024 sc1 lds\n"
    "v_sub_f32 v69, 0, v13\n"
    "v_sub_f32 v70, 0, v13\n"
    "v_sub_f32 v71, 0, v13\n"
    "v_sub_f32 v72, 0, v113\n"
    "v_mfma_f32_16x16x32_bf16 v[160:163], a[96:99], v[100:103], 0\n"
    "buffer_load_dwordx4 v12, s[44:47], s90 offen offset:2048 sc1 lds\n"
    "v_sub_f32 v73, 0, v113\n"
    "v_sub_f32 v74, 0, v113\n"
    "v_sub_f32 v75, 0, v113\n"
    "v_sub_f32 v76, 0, v14\n"
    "v_sub_f32 v77, 0, v14\n"
    "v_mfma_f32_16x16x32_bf16 v[164:167], a[100:103], v[100:103], 0\n"
    "buffer_load_dwordx4 v12, s[44:47], s90 offen offset:3072 sc1 lds\n"
    "v_sub_f32 v78, 0, v14\n"
    "v_sub_f32 v79, 0, v14\n"
    "v_sub_f32 v32, v32, v113\n"
    "v_sub_f32 v33, v33, v113\n"
    "v_mfma_f32_16x16x32_bf16 v[168:171], a[104:107], v[100:103], 0\n"
    "s_sub_u32 s90, s90, 0x4000\n"
    "s_cselect_b32 s90, s87, s90\n"
    "s_cselect_b32 s46, s82, s46\n"
    "s_cselect_b32 s82, 0, s82\n"
    "v_sub_f32 v34, v34, v113\n"
    "v_sub_f32 v35, v35, v113\n"
    "v_sub_f32 v36, v36, v113\n"
    "v_sub_f32 v37, v37, v113\n"
    "v_sub_f32 v38, v38, v113\n"
    "v_mfma_f32_16x16x32_bf16 v[172:175], a[108:111], v[100:103], 0\n"
    "s_add_u32 s73, s86, 0x8000\n"
    "s_and_b32 s73, s73, 0x1ffff\n"
    "s_add_u32 m0, s73, s88\n"
    "s_nop 0\n"
    "v_sub_f32 v39, v39, v113\n"
    "v_sub_f32 v40, v40, v14\n"
    "v_sub_f32 v41, v41, v14\n"
    "v_sub_f32 v42, v42, v14\n"
    "v_mfma_f32_16x16x32_bf16 v[176:179], a[112:115], v[100:103], 0\n"
    "buffer_load_dwordx4 v12, s[40:43], s89 offen offset:0 sc1 lds\n"
    "v_sub_f32 v43, v43, v14\n"
    "v_sub_f32 v44, v44, v14\n"
    "v_sub_f32 v45, v45, v14\n"
    "v_sub_f32 v46, v46, v14\n"
    "v_sub_f32 v47, v47, v14\n"
    "v_mfma_f32_16x16x32_bf16 v[180:183], a[116:119], v[100:103], 0\n"
    "buffer_load_dwordx4 v12, s[40:43], s89 offen offset:1024 sc1 lds\n"
    "v_exp_f32 v32, v32\n"
    "v_exp_f32 v33, v33\n"
    "v_exp_f32 v34, v34\n"
    "v_exp_f32 v35, v35\n"
    "v_mfma_f32_16x16x32_bf16 v[184:187], a[120:123], v[100:103], 0\n"
    "buffer_load_dwordx4 v12, s[40:43], s89 offen offset:2048 sc1 lds\n"
    "v_exp_f32 v36, v36\n"
    "v_exp_f32 v37, v37\n"
    "v_exp_f32 v38, v38\n"
    "v_exp_f32 v39, v39\n"
    "v_exp_f32 v40, v40\n"
    "v_mfma_f32_16x16x32_bf16 v[188:191], a[124:127], v[100:103], 0\n"
    "buffer_load_dwordx4 v12, s[40:43], s89 offen offset:3072 sc1 lds\n"
    "v_exp_f32 v41, v41\n"
    "v_exp_f32 v42, v42\n"
    "v_exp_f32 v43, v43\n"
    "v_cvt_pk_bf16_f32 v104, v32, v33\n"
    "v_mfma_f32_16x16x32_bf16 v[84:87], a[32:35], v[100:103], 0\n"
    "v_cvt_pk_bf16_f32 v105, v34, v35\n"
    "v_cvt_pk_bf16_f32 v106, v36, v37\n"
    "v_cvt_pk_bf16_f32 v107, v38, v39\n"
    "v_cvt_pk_bf16_f32 v108, v40, v41\n"
    "s_waitcnt vmcnt(16)\n"
    "v_mov_b32 v112, s85\n"
    "v_mov_b32 v116, s64\n"
    "v_mov_b32 v117, s65\n"
    "s_mov_b64 exec, 1\n"
    "buffer_store_dwordx2 v[116:117], v112, s[56:59], 0 offen sc1\n"
    "s_mov_b64 exec, -1\n"
    "s_xor_b32 s86, s86, 0x10000\n"
    "s_lshl_b32 s91, s39, 1\n"
    "s_sub_u32 s91, s91, 1\n"
    "s_cbranch_scc1 .Lat_drain1_%=\n"
    "s_waitcnt lgkmcnt(0)\n"
    ".p2align 6\n"
    ".Lat_loop1_%=:\n"
    "v_mfma_f32_16x16x32_bf16 v[16:19], a[64:67], a[192:195], v[64:67]\n"
    "v_cvt_pk_bf16_f32 v109, v42, v43\n"
    "s_xor_b32 s73, s86, 0x14000\n"
    "v_mfma_f32_16x16x32_bf16 v[16:19], a[68:71], a[196:199], v[16:19]\n"
    "s_add_u32 m0, s73, s88\n"
    "v_exp_f32 v44, v44\n"
    "v_mfma_f32_16x16x32_bf16 v[16:19], a[72:75], a[200:203], v[16:19]\n"
    "buffer_load_dwordx4 v12, s[44:47], s90 offen offset:0 sc1 lds\n"
    "v_mfma_f32_16x16x32_bf16 v[16:19], a[76:79], a[204:207], v[16:19]\n"
    "s_sub_u32 s89, s89, 0x4000\n"
    "v_exp_f32 v45, v45\n"
    "v_mfma_f32_16x16x32_bf16 v[20:23], a[80:83], a[192:195], v[64:67]\n"
    "v_cvt_pk_bf16_f32 v110, v44, v45\n"
    "v_mfma_f32_16x16x32_bf16 v[20:23], a[84:87], a[196:199], v[20:23]\n"
    "s_cselect_b32 s89, s87, s89\n"
    "v_exp_f32 v46, v46\n"
    "v_mfma_f32_16x16x32_bf16 v[20:23], a[88:91], a[200:203], v[20:23]\n"
    "buffer_load_dwordx4 v12, s[44:47], s90 offen offset:1024 sc1 lds\n"
    "v_mfma_f32_16x16x32_bf16 v[20:23], a[92:95], a[204:207], v[20:23]\n"
    "s_cselect_b32 s42, s81, s42\n"
    "v_exp_f32 v47, v47\n"
    "v_mfma_f32_16x16x32_bf16 v[24:27], a[64:67], a[208:211], v[68:71]\n"
    "v_cvt_pk_bf16_f32 v111, v46, v47\n"
    "v_mfma_f32_16x16x32_bf16 v[24:27], a[68:71], a[212:215], v[24:27]\n"
    "s_cselect_b32 s81, 0, s81\n"
    "v_exp_f32 v16, v16\n"
    "v_mfma_f32_16x16x32_bf16 v[24:27], a[72:75], a[216:219], v[24:27]\n"
    "buffer_load_dwordx4 v12, s[44:47], s90 offen offset:2048 sc1 lds\n"
    "v_mfma_f32_16x16x32_bf16 v[24:27], a[76:79], a[220:223], v[24:27]\n"
    "s_add_u32 s93, s86, 0x8000\n"
    "v_exp_f32 v17, v17\n"
    "v_mfma_f32_16x16x32_bf16 v[28:31], a[80:83], a[208:211], v[68:71]\n"
    "v_cvt_pk_bf16_f32 v96, v16, v17\n"
    "v_mfma_f32_16x16x32_bf16 v[28:31], a[84:87], a[212:215], v[28:31]\n"
    "s_and_b32 s93, s93, 0x1ffff\n"
    "v_exp_f32 v18, v18\n"
    "v_mfma_f32_16x16x32_bf16 v[28:31], a[88:91], a[216:219], v[28:31]\n"
    "buffer_load_dwordx4 v12, s[44:47], s90 offen offset:3072 sc1 lds\n"
    "v_mfma_f32_16x16x32_bf16 v[28:31], a[92:95], a[220:223], v[28:31]\n"
    "s_sub_u32 s90, s90, 0x4000\n"
    "v_exp_f32 v19, v19\n"
    "v_mfma_f32_16x16x32_bf16 v[192:195], a[96:99], v[104:107], v[192:195]\n"
    "v_cvt_pk_bf16_f32 v97, v18, v19\n"
    "s_cselect_b32 s90, s87, s90\n"
    "v_mfma_f32_16x16x32_bf16 v[196:199], a[100:103], v[104:107], v[196:199]\n"
    "s_cselect_b32 s46, s82, s46\n"
    "v_exp_f32 v20, v20\n"
    "v_mfma_f32_16x16x32_bf16 v[200:203], a[104:107], v[104:107], v[200:203]\n"
    "s_cselect_b32 s82, 0, s82\n"
    "v_mfma_f32_16x16x32_bf16 v[204:207], a[108:111], v[104:107], v[204:207]\n"
    "v_exp_f32 v21, v21\n"
    "v_mfma_f32_16x16x32_bf16 v[208:211], a[112:115], v[104:107], v[208:211]\n"
    "v_cvt_pk_bf16_f32 v98, v20, v21\n"
    "v_mfma_f32_16x16x32_bf16 v[212:215], a[116:119], v[104:107], v[212:215]\n"
    "s_sub_u32 s95, s95, 1\n"
    "v_exp_f32 v22, v22\n"
    "v_mfma_f32_16x16x32_bf16 v[216:219], a[120:123], v[104:107], v[216:219]\n"
    "s_cbranch_scc1 .Lat_side1_%=\n"
    ".Lat_back1_%=:\n"
    "v_mfma_f32_16x16x32_bf16 v[220:223], a[124:127], v[104:107], v[220:223]\n"
    "v_exp_f32 v23, v23\n"
    "v_mfma_f32_16x16x32_bf16 v[88:91], a[32:35], v[104:107], v[88:91]\n"
    "v_cvt_pk_bf16_f32 v99, v22, v23\n"
    "v_mfma_f32_16x16x32_bf16 v[224:227], a[96:99], v[108:111], v[224:227]\n"
    "s_waitcnt vmcnt(16)\n"
    "s_barrier\n"
    "v_mfma_f32_16x16x32_bf16 v[228:231], a[100:103], v[108:111], v[228:231]\n"
    "ds_read_b128 a[96:99], v10 offset:16384\n"
    "v_mfma_f32_16x16x32_bf16 v[232:235], a[104:107], v[108:111], v[232:235]\n"
    "v_exp_f32 v24, v24\n"
    "v_mfma_f32_16x16x32_bf16 v[236:239], a[108:111], v[108:111], v[236:239]\n"
    "ds_read_b128 a[100:103], v10 offset:17408\n"
    "v_mfma_f32_16x16x32_bf16 v[240:243], a[112:115], v[108:111], v[240:243]\n"
    "v_exp_f32 v25, v25\n"
    "v_mfma_f32_16x16x32_bf16 v[244:247], a[116:119], v[108:111], v[244:247]\n"
    "ds_read_b128 a[104:107], v10 offset:18432\n"
    "v_cvt_pk_bf16_f32 v100, v24, v25\n"
    "v_mfma_f32_16x16x32_bf16 v[248:251], a[120:123], v[108:111], v[248:251]\n"
    "v_exp_f32 v26, v26\n"
    "v_mfma_f32_16x16x32_bf16 v[252:255], a[124:127], v[108:111], v[252:255]\n"
    "ds_read_b128 a[108:111], v10 offset:19456\n"
    "v_mfma_f32_16x16x32_bf16 v[92:95], a[32:35], v[108:111], v[92:95]\n"
    "v_exp_f32 v27, v27\n"
    "v_mfma_f32_16x16x32_bf16 v[32:35], a[64:67], a[224:227], v[72:75]\n"
    "ds_read_b128 a[112:115], v10 offset:20480\n"
    "v_cvt_pk_bf16_f32 v101, v26, v27\n"
    "v_mfma_f32_16x16x32_bf16 v[32:35], a[68:71], a[228:231], v[32:35]\n"
    "v_exp_f32 v28, v28\n"
    "v_mfma_f32_16x16x32_bf16 v[32:35], a[72:75], a[232:235], v[32:35]\n"
    "ds_read_b128 a[116:119], v10 offset:21504\n"
    "v_mfma_f32_16x16x32_bf16 v[32:35], a[76:79], a[236:239], v[32:35]\n"
    "v_exp_f32 v29, v29\n"
    "v_mfma_f32_16x16x32_bf16 v[36:39], a[80:83], a[224:227], v[72:75]\n"
    "ds_read_b128 a[120:123], v10 offset:22528\n"
    "v_cvt_pk_bf16_f32 v102, v28, v29\n"
    "v_mfma_f32_16x16x32_bf16 v[36:39], a[84:87], a[228:231], v[36:39]\n"
    "v_exp_f32 v30, v30\n"
    "v_mfma_f32_16x16x32_bf16 v[36:39], a[88:91], a[232:235], v[36:39]\n"
    "ds_read_b128 a[124:127], v10 offset:23552\n"
    "v_mfma_f32_16x16x32_bf16 v[36:39], a[92:95], a[236:239], v[36:39]\n"
    "v_exp_f32 v31, v31\n"
    "v_mfma_f32_16x16x32_bf16 v[40:43], a[64:67], a[240:243], v[76:79]\n"
    "v_cvt_pk_bf16_f32 v103, v30, v31\n"
    "v_add_u32 v10, s93, v9\n"
    "v_mfma_f32_16x16x32_bf16 v[40:43], a[68:71], a[244:247], v[40:43]\n"
    "v_exp_f32 v32, v32\n"
    "v_mfma_f32_16x16x32_bf16 v[40:43], a[72:75], a[248:251], v[40:43]\n"
    "ds_read_b128 a[64:67], v10 offset:0\n"
    "v_mfma_f32_16x16x32_bf16 v[40:43], a[76:79], a[252:255], v[40:43]\n"
    "v_exp_f32 v33, v33\n"
    "v_mfma_f32_16x16x32_bf16 v[44:47], a[80:83], a[240:243], v[76:79]\n"
    "ds_read_b128 a[68:71], v10 offset:1024\n"
    "v_cvt_pk_bf16_f32 v104, v32, v33\n"
    "v_mfma_f32_16x16x32_bf16 v[44:47], a[84:87], a[244:247], v[44:47]\n"
    "v_exp_f32 v34, v34\n"
    "v_mfma_f32_16x16x32_bf16 v[44:47], a[88:91], a[248:251], v[44:47]\n"
    "ds_read_b128 a[72:75], v10 offset:2048\n"
    "v_mfma_f32_16x16x32_bf16 v[44:47], a[92:95], a[252:255], v[44:47]\n"
    "s_waitcnt lgkmcnt(3)\n"
    "v_exp_f32 v35, v35\n"
    "v_mfma_f32_16x16x32_bf16 v[128:131], a[96:99], v[96:99], v[128:131]\n"
    "ds_read_b128 a[76:79], v10 offset:3072\n"
    "v_cvt_pk_bf16_f32 v105, v34, v35\n"
    "v_mfma_f32_16x16x32_bf16 v[132:135], a[100:103], v[96:99], v[132:135]\n"
    "v_exp_f32 v36, v36\n"
    "v_mfma_f32_16x16x32_bf16 v[136:139], a[104:107], v[96:99], v[136:139]\n"
    "ds_read_b128 a[80:83], v10 offset:4096\n"
    "v_mfma_f32_16x16x32_bf16 v[140:143], a[108:111], v[96:99], v[140:143]\n"
    "v_exp_f32 v37, v37\n"
    "v_mfma_f32_16x16x32_bf16 v[144:147], a[112:115], v[96:99], v[144:147]\n"
    "ds_read_b128 a[84:87], v10 offset:5120\n"
    "v_cvt_pk_bf16_f32 v106, v36, v37\n"
    "v_mfma_f32_16x16x32_bf16 v[148:151], a[116:119], v[96:99], v[148:151]\n"
    "v_exp_f32 v38, v38\n"
    "v_mfma_f32_16x16x32_bf16 v[152:155], a[120:123], v[96:99], v[152:155]\n"
    "ds_read_b128 a[88:91], v10 offset:6144\n"
    "v_mfma_f32_16x16x32_bf16 v[156:159], a[124:127], v[96:99], v[156:159]\n"
    "s_add_u32 m0, s86, s88\n"
    "v_exp_f32 v39, v39\n"
    "v_mfma_f32_16x16x32_bf16 v[80:83], a[32:35], v[96:99], v[80:83]\n"
    "ds_read_b128 a[92:95], v10 offset:7168\n"
    "v_cvt_pk_bf16_f32 v107, v38, v39\n"
    "v_mfma_f32_16x16x32_bf16 v[160:163], a[96:99], v[100:103], v[160:163]\n"
    "v_mfma_f32_16x16x32_bf16 v[164:167], a[100:103], v[100:103], v[164:167]\n"
    "buffer_load_dwordx4 v12, s[40:43], s89 offen offset:0 sc1 lds\n"
    "v_mfma_f32_16x16x32_bf16 v[168:171], a[104:107], v[100:103], v[168:171]\n"
    "s_mov_b32 s86, s93\n"
    "v_exp_f32 v40, v40\n"
    "v_mfma_f32_16x16x32_bf16 v[172:175], a[108:111], v[100:103], v[172:175]\n"
    "buffer_load_dwordx4 v12, s[40:43], s89 offen offset:1024 sc1 lds\n"
    "v_mfma_f32_16x16x32_bf16 v[176:179], a[112:115], v[100:103], v[176:179]\n"
    "v_exp_f32 v41, v41\n"
    "v_mfma_f32_16x16x32_bf16 v[180:183], a[116:119], v[100:103], v[180:183]\n"
    "buffer_load_dwordx4 v12, s[40:43], s89 offen offset:2048 sc1 lds\n"
    "v_cvt_pk_bf16_f32 v108, v40, v41\n"
    "v_mfma_f32_16x16x32_bf16 v[184:187], a[120:123], v[100:103], v[184:187]\n"
    "s_sub_u32 s91, s91, 1\n"
    "v_exp_f32 v42, v42\n"
    "v_mfma_f32_16x16x32_bf16 v[188:191], a[124:127], v[100:103], v[188:191]\n"
    "buffer_load_dwordx4 v12, s[40:43], s89 offen offset:3072 sc1 lds\n"
    "v_mfma_f32_16x16x32_bf16 v[84:87], a[32:35], v[100:103], v[84:87]\n"
    "s_waitcnt lgkmcnt(0)\n"
    "v_exp_f32 v43, v43\n"
    "s_cbranch_scc0 .Lat_loop1_%=\n"
    ".Lcodeq_%=:\n"
    ".Lat_drain1_%=:\n"
    "s_sub_u32 s89, s89, 0x4000\n"
    "s_cselect_b32 s89, s87, s89\n"
    "s_cselect_b32 s42, s81, s42\n"
    "s_cselect_b32 s81, 0, s81\n"
    "s_lshl_b32 s73, s68, 15\n"
    "s_add_u32 s73, s73, 0x4000\n"
    "s_cmp_eq_u32 s68, 1\n"
    "s_cselect_b32 s73, 0, s73\n"
    "s_xor_b32 s73, s73, s86\n"
    "s_cmp_eq_u32 s68, 0\n"
    "s_cselect_b32 s73, 0x20000, s73\n"
    "s_add_u32 s73, s73, s69\n"
    "v_add_u32 v114, s73, v8\n"
    "s_waitcnt vmcnt(12) lgkmcnt(0)\n"
    "s_barrier\n"
    "s_cmp_lg_u32 s71, 0\n"
    "s_cbranch_scc1 .Lat_epi1_%=\n"
    "v_mfma_f32_16x16x32_bf16 v[192:195], a[96:99], v[104:107], v[192:195]\n"
    "v_exp_f32 v44, v44\n"
    "v_exp_f32 v45, v45\n"
    "v_mfma_f32_16x16x32_bf16 v[196:199], a[100:103], v[104:107], v[196:199]\n"
    "v_exp_f32 v46, v46\n"
    "v_mfma_f32_16x16x32_bf16 v[200:203], a[104:107], v[104:107], v[200:203]\n"
    "v_exp_f32 v47, v47\n"
    "v_mfma_f32_16x16x32_bf16 v[204:207], a[108:111], v[104:107], v[204:207]\n"
    "v_cvt_pk_bf16_f32 v109, v42, v43\n"
    "v_mfma_f32_16x16x32_bf16 v[208:211], a[112:115], v[104:107], v[208:211]\n"
    "v_cvt_pk_bf16_f32 v110, v44, v45\n"
    "v_mfma_f32_16x16x32_bf16 v[212:215], a[116:119], v[104:107], v[212:215]\n"
    "v_cvt_pk_bf16_f32 v111, v46, v47\n"
    "v_mfma_f32_16x16x32_bf16 v[216:219], a[120:123], v[104:107], v[216:219]\n"
    "s_xor_b32 s93, s68, 2\n"
    "s_lshl_b32 s73, s93, 15\n"
    "s_add_u32 s73, s73, 0x4000\n"
    "s_cmp_eq_u32 s93, 1\n"
    "s_cselect_b32 s73, 0, s73\n"
    "s_xor_b32 s73, s73, s86\n"
    "s_cmp_eq_u32 s93, 0\n"
    "v_mfma_f32_16x16x32_bf16 v[220:223], a[124:127], v[104:107], v[220:223]\n"
    "s_cselect_b32 s73, 0x20000, s73\n"
    "s_add_u32 s73, s73, s69\n"
    "v_add_u32 v115, s73, v8\n"
    "s_sub_u32 s73, 3, s72\n"
    "s_cmp_eq_u32 s71, 0\n"
    "s_cselect_b32 s73, s72, s73\n"
    "v_mfma_f32_16x16x32_bf16 v[88:91], a[32:35], v[104:107], v[88:91]\n"
    "s_lshl_b32 s74, s39, 2\n"
    "s_add_u32 s73, s73, s74\n"
    "s_lshl_b32 s73, s73, 18\n"
    "s_lshl_b32 s74, s66, 13\n"
    "s_add_u32 s92, s73, s74\n"
    "s_mov_b64 s[60:61], %9\n"
    "s_add_u32 s60, s60, 0x2fffc00\n"
    "s_addc_u32 s61, s61, 0\n"
    "s_mov_b32 s62, 0x1700400\n"
    "s_mov_b32 s63, 0x20000\n"
    "v_mfma_f32_16x16x32_bf16 v[224:227], a[96:99], v[108:111], v[224:227]\n"
    "s_mov_b64 s[52:53], %8\n"
    "s_mov_b32 s54, 0x1c0000\n"
    "s_mov_b32 s55, 0x20000\n"
    "s_and_b32 s73, s83, 1\n"
    "s_lshl_b32 s73, s73, 10\n"
    "v_add_u32 v3, 0x400, v8\n"
    "v_mfma_f32_16x16x32_bf16 v[228:231], a[100:103], v[108:111], v[228:231]\n"
    "v_add_u32 v2, s73, v3\n"
    "v_subrev_u32 v3, s73, v3\n"
    "s_mul_i32 s75, s80, 0x1c000\n"
    "s_mul_i32 s73, s83, 0x7000\n"
    "s_add_u32 s75, s75, s73\n"
    "s_add_u32 s75, s75, 0x1000000\n"
    "v_mfma_f32_16x16x32_bf16 v[232:235], a[104:107], v[108:111], v[232:235]\n"
    "s_mul_i32 s76, s83, 0x70000\n"
    "s_lshl_b32 s73, s71, 15\n"
    "s_add_u32 s76, s76, s73\n"
    "s_lshl_b32 s73, s79, 8\n"
    "s_add_u32 s76, s76, s73\n"
    "s_lshl_b32 s73, s72, 7\n"
    "v_mfma_f32_16x16x32_bf16 v[236:239], a[108:111], v[108:111], v[236:239]\n"
    "s_add_u32 s76, s76, s73\n"
    "v_and_b32 v112, 0x1c0, v8\n"
    "v_lshlrev_b32 v121, 4, v112\n"
    "v_lshrrev_b32 v112, 3, v8\n"
    "v_and_b32 v112, 0x60, v112\n"
    "v_and_b32 v113, 0x30, v8\n"
    "v_xor_b32 v112, v112, v113\n"
    "v_mfma_f32_16x16x32_bf16 v[240:243], a[112:115], v[108:111], v[240:243]\n"
    "v_add_u32 v121, v121, v112\n"
    "v_add_u32 v4, 0x1c00, v121\n"
    "v_add_u32 v10, 0x1c00, v4\n"
    "v_add_u32 v9, 0x1c00, v10\n"
    "s_xor_b32 s74, s86, 0x10000\n"
    "s_lshl_b32 s73, s71, 13\n"
    "v_mfma_f32_16x16x32_bf16 v[244:247], a[116:119], v[108:111], v[244:247]\n"
    "s_add_u32 s73, s73, s69\n"
    "v_add_u32 v28, s73, v8\n"
    "v_add_u32 v16, s86, v28\n"
    "v_add_u32 v17, s74, v28\n"
    "v_add_u32 v18, 0x20000, v28\n"
    "v_and_b32 v28, 0x1c0, v8\n"
    "v_mfma_f32_16x16x32_bf16 v[248:251], a[120:123], v[108:111], v[248:251]\n"
    "v_lshrrev_b32 v29, 3, v8\n"
    "v_and_b32 v29, 0x20, v29\n"
    "v_add_u32 v28, v28, v29\n"
    "v_lshrrev_b32 v29, 1, v8\n"
    "v_and_b32 v29, 0x18, v29\n"
    "v_add_u32 v28, v28, v29\n"
    "v_mfma_f32_16x16x32_bf16 v[252:255], a[124:127], v[108:111], v[252:255]\n"
    "s_lshl_b32 s73, s72, 12\n"
    "s_add_u32 s73, s73, s69\n"
    "v_add_u32 v28, s73, v28\n"
    "v_and_b32 v29, 0x200, v8\n"
    "v_lshlrev_b32 v29, 1, v29\n"
    "v_add_u32 v28, v28, v29\n"
    "v_mfma_f32_16x16x32_bf16 v[92:95], a[32:35], v[108:111], v[92:95]\n"
    "v_add_u32 v19, s86, v28\n"
    "v_xor_b32 v20, 32, v19\n"
    "v_add_u32 v21, s74, v28\n"
    "v_xor_b32 v22, 32, v21\n"
    "v_add_u32 v23, 0x20000, v28\n"
    "v_xor_b32 v24, 32, v23\n"
    "s_nop 7\n"
    "ds_write_b128 v114, v[192:195] offset:0\n"
    "ds_write_b128 v114, v[196:199] offset:1024\n"
    "ds_write_b128 v114, v[200:203] offset:2048\n"
    "ds_write_b128 v114, v[204:207] offset:3072\n"
    "ds_write_b128 v114, v[208:211] offset:4096\n"
    "ds_write_b128 v114, v[212:215] offset:5120\n"
    "ds_write_b128 v114, v[216:219] offset:6144\n"
    "ds_write_b128 v114, v[220:223] offset:7168\n"
    "ds_write_b128 v114, v[224:227] offset:8192\n"
    "ds_write_b128 v114, v[228:231] offset:9216\n"
    "ds_write_b128 v114, v[232:235] offset:10240\n"
    "ds_write_b128 v114, v[236:239] offset:11264\n"
    "ds_write_b128 v114, v[240:243] offset:12288\n"
    "ds_write_b128 v114, v[244:247] offset:13312\n"
    "ds_write_b128 v114, v[248:251] offset:14336\n"
    "ds_write_b128 v114, v[252:255] offset:15360\n"
    "ds_write2st64_b32 v122, v88, v92 offset0:0 offset1:12\n"
    "s_waitcnt vmcnt(0)\n"
    "s_xor_b32 s73, s86, 0x8000\n"
    "s_add_u32 m0, s73, s88\n"
    "s_nop 0\n"
    "buffer_load_dwordx4 v2, s[60:63], s75 offen offset:0 sc1 lds\n"
    "buffer_load_dwordx4 v3, s[60:63], s75 offen offset:1024 sc1 lds\n"
    "buffer_load_dwordx4 v2, s[60:63], s75 offen offset:2048 sc1 lds\n"
    "buffer_load_dwordx4 v3, s[60:63], s75 offen offset:3072 sc1 lds\n"
    "s_xor_b32 s73, s86, 0xc000\n"
    "s_add_u32 m0, s73, s88\n"
    "s_add_u32 s75, s75, 0x1000\n"
    "buffer_load_dwordx4 v121, s[52:55], s76 offen offset:0 lds\n"
    "buffer_load_dwordx4 v4, s[52:55], s76 offen offset:1024 lds\n"
    "buffer_load_dwordx4 v10, s[52:55], s76 offen offset:2048 lds\n"
    "buffer_load_dwordx4 v9, s[52:55], s76 offen offset:3072 lds\n"
    "s_add_u32 s76, s76, 0x10000\n"
    "s_waitcnt lgkmcnt(0)\n"
    "s_barrier\n"
    "ds_read2st64_b32 v[126:127], v123 offset0:0 offset1:12\n"
    "ds_read_b128 v[192:195], v115 offset:0\n"
    "ds_read_b128 v[196:199], v115 offset:1024\n"
    "ds_read_b128 v[200:203], v115 offset:2048\n"
    "ds_read_b128 v[204:207], v115 offset:3072\n"
    "ds_read_b128 v[208:211], v115 offset:4096\n"
    "ds_read_b128 v[212:215], v115 offset:5120\n"
    "s_waitcnt lgkmcnt(6)\n"
    "v_add_f32 v126, v80, v126\n"
    "v_add_f32 v127, v84, v127\n"
    "v_rcp_f32 v116, v126\n"
    "v_rcp_f32 v117, v126\n"
    "s_xor_b32 s73, s86, 0x10000\n"
    "s_add_u32 m0, s73, s88\n"
    "s_nop 0\n"
    "v_rcp_f32 v64, v127\n"
    "v_rcp_f32 v65, v127\n"
    "ds_read_b128 v[216:219], v115 offset:6144\n"
    "ds_read_b128 v[220:223], v115 offset:7168\n"
    "s_waitcnt lgkmcnt(7)\n"
    "v_pk_add_f32 v[128:129], v[128:129], v[192:193]\n"
    "buffer_load_dwordx4 v2, s[60:63], s75 offen offset:0 sc1 lds\n"
    "v_pk_add_f32 v[130:131], v[130:131], v[194:195]\n"
    "s_waitcnt lgkmcnt(6)\n"
    "v_pk_add_f32 v[132:133], v[132:133], v[196:197]\n"
    "v_pk_add_f32 v[134:135], v[134:135], v[198:199]\n"
    "v_pk_mul_f32 v[128:129], v[128:129], v[116:117]\n"
    "v_pk_mul_f32 v[130:131], v[130:131], v[116:117]\n"
    "buffer_load_dwordx4 v3, s[60:63], s75 offen offset:1024 sc1 lds\n"
    "v_pk_mul_f32 v[132:133], v[132:133], v[116:117]\n"
    "v_pk_mul_f32 v[134:135], v[134:135], v[116:117]\n"
    "v_cvt_pk_bf16_f32 v80, v128, v129\n"
    "v_cvt_pk_bf16_f32 v81, v130, v131\n"
    "v_cvt_pk_bf16_f32 v82, v132, v133\n"
    "buffer_load_dwordx4 v2, s[60:63], s75 offen offset:2048 sc1 lds\n"
    "v_cvt_pk_bf16_f32 v83, v134, v135\n"
    "buffer_store_dwordx4 v[80:83], v8, s[48:51], s92 offen offset:0 sc1\n"
    "ds_read_b128 v[224:227], v115 offset:8192\n"
    "ds_read_b128 v[228:231], v115 offset:9216\n"
    "s_waitcnt lgkmcnt(7)\n"
    "v_pk_add_f32 v[136:137], v[136:137], v[200:201]\n"
    "buffer_load_dwordx4 v3, s[60:63], s75 offen offset:3072 sc1 lds\n"
    "v_pk_add_f32 v[138:139], v[138:139], v[202:203]\n"
    "s_waitcnt lgkmcnt(6)\n"
    "v_pk_add_f32 v[140:141], v[140:141], v[204:205]\n"
    "v_pk_add_f32 v[142:143], v[142:143], v[206:207]\n"
    "v_pk_mul_f32 v[136:137], v[136:137], v[116:117]\n"
    "v_pk_mul_f32 v[138:139], v[138:139], v[116:117]\n"
    "s_xor_b32 s73, s86, 0x4000\n"
    "s_add_u32 m0, s73, s88\n"
    "s_add_u32 s75, s75, 0x1000\n"
    "v_pk_mul_f32 v[140:141], v[140:141], v[116:117]\n"
    "v_pk_mul_f32 v[142:143], v[142:143], v[116:117]\n"
    "v_cvt_pk_bf16_f32 v84, v136, v137\n"
    "v_cvt_pk_bf16_f32 v85, v138, v139\n"
    "v_cvt_pk_bf16_f32 v86, v140, v141\n"
    "buffer_load_dwordx4 v121, s[52:55], s76 offen offset:0 lds\n"
    "v_cvt_pk_bf16_f32 v87, v142, v143\n"
    "buffer_store_dwordx4 v[84:87], v8, s[48:51], s92 offen offset:2048 sc1\n"
    "ds_read_b128 v[232:235], v115 offset:10240\n"
    "ds_read_b128 v[236:239], v115 offset:11264\n"
    "s_waitcnt lgkmcnt(7)\n"
    "v_pk_add_f32 v[144:145], v[144:145], v[208:209]\n"
    "buffer_load_dwordx4 v4, s[52:55], s76 offen offset:1024 lds\n"
    "v_pk_add_f32 v[146:147], v[146:147], v[210:211]\n"
    "s_waitcnt lgkmcnt(6)\n"
    "v_pk_add_f32 v[148:149], v[148:149], v[212:213]\n"
    "v_pk_add_f32 v[150:151], v[150:151], v[214:215]\n"
    "v_pk_mul_f32 v[144:145], v[144:145], v[116:117]\n"
    "v_pk_mul_f32 v[146:147], v[146:147], v[116:117]\n"
    "buffer_load_dwordx4 v10, s[52:55], s76 offen offset:2048 lds\n"
    "v_pk_mul_f32 v[148:149], v[148:149], v[116:117]\n"
    "v_pk_mul_f32 v[150:151], v[150:151], v[116:117]\n"
    "v_cvt_pk_bf16_f32 v88, v144, v145\n"
    "v_cvt_pk_bf16_f32 v89, v146, v147\n"
    "v_cvt_pk_bf16_f32 v90, v148, v149\n"
    "buffer_load_dwordx4 v9, s[52:55], s76 offen offset:3072 lds\n"
    "v_cvt_pk_bf16_f32 v91, v150, v151\n"
    "buffer_store_dwordx4 v[88:91], v120, s[48:51], s92 offen offset:0 sc1\n"
    "ds_read_b128 v[240:243], v115 offset:12288\n"
    "ds_read_b128 v[244:247], v115 offset:13312\n"
    "s_waitcnt lgkmcnt(7)\n"
    "v_pk_add_f32 v[152:153], v[152:153], v[216:217]\n"
    "s_add_u32 s76, s76, 0x10000\n"
    "v_pk_add_f32 v[154:155], v[154:155], v[218:219]\n"
    "s_waitcnt lgkmcnt(6)\n"
    "v_pk_add_f32 v[156:157], v[156:157], v[220:221]\n"
    "v_pk_add_f32 v[158:159], v[158:159], v[222:223]\n"
    "v_pk_mul_f32 v[152:153], v[152:153], v[116:117]\n"
    "v_pk_mul_f32 v[154:155], v[154:155], v[116:117]\n"
    "v_pk_mul_f32 v[156:157], v[156:157], v[116:117]\n"
    "v_pk_mul_f32 v[158:159], v[158:159], v[116:117]\n"
    "v_cvt_pk_bf16_f32 v92, v152, v153\n"
    "v_cvt_pk_bf16_f32 v93, v154, v155\n"
    "v_cvt_pk_bf16_f32 v94, v156, v157\n"
    "v_cvt_pk_bf16_f32 v95, v158, v159\n"
    "buffer_store_dwordx4 v[92:95], v120, s[48:51], s92 offen offset:2048 sc1\n"
    "ds_read_b128 v[248:251], v115 offset:14336\n"
    "ds_read_b128 v[252:255], v115 offset:15360\n"
    "s_waitcnt lgkmcnt(7)\n"
    "v_pk_add_f32 v[160:161], v[160:161], v[224:225]\n"
    "v_pk_add_f32 v[162:163], v[162:163], v[226:227]\n"
    "s_waitcnt lgkmcnt(6)\n"
    "v_pk_add_f32 v[164:165], v[164:165], v[228:229]\n"
    "v_pk_add_f32 v[166:167], v[166:167], v[230:231]\n"
    "v_pk_mul_f32 v[160:161], v[160:161], v[64:65]\n"
    "v_pk_mul_f32 v[162:163], v[162:163], v[64:65]\n"
    "v_pk_mul_f32 v[164:165], v[164:165], v[64:65]\n"
    "v_pk_mul_f32 v[166:167], v[166:167], v[64:65]\n"
    "v_cvt_pk_bf16_f32 v96, v160, v161\n"
    "v_cvt_pk_bf16_f32 v97, v162, v163\n"
    "v_cvt_pk_bf16_f32 v98, v164, v165\n"
    "v_cvt_pk_bf16_f32 v99, v166, v167\n"
    "buffer_store_dwordx4 v[96:99], v8, s[48:51], s92 offen offset:1024 sc1\n"
    "s_waitcnt lgkmcnt(5)\n"
    "v_pk_add_f32 v[168:169], v[168:169], v[232:233]\n"
    "v_pk_add_f32 v[170:171], v[170:171], v[234:235]\n"
    "s_waitcnt lgkmcnt(4)\n"
    "v_pk_add_f32 v[172:173], v[172:173], v[236:237]\n"
    "v_pk_add_f32 v[174:175], v[174:175], v[238:239]\n"
    "v_pk_mul_f32 v[168:169], v[168:169], v[64:65]\n"
    "v_pk_mul_f32 v[170:171], v[170:171], v[64:65]\n"
    "v_pk_mul_f32 v[172:173], v[172:173], v[64:65]\n"
    "v_pk_mul_f32 v[174:175], v[174:175], v[64:65]\n"
    "v_cvt_pk_bf16_f32 v100, v168, v169\n"
    "v_cvt_pk_bf16_f32 v101, v170, v171\n"
    "v_cvt_pk_bf16_f32 v102, v172, v173\n"
    "v_cvt_pk_bf16_f32 v103, v174, v175\n"
    "buffer_store_dwordx4 v[100:103], v8, s[48:51], s92 offen offset:3072 sc1\n"
    "s_waitcnt lgkmcnt(3)\n"
    "v_pk_add_f32 v[176:177], v[176:177], v[240:241]\n"
    "v_pk_add_f32 v[178:179], v[178:179], v[242:243]\n"
    "s_waitcnt lgkmcnt(2)\n"
    "v_pk_add_f32 v[180:181], v[180:181], v[244:245]\n"
    "v_pk_add_f32 v[182:183], v[182:183], v[246:247]\n"
    "v_pk_mul_f32 v[176:177], v[176:177], v[64:65]\n"
    "v_pk_mul_f32 v[178:179], v[178:179], v[64:65]\n"
    "v_pk_mul_f32 v[180:181], v[180:181], v[64:65]\n"
    "v_pk_mul_f32 v[182:183], v[182:183], v[64:65]\n"
    "v_cvt_pk_bf16_f32 v104, v176, v177\n"
    "v_cvt_pk_bf16_f32 v105, v178, v179\n"
    "v_cvt_pk_bf16_f32 v106, v180, v181\n"
    "v_cvt_pk_bf16_f32 v107, v182, v183\n"
    "buffer_store_dwordx4 v[104:107], v120, s[48:51], s92 offen offset:1024 sc1\n"
    "s_waitcnt lgkmcnt(1)\n"
    "v_pk_add_f32 v[184:185], v[184:185], v[248:249]\n"
    "v_pk_add_f32 v[186:187], v[186:187], v[250:251]\n"
    "s_waitcnt lgkmcnt(0)\n"
    "v_pk_add_f32 v[188:189], v[188:189], v[252:253]\n"
    "v_pk_add_f32 v[190:191], v[190:191], v[254:255]\n"
    "v_pk_mul_f32 v[184:185], v[184:185], v[64:65]\n"
    "v_pk_mul_f32 v[186:187], v[186:187], v[64:65]\n"
    "v_pk_mul_f32 v[188:189], v[188:189], v[64:65]\n"
    "v_pk_mul_f32 v[190:191], v[190:191], v[64:65]\n"
    "v_cvt_pk_bf16_f32 v108, v184, v185\n"
    "v_cvt_pk_bf16_f32 v109, v186, v187\n"
    "v_cvt_pk_bf16_f32 v110, v188, v189\n"
    "v_cvt_pk_bf16_f32 v111, v190, v191\n"
    "buffer_store_dwordx4 v[108:111], v120, s[48:51], s92 offen offset:3072 sc1\n"
    "v_add_f32 v126, v126, v127\n"
    "v_and_b32 v127, 0xfffffcff, v122\n"
    "v_mfma_f32_16x16x32_bf16 v[68:71], a[32:35], v[104:107], 0\n"
    "v_mfma_f32_16x16x32_bf16 v[68:71], a[32:35], v[108:111], v[68:71]\n"
    "v_mfma_f32_16x16x32_bf16 v[68:71], a[32:35], v[80:83], v[68:71]\n"
    "v_mfma_f32_16x16x32_bf16 v[68:71], a[32:35], v[84:87], v[68:71]\n"
    "v_mfma_f32_16x16x32_bf16 v[68:71], a[32:35], v[88:91], v[68:71]\n"
    "v_mfma_f32_16x16x32_bf16 v[68:71], a[32:35], v[92:95], v[68:71]\n"
    "v_mfma_f32_16x16x32_bf16 v[68:71], a[32:35], v[96:99], v[68:71]\n"
    "v_mfma_f32_16x16x32_bf16 v[68:71], a[32:35], v[100:103], v[68:71]\n"
    "s_nop 7\n"
    "v_fma_f32 v126, v68, 0, v126\n"
    "ds_write_b32 v122, v126 offset:2048\n"
    "s_branch .Lat_v1_%=\n"
    ".Lat_epi1_%=:\n"
    "v_mfma_f32_16x16x32_bf16 v[192:195], a[96:99], v[104:107], v[192:195]\n"
    "v_exp_f32 v44, v44\n"
    "v_exp_f32 v45, v45\n"
    "v_mfma_f32_16x16x32_bf16 v[196:199], a[100:103], v[104:107], v[196:199]\n"
    "ds_write_b128 v114, v[128:131] offset:0\n"
    "v_exp_f32 v46, v46\n"
    "v_mfma_f32_16x16x32_bf16 v[200:203], a[104:107], v[104:107], v[200:203]\n"
    "ds_write_b128 v114, v[132:135] offset:1024\n"
    "v_exp_f32 v47, v47\n"
    "v_mfma_f32_16x16x32_bf16 v[204:207], a[108:111], v[104:107], v[204:207]\n"
    "ds_write_b128 v114, v[136:139] offset:2048\n"
    "v_cvt_pk_bf16_f32 v109, v42, v43\n"
    "v_mfma_f32_16x16x32_bf16 v[208:211], a[112:115], v[104:107], v[208:211]\n"
    "ds_write_b128 v114, v[140:143] offset:3072\n"
    "v_cvt_pk_bf16_f32 v110, v44, v45\n"
    "v_mfma_f32_16x16x32_bf16 v[212:215], a[116:119], v[104:107], v[212:215]\n"
    "ds_write_b128 v114, v[144:147] offset:4096\n"
    "v_cvt_pk_bf16_f32 v111, v46, v47\n"
    "v_mfma_f32_16x16x32_bf16 v[216:219], a[120:123], v[104:107], v[216:219]\n"
    "ds_write_b128 v114, v[148:151] offset:5120\n"
    "s_xor_b32 s93, s68, 2\n"
    "s_lshl_b32 s73, s93, 15\n"
    "s_add_u32 s73, s73, 0x4000\n"
    "s_cmp_eq_u32 s93, 1\n"
    "s_cselect_b32 s73, 0, s73\n"
    "s_xor_b32 s73, s73, s86\n"
    "s_cmp_eq_u32 s93, 0\n"
    "v_mfma_f32_16x16x32_bf16 v[220:223], a[124:127], v[104:107], v[220:223]\n"
    "ds_write_b128 v114, v[152:155] offset:6144\n"
    "s_cselect_b32 s73, 0x20000, s73\n"
    "s_add_u32 s73, s73, s69\n"
    "v_add_u32 v115, s73, v8\n"
    "s_sub_u32 s73, 3, s72\n"
    "s_cmp_eq_u32 s71, 0\n"
    "s_cselect_b32 s73, s72, s73\n"
    "v_mfma_f32_16x16x32_bf16 v[88:91], a[32:35], v[104:107], v[88:91]\n"
    "ds_write_b128 v114, v[156:159] offset:7168\n"
    "s_lshl_b32 s74, s39, 2\n"
    "s_add_u32 s73, s73, s74\n"
    "s_lshl_b32 s73, s73, 18\n"
    "s_lshl_b32 s74, s66, 13\n"
    "s_add_u32 s92, s73, s74\n"
    "s_mov_b64 s[60:61], %9\n"
    "s_add_u32 s60, s60, 0x2fffc00\n"
    "s_addc_u32 s61, s61, 0\n"
    "s_mov_b32 s62, 0x1700400\n"
    "s_mov_b32 s63, 0x20000\n"
    "v_mfma_f32_16x16x32_bf16 v[224:227], a[96:99], v[108:111], v[224:227]\n"
    "ds_write_b128 v114, v[160:163] offset:8192\n"
    "s_mov_b64 s[52:53], %8\n"
    "s_mov_b32 s54, 0x1c0000\n"
    "s_mov_b32 s55, 0x20000\n"
    "s_and_b32 s73, s83, 1\n"
    "s_lshl_b32 s73, s73, 10\n"
    "v_add_u32 v3, 0x400, v8\n"
    "v_mfma_f32_16x16x32_bf16 v[228:231], a[100:103], v[108:111], v[228:231]\n"
    "ds_write_b128 v114, v[164:167] offset:9216\n"
    "v_add_u32 v2, s73, v3\n"
    "v_subrev_u32 v3, s73, v3\n"
    "s_mul_i32 s75, s80, 0x1c000\n"
    "s_mul_i32 s73, s83, 0x7000\n"
    "s_add_u32 s75, s75, s73\n"
    "s_add_u32 s75, s75, 0x1000000\n"
    "v_mfma_f32_16x16x32_bf16 v[232:235], a[104:107], v[108:111], v[232:235]\n"
    "ds_write_b128 v114, v[168:171] offset:10240\n"
    "s_mul_i32 s76, s83, 0x70000\n"
    "s_lshl_b32 s73, s71, 15\n"
    "s_add_u32 s76, s76, s73\n"
    "s_lshl_b32 s73, s79, 8\n"
    "s_add_u32 s76, s76, s73\n"
    "s_lshl_b32 s73, s72, 7\n"
    "v_mfma_f32_16x16x32_bf16 v[236:239], a[108:111], v[108:111], v[236:239]\n"
    "ds_write_b128 v114, v[172:175] offset:11264\n"
    "s_add_u32 s76, s76, s73\n"
    "v_and_b32 v112, 0x1c0, v8\n"
    "v_lshlrev_b32 v121, 4, v112\n"
    "v_lshrrev_b32 v112, 3, v8\n"
    "v_and_b32 v112, 0x60, v112\n"
    "v_and_b32 v113, 0x30, v8\n"
    "v_xor_b32 v112, v112, v113\n"
    "v_mfma_f32_16x16x32_bf16 v[240:243], a[112:115], v[108:111], v[240:243]\n"
    "ds_write_b128 v114, v[176:179] offset:12288\n"
    "v_add_u32 v121, v121, v112\n"
    "v_add_u32 v4, 0x1c00, v121\n"
    "v_add_u32 v10, 0x1c00, v4\n"
    "v_add_u32 v9, 0x1c00, v10\n"
    "s_xor_b32 s74, s86, 0x10000\n"
    "s_lshl_b32 s73, s71, 13\n"
    "v_mfma_f32_16x16x32_bf16 v[244:247], a[116:119], v[108:111], v[244:247]\n"
    "ds_write_b128 v114, v[180:183] offset:13312\n"
    "s_add_u32 s73, s73, s69\n"
    "v_add_u32 v28, s73, v8\n"
    "v_add_u32 v16, s86, v28\n"
    "v_add_u32 v17, s74, v28\n"
    "v_add_u32 v18, 0x20000, v28\n"
    "v_and_b32 v28, 0x1c0, v8\n"
    "v_mfma_f32_16x16x32_bf16 v[248:251], a[120:123], v[108:111], v[248:251]\n"
    "ds_write_b128 v114, v[184:187] offset:14336\n"
    "v_lshrrev_b32 v29, 3, v8\n"
    "v_and_b32 v29, 0x20, v29\n"
    "v_add_u32 v28, v28, v29\n"
    "v_lshrrev_b32 v29, 1, v8\n"
    "v_and_b32 v29, 0x18, v29\n"
    "v_add_u32 v28, v28, v29\n"
    "v_mfma_f32_16x16x32_bf16 v[252:255], a[124:127], v[108:111], v[252:255]\n"
    "ds_write_b128 v114, v[188:191] offset:15360\n"
    "s_lshl_b32 s73, s72, 12\n"
    "s_add_u32 s73, s73, s69\n"
    "v_add_u32 v28, s73, v28\n"
    "v_and_b32 v29, 0x200, v8\n"
    "v_lshlrev_b32 v29, 1, v29\n"
    "v_add_u32 v28, v28, v29\n"
    "v_mfma_f32_16x16x32_bf16 v[92:95], a[32:35], v[108:111], v[92:95]\n"
    "ds_write2st64_b32 v122, v80, v84 offset0:0 offset1:12\n"
    "v_add_u32 v19, s86, v28\n"
    "v_xor_b32 v20, 32, v19\n"
    "v_add_u32 v21, s74, v28\n"
    "v_xor_b32 v22, 32, v21\n"
    "v_add_u32 v23, 0x20000, v28\n"
    "v_xor_b32 v24, 32, v23\n"
    "s_waitcnt vmcnt(0)\n"
    "s_xor_b32 s73, s86, 0x8000\n"
    "s_add_u32 m0, s73, s88\n"
    "s_nop 0\n"
    "buffer_load_dwordx4 v2, s[60:63], s75 offen offset:0 sc1 lds\n"
    "buffer_load_dwordx4 v3, s[60:63], s75 offen offset:1024 sc1 lds\n"
    "buffer_load_dwordx4 v2, s[60:63], s75 offen offset:2048 sc1 lds\n"
    "buffer_load_dwordx4 v3, s[60:63], s75 offen offset:3072 sc1 lds\n"
    "s_xor_b32 s73, s86, 0xc000\n"
    "s_add_u32 m0, s73, s88\n"
    "s_add_u32 s75, s75, 0x1000\n"
    "buffer_load_dwordx4 v121, s[52:55], s76 offen offset:0 lds\n"
    "buffer_load_dwordx4 v4, s[52:55], s76 offen offset:1024 lds\n"
    "buffer_load_dwordx4 v10, s[52:55], s76 offen offset:2048 lds\n"
    "buffer_load_dwordx4 v9, s[52:55], s76 offen offset:3072 lds\n"
    "s_add_u32 s76, s76, 0x10000\n"
    "s_waitcnt lgkmcnt(0)\n"
    "s_barrier\n"
    "ds_read2st64_b32 v[126:127], v123 offset0:0 offset1:12\n"
    "ds_read_b128 v[128:131], v115 offset:0\n"
    "ds_read_b128 v[132:135], v115 offset:1024\n"
    "ds_read_b128 v[136:139], v115 offset:2048\n"
    "ds_read_b128 v[140:143], v115 offset:3072\n"
    "ds_read_b128 v[144:147], v115 offset:4096\n"
    "ds_read_b128 v[148:151], v115 offset:5120\n"
    "s_waitcnt lgkmcnt(6)\n"
    "v_add_f32 v126, v88, v126\n"
    "v_add_f32 v127, v92, v127\n"
    "v_rcp_f32 v116, v126\n"
    "v_rcp_f32 v117, v126\n"
    "s_xor_b32 s73, s86, 0x10000\n"
    "s_add_u32 m0, s73, s88\n"
    "s_nop 0\n"
    "v_rcp_f32 v64, v127\n"
    "v_rcp_f32 v65, v127\n"
    "ds_read_b128 v[152:155], v115 offset:6144\n"
    "ds_read_b128 v[156:159], v115 offset:7168\n"
    "s_waitcnt lgkmcnt(7)\n"
    "v_pk_add_f32 v[192:193], v[192:193], v[128:129]\n"
    "buffer_load_dwordx4 v2, s[60:63], s75 offen offset:0 sc1 lds\n"
    "v_pk_add_f32 v[194:195], v[194:195], v[130:131]\n"
    "s_waitcnt lgkmcnt(6)\n"
    "v_pk_add_f32 v[196:197], v[196:197], v[132:133]\n"
    "v_pk_add_f32 v[198:199], v[198:199], v[134:135]\n"
    "v_pk_mul_f32 v[192:193], v[192:193], v[116:117]\n"
    "v_pk_mul_f32 v[194:195], v[194:195], v[116:117]\n"
    "buffer_load_dwordx4 v3, s[60:63], s75 offen offset:1024 sc1 lds\n"
    "v_pk_mul_f32 v[196:197], v[196:197], v[116:117]\n"
    "v_pk_mul_f32 v[198:199], v[198:199], v[116:117]\n"
    "v_cvt_pk_bf16_f32 v80, v192, v193\n"
    "v_cvt_pk_bf16_f32 v81, v194, v195\n"
    "v_cvt_pk_bf16_f32 v82, v196, v197\n"
    "buffer_load_dwordx4 v2, s[60:63], s75 offen offset:2048 sc1 lds\n"
    "v_cvt_pk_bf16_f32 v83, v198, v199\n"
    "buffer_store_dwordx4 v[80:83], v8, s[48:51], s92 offen offset:0 sc1\n"
    "ds_read_b128 v[160:163], v115 offset:8192\n"
    "ds_read_b128 v[164:167], v115 offset:9216\n"
    "s_waitcnt lgkmcnt(7)\n"
    "v_pk_add_f32 v[200:201], v[200:201], v[136:137]\n"
    "buffer_load_dwordx4 v3, s[60:63], s75 offen offset:3072 sc1 lds\n"
    "v_pk_add_f32 v[202:203], v[202:203], v[138:139]\n"
    "s_waitcnt lgkmcnt(6)\n"
    "v_pk_add_f32 v[204:205], v[204:205], v[140:141]\n"
    "v_pk_add_f32 v[206:207], v[206:207], v[142:143]\n"
    "v_pk_mul_f32 v[200:201], v[200:201], v[116:117]\n"
    "v_pk_mul_f32 v[202:203], v[202:203], v[116:117]\n"
    "s_xor_b32 s73, s86, 0x4000\n"
    "s_add_u32 m0, s73, s88\n"
    "s_add_u32 s75, s75, 0x1000\n"
    "v_pk_mul_f32 v[204:205], v[204:205], v[116:117]\n"
    "v_pk_mul_f32 v[206:207], v[206:207], v[116:117]\n"
    "v_cvt_pk_bf16_f32 v84, v200, v201\n"
    "v_cvt_pk_bf16_f32 v85, v202, v203\n"
    "v_cvt_pk_bf16_f32 v86, v204, v205\n"
    "buffer_load_dwordx4 v121, s[52:55], s76 offen offset:0 lds\n"
    "v_cvt_pk_bf16_f32 v87, v206, v207\n"
    "buffer_store_dwordx4 v[84:87], v8, s[48:51], s92 offen offset:2048 sc1\n"
    "ds_read_b128 v[168:171], v115 offset:10240\n"
    "ds_read_b128 v[172:175], v115 offset:11264\n"
    "s_waitcnt lgkmcnt(7)\n"
    "v_pk_add_f32 v[208:209], v[208:209], v[144:145]\n"
    "buffer_load_dwordx4 v4, s[52:55], s76 offen offset:1024 lds\n"
    "v_pk_add_f32 v[210:211], v[210:211], v[146:147]\n"
    "s_waitcnt lgkmcnt(6)\n"
    "v_pk_add_f32 v[212:213], v[212:213], v[148:149]\n"
    "v_pk_add_f32 v[214:215], v[214:215], v[150:151]\n"
    "v_pk_mul_f32 v[208:209], v[208:209], v[116:117]\n"
    "v_pk_mul_f32 v[210:211], v[210:211], v[116:117]\n"
    "buffer_load_dwordx4 v10, s[52:55], s76 offen offset:2048 lds\n"
    "v_pk_mul_f32 v[212:213], v[212:213], v[116:117]\n"
    "v_pk_mul_f32 v[214:215], v[214:215], v[116:117]\n"
    "v_cvt_pk_bf16_f32 v88, v208, v209\n"
    "v_cvt_pk_bf16_f32 v89, v210, v211\n"
    "v_cvt_pk_bf16_f32 v90, v212, v213\n"
    "buffer_load_dwordx4 v9, s[52:55], s76 offen offset:3072 lds\n"
    "v_cvt_pk_bf16_f32 v91, v214, v215\n"
    "buffer_store_dwordx4 v[88:91], v120, s[48:51], s92 offen offset:0 sc1\n"
    "ds_read_b128 v[176:179], v115 offset:12288\n"
    "ds_read_b128 v[180:183], v115 offset:13312\n"
    "s_waitcnt lgkmcnt(7)\n"
    "v_pk_add_f32 v[216:217], v[216:217], v[152:153]\n"
    "s_add_u32 s76, s76, 0x10000\n"
    "v_pk_add_f32 v[218:219], v[218:219], v[154:155]\n"
    "s_waitcnt lgkmcnt(6)\n"
    "v_pk_add_f32 v[220:221], v[220:221], v[156:157]\n"
    "v_pk_add_f32 v[222:223], v[222:223], v[158:159]\n"
    "v_pk_mul_f32 v[216:217], v[216:217], v[116:117]\n"
    "v_pk_mul_f32 v[218:219], v[218:219], v[116:117]\n"
    "v_pk_mul_f32 v[220:221], v[220:221], v[116:117]\n"
    "v_pk_mul_f32 v[222:223], v[222:223], v[116:117]\n"
    "v_cvt_pk_bf16_f32 v92, v216, v217\n"
    "v_cvt_pk_bf16_f32 v93, v218, v219\n"
    "v_cvt_pk_bf16_f32 v94, v220, v221\n"
    "v_cvt_pk_bf16_f32 v95, v222, v223\n"
    "buffer_store_dwordx4 v[92:95], v120, s[48:51], s92 offen offset:2048 sc1\n"
    "ds_read_b128 v[184:187], v115 offset:14336\n"
    "ds_read_b128 v[188:191], v115 offset:15360\n"
    "s_waitcnt lgkmcnt(7)\n"
    "v_pk_add_f32 v[224:225], v[224:225], v[160:161]\n"
    "v_pk_add_f32 v[226:227], v[226:227], v[162:163]\n"
    "s_waitcnt lgkmcnt(6)\n"
    "v_pk_add_f32 v[228:229], v[228:229], v[164:165]\n"
    "v_pk_add_f32 v[230:231], v[230:231], v[166:167]\n"
    "v_pk_mul_f32 v[224:225], v[224:225], v[64:65]\n"
    "v_pk_mul_f32 v[226:227], v[226:227], v[64:65]\n"
    "v_pk_mul_f32 v[228:229], v[228:229], v[64:65]\n"
    "v_pk_mul_f32 v[230:231], v[230:231], v[64:65]\n"
    "v_cvt_pk_bf16_f32 v96, v224, v225\n"
    "v_cvt_pk_bf16_f32 v97, v226, v227\n"
    "v_cvt_pk_bf16_f32 v98, v228, v229\n"
    "v_cvt_pk_bf16_f32 v99, v230, v231\n"
    "buffer_store_dwordx4 v[96:99], v8, s[48:51], s92 offen offset:1024 sc1\n"
    "s_waitcnt lgkmcnt(5)\n"
    "v_pk_add_f32 v[232:233], v[232:233], v[168:169]\n"
    "v_pk_add_f32 v[234:235], v[234:235], v[170:171]\n"
    "s_waitcnt lgkmcnt(4)\n"
    "v_pk_add_f32 v[236:237], v[236:237], v[172:173]\n"
    "v_pk_add_f32 v[238:239], v[238:239], v[174:175]\n"
    "v_pk_mul_f32 v[232:233], v[232:233], v[64:65]\n"
    "v_pk_mul_f32 v[234:235], v[234:235], v[64:65]\n"
    "v_pk_mul_f32 v[236:237], v[236:237], v[64:65]\n"
    "v_pk_mul_f32 v[238:239], v[238:239], v[64:65]\n"
    "v_cvt_pk_bf16_f32 v100, v232, v233\n"
    "v_cvt_pk_bf16_f32 v101, v234, v235\n"
    "v_cvt_pk_bf16_f32 v102, v236, v237\n"
    "v_cvt_pk_bf16_f32 v103, v238, v239\n"
    "buffer_store_dwordx4 v[100:103], v8, s[48:51], s92 offen offset:3072 sc1\n"
    "s_waitcnt lgkmcnt(3)\n"
    "v_pk_add_f32 v[240:241], v[240:241], v[176:177]\n"
    "v_pk_add_f32 v[242:243], v[242:243], v[178:179]\n"
    "s_waitcnt lgkmcnt(2)\n"
    "v_pk_add_f32 v[244:245], v[244:245], v[180:181]\n"
    "v_pk_add_f32 v[246:247], v[246:247], v[182:183]\n"
    "v_pk_mul_f32 v[240:241], v[240:241], v[64:65]\n"
    "v_pk_mul_f32 v[242:243], v[242:243], v[64:65]\n"
    "v_pk_mul_f32 v[244:245], v[244:245], v[64:65]\n"
    "v_pk_mul_f32 v[246:247], v[246:247], v[64:65]\n"
    "v_cvt_pk_bf16_f32 v104, v240, v241\n"
    "v_cvt_pk_bf16_f32 v105, v242, v243\n"
    "v_cvt_pk_bf16_f32 v106, v244, v245\n"
    "v_cvt_pk_bf16_f32 v107, v246, v247\n"
    "buffer_store_dwordx4 v[104:107], v120, s[48:51], s92 offen offset:1024 sc1\n"
    "s_waitcnt lgkmcnt(1)\n"
    "v_pk_add_f32 v[248:249], v[248:249], v[184:185]\n"
    "v_pk_add_f32 v[250:251], v[250:251], v[186:187]\n"
    "s_waitcnt lgkmcnt(0)\n"
    "v_pk_add_f32 v[252:253], v[252:253], v[188:189]\n"
    "v_pk_add_f32 v[254:255], v[254:255], v[190:191]\n"
    "v_pk_mul_f32 v[248:249], v[248:249], v[64:65]\n"
    "v_pk_mul_f32 v[250:251], v[250:251], v[64:65]\n"
    "v_pk_mul_f32 v[252:253], v[252:253], v[64:65]\n"
    "v_pk_mul_f32 v[254:255], v[254:255], v[64:65]\n"
    "v_cvt_pk_bf16_f32 v108, v248, v249\n"
    "v_cvt_pk_bf16_f32 v109, v250, v251\n"
    "v_cvt_pk_bf16_f32 v110, v252, v253\n"
    "v_cvt_pk_bf16_f32 v111, v254, v255\n"
    "buffer_store_dwordx4 v[108:111], v120, s[48:51], s92 offen offset:3072 sc1\n"
    "v_add_f32 v126, v126, v127\n"
    "v_and_b32 v127, 0xfffffcff, v122\n"
    "v_mfma_f32_16x16x32_bf16 v[68:71], a[32:35], v[104:107], 0\n"
    "v_mfma_f32_16x16x32_bf16 v[68:71], a[32:35], v[108:111], v[68:71]\n"
    "v_mfma_f32_16x16x32_bf16 v[68:71], a[32:35], v[80:83], v[68:71]\n"
    "v_mfma_f32_16x16x32_bf16 v[68:71], a[32:35], v[84:87], v[68:71]\n"
    "v_mfma_f32_16x16x32_bf16 v[68:71], a[32:35], v[88:91], v[68:71]\n"
    "v_mfma_f32_16x16x32_bf16 v[68:71], a[32:35], v[92:95], v[68:71]\n"
    "v_mfma_f32_16x16x32_bf16 v[68:71], a[32:35], v[96:99], v[68:71]\n"
    "v_mfma_f32_16x16x32_bf16 v[68:71], a[32:35], v[100:103], v[68:71]\n"
    "s_nop 7\n"
    "v_fma_f32 v126, v68, 0, v126\n"
    "ds_write_b32 v122, v126 offset:2048\n"
    ".Lat_v1_%=:\n"
    "s_waitcnt vmcnt(16) lgkmcnt(0)\n"
    "s_barrier\n"
    "ds_read2st64_b32 v[116:117], v127 offset0:8 offset1:9\n"
    "ds_read2st64_b32 v[118:119], v127 offset0:10 offset1:11\n"
    "ds_read_b128 a[128:131], v16 offset:32768\n"
    "ds_read_b128 a[132:135], v16 offset:33792\n"
    "ds_read_b128 a[136:139], v16 offset:36864\n"
    "ds_read_b128 a[140:143], v16 offset:37888\n"
    "ds_read_b64_tr_b16 a[160:161], v19 offset:49152\n"
    "ds_read_b64_tr_b16 a[162:163], v19 offset:51200\n"
    "ds_read_b64_tr_b16 a[164:165], v20 offset:49152\n"
    "ds_read_b64_tr_b16 a[166:167], v20 offset:51200\n"
    "ds_read_b64_tr_b16 a[168:169], v19 offset:49664\n"
    "ds_read_b64_tr_b16 a[170:171], v19 offset:51712\n"
    "ds_read_b64_tr_b16 a[172:173], v20 offset:49664\n"
    "ds_read_b64_tr_b16 a[174:175], v20 offset:51712\n"
    "ds_read_b128 a[144:147], v16 offset:34816\n"
    "ds_read_b128 a[148:151], v16 offset:35840\n"
    "ds_read_b128 a[152:155], v16 offset:38912\n"
    "ds_read_b128 a[156:159], v16 offset:39936\n"
    "ds_read_b64_tr_b16 a[176:177], v19 offset:57344\n"
    "ds_read_b64_tr_b16 a[178:179], v19 offset:59392\n"
    "ds_read_b64_tr_b16 a[180:181], v20 offset:57344\n"
    "ds_read_b64_tr_b16 a[182:183], v20 offset:59392\n"
    "ds_read_b64_tr_b16 a[184:185], v19 offset:57856\n"
    "ds_read_b64_tr_b16 a[186:187], v19 offset:59904\n"
    "ds_read_b64_tr_b16 a[188:189], v20 offset:57856\n"
    "ds_read_b64_tr_b16 a[190:191], v20 offset:59904\n"
    "s_waitcnt lgkmcnt(15)\n"
    "v_pk_add_f32 v[118:119], v[118:119], v[116:117]\n"
    "v_add_f32 v126, v118, v119\n"
    "v_cmp_ngt_f32 vcc, 0x67800000, v126\n"
    "s_nop 4\n"
    "s_cbranch_vccnz .Lat_redo1_%=\n"
    ".Lat_ok1_%=:\n"
    "s_lshr_b32 s85, s92, 10\n"
    "s_add_u32 s85, s85, 0x5000\n"
    "v_mov_b32 v36, s64\n"
    "v_mov_b32 v37, s65\n"
    "v_mov_b32 v34, s85\n"
    "s_mov_b64 s[92:93], %5\n"
    "s_mov_b32 s94, 0x400000\n"
    "s_mov_b32 s95, 0x20000\n"
    "v_and_b32 v5, 0x30, v8\n"
    "s_lshl_b32 s73, s80, 5\n"
    "s_lshl_b32 s74, s83, 3\n"
    "s_add_u32 s74, s74, s77\n"
    "s_add_u32 s73, s73, s74\n"
    "s_lshl_b32 s90, s73, 13\n"
    "s_lshl_b32 s91, s74, 17\n"
    "s_lshl_b32 s73, s73, 3\n"
    "s_add_u32 s73, s73, 0x5000\n"
    "v_add_u32 v5, s73, v5\n"
    "buffer_load_dwordx2 v[6:7], v5, s[56:59], 0 offen sc1\n"
    "s_and_b32 s96, s76, 0xffff\n"
    "s_add_u32 s91, s91, s96\n"
    "s_waitcnt lgkmcnt(0)\n"
    "v_mfma_f32_16x16x32_bf16 v[144:147], a[160:163], a[132:135], 0\n"
    "s_waitcnt vmcnt(7)\n"
    "s_barrier\n"
    "v_mfma_f32_16x16x32_bf16 v[144:147], a[176:179], a[148:151], v[144:147]\n"
    "ds_read_b128 a[64:67], v17 offset:0\n"
    "ds_read_b128 a[68:71], v17 offset:1024\n"
    "v_mfma_f32_16x16x32_bf16 v[148:151], a[164:167], a[132:135], 0\n"
    "ds_read_b128 a[72:75], v17 offset:4096\n"
    "ds_read_b128 a[76:79], v17 offset:5120\n"
    "s_xor_b32 s73, s86, 0x18000\n"
    "s_add_u32 m0, s73, s88\n"
    "v_mfma_f32_16x16x32_bf16 v[148:151], a[180:183], a[148:151], v[148:151]\n"
    "buffer_load_dwordx4 v2, s[60:63], s75 offen offset:0 sc1 lds\n"
    "v_mfma_f32_16x16x32_bf16 v[152:155], a[168:171], a[132:135], 0\n"
    "ds_read_b64_tr_b16 a[96:97], v19 offset:16384\n"
    "ds_read_b64_tr_b16 a[98:99], v19 offset:18432\n"
    "v_mfma_f32_16x16x32_bf16 v[152:155], a[184:187], a[148:151], v[152:155]\n"
    "buffer_load_dwordx4 v3, s[60:63], s75 offen offset:1024 sc1 lds\n"
    "v_mfma_f32_16x16x32_bf16 v[156:159], a[172:175], a[132:135], 0\n"
    "ds_read_b64_tr_b16 a[100:101], v20 offset:16384\n"
    "ds_read_b64_tr_b16 a[102:103], v20 offset:18432\n"
    "v_mfma_f32_16x16x32_bf16 v[156:159], a[188:191], a[148:151], v[156:159]\n"
    "buffer_load_dwordx4 v2, s[60:63], s75 offen offset:2048 sc1 lds\n"
    "v_mfma_f32_16x16x32_bf16 v[160:163], a[160:163], a[136:139], 0\n"
    "ds_read_b64_tr_b16 a[104:105], v19 offset:16896\n"
    "ds_read_b64_tr_b16 a[106:107], v19 offset:18944\n"
    "v_mfma_f32_16x16x32_bf16 v[160:163], a[176:179], a[152:155], v[160:163]\n"
    "buffer_load_dwordx4 v3, s[60:63], s75 offen offset:3072 sc1 lds\n"
    "v_mfma_f32_16x16x32_bf16 v[164:167], a[164:167], a[136:139], 0\n"
    "ds_read_b64_tr_b16 a[108:109], v20 offset:16896\n"
    "ds_read_b64_tr_b16 a[110:111], v20 offset:18944\n"
    "s_xor_b32 s73, s86, 0x14000\n"
    "s_add_u32 m0, s73, s88\n"
    "s_add_u32 s75, s75, 0x1000\n"
    "v_mfma_f32_16x16x32_bf16 v[164:167], a[180:183], a[152:155], v[164:167]\n"
    "buffer_load_dwordx4 v121, s[52:55], s76 offen offset:0 lds\n"
    "v_mfma_f32_16x16x32_bf16 v[168:171], a[168:171], a[136:139], 0\n"
    "ds_read_b128 a[80:83], v17 offset:2048\n"
    "ds_read_b128 a[84:87], v17 offset:3072\n"
    "v_mfma_f32_16x16x32_bf16 v[168:171], a[184:187], a[152:155], v[168:171]\n"
    "buffer_load_dwordx4 v4, s[52:55], s76 offen offset:1024 lds\n"
    "v_mfma_f32_16x16x32_bf16 v[172:175], a[172:175], a[136:139], 0\n"
    "ds_read_b128 a[88:91], v17 offset:6144\n"
    "ds_read_b128 a[92:95], v17 offset:7168\n"
    "v_mfma_f32_16x16x32_bf16 v[172:175], a[188:191], a[152:155], v[172:175]\n"
    "buffer_load_dwordx4 v10, s[52:55], s76 offen offset:2048 lds\n"
    "v_mfma_f32_16x16x32_bf16 v[176:179], a[160:163], a[140:143], 0\n"
    "ds_read_b64_tr_b16 a[112:113], v19 offset:24576\n"
    "ds_read_b64_tr_b16 a[114:115], v19 offset:26624\n"
    "v_mfma_f32_16x16x32_bf16 v[176:179], a[176:179], a[156:159], v[176:179]\n"
    "buffer_load_dwordx4 v9, s[52:55], s76 offen offset:3072 lds\n"
    "v_mfma_f32_16x16x32_bf16 v[180:183], a[164:167], a[140:143], 0\n"
    "ds_read_b64_tr_b16 a[116:117], v20 offset:24576\n"
    "ds_read_b64_tr_b16 a[118:119], v20 offset:26624\n"
    "s_add_u32 s76, s76, 0x10000\n"
    "s_xor_b32 s73, s86, 0x0\n"
    "s_add_u32 m0, s73, s88\n"
    "v_mfma_f32_16x16x32_bf16 v[180:183], a[180:183], a[156:159], v[180:183]\n"
    "buffer_load_dwordx4 v2, s[60:63], s75 offen offset:0 sc1 lds\n"
    "v_mfma_f32_16x16x32_bf16 v[184:187], a[168:171], a[140:143], 0\n"
    "ds_read_b64_tr_b16 a[120:121], v19 offset:25088\n"
    "ds_read_b64_tr_b16 a[122:123], v19 offset:27136\n"
    "v_mfma_f32_16x16x32_bf16 v[184:187], a[184:187], a[156:159], v[184:187]\n"
    "buffer_load_dwordx4 v3, s[60:63], s75 offen offset:1024 sc1 lds\n"
    "v_mfma_f32_16x16x32_bf16 v[188:191], a[172:175], a[140:143], 0\n"
    "ds_read_b64_tr_b16 a[124:125], v20 offset:25088\n"
    "ds_read_b64_tr_b16 a[126:127], v20 offset:27136\n"
    "v_mfma_f32_16x16x32_bf16 v[188:191], a[188:191], a[156:159], v[188:191]\n"
    "buffer_load_dwordx4 v2, s[60:63], s75 offen offset:2048 sc1 lds\n"
    "v_mfma_f32_16x16x32_bf16 v[128:131], a[160:163], a[128:131], 0\n"
    "s_mov_b64 s[40:41], %9\n"
    "s_mov_b32 s42, 0x1000000\n"
    "v_mfma_f32_16x16x32_bf16 v[128:131], a[176:179], a[144:147], v[128:131]\n"
    "buffer_load_dwordx4 v3, s[60:63], s75 offen offset:3072 sc1 lds\n"
    "v_mfma_f32_16x16x32_bf16 v[132:135], a[164:167], a[128:131], 0\n"
    "s_xor_b32 s73, s86, 0x1c000\n"
    "s_add_u32 m0, s73, s88\n"
    "s_add_u32 s75, s75, 0x1000\n"
    "v_mfma_f32_16x16x32_bf16 v[132:135], a[180:183], a[144:147], v[132:135]\n"
    "buffer_load_dwordx4 v121, s[52:55], s76 offen offset:0 lds\n"
    "v_mfma_f32_16x16x32_bf16 v[136:139], a[168:171], a[128:131], 0\n"
    "buffer_load_dwordx4 v4, s[52:55], s76 offen offset:1024 lds\n"
    "v_mfma_f32_16x16x32_bf16 v[136:139], a[184:187], a[144:147], v[136:139]\n"
    "s_mov_b32 s43, 0x20000\n"
    "s_lshl_b32 s87, s78, 2\n"
    "v_mfma_f32_16x16x32_bf16 v[140:143], a[172:175], a[128:131], 0\n"
    "buffer_load_dwordx4 v10, s[52:55], s76 offen offset:2048 lds\n"
    "v_mfma_f32_16x16x32_bf16 v[140:143], a[188:191], a[144:147], v[140:143]\n"
    "buffer_load_dwordx4 v9, s[52:55], s76 offen offset:3072 lds\n"
    "s_waitcnt lgkmcnt(0)\n"
    "v_mfma_f32_16x16x32_bf16 v[144:147], a[96:99], a[68:71], v[144:147]\n"
    "s_waitcnt vmcnt(8)\n"
    "s_barrier\n"
    "v_mfma_f32_16x16x32_bf16 v[144:147], a[112:115], a[84:87], v[144:147]\n"
    "ds_read_b128 a[128:131], v17 offset:32768\n"
    "ds_read_b128 a[132:135], v17 offset:33792\n"
    "s_add_u32 s76, s76, 0x10000\n"
    "v_mfma_f32_16x16x32_bf16 v[148:151], a[100:103], a[68:71], v[148:151]\n"
    "ds_read_b128 a[136:139], v17 offset:36864\n"
    "ds_read_b128 a[140:143], v17 offset:37888\n"
    "s_add_u32 m0, s88, 0x20000\n"
    "v_mfma_f32_16x16x32_bf16 v[148:151], a[116:119], a[84:87], v[148:151]\n"
    "buffer_load_dwordx4 v2, s[60:63], s75 offen offset:0 sc1 lds\n"
    "v_mfma_f32_16x16x32_bf16 v[152:155], a[104:107], a[68:71], v[152:155]\n"
    "ds_read_b64_tr_b16 a[160:161], v21 offset:16384\n"
    "ds_read_b64_tr_b16 a[162:163], v21 offset:18432\n"
    "v_mfma_f32_16x16x32_bf16 v[152:155], a[120:123], a[84:87], v[152:155]\n"
    "buffer_load_dwordx4 v3, s[60:63], s75 offen offset:1024 sc1 lds\n"
    "v_mfma_f32_16x16x32_bf16 v[156:159], a[108:111], a[68:71], v[156:159]\n"
    "ds_read_b64_tr_b16 a[164:165], v22 offset:16384\n"
    "ds_read_b64_tr_b16 a[166:167], v22 offset:18432\n"
    "v_mfma_f32_16x16x32_bf16 v[156:159], a[124:127], a[84:87], v[156:159]\n"
    "buffer_load_dwordx4 v2, s[60:63], s75 offen offset:2048 sc1 lds\n"
    "v_mfma_f32_16x16x32_bf16 v[160:163], a[96:99], a[72:75], v[160:163]\n"
    "ds_read_b64_tr_b16 a[168:169], v21 offset:16896\n"
    "ds_read_b64_tr_b16 a[170:171], v21 offset:18944\n"
    "v_mfma_f32_16x16x32_bf16 v[160:163], a[112:115], a[88:91], v[160:163]\n"
    "buffer_load_dwordx4 v3, s[60:63], s75 offen offset:3072 sc1 lds\n"
    "v_mfma_f32_16x16x32_bf16 v[164:167], a[100:103], a[72:75], v[164:167]\n"
    "ds_read_b64_tr_b16 a[172:173], v22 offset:16896\n"
    "ds_read_b64_tr_b16 a[174:175], v22 offset:18944\n"
    "s_add_u32 m0, s88, 0x24000\n"
    "s_add_u32 s75, s75, 0x1000\n"
    "v_mfma_f32_16x16x32_bf16 v[164:167], a[116:119], a[88:91], v[164:167]\n"
    "buffer_load_dwordx4 v121, s[52:55], s76 offen offset:0 lds\n"
    "v_mfma_f32_16x16x32_bf16 v[168:171], a[104:107], a[72:75], v[168:171]\n"
    "ds_read_b128 a[144:147], v17 offset:34816\n"
    "ds_read_b128 a[148:151], v17 offset:35840\n"
    "v_mfma_f32_16x16x32_bf16 v[168:171], a[120:123], a[88:91], v[168:171]\n"
    "buffer_load_dwordx4 v4, s[52:55], s76 offen offset:1024 lds\n"
    "v_mfma_f32_16x16x32_bf16 v[172:175], a[108:111], a[72:75], v[172:175]\n"
    "ds_read_b128 a[152:155], v17 offset:38912\n"
    "ds_read_b128 a[156:159], v17 offset:39936\n"
    "v_mfma_f32_16x16x32_bf16 v[172:175], a[124:127], a[88:91], v[172:175]\n"
    "buffer_load_dwordx4 v10, s[52:55], s76 offen offset:2048 lds\n"
    "v_mfma_f32_16x16x32_bf16 v[176:179], a[96:99], a[76:79], v[176:179]\n"
    "ds_read_b64_tr_b16 a[176:177], v21 offset:24576\n"
    "ds_read_b64_tr_b16 a[178:179], v21 offset:26624\n"
    "v_mfma_f32_16x16x32_bf16 v[176:179], a[112:115], a[92:95], v[176:179]\n"
    "buffer_load_dwordx4 v9, s[52:55], s76 offen offset:3072 lds\n"
    "v_mfma_f32_16x16x32_bf16 v[180:183], a[100:103], a[76:79], v[180:183]\n"
    "ds_read_b64_tr_b16 a[180:181], v22 offset:24576\n"
    "ds_read_b64_tr_b16 a[182:183], v22 offset:26624\n"
    "s_add_u32 s76, s76, 0x10000\n"
    "s_xor_b32 s73, s86, 0x8000\n"
    "s_add_u32 m0, s73, s88\n"
    "v_mfma_f32_16x16x32_bf16 v[180:183], a[116:119], a[92:95], v[180:183]\n"
    "buffer_load_dwordx4 v2, s[60:63], s75 offen offset:0 sc1 lds\n"
    "v_mfma_f32_16x16x32_bf16 v[184:187], a[104:107], a[76:79], v[184:187]\n"
    "ds_read_b64_tr_b16 a[184:185], v21 offset:25088\n"
    "ds_read_b64_tr_b16 a[186:187], v21 offset:27136\n"
    "v_mfma_f32_16x16x32_bf16 v[184:187], a[120:123], a[92:95], v[184:187]\n"
    "buffer_load_dwordx4 v3, s[60:63], s75 offen offset:1024 sc1 lds\n"
    "v_mfma_f32_16x16x32_bf16 v[188:191], a[108:111], a[76:79], v[188:191]\n"
    "ds_read_b64_tr_b16 a[188:189], v22 offset:25088\n"
    "ds_read_b64_tr_b16 a[190:191], v22 offset:27136\n"
    "v_mfma_f32_16x16x32_bf16 v[188:191], a[124:127], a[92:95], v[188:191]\n"
    "buffer_load_dwordx4 v2, s[60:63], s75 offen offset:2048 sc1 lds\n"
    "v_mfma_f32_16x16x32_bf16 v[128:131], a[96:99], a[64:67], v[128:131]\n"
    "s_add_u32 s87, s87, s79\n"
    "s_lshl_b32 s87, s87, 18\n"
    "v_mfma_f32_16x16x32_bf16 v[128:131], a[112:115], a[80:83], v[128:131]\n"
    "buffer_load_dwordx4 v3, s[60:63], s75 offen offset:3072 sc1 lds\n"
    "v_mfma_f32_16x16x32_bf16 v[132:135], a[100:103], a[64:67], v[132:135]\n"
    "s_xor_b32 s73, s86, 0xc000\n"
    "s_add_u32 m0, s73, s88\n"
    "s_add_u32 s75, s75, 0x1000\n"
    "v_mfma_f32_16x16x32_bf16 v[132:135], a[116:119], a[80:83], v[132:135]\n"
    "buffer_load_dwordx4 v121, s[52:55], s76 offen offset:0 lds\n"
    "v_mfma_f32_16x16x32_bf16 v[136:139], a[104:107], a[64:67], v[136:139]\n"
    "buffer_load_dwordx4 v4, s[52:55], s76 offen offset:1024 lds\n"
    "v_mfma_f32_16x16x32_bf16 v[136:139], a[120:123], a[80:83], v[136:139]\n"
    "s_lshl_b32 s96, s68, 11\n"
    "s_add_u32 s87, s87, s96\n"
    "v_mfma_f32_16x16x32_bf16 v[140:143], a[108:111], a[64:67], v[140:143]\n"
    "buffer_load_dwordx4 v10, s[52:55], s76 offen offset:2048 lds\n"
    "v_mfma_f32_16x16x32_bf16 v[140:143], a[124:127], a[80:83], v[140:143]\n"
    "buffer_load_dwordx4 v9, s[52:55], s76 offen offset:3072 lds\n"
    "s_waitcnt lgkmcnt(0)\n"
    "v_mfma_f32_16x16x32_bf16 v[144:147], a[160:163], a[132:135], v[144:147]\n"
    "s_waitcnt vmcnt(16)\n"
    "s_barrier\n"
    "v_mfma_f32_16x16x32_bf16 v[144:147], a[176:179], a[148:151], v[144:147]\n"
    "ds_read_b128 a[64:67], v16 offset:0\n"
    "s_xor_b32 s73, s86, 0x10000\n"
    "v_mfma_f32_16x16x32_bf16 v[148:151], a[164:167], a[132:135], v[148:151]\n"
    "ds_read_b128 a[68:71], v16 offset:1024\n"
    "s_add_u32 m0, s73, s88\n"
    "v_mfma_f32_16x16x32_bf16 v[148:151], a[180:183], a[148:151], v[148:151]\n"
    "buffer_load_dwordx4 v2, s[60:63], s75 offen offset:0 sc1 lds\n"
    "v_mfma_f32_16x16x32_bf16 v[152:155], a[168:171], a[132:135], v[152:155]\n"
    "ds_read_b128 a[72:75], v16 offset:4096\n"
    "s_add_u32 s76, s76, 0x10000\n"
    "v_mfma_f32_16x16x32_bf16 v[152:155], a[184:187], a[148:151], v[152:155]\n"
    "ds_read_b128 a[76:79], v16 offset:5120\n"
    "s_lshl_b32 s96, s83, 14\n"
    "v_mfma_f32_16x16x32_bf16 v[156:159], a[172:175], a[132:135], v[156:159]\n"
    "ds_read_b64_tr_b16 a[96:97], v21 offset:49152\n"
    "s_add_u32 s84, s87, s96\n"
    "v_mfma_f32_16x16x32_bf16 v[156:159], a[188:191], a[148:151], v[156:159]\n"
    "buffer_load_dwordx4 v3, s[60:63], s75 offen offset:1024 sc1 lds\n"
    "v_mfma_f32_16x16x32_bf16 v[160:163], a[160:163], a[136:139], v[160:163]\n"
    "ds_read_b64_tr_b16 a[98:99], v21 offset:51200\n"
    "ds_read_b64_tr_b16 a[100:101], v22 offset:49152\n"
    "v_mfma_f32_16x16x32_bf16 v[160:163], a[176:179], a[152:155], v[160:163]\n"
    "ds_read_b64_tr_b16 a[102:103], v22 offset:51200\n"
    "s_lshl_b32 s96, s83, 16\n"
    "v_mfma_f32_16x16x32_bf16 v[164:167], a[164:167], a[136:139], v[164:167]\n"
    "ds_read_b64_tr_b16 a[104:105], v21 offset:49664\n"
    "ds_read_b64_tr_b16 a[106:107], v21 offset:51712\n"
    "v_mfma_f32_16x16x32_bf16 v[164:167], a[180:183], a[152:155], v[164:167]\n"
    "buffer_load_dwordx4 v2, s[60:63], s75 offen offset:2048 sc1 lds\n"
    "v_mfma_f32_16x16x32_bf16 v[168:171], a[168:171], a[136:139], v[168:171]\n"
    "ds_read_b64_tr_b16 a[108:109], v22 offset:49664\n"
    "s_add_u32 s87, s87, s96\n"
    "v_mfma_f32_16x16x32_bf16 v[168:171], a[184:187], a[152:155], v[168:171]\n"
    "ds_read_b64_tr_b16 a[110:111], v22 offset:51712\n"
    "s_lshl_b32 s96, s78, 2\n"
    "v_mfma_f32_16x16x32_bf16 v[172:175], a[172:175], a[136:139], v[172:175]\n"
    "ds_read_b128 a[80:83], v16 offset:2048\n"
    "s_add_u32 s96, s96, s79\n"
    "v_mfma_f32_16x16x32_bf16 v[172:175], a[188:191], a[152:155], v[172:175]\n"
    "buffer_load_dwordx4 v3, s[60:63], s75 offen offset:3072 sc1 lds\n"
    "v_mfma_f32_16x16x32_bf16 v[176:179], a[160:163], a[140:143], v[176:179]\n"
    "ds_read_b128 a[84:87], v16 offset:3072\n"
    "s_xor_b32 s73, s86, 0x4000\n"
    "v_mfma_f32_16x16x32_bf16 v[176:179], a[176:179], a[156:159], v[176:179]\n"
    "ds_read_b128 a[88:91], v16 offset:6144\n"
    "s_add_u32 m0, s73, s88\n"
    "v_mfma_f32_16x16x32_bf16 v[180:183], a[164:167], a[140:143], v[180:183]\n"
    "ds_read_b128 a[92:95], v16 offset:7168\n"
    "s_mov_b32 s75, s90\n"
    "v_mfma_f32_16x16x32_bf16 v[180:183], a[180:183], a[156:159], v[180:183]\n"
    "buffer_load_dwordx4 v121, s[52:55], s76 offen offset:0 lds\n"
    "v_mfma_f32_16x16x32_bf16 v[184:187], a[168:171], a[140:143], v[184:187]\n"
    "ds_read_b64_tr_b16 a[112:113], v21 offset:57344\n"
    "s_waitcnt vmcnt(37)\n"
    "v_mfma_f32_16x16x32_bf16 v[184:187], a[184:187], a[156:159], v[184:187]\n"
    "ds_read_b64_tr_b16 a[114:115], v21 offset:59392\n"
    "ds_read_b64_tr_b16 a[116:117], v22 offset:57344\n"
    "v_mfma_f32_16x16x32_bf16 v[188:191], a[172:175], a[140:143], v[188:191]\n"
    "ds_read_b64_tr_b16 a[118:119], v22 offset:59392\n"
    "v_cmp_ne_u64 vcc, s[64:65], v[6:7]\n"
    "v_mfma_f32_16x16x32_bf16 v[188:191], a[188:191], a[156:159], v[188:191]\n"
    "buffer_load_dwordx4 v4, s[52:55], s76 offen offset:1024 lds\n"
    "v_mfma_f32_16x16x32_bf16 v[128:131], a[160:163], a[128:131], v[128:131]\n"
    "ds_read_b64_tr_b16 a[120:121], v21 offset:57856\n"
    "ds_read_b64_tr_b16 a[122:123], v21 offset:59904\n"
    "v_mfma_f32_16x16x32_bf16 v[128:131], a[176:179], a[144:147], v[128:131]\n"
    "ds_read_b64_tr_b16 a[124:125], v22 offset:57856\n"
    "ds_read_b64_tr_b16 a[126:127], v22 offset:59904\n"
    "v_mfma_f32_16x16x32_bf16 v[132:135], a[164:167], a[128:131], v[132:135]\n"
    "s_lshl_b32 s96, s96, 2\n"
    "s_add_u32 s96, s96, s68\n"
    "v_mfma_f32_16x16x32_bf16 v[132:135], a[180:183], a[144:147], v[132:135]\n"
    "buffer_load_dwordx4 v10, s[52:55], s76 offen offset:2048 lds\n"
    "v_mfma_f32_16x16x32_bf16 v[136:139], a[168:171], a[128:131], v[136:139]\n"
    "s_lshl_b32 s96, s96, 5\n"
    "s_add_u32 s96, s96, 0xb000\n"
    "v_mfma_f32_16x16x32_bf16 v[136:139], a[184:187], a[144:147], v[136:139]\n"
    "s_waitcnt vmcnt(40)\n"
    "s_mov_b64 exec, 1\n"
    "buffer_store_dwordx2 v[36:37], v34, s[56:59], 0 offen sc1\n"
    "s_mov_b64 exec, -1\n"
    "v_mfma_f32_16x16x32_bf16 v[140:143], a[172:175], a[128:131], v[140:143]\n"
    "s_cbranch_vccnz .Lc20_%=\n"
    ".Lb20_%=:\n"
    "v_mfma_f32_16x16x32_bf16 v[140:143], a[188:191], a[144:147], v[140:143]\n"
    "buffer_load_dwordx4 v9, s[52:55], s76 offen offset:3072 lds\n"
    "s_waitcnt lgkmcnt(0)\n"
    "v_mfma_f32_16x16x32_bf16 v[144:147], a[96:99], a[68:71], v[144:147]\n"
    "s_waitcnt vmcnt(17)\n"
    "s_barrier\n"
    "v_mfma_f32_16x16x32_bf16 v[144:147], a[112:115], a[84:87], v[144:147]\n"
    "ds_read_b128 a[128:131], v18 offset:0\n"
    "s_xor_b32 s73, s86, 0x18000\n"
    "v_mfma_f32_16x16x32_bf16 v[148:151], a[100:103], a[68:71], v[148:151]\n"
    "ds_read_b128 a[132:135], v18 offset:1024\n"
    "s_add_u32 m0, s73, s88\n"
    "v_mfma_f32_16x16x32_bf16 v[148:151], a[116:119], a[84:87], v[148:151]\n"
    "buffer_load_dwordx4 v2, s[60:63], s75 offen offset:0 sc1 lds\n"
    "v_mfma_f32_16x16x32_bf16 v[152:155], a[104:107], a[68:71], v[152:155]\n"
    "ds_read_b128 a[136:139], v18 offset:4096\n"
    "s_mov_b32 s76, s91\n"
    "v_mfma_f32_16x16x32_bf16 v[152:155], a[120:123], a[84:87], v[152:155]\n"
    "ds_read_b128 a[140:143], v18 offset:5120\n"
    "s_lshl_b32 s97, s83, 3\n"
    "v_mfma_f32_16x16x32_bf16 v[156:159], a[108:111], a[68:71], v[156:159]\n"
    "ds_read_b64_tr_b16 a[160:161], v23 offset:16384\n"
    "v_lshrrev_b32 v32, 1, v8\n"
    "v_mfma_f32_16x16x32_bf16 v[156:159], a[124:127], a[84:87], v[156:159]\n"
    "buffer_load_dwordx4 v3, s[60:63], s75 offen offset:1024 sc1 lds\n"
    "v_mfma_f32_16x16x32_bf16 v[160:163], a[96:99], a[72:75], v[160:163]\n"
    "ds_read_b64_tr_b16 a[162:163], v23 offset:18432\n"
    "ds_read_b64_tr_b16 a[164:165], v24 offset:16384\n"
    "v_mfma_f32_16x16x32_bf16 v[160:163], a[112:115], a[88:91], v[160:163]\n"
    "ds_read_b64_tr_b16 a[166:167], v24 offset:18432\n"
    "v_and_b32 v32, 0x18, v32\n"
    "v_mfma_f32_16x16x32_bf16 v[164:167], a[100:103], a[72:75], v[164:167]\n"
    "ds_read_b64_tr_b16 a[168:169], v23 offset:16896\n"
    "ds_read_b64_tr_b16 a[170:171], v23 offset:18944\n"
    "v_mfma_f32_16x16x32_bf16 v[164:167], a[116:119], a[88:91], v[164:167]\n"
    "buffer_load_dwordx4 v2, s[60:63], s75 offen offset:2048 sc1 lds\n"
    "v_mfma_f32_16x16x32_bf16 v[168:171], a[104:107], a[72:75], v[168:171]\n"
    "ds_read_b64_tr_b16 a[172:173], v24 offset:16896\n"
    "v_max_u32 v32, 8, v32\n"
    "v_mfma_f32_16x16x32_bf16 v[168:171], a[120:123], a[88:91], v[168:171]\n"
    "ds_read_b64_tr_b16 a[174:175], v24 offset:18944\n"
    "v_xor_b32 v32, s97, v32\n"
    "v_mfma_f32_16x16x32_bf16 v[172:175], a[108:111], a[72:75], v[172:175]\n"
    "ds_read_b128 a[144:147], v18 offset:2048\n"
    "v_add_u32 v32, s96, v32\n"
    "v_mfma_f32_16x16x32_bf16 v[172:175], a[124:127], a[88:91], v[172:175]\n"
    "buffer_load_dwordx4 v3, s[60:63], s75 offen offset:3072 sc1 lds\n"
    "v_mfma_f32_16x16x32_bf16 v[176:179], a[96:99], a[76:79], v[176:179]\n"
    "ds_read_b128 a[148:151], v18 offset:3072\n"
    "s_xor_b32 s73, s86, 0x14000\n"
    "v_mfma_f32_16x16x32_bf16 v[176:179], a[112:115], a[92:95], v[176:179]\n"
    "ds_read_b128 a[152:155], v18 offset:6144\n"
    "s_add_u32 m0, s73, s88\n"
    "v_mfma_f32_16x16x32_bf16 v[180:183], a[100:103], a[76:79], v[180:183]\n"
    "ds_read_b128 a[156:159], v18 offset:7168\n"
    "s_add_u32 s75, s75, 0x1000\n"
    "v_mfma_f32_16x16x32_bf16 v[180:183], a[116:119], a[92:95], v[180:183]\n"
    "buffer_load_dwordx4 v121, s[92:95], s76 offen offset:0 lds\n"
    "v_mfma_f32_16x16x32_bf16 v[184:187], a[104:107], a[76:79], v[184:187]\n"
    "ds_read_b64_tr_b16 a[176:177], v23 offset:24576\n"
    "s_add_u32 s96, s96, s97\n"
    "v_mfma_f32_16x16x32_bf16 v[184:187], a[120:123], a[92:95], v[184:187]\n"
    "ds_read_b64_tr_b16 a[178:179], v23 offset:26624\n"
    "ds_read_b64_tr_b16 a[180:181], v24 offset:24576\n"
    "v_mfma_f32_16x16x32_bf16 v[188:191], a[108:111], a[76:79], v[188:191]\n"
    "ds_read_b64_tr_b16 a[182:183], v24 offset:26624\n"
    "v_mov_b32 v33, s96\n"
    "v_mfma_f32_16x16x32_bf16 v[188:191], a[124:127], a[92:95], v[188:191]\n"
    "buffer_load_dwordx4 v4, s[92:95], s76 offen offset:1024 lds\n"
    "v_mfma_f32_16x16x32_bf16 v[128:131], a[96:99], a[64:67], v[128:131]\n"
    "ds_read_b64_tr_b16 a[184:185], v23 offset:25088\n"
    "ds_read_b64_tr_b16 a[186:187], v23 offset:27136\n"
    "v_mfma_f32_16x16x32_bf16 v[128:131], a[112:115], a[80:83], v[128:131]\n"
    "ds_read_b64_tr_b16 a[188:189], v24 offset:25088\n"
    "ds_read_b64_tr_b16 a[190:191], v24 offset:27136\n"
    "v_mfma_f32_16x16x32_bf16 v[132:135], a[100:103], a[64:67], v[132:135]\n"
    "v_and_b32 v28, 0xf0, v8\n"
    "v_lshlrev_b32 v26, 6, v28\n"
    "v_mfma_f32_16x16x32_bf16 v[132:135], a[116:119], a[80:83], v[132:135]\n"
    "buffer_load_dwordx4 v10, s[92:95], s76 offen offset:2048 lds\n"
    "v_mfma_f32_16x16x32_bf16 v[136:139], a[104:107], a[64:67], v[136:139]\n"
    "v_lshrrev_b32 v29, 5, v8\n"
    "v_and_b32 v29, 0x18, v29\n"
    "v_mfma_f32_16x16x32_bf16 v[136:139], a[120:123], a[80:83], v[136:139]\n"
    "s_lshl_b32 s96, s78, 1\n"
    "s_add_u32 s96, s96, s71\n"
    "v_mfma_f32_16x16x32_bf16 v[140:143], a[108:111], a[64:67], v[140:143]\n"
    "s_lshl_b32 s96, s96, 2\n"
    "s_add_u32 s96, s96, s83\n"
    "v_mfma_f32_16x16x32_bf16 v[140:143], a[124:127], a[80:83], v[140:143]\n"
    "buffer_load_dwordx4 v9, s[92:95], s76 offen offset:3072 lds\n"
    "s_waitcnt lgkmcnt(0)\n"
    "v_mfma_f32_16x16x32_bf16 v[144:147], a[160:163], a[132:135], v[144:147]\n"
    "s_waitcnt vmcnt(17)\n"
    "s_barrier\n"
    "v_mfma_f32_16x16x32_bf16 v[144:147], a[176:179], a[148:151], v[144:147]\n"
    "ds_read_b128 a[64:67], v16 offset:32768\n"
    "s_xor_b32 s73, s86, 0x0\n"
    "v_mfma_f32_16x16x32_bf16 v[148:151], a[164:167], a[132:135], v[148:151]\n"
    "ds_read_b128 a[68:71], v16 offset:33792\n"
    "s_add_u32 m0, s73, s88\n"
    "v_mfma_f32_16x16x32_bf16 v[148:151], a[180:183], a[148:151], v[148:151]\n"
    "buffer_load_dwordx4 v2, s[60:63], s75 offen offset:0 sc1 lds\n"
    "v_mfma_f32_16x16x32_bf16 v[152:155], a[168:171], a[132:135], v[152:155]\n"
    "ds_read_b128 a[72:75], v16 offset:36864\n"
    "s_add_u32 s76, s76, 0x10000\n"
    "v_mfma_f32_16x16x32_bf16 v[152:155], a[184:187], a[148:151], v[152:155]\n"
    "ds_read_b128 a[76:79], v16 offset:37888\n"
    "s_lshl_b32 s96, s96, 14\n"
    "v_mfma_f32_16x16x32_bf16 v[156:159], a[172:175], a[132:135], v[156:159]\n"
    "ds_read_b64_tr_b16 a[96:97], v19 offset:49152\n"
    "s_lshl_b32 s97, s79, 8\n"
    "v_mfma_f32_16x16x32_bf16 v[156:159], a[188:191], a[148:151], v[156:159]\n"
    "buffer_load_dwordx4 v3, s[60:63], s75 offen offset:1024 sc1 lds\n"
    "v_mfma_f32_16x16x32_bf16 v[160:163], a[160:163], a[136:139], v[160:163]\n"
    "ds_read_b64_tr_b16 a[98:99], v19 offset:51200\n"
    "ds_read_b64_tr_b16 a[100:101], v20 offset:49152\n"
    "v_mfma_f32_16x16x32_bf16 v[160:163], a[176:179], a[152:155], v[160:163]\n"
    "ds_read_b64_tr_b16 a[102:103], v20 offset:51200\n"
    "s_add_u32 s96, s96, s97\n"
    "v_mfma_f32_16x16x32_bf16 v[164:167], a[164:167], a[136:139], v[164:167]\n"
    "ds_read_b64_tr_b16 a[104:105], v19 offset:49664\n"
    "ds_read_b64_tr_b16 a[106:107], v19 offset:51712\n"
    "v_mfma_f32_16x16x32_bf16 v[164:167], a[180:183], a[152:155], v[164:167]\n"
    "buffer_load_dwordx4 v2, s[60:63], s75 offen offset:2048 sc1 lds\n"
    "v_mfma_f32_16x16x32_bf16 v[168:171], a[168:171], a[136:139], v[168:171]\n"
    "ds_read_b64_tr_b16 a[108:109], v20 offset:49664\n"
    "s_lshl_b32 s97, s72, 7\n"
    "v_mfma_f32_16x16x32_bf16 v[168:171], a[184:187], a[152:155], v[168:171]\n"
    "ds_read_b64_tr_b16 a[110:111], v20 offset:51712\n"
    "s_add_u32 s96, s96, s97\n"
    "v_mfma_f32_16x16x32_bf16 v[172:175], a[172:175], a[136:139], v[172:175]\n"
    "ds_read_b128 a[80:83], v16 offset:34816\n"
    "v_add_u32 v26, s96, v26\n"
    "v_mfma_f32_16x16x32_bf16 v[172:175], a[188:191], a[152:155], v[172:175]\n"
    "buffer_load_dwordx4 v3, s[60:63], s75 offen offset:3072 sc1 lds\n"
    "v_mfma_f32_16x16x32_bf16 v[176:179], a[160:163], a[140:143], v[176:179]\n"
    "ds_read_b128 a[84:87], v16 offset:35840\n"
    "s_xor_b32 s73, s86, 0x1c000\n"
    "v_mfma_f32_16x16x32_bf16 v[176:179], a[176:179], a[156:159], v[176:179]\n"
    "ds_read_b128 a[88:91], v16 offset:38912\n"
    "s_add_u32 m0, s73, s88\n"
    "v_mfma_f32_16x16x32_bf16 v[180:183], a[164:167], a[140:143], v[180:183]\n"
    "ds_read_b128 a[92:95], v16 offset:39936\n"
    "s_add_u32 s75, s75, 0x3000\n"
    "v_mfma_f32_16x16x32_bf16 v[180:183], a[180:183], a[156:159], v[180:183]\n"
    "buffer_load_dwordx4 v121, s[92:95], s76 offen offset:0 lds\n"
    "v_mfma_f32_16x16x32_bf16 v[184:187], a[168:171], a[140:143], v[184:187]\n"
    "ds_read_b64_tr_b16 a[112:113], v19 offset:57344\n"
    "v_add_u32 v27, v26, v29\n"
    "v_mfma_f32_16x16x32_bf16 v[184:187], a[184:187], a[156:159], v[184:187]\n"
    "ds_read_b64_tr_b16 a[114:115], v19 offset:59392\n"
    "ds_read_b64_tr_b16 a[116:117], v20 offset:57344\n"
    "v_mfma_f32_16x16x32_bf16 v[188:191], a[172:175], a[140:143], v[188:191]\n"
    "ds_read_b64_tr_b16 a[118:119], v20 offset:59392\n"
    "v_and_b32 v28, 8, v29\n"
    "v_mfma_f32_16x16x32_bf16 v[188:191], a[188:191], a[156:159], v[188:191]\n"
    "buffer_load_dwordx4 v4, s[92:95], s76 offen offset:1024 lds\n"
    "v_mfma_f32_16x16x32_bf16 v[128:131], a[160:163], a[128:131], v[128:131]\n"
    "ds_read_b64_tr_b16 a[120:121], v19 offset:57856\n"
    "ds_read_b64_tr_b16 a[122:123], v19 offset:59904\n"
    "v_mfma_f32_16x16x32_bf16 v[128:131], a[176:179], a[144:147], v[128:131]\n"
    "ds_read_b64_tr_b16 a[124:125], v20 offset:57856\n"
    "ds_read_b64_tr_b16 a[126:127], v20 offset:59904\n"
    "v_mfma_f32_16x16x32_bf16 v[132:135], a[164:167], a[128:131], v[132:135]\n"
    "v_lshlrev_b32 v28, 2, v28\n"
    "v_and_b32 v29, 16, v29\n"
    "v_mfma_f32_16x16x32_bf16 v[132:135], a[180:183], a[144:147], v[132:135]\n"
    "buffer_load_dwordx4 v10, s[92:95], s76 offen offset:2048 lds\n"
    "v_mfma_f32_16x16x32_bf16 v[136:139], a[168:171], a[128:131], v[136:139]\n"
    "v_add3_u32 v26, v26, v28, v29\n"
    "s_mov_b64 s[48:49], %0\n"
    "v_mfma_f32_16x16x32_bf16 v[136:139], a[184:187], a[144:147], v[136:139]\n"
    "s_mov_b32 s50, 0x200000\n"
    "s_mov_b32 s51, 0x20000\n"
    "v_mfma_f32_16x16x32_bf16 v[140:143], a[172:175], a[128:131], v[140:143]\n"
    "s_mov_b64 s[44:45], %10\n"
    "s_mov_b32 s46, 0x200000\n"
    "v_mfma_f32_16x16x32_bf16 v[140:143], a[188:191], a[144:147], v[140:143]\n"
    "buffer_load_dwordx4 v9, s[92:95], s76 offen offset:3072 lds\n"
    "s_waitcnt lgkmcnt(0)\n"
    "v_mfma_f32_16x16x32_bf16 v[144:147], a[96:99], a[68:71], v[144:147]\n"
    "s_waitcnt vmcnt(16)\n"
    "s_barrier\n"
    "v_mfma_f32_16x16x32_bf16 v[144:147], a[112:115], a[84:87], v[144:147]\n"
    "ds_read_b128 a[128:131], v17 offset:0\n"
    "s_mov_b32 s47, 0x20000\n"
    "v_mfma_f32_16x16x32_bf16 v[148:151], a[100:103], a[68:71], v[148:151]\n"
    "ds_read_b128 a[132:135], v17 offset:1024\n"
    "s_add_u32 m0, s88, 0x20000\n"
    "v_mfma_f32_16x16x32_bf16 v[148:151], a[116:119], a[84:87], v[148:151]\n"
    "buffer_load_dwordx4 v2, s[60:63], s75 offen offset:0 sc1 lds\n"
    "v_mfma_f32_16x16x32_bf16 v[152:155], a[104:107], a[68:71], v[152:155]\n"
    "ds_read_b128 a[136:139], v17 offset:4096\n"
    "s_add_u32 s76, s76, 0x30000\n"
    "v_mfma_f32_16x16x32_bf16 v[152:155], a[120:123], a[84:87], v[152:155]\n"
    "ds_read_b128 a[140:143], v17 offset:5120\n"
    "v_mfma_f32_16x16x32_bf16 v[156:159], a[108:111], a[68:71], v[156:159]\n"
    "ds_read_b64_tr_b16 a[160:161], v19 offset:16384\n"
    "v_mfma_f32_16x16x32_bf16 v[156:159], a[124:127], a[84:87], v[156:159]\n"
    "buffer_load_dwordx4 v3, s[60:63], s75 offen offset:1024 sc1 lds\n"
    "v_mfma_f32_16x16x32_bf16 v[160:163], a[96:99], a[72:75], v[160:163]\n"
    "ds_read_b64_tr_b16 a[162:163], v19 offset:18432\n"
    "ds_read_b64_tr_b16 a[164:165], v20 offset:16384\n"
    "v_mfma_f32_16x16x32_bf16 v[160:163], a[112:115], a[88:91], v[160:163]\n"
    "ds_read_b64_tr_b16 a[166:167], v20 offset:18432\n"
    "v_mfma_f32_16x16x32_bf16 v[164:167], a[100:103], a[72:75], v[164:167]\n"
    "ds_read_b64_tr_b16 a[168:169], v19 offset:16896\n"
    "ds_read_b64_tr_b16 a[170:171], v19 offset:18944\n"
    "v_mfma_f32_16x16x32_bf16 v[164:167], a[116:119], a[88:91], v[164:167]\n"
    "buffer_load_dwordx4 v2, s[60:63], s75 offen offset:2048 sc1 lds\n"
    "v_mfma_f32_16x16x32_bf16 v[168:171], a[104:107], a[72:75], v[168:171]\n"
    "ds_read_b64_tr_b16 a[172:173], v20 offset:16896\n"
    "v_mfma_f32_16x16x32_bf16 v[168:171], a[120:123], a[88:91], v[168:171]\n"
    "ds_read_b64_tr_b16 a[174:175], v20 offset:18944\n"
    "v_mfma_f32_16x16x32_bf16 v[172:175], a[108:111], a[72:75], v[172:175]\n"
    "ds_read_b128 a[144:147], v17 offset:2048\n"
    "v_mfma_f32_16x16x32_bf16 v[172:175], a[124:127], a[88:91], v[172:175]\n"
    "buffer_load_dwordx4 v3, s[60:63], s75 offen offset:3072 sc1 lds\n"
    "v_mfma_f32_16x16x32_bf16 v[176:179], a[96:99], a[76:79], v[176:179]\n"
    "ds_read_b128 a[148:151], v17 offset:3072\n"
    "s_add_u32 m0, s88, 0x24000\n"
    "v_mfma_f32_16x16x32_bf16 v[176:179], a[112:115], a[92:95], v[176:179]\n"
    "ds_read_b128 a[152:155], v17 offset:6144\n"
    "s_add_u32 s75, s75, 0x1000\n"
    "v_mfma_f32_16x16x32_bf16 v[180:183], a[100:103], a[76:79], v[180:183]\n"
    "ds_read_b128 a[156:159], v17 offset:7168\n"
    "v_mfma_f32_16x16x32_bf16 v[180:183], a[116:119], a[92:95], v[180:183]\n"
    "buffer_load_dwordx4 v121, s[92:95], s76 offen offset:0 lds\n"
    "v_mfma_f32_16x16x32_bf16 v[184:187], a[104:107], a[76:79], v[184:187]\n"
    "ds_read_b64_tr_b16 a[176:177], v19 offset:24576\n"
    "v_mfma_f32_16x16x32_bf16 v[184:187], a[120:123], a[92:95], v[184:187]\n"
    "ds_read_b64_tr_b16 a[178:179], v19 offset:26624\n"
    "ds_read_b64_tr_b16 a[180:181], v20 offset:24576\n"
    "v_mfma_f32_16x16x32_bf16 v[188:191], a[108:111], a[76:79], v[188:191]\n"
    "ds_read_b64_tr_b16 a[182:183], v20 offset:26624\n"
    "v_mfma_f32_16x16x32_bf16 v[188:191], a[124:127], a[92:95], v[188:191]\n"
    "buffer_load_dwordx4 v4, s[92:95], s76 offen offset:1024 lds\n"
    "v_mfma_f32_16x16x32_bf16 v[128:131], a[96:99], a[64:67], v[128:131]\n"
    "ds_read_b64_tr_b16 a[184:185], v19 offset:25088\n"
    "ds_read_b64_tr_b16 a[186:187], v19 offset:27136\n"
    "v_mfma_f32_16x16x32_bf16 v[128:131], a[112:115], a[80:83], v[128:131]\n"
    "ds_read_b64_tr_b16 a[188:189], v20 offset:25088\n"
    "ds_read_b64_tr_b16 a[190:191], v20 offset:27136\n"
    "v_mfma_f32_16x16x32_bf16 v[132:135], a[100:103], a[64:67], v[132:135]\n"
    "v_mfma_f32_16x16x32_bf16 v[132:135], a[116:119], a[80:83], v[132:135]\n"
    "buffer_load_dwordx4 v10, s[92:95], s76 offen offset:2048 lds\n"
    "v_mfma_f32_16x16x32_bf16 v[136:139], a[104:107], a[64:67], v[136:139]\n"
    "v_mfma_f32_16x16x32_bf16 v[136:139], a[120:123], a[80:83], v[136:139]\n"
    "v_mfma_f32_16x16x32_bf16 v[140:143], a[108:111], a[64:67], v[140:143]\n"
    "v_mfma_f32_16x16x32_bf16 v[140:143], a[124:127], a[80:83], v[140:143]\n"
    "buffer_load_dwordx4 v9, s[92:95], s76 offen offset:3072 lds\n"
    "s_waitcnt lgkmcnt(0)\n"
    "v_mfma_f32_16x16x32_bf16 v[144:147], a[160:163], a[132:135], v[144:147]\n"
    "s_waitcnt vmcnt(16)\n"
    "s_barrier\n"
    "v_mfma_f32_16x16x32_bf16 v[144:147], a[176:179], a[148:151], v[144:147]\n"
    "ds_read_b128 a[64:67], v17 offset:32768\n"
    "s_xor_b32 s73, s86, 0x8000\n"
    "v_mfma_f32_16x16x32_bf16 v[148:151], a[164:167], a[132:135], v[148:151]\n"
    "ds_read_b128 a[68:71], v17 offset:33792\n"
    "s_add_u32 m0, s73, s88\n"
    "v_mfma_f32_16x16x32_bf16 v[148:151], a[180:183], a[148:151], v[148:151]\n"
    "buffer_load_dwordx4 v2, s[60:63], s75 offen offset:0 sc1 lds\n"
    "v_mfma_f32_16x16x32_bf16 v[152:155], a[168:171], a[132:135], v[152:155]\n"
    "ds_read_b128 a[72:75], v17 offset:36864\n"
    "s_add_u32 s76, s76, 0x10000\n"
    "v_mfma_f32_16x16x32_bf16 v[152:155], a[184:187], a[148:151], v[152:155]\n"
    "ds_read_b128 a[76:79], v17 offset:37888\n"
    "v_mfma_f32_16x16x32_bf16 v[156:159], a[172:175], a[132:135], v[156:159]\n"
    "ds_read_b64_tr_b16 a[96:97], v21 offset:16384\n"
    "v_mfma_f32_16x16x32_bf16 v[156:159], a[188:191], a[148:151], v[156:159]\n"
    "buffer_load_dwordx4 v3, s[60:63], s75 offen offset:1024 sc1 lds\n"
    "v_mfma_f32_16x16x32_bf16 v[160:163], a[160:163], a[136:139], v[160:163]\n"
    "ds_read_b64_tr_b16 a[98:99], v21 offset:18432\n"
    "ds_read_b64_tr_b16 a[100:101], v22 offset:16384\n"
    "v_mfma_f32_16x16x32_bf16 v[160:163], a[176:179], a[152:155], v[160:163]\n"
    "ds_read_b64_tr_b16 a[102:103], v22 offset:18432\n"
    "v_mfma_f32_16x16x32_bf16 v[164:167], a[164:167], a[136:139], v[164:167]\n"
    "ds_read_b64_tr_b16 a[104:105], v21 offset:16896\n"
    "ds_read_b64_tr_b16 a[106:107], v21 offset:18944\n"
    "v_mfma_f32_16x16x32_bf16 v[164:167], a[180:183], a[152:155], v[164:167]\n"
    "buffer_load_dwordx4 v2, s[60:63], s75 offen offset:2048 sc1 lds\n"
    "v_mfma_f32_16x16x32_bf16 v[168:171], a[168:171], a[136:139], v[168:171]\n"
    "ds_read_b64_tr_b16 a[108:109], v22 offset:16896\n"
    "v_mfma_f32_16x16x32_bf16 v[168:171], a[184:187], a[152:155], v[168:171]\n"
    "ds_read_b64_tr_b16 a[110:111], v22 offset:18944\n"
    "v_mfma_f32_16x16x32_bf16 v[172:175], a[172:175], a[136:139], v[172:175]\n"
    "ds_read_b128 a[80:83], v17 offset:34816\n"
    "v_mfma_f32_16x16x32_bf16 v[172:175], a[188:191], a[152:155], v[172:175]\n"
    "buffer_load_dwordx4 v3, s[60:63], s75 offen offset:3072 sc1 lds\n"
    "v_mfma_f32_16x16x32_bf16 v[176:179], a[160:163], a[140:143], v[176:179]\n"
    "ds_read_b128 a[84:87], v17 offset:35840\n"
    "s_xor_b32 s73, s86, 0xc000\n"
    "v_mfma_f32_16x16x32_bf16 v[176:179], a[176:179], a[156:159], v[176:179]\n"
    "ds_read_b128 a[88:91], v17 offset:38912\n"
    "s_add_u32 m0, s73, s88\n"
    "v_mfma_f32_16x16x32_bf16 v[180:183], a[164:167], a[140:143], v[180:183]\n"
    "ds_read_b128 a[92:95], v17 offset:39936\n"
    "s_add_u32 s75, s75, 0x3000\n"
    "v_mfma_f32_16x16x32_bf16 v[180:183], a[180:183], a[156:159], v[180:183]\n"
    "buffer_load_dwordx4 v121, s[92:95], s76 offen offset:0 lds\n"
    "v_mfma_f32_16x16x32_bf16 v[184:187], a[168:171], a[140:143], v[184:187]\n"
    "ds_read_b64_tr_b16 a[112:113], v21 offset:24576\n"
    "v_mfma_f32_16x16x32_bf16 v[184:187], a[184:187], a[156:159], v[184:187]\n"
    "ds_read_b64_tr_b16 a[114:115], v21 offset:26624\n"
    "ds_read_b64_tr_b16 a[116:117], v22 offset:24576\n"
    "v_mfma_f32_16x16x32_bf16 v[188:191], a[172:175], a[140:143], v[188:191]\n"
    "ds_read_b64_tr_b16 a[118:119], v22 offset:26624\n"
    "v_mfma_f32_16x16x32_bf16 v[188:191], a[188:191], a[156:159], v[188:191]\n"
    "buffer_load_dwordx4 v4, s[92:95], s76 offen offset:1024 lds\n"
    "v_mfma_f32_16x16x32_bf16 v[128:131], a[160:163], a[128:131], v[128:131]\n"
    "ds_read_b64_tr_b16 a[120:121], v21 offset:25088\n"
    "ds_read_b64_tr_b16 a[122:123], v21 offset:27136\n"
    "v_mfma_f32_16x16x32_bf16 v[128:131], a[176:179], a[144:147], v[128:131]\n"
    "ds_read_b64_tr_b16 a[124:125], v22 offset:25088\n"
    "ds_read_b64_tr_b16 a[126:127], v22 offset:27136\n"
    "v_mfma_f32_16x16x32_bf16 v[132:135], a[164:167], a[128:131], v[132:135]\n"
    "v_mfma_f32_16x16x32_bf16 v[132:135], a[180:183], a[144:147], v[132:135]\n"
    "buffer_load_dwordx4 v10, s[92:95], s76 offen offset:2048 lds\n"
    "v_mfma_f32_16x16x32_bf16 v[136:139], a[168:171], a[128:131], v[136:139]\n"
    "v_mfma_f32_16x16x32_bf16 v[136:139], a[184:187], a[144:147], v[136:139]\n"
    "v_mfma_f32_16x16x32_bf16 v[140:143], a[172:175], a[128:131], v[140:143]\n"
    "v_mfma_f32_16x16x32_bf16 v[140:143], a[188:191], a[144:147], v[140:143]\n"
    "buffer_load_dwordx4 v9, s[92:95], s76 offen offset:3072 lds\n"
    "s_waitcnt lgkmcnt(0)\n"
    "v_mfma_f32_16x16x32_bf16 v[208:211], a[96:99], a[68:71], 0\n"
    "s_waitcnt vmcnt(16)\n"
    "s_barrier\n"
    "v_mfma_f32_16x16x32_bf16 v[208:211], a[112:115], a[84:87], v[208:211]\n"
    "ds_read_b128 a[128:131], v16 offset:0\n"
    "s_xor_b32 s73, s86, 0x10000\n"
    "v_mfma_f32_16x16x32_bf16 v[212:215], a[100:103], a[68:71], 0\n"
    "ds_read_b128 a[132:135], v16 offset:1024\n"
    "s_add_u32 m0, s73, s88\n"
    "v_mfma_f32_16x16x32_bf16 v[212:215], a[116:119], a[84:87], v[212:215]\n"
    "buffer_load_dwordx4 v2, s[60:63], s75 offen offset:0 sc1 lds\n"
    "v_mfma_f32_16x16x32_bf16 v[216:219], a[104:107], a[68:71], 0\n"
    "ds_read_b128 a[136:139], v16 offset:4096\n"
    "s_add_u32 s76, s76, 0x30000\n"
    "v_mfma_f32_16x16x32_bf16 v[216:219], a[120:123], a[84:87], v[216:219]\n"
    "ds_read_b128 a[140:143], v16 offset:5120\n"
    "v_mfma_f32_16x16x32_bf16 v[220:223], a[108:111], a[68:71], 0\n"
    "ds_read_b64_tr_b16 a[160:161], v21 offset:49152\n"
    "v_mfma_f32_16x16x32_bf16 v[220:223], a[124:127], a[84:87], v[220:223]\n"
    "buffer_load_dwordx4 v3, s[60:63], s75 offen offset:1024 sc1 lds\n"
    "v_mfma_f32_16x16x32_bf16 v[224:227], a[96:99], a[72:75], 0\n"
    "ds_read_b64_tr_b16 a[162:163], v21 offset:51200\n"
    "ds_read_b64_tr_b16 a[164:165], v22 offset:49152\n"
    "v_mfma_f32_16x16x32_bf16 v[224:227], a[112:115], a[88:91], v[224:227]\n"
    "ds_read_b64_tr_b16 a[166:167], v22 offset:51200\n"
    "v_mfma_f32_16x16x32_bf16 v[228:231], a[100:103], a[72:75], 0\n"
    "ds_read_b64_tr_b16 a[168:169], v21 offset:49664\n"
    "ds_read_b64_tr_b16 a[170:171], v21 offset:51712\n"
    "v_mfma_f32_16x16x32_bf16 v[228:231], a[116:119], a[88:91], v[228:231]\n"
    "buffer_load_dwordx4 v2, s[60:63], s75 offen offset:2048 sc1 lds\n"
    "v_mfma_f32_16x16x32_bf16 v[232:235], a[104:107], a[72:75], 0\n"
    "ds_read_b64_tr_b16 a[172:173], v22 offset:49664\n"
    "s_xor_b32 s74, s83, 1\n"
    "v_mfma_f32_16x16x32_bf16 v[232:235], a[120:123], a[88:91], v[232:235]\n"
    "ds_read_b64_tr_b16 a[174:175], v22 offset:51712\n"
    "s_lshl_b32 s74, s74, 16\n"
    "v_mfma_f32_16x16x32_bf16 v[236:239], a[108:111], a[72:75], 0\n"
    "ds_read_b128 a[144:147], v16 offset:2048\n"
    "s_add_u32 s74, s74, s84\n"
    "v_mfma_f32_16x16x32_bf16 v[236:239], a[124:127], a[88:91], v[236:239]\n"
    "buffer_load_dwordx4 v3, s[60:63], s75 offen offset:3072 sc1 lds\n"
    "v_mfma_f32_16x16x32_bf16 v[240:243], a[96:99], a[76:79], 0\n"
    "ds_read_b128 a[148:151], v16 offset:3072\n"
    "s_xor_b32 s73, s86, 0x4000\n"
    "v_mfma_f32_16x16x32_bf16 v[240:243], a[112:115], a[92:95], v[240:243]\n"
    "ds_read_b128 a[152:155], v16 offset:6144\n"
    "s_add_u32 m0, s73, s88\n"
    "v_mfma_f32_16x16x32_bf16 v[244:247], a[100:103], a[76:79], 0\n"
    "ds_read_b128 a[156:159], v16 offset:7168\n"
    "s_add_u32 s75, s75, 0x1000\n"
    "v_mfma_f32_16x16x32_bf16 v[244:247], a[116:119], a[92:95], v[244:247]\n"
    "buffer_load_dwordx4 v121, s[92:95], s76 offen offset:0 lds\n"
    "v_mfma_f32_16x16x32_bf16 v[248:251], a[104:107], a[76:79], 0\n"
    "ds_read_b64_tr_b16 a[176:177], v21 offset:57344\n"
    "v_mfma_f32_16x16x32_bf16 v[248:251], a[120:123], a[92:95], v[248:251]\n"
    "ds_read_b64_tr_b16 a[178:179], v21 offset:59392\n"
    "ds_read_b64_tr_b16 a[180:181], v22 offset:57344\n"
    "v_mfma_f32_16x16x32_bf16 v[252:255], a[108:111], a[76:79], 0\n"
    "ds_read_b64_tr_b16 a[182:183], v22 offset:59392\n"
    "v_mfma_f32_16x16x32_bf16 v[252:255], a[124:127], a[92:95], v[252:255]\n"
    "buffer_load_dwordx4 v4, s[92:95], s76 offen offset:1024 lds\n"
    "v_mfma_f32_16x16x32_bf16 v[192:195], a[96:99], a[64:67], 0\n"
    "ds_read_b64_tr_b16 a[184:185], v21 offset:57856\n"
    "ds_read_b64_tr_b16 a[186:187], v21 offset:59904\n"
    "v_mfma_f32_16x16x32_bf16 v[192:195], a[112:115], a[80:83], v[192:195]\n"
    "ds_read_b64_tr_b16 a[188:189], v22 offset:57856\n"
    "ds_read_b64_tr_b16 a[190:191], v22 offset:59904\n"
    "v_mfma_f32_16x16x32_bf16 v[196:199], a[100:103], a[64:67], 0\n"
    "v_cvt_pk_bf16_f32 v52, v144, v145\n"
    "v_cvt_pk_bf16_f32 v53, v146, v147\n"
    "v_mfma_f32_16x16x32_bf16 v[196:199], a[116:119], a[80:83], v[196:199]\n"
    "buffer_load_dwordx4 v10, s[92:95], s76 offen offset:2048 lds\n"
    "v_mfma_f32_16x16x32_bf16 v[200:203], a[104:107], a[64:67], 0\n"
    "v_cvt_pk_bf16_f32 v54, v148, v149\n"
    "v_cvt_pk_bf16_f32 v55, v150, v151\n"
    "v_mfma_f32_16x16x32_bf16 v[200:203], a[120:123], a[80:83], v[200:203]\n"
    "buffer_store_dwordx4 v[52:55], v8, s[40:43], s74 offen offset:0 sc1\n"
    "v_mfma_f32_16x16x32_bf16 v[204:207], a[108:111], a[64:67], 0\n"
    "v_mfma_f32_16x16x32_bf16 v[204:207], a[124:127], a[80:83], v[204:207]\n"
    "buffer_load_dwordx4 v9, s[92:95], s76 offen offset:3072 lds\n"
    "s_waitcnt lgkmcnt(0)\n"
    "v_mfma_f32_16x16x32_bf16 v[208:211], a[160:163], a[132:135], v[208:211]\n"
    "s_waitcnt vmcnt(17)\n"
    "s_barrier\n"
    "v_mfma_f32_16x16x32_bf16 v[208:211], a[176:179], a[148:151], v[208:211]\n"
    "ds_read_b128 a[64:67], v18 offset:0\n"
    "s_xor_b32 s73, s86, 0x18000\n"
    "v_mfma_f32_16x16x32_bf16 v[212:215], a[164:167], a[132:135], v[212:215]\n"
    "ds_read_b128 a[68:71], v18 offset:1024\n"
    "s_add_u32 m0, s73, s88\n"
    "v_mfma_f32_16x16x32_bf16 v[212:215], a[180:183], a[148:151], v[212:215]\n"
    "buffer_load_dwordx4 v2, s[60:63], s75 offen offset:0 sc1 lds\n"
    "v_mfma_f32_16x16x32_bf16 v[216:219], a[168:171], a[132:135], v[216:219]\n"
    "ds_read_b128 a[72:75], v18 offset:4096\n"
    "s_add_u32 s76, s76, 0x10000\n"
    "v_mfma_f32_16x16x32_bf16 v[216:219], a[184:187], a[148:151], v[216:219]\n"
    "ds_read_b128 a[76:79], v18 offset:5120\n"
    "v_xor_b32 v5, 8, v5\n"
    "v_mfma_f32_16x16x32_bf16 v[220:223], a[172:175], a[132:135], v[220:223]\n"
    "ds_read_b64_tr_b16 a[96:97], v23 offset:16384\n"
    "v_mfma_f32_16x16x32_bf16 v[220:223], a[188:191], a[148:151], v[220:223]\n"
    "buffer_load_dwordx4 v3, s[60:63], s75 offen offset:1024 sc1 lds\n"
    "v_mfma_f32_16x16x32_bf16 v[224:227], a[160:163], a[136:139], v[224:227]\n"
    "ds_read_b64_tr_b16 a[98:99], v23 offset:18432\n"
    "ds_read_b64_tr_b16 a[100:101], v24 offset:16384\n"
    "v_mfma_f32_16x16x32_bf16 v[224:227], a[176:179], a[152:155], v[224:227]\n"
    "ds_read_b64_tr_b16 a[102:103], v24 offset:18432\n"
    "v_mfma_f32_16x16x32_bf16 v[228:231], a[164:167], a[136:139], v[228:231]\n"
    "ds_read_b64_tr_b16 a[104:105], v23 offset:16896\n"
    "ds_read_b64_tr_b16 a[106:107], v23 offset:18944\n"
    "v_mfma_f32_16x16x32_bf16 v[228:231], a[180:183], a[152:155], v[228:231]\n"
    "buffer_load_dwordx4 v2, s[60:63], s75 offen offset:2048 sc1 lds\n"
    "v_mfma_f32_16x16x32_bf16 v[232:235], a[168:171], a[136:139], v[232:235]\n"
    "ds_read_b64_tr_b16 a[108:109], v24 offset:16896\n"
    "s_xor_b32 s74, s83, 1\n"
    "v_mfma_f32_16x16x32_bf16 v[232:235], a[184:187], a[152:155], v[232:235]\n"
    "ds_read_b64_tr_b16 a[110:111], v24 offset:18944\n"
    "s_lshl_b32 s74, s74, 16\n"
    "v_mfma_f32_16x16x32_bf16 v[236:239], a[172:175], a[136:139], v[236:239]\n"
    "ds_read_b128 a[80:83], v18 offset:2048\n"
    "s_add_u32 s74, s74, s84\n"
    "v_mfma_f32_16x16x32_bf16 v[236:239], a[188:191], a[152:155], v[236:239]\n"
    "buffer_load_dwordx4 v3, s[60:63], s75 offen offset:3072 sc1 lds\n"
    "v_mfma_f32_16x16x32_bf16 v[240:243], a[160:163], a[140:143], v[240:243]\n"
    "ds_read_b128 a[84:87], v18 offset:3072\n"
    "s_xor_b32 s73, s86, 0x14000\n"
    "v_mfma_f32_16x16x32_bf16 v[240:243], a[176:179], a[156:159], v[240:243]\n"
    "ds_read_b128 a[88:91], v18 offset:6144\n"
    "s_add_u32 m0, s73, s88\n"
    "v_mfma_f32_16x16x32_bf16 v[244:247], a[164:167], a[140:143], v[244:247]\n"
    "ds_read_b128 a[92:95], v18 offset:7168\n"
    "s_add_u32 s75, s75, 0x3000\n"
    "v_mfma_f32_16x16x32_bf16 v[244:247], a[180:183], a[156:159], v[244:247]\n"
    "buffer_load_dwordx4 v121, s[92:95], s76 offen offset:0 lds\n"
    "v_mfma_f32_16x16x32_bf16 v[248:251], a[168:171], a[140:143], v[248:251]\n"
    "ds_read_b64_tr_b16 a[112:113], v23 offset:24576\n"
    "v_mfma_f32_16x16x32_bf16 v[248:251], a[184:187], a[156:159], v[248:251]\n"
    "ds_read_b64_tr_b16 a[114:115], v23 offset:26624\n"
    "ds_read_b64_tr_b16 a[116:117], v24 offset:24576\n"
    "v_mfma_f32_16x16x32_bf16 v[252:255], a[172:175], a[140:143], v[252:255]\n"
    "ds_read_b64_tr_b16 a[118:119], v24 offset:26624\n"
    "v_mfma_f32_16x16x32_bf16 v[252:255], a[188:191], a[156:159], v[252:255]\n"
    "buffer_load_dwordx4 v4, s[92:95], s76 offen offset:1024 lds\n"
    "v_mfma_f32_16x16x32_bf16 v[192:195], a[160:163], a[128:131], v[192:195]\n"
    "ds_read_b64_tr_b16 a[120:121], v23 offset:25088\n"
    "ds_read_b64_tr_b16 a[122:123], v23 offset:27136\n"
    "v_mfma_f32_16x16x32_bf16 v[192:195], a[176:179], a[144:147], v[192:195]\n"
    "ds_read_b64_tr_b16 a[124:125], v24 offset:25088\n"
    "ds_read_b64_tr_b16 a[126:127], v24 offset:27136\n"
    "v_mfma_f32_16x16x32_bf16 v[196:199], a[164:167], a[128:131], v[196:199]\n"
    "v_cvt_pk_bf16_f32 v56, v152, v153\n"
    "v_cvt_pk_bf16_f32 v57, v154, v155\n"
    "v_mfma_f32_16x16x32_bf16 v[196:199], a[180:183], a[144:147], v[196:199]\n"
    "buffer_load_dwordx4 v10, s[92:95], s76 offen offset:2048 lds\n"
    "v_mfma_f32_16x16x32_bf16 v[200:203], a[168:171], a[128:131], v[200:203]\n"
    "v_cvt_pk_bf16_f32 v58, v156, v157\n"
    "v_cvt_pk_bf16_f32 v59, v158, v159\n"
    "v_mfma_f32_16x16x32_bf16 v[200:203], a[184:187], a[144:147], v[200:203]\n"
    "buffer_store_dwordx4 v[56:59], v8, s[40:43], s74 offen offset:1024 sc1\n"
    "v_mfma_f32_16x16x32_bf16 v[204:207], a[172:175], a[128:131], v[204:207]\n"
    "buffer_load_dwordx2 v[6:7], v5, s[56:59], 0 offen sc1\n"
    "v_mfma_f32_16x16x32_bf16 v[204:207], a[188:191], a[144:147], v[204:207]\n"
    "buffer_load_dwordx4 v9, s[92:95], s76 offen offset:3072 lds\n"
    "s_waitcnt lgkmcnt(0)\n"
    "v_mfma_f32_16x16x32_bf16 v[208:211], a[96:99], a[68:71], v[208:211]\n"
    "s_waitcnt vmcnt(19)\n"
    "s_barrier\n"
    "v_mfma_f32_16x16x32_bf16 v[208:211], a[112:115], a[84:87], v[208:211]\n"
    "ds_read_b128 a[128:131], v16 offset:32768\n"
    "s_xor_b32 s73, s86, 0x0\n"
    "v_mfma_f32_16x16x32_bf16 v[212:215], a[100:103], a[68:71], v[212:215]\n"
    "ds_read_b128 a[132:135], v16 offset:33792\n"
    "s_add_u32 m0, s73, s88\n"
    "v_mfma_f32_16x16x32_bf16 v[212:215], a[116:119], a[84:87], v[212:215]\n"
    "buffer_load_dwordx4 v2, s[60:63], s75 offen offset:0 sc1 lds\n"
    "v_mfma_f32_16x16x32_bf16 v[216:219], a[104:107], a[68:71], v[216:219]\n"
    "ds_read_b128 a[136:139], v16 offset:36864\n"
    "s_add_u32 s76, s76, 0x30000\n"
    "v_mfma_f32_16x16x32_bf16 v[216:219], a[120:123], a[84:87], v[216:219]\n"
    "ds_read_b128 a[140:143], v16 offset:37888\n"
    "v_mfma_f32_16x16x32_bf16 v[220:223], a[108:111], a[68:71], v[220:223]\n"
    "ds_read_b64_tr_b16 a[160:161], v19 offset:49152\n"
    "v_mfma_f32_16x16x32_bf16 v[220:223], a[124:127], a[84:87], v[220:223]\n"
    "buffer_load_dwordx4 v3, s[60:63], s75 offen offset:1024 sc1 lds\n"
    "v_mfma_f32_16x16x32_bf16 v[224:227], a[96:99], a[72:75], v[224:227]\n"
    "ds_read_b64_tr_b16 a[162:163], v19 offset:51200\n"
    "ds_read_b64_tr_b16 a[164:165], v20 offset:49152\n"
    "v_mfma_f32_16x16x32_bf16 v[224:227], a[112:115], a[88:91], v[224:227]\n"
    "ds_read_b64_tr_b16 a[166:167], v20 offset:51200\n"
    "v_mfma_f32_16x16x32_bf16 v[228:231], a[100:103], a[72:75], v[228:231]\n"
    "ds_read_b64_tr_b16 a[168:169], v19 offset:49664\n"
    "ds_read_b64_tr_b16 a[170:171], v19 offset:51712\n"
    "v_mfma_f32_16x16x32_bf16 v[228:231], a[116:119], a[88:91], v[228:231]\n"
    "buffer_load_dwordx4 v2, s[60:63], s75 offen offset:2048 sc1 lds\n"
    "v_mfma_f32_16x16x32_bf16 v[232:235], a[104:107], a[72:75], v[232:235]\n"
    "ds_read_b64_tr_b16 a[172:173], v20 offset:49664\n"
    "s_xor_b32 s74, s83, 2\n"
    "v_mfma_f32_16x16x32_bf16 v[232:235], a[120:123], a[88:91], v[232:235]\n"
    "ds_read_b64_tr_b16 a[174:175], v20 offset:51712\n"
    "s_lshl_b32 s74, s74, 16\n"
    "v_mfma_f32_16x16x32_bf16 v[236:239], a[108:111], a[72:75], v[236:239]\n"
    "ds_read_b128 a[144:147], v16 offset:34816\n"
    "s_add_u32 s74, s74, s84\n"
    "v_mfma_f32_16x16x32_bf16 v[236:239], a[124:127], a[88:91], v[236:239]\n"
    "buffer_load_dwordx4 v3, s[60:63], s75 offen offset:3072 sc1 lds\n"
    "v_mfma_f32_16x16x32_bf16 v[240:243], a[96:99], a[76:79], v[240:243]\n"
    "ds_read_b128 a[148:151], v16 offset:35840\n"
    "s_xor_b32 s73, s86, 0x1c000\n"
    "v_mfma_f32_16x16x32_bf16 v[240:243], a[112:115], a[92:95], v[240:243]\n"
    "ds_read_b128 a[152:155], v16 offset:38912\n"
    "s_add_u32 m0, s73, s88\n"
    "v_mfma_f32_16x16x32_bf16 v[244:247], a[100:103], a[76:79], v[244:247]\n"
    "ds_read_b128 a[156:159], v16 offset:39936\n"
    "s_add_u32 s75, s75, 0x1000\n"
    "v_mfma_f32_16x16x32_bf16 v[244:247], a[116:119], a[92:95], v[244:247]\n"
    "buffer_load_dwordx4 v121, s[92:95], s76 offen offset:0 lds\n"
    "v_mfma_f32_16x16x32_bf16 v[248:251], a[104:107], a[76:79], v[248:251]\n"
    "ds_read_b64_tr_b16 a[176:177], v19 offset:57344\n"
    "v_mfma_f32_16x16x32_bf16 v[248:251], a[120:123], a[92:95], v[248:251]\n"
    "ds_read_b64_tr_b16 a[178:179], v19 offset:59392\n"
    "ds_read_b64_tr_b16 a[180:181], v20 offset:57344\n"
    "v_mfma_f32_16x16x32_bf16 v[252:255], a[108:111], a[76:79], v[252:255]\n"
    "ds_read_b64_tr_b16 a[182:183], v20 offset:59392\n"
    "v_mfma_f32_16x16x32_bf16 v[252:255], a[124:127], a[92:95], v[252:255]\n"
    "buffer_load_dwordx4 v4, s[92:95], s76 offen offset:1024 lds\n"
    "v_mfma_f32_16x16x32_bf16 v[192:195], a[96:99], a[64:67], v[192:195]\n"
    "ds_read_b64_tr_b16 a[184:185], v19 offset:57856\n"
    "ds_read_b64_tr_b16 a[186:187], v19 offset:59904\n"
    "v_mfma_f32_16x16x32_bf16 v[192:195], a[112:115], a[80:83], v[192:195]\n"
    "ds_read_b64_tr_b16 a[188:189], v20 offset:57856\n"
    "ds_read_b64_tr_b16 a[190:191], v20 offset:59904\n"
    "v_mfma_f32_16x16x32_bf16 v[196:199], a[100:103], a[64:67], v[196:199]\n"
    "v_cvt_pk_bf16_f32 v44, v160, v161\n"
    "v_cvt_pk_bf16_f32 v45, v162, v163\n"
    "v_mfma_f32_16x16x32_bf16 v[196:199], a[116:119], a[80:83], v[196:199]\n"
    "buffer_load_dwordx4 v10, s[92:95], s76 offen offset:2048 lds\n"
    "v_mfma_f32_16x16x32_bf16 v[200:203], a[104:107], a[64:67], v[200:203]\n"
    "v_cvt_pk_bf16_f32 v46, v164, v165\n"
    "v_cvt_pk_bf16_f32 v47, v166, v167\n"
    "v_mfma_f32_16x16x32_bf16 v[200:203], a[120:123], a[80:83], v[200:203]\n"
    "buffer_store_dwordx4 v[44:47], v8, s[40:43], s74 offen offset:0 sc1\n"
    "v_mfma_f32_16x16x32_bf16 v[204:207], a[108:111], a[64:67], v[204:207]\n"
    "v_mfma_f32_16x16x32_bf16 v[204:207], a[124:127], a[80:83], v[204:207]\n"
    "buffer_load_dwordx4 v9, s[92:95], s76 offen offset:3072 lds\n"
    "s_waitcnt lgkmcnt(0)\n"
    "v_mfma_f32_16x16x32_bf16 v[208:211], a[160:163], a[132:135], v[208:211]\n"
    "s_waitcnt vmcnt(19)\n"
    "s_barrier\n"
    "v_mfma_f32_16x16x32_bf16 v[208:211], a[176:179], a[148:151], v[208:211]\n"
    "ds_read_b128 a[64:67], v17 offset:0\n"
    "v_mfma_f32_16x16x32_bf16 v[212:215], a[164:167], a[132:135], v[212:215]\n"
    "ds_read_b128 a[68:71], v17 offset:1024\n"
    "s_add_u32 m0, s88, 0x20000\n"
    "v_mfma_f32_16x16x32_bf16 v[212:215], a[180:183], a[148:151], v[212:215]\n"
    "buffer_load_dwordx4 v2, s[60:63], s75 offen offset:0 sc1 lds\n"
    "v_mfma_f32_16x16x32_bf16 v[216:219], a[168:171], a[132:135], v[216:219]\n"
    "ds_read_b128 a[72:75], v17 offset:4096\n"
    "s_add_u32 s76, s76, 0x10000\n"
    "v_mfma_f32_16x16x32_bf16 v[216:219], a[184:187], a[148:151], v[216:219]\n"
    "ds_read_b128 a[76:79], v17 offset:5120\n"
    "v_mfma_f32_16x16x32_bf16 v[220:223], a[172:175], a[132:135], v[220:223]\n"
    "ds_read_b64_tr_b16 a[96:97], v19 offset:16384\n"
    "v_mfma_f32_16x16x32_bf16 v[220:223], a[188:191], a[148:151], v[220:223]\n"
    "buffer_load_dwordx4 v3, s[60:63], s75 offen offset:1024 sc1 lds\n"
    "v_mfma_f32_16x16x32_bf16 v[224:227], a[160:163], a[136:139], v[224:227]\n"
    "ds_read_b64_tr_b16 a[98:99], v19 offset:18432\n"
    "ds_read_b64_tr_b16 a[100:101], v20 offset:16384\n"
    "v_mfma_f32_16x16x32_bf16 v[224:227], a[176:179], a[152:155], v[224:227]\n"
    "ds_read_b64_tr_b16 a[102:103], v20 offset:18432\n"
    "v_mfma_f32_16x16x32_bf16 v[228:231], a[164:167], a[136:139], v[228:231]\n"
    "ds_read_b64_tr_b16 a[104:105], v19 offset:16896\n"
    "ds_read_b64_tr_b16 a[106:107], v19 offset:18944\n"
    "v_mfma_f32_16x16x32_bf16 v[228:231], a[180:183], a[152:155], v[228:231]\n"
    "buffer_load_dwordx4 v2, s[60:63], s75 offen offset:2048 sc1 lds\n"
    "v_mfma_f32_16x16x32_bf16 v[232:235], a[168:171], a[136:139], v[232:235]\n"
    "ds_read_b64_tr_b16 a[108:109], v20 offset:16896\n"
    "s_xor_b32 s74, s83, 2\n"
    "v_mfma_f32_16x16x32_bf16 v[232:235], a[184:187], a[152:155], v[232:235]\n"
    "ds_read_b64_tr_b16 a[110:111], v20 offset:18944\n"
    "s_lshl_b32 s74, s74, 16\n"
    "v_mfma_f32_16x16x32_bf16 v[236:239], a[172:175], a[136:139], v[236:239]\n"
    "ds_read_b128 a[80:83], v17 offset:2048\n"
    "s_add_u32 s74, s74, s84\n"
    "v_mfma_f32_16x16x32_bf16 v[236:239], a[188:191], a[152:155], v[236:239]\n"
    "buffer_load_dwordx4 v3, s[60:63], s75 offen offset:3072 sc1 lds\n"
    "v_mfma_f32_16x16x32_bf16 v[240:243], a[160:163], a[140:143], v[240:243]\n"
    "ds_read_b128 a[84:87], v17 offset:3072\n"
    "s_add_u32 m0, s88, 0x24000\n"
    "v_mfma_f32_16x16x32_bf16 v[240:243], a[176:179], a[156:159], v[240:243]\n"
    "ds_read_b128 a[88:91], v17 offset:6144\n"
    "s_xor_b32 s75, s90, 0x2000\n"
    "v_mfma_f32_16x16x32_bf16 v[244:247], a[164:167], a[140:143], v[244:247]\n"
    "ds_read_b128 a[92:95], v17 offset:7168\n"
    "v_mfma_f32_16x16x32_bf16 v[244:247], a[180:183], a[156:159], v[244:247]\n"
    "buffer_load_dwordx4 v121, s[92:95], s76 offen offset:0 lds\n"
    "v_mfma_f32_16x16x32_bf16 v[248:251], a[168:171], a[140:143], v[248:251]\n"
    "ds_read_b64_tr_b16 a[112:113], v19 offset:24576\n"
    "s_waitcnt vmcnt(15)\n"
    "v_mfma_f32_16x16x32_bf16 v[248:251], a[184:187], a[156:159], v[248:251]\n"
    "ds_read_b64_tr_b16 a[114:115], v19 offset:26624\n"
    "ds_read_b64_tr_b16 a[116:117], v20 offset:24576\n"
    "v_mfma_f32_16x16x32_bf16 v[252:255], a[172:175], a[140:143], v[252:255]\n"
    "ds_read_b64_tr_b16 a[118:119], v20 offset:26624\n"
    "v_cmp_ne_u64 vcc, s[64:65], v[6:7]\n"
    "v_mfma_f32_16x16x32_bf16 v[252:255], a[188:191], a[156:159], v[252:255]\n"
    "buffer_load_dwordx4 v4, s[92:95], s76 offen offset:1024 lds\n"
    "v_mfma_f32_16x16x32_bf16 v[192:195], a[160:163], a[128:131], v[192:195]\n"
    "ds_read_b64_tr_b16 a[120:121], v19 offset:25088\n"
    "ds_read_b64_tr_b16 a[122:123], v19 offset:27136\n"
    "v_mfma_f32_16x16x32_bf16 v[192:195], a[176:179], a[144:147], v[192:195]\n"
    "ds_read_b64_tr_b16 a[124:125], v20 offset:25088\n"
    "ds_read_b64_tr_b16 a[126:127], v20 offset:27136\n"
    "v_mfma_f32_16x16x32_bf16 v[196:199], a[164:167], a[128:131], v[196:199]\n"
    "v_cvt_pk_bf16_f32 v48, v168, v169\n"
    "v_cvt_pk_bf16_f32 v49, v170, v171\n"
    "v_mfma_f32_16x16x32_bf16 v[196:199], a[180:183], a[144:147], v[196:199]\n"
    "buffer_load_dwordx4 v10, s[92:95], s76 offen offset:2048 lds\n"
    "v_mfma_f32_16x16x32_bf16 v[200:203], a[168:171], a[128:131], v[200:203]\n"
    "v_cvt_pk_bf16_f32 v50, v172, v173\n"
    "v_cvt_pk_bf16_f32 v51, v174, v175\n"
    "v_mfma_f32_16x16x32_bf16 v[200:203], a[184:187], a[144:147], v[200:203]\n"
    "buffer_store_dwordx4 v[48:51], v8, s[40:43], s74 offen offset:1024 sc1\n"
    "v_mfma_f32_16x16x32_bf16 v[204:207], a[172:175], a[128:131], v[204:207]\n"
    "s_cbranch_vccnz .Lc22_%=\n"
    ".Lb22_%=:\n"
    "v_mfma_f32_16x16x32_bf16 v[204:207], a[188:191], a[144:147], v[204:207]\n"
    "buffer_load_dwordx4 v9, s[92:95], s76 offen offset:3072 lds\n"
    "s_waitcnt lgkmcnt(0)\n"
    "v_mfma_f32_16x16x32_bf16 v[208:211], a[96:99], a[68:71], v[208:211]\n"
    "s_waitcnt vmcnt(18)\n"
    "s_barrier\n"
    "v_mfma_f32_16x16x32_bf16 v[208:211], a[112:115], a[84:87], v[208:211]\n"
    "ds_read_b128 a[128:131], v17 offset:32768\n"
    "s_xor_b32 s73, s86, 0x8000\n"
    "v_mfma_f32_16x16x32_bf16 v[212:215], a[100:103], a[68:71], v[212:215]\n"
    "ds_read_b128 a[132:135], v17 offset:33792\n"
    "s_add_u32 m0, s73, s88\n"
    "v_mfma_f32_16x16x32_bf16 v[212:215], a[116:119], a[84:87], v[212:215]\n"
    "buffer_load_dwordx4 v2, s[60:63], s75 offen offset:0 sc1 lds\n"
    "v_mfma_f32_16x16x32_bf16 v[216:219], a[104:107], a[68:71], v[216:219]\n"
    "ds_read_b128 a[136:139], v17 offset:36864\n"
    "s_xor_b32 s76, s91, 0x20000\n"
    "v_mfma_f32_16x16x32_bf16 v[216:219], a[120:123], a[84:87], v[216:219]\n"
    "ds_read_b128 a[140:143], v17 offset:37888\n"
    "v_mfma_f32_16x16x32_bf16 v[220:223], a[108:111], a[68:71], v[220:223]\n"
    "ds_read_b64_tr_b16 a[160:161], v21 offset:16384\n"
    "v_mfma_f32_16x16x32_bf16 v[220:223], a[124:127], a[84:87], v[220:223]\n"
    "buffer_load_dwordx4 v3, s[60:63], s75 offen offset:1024 sc1 lds\n"
    "v_mfma_f32_16x16x32_bf16 v[224:227], a[96:99], a[72:75], v[224:227]\n"
    "ds_read_b64_tr_b16 a[162:163], v21 offset:18432\n"
    "ds_read_b64_tr_b16 a[164:165], v22 offset:16384\n"
    "v_mfma_f32_16x16x32_bf16 v[224:227], a[112:115], a[88:91], v[224:227]\n"
    "ds_read_b64_tr_b16 a[166:167], v22 offset:18432\n"
    "v_mfma_f32_16x16x32_bf16 v[228:231], a[100:103], a[72:75], v[228:231]\n"
    "ds_read_b64_tr_b16 a[168:169], v21 offset:16896\n"
    "ds_read_b64_tr_b16 a[170:171], v21 offset:18944\n"
    "v_mfma_f32_16x16x32_bf16 v[228:231], a[116:119], a[88:91], v[228:231]\n"
    "buffer_load_dwordx4 v2, s[60:63], s75 offen offset:2048 sc1 lds\n"
    "v_mfma_f32_16x16x32_bf16 v[232:235], a[104:107], a[72:75], v[232:235]\n"
    "ds_read_b64_tr_b16 a[172:173], v22 offset:16896\n"
    "s_xor_b32 s74, s83, 3\n"
    "v_mfma_f32_16x16x32_bf16 v[232:235], a[120:123], a[88:91], v[232:235]\n"
    "ds_read_b64_tr_b16 a[174:175], v22 offset:18944\n"
    "s_lshl_b32 s74, s74, 16\n"
    "v_mfma_f32_16x16x32_bf16 v[236:239], a[108:111], a[72:75], v[236:239]\n"
    "ds_read_b128 a[144:147], v17 offset:34816\n"
    "s_add_u32 s74, s74, s84\n"
    "v_mfma_f32_16x16x32_bf16 v[236:239], a[124:127], a[88:91], v[236:239]\n"
    "buffer_load_dwordx4 v3, s[60:63], s75 offen offset:3072 sc1 lds\n"
    "v_mfma_f32_16x16x32_bf16 v[240:243], a[96:99], a[76:79], v[240:243]\n"
    "ds_read_b128 a[148:151], v17 offset:35840\n"
    "s_xor_b32 s73, s86, 0xc000\n"
    "v_mfma_f32_16x16x32_bf16 v[240:243], a[112:115], a[92:95], v[240:243]\n"
    "ds_read_b128 a[152:155], v17 offset:38912\n"
    "s_add_u32 m0, s73, s88\n"
    "v_mfma_f32_16x16x32_bf16 v[244:247], a[100:103], a[76:79], v[244:247]\n"
    "ds_read_b128 a[156:159], v17 offset:39936\n"
    "s_add_u32 s75, s75, 0x1000\n"
    "v_mfma_f32_16x16x32_bf16 v[244:247], a[116:119], a[92:95], v[244:247]\n"
    "buffer_load_dwordx4 v121, s[92:95], s76 offen offset:0 lds\n"
    "v_mfma_f32_16x16x32_bf16 v[248:251], a[104:107], a[76:79], v[248:251]\n"
    "ds_read_b64_tr_b16 a[176:177], v21 offset:24576\n"
    "v_mfma_f32_16x16x32_bf16 v[248:251], a[120:123], a[92:95], v[248:251]\n"
    "ds_read_b64_tr_b16 a[178:179], v21 offset:26624\n"
    "ds_read_b64_tr_b16 a[180:181], v22 offset:24576\n"
    "v_mfma_f32_16x16x32_bf16 v[252:255], a[108:111], a[76:79], v[252:255]\n"
    "ds_read_b64_tr_b16 a[182:183], v22 offset:26624\n"
    "v_mfma_f32_16x16x32_bf16 v[252:255], a[124:127], a[92:95], v[252:255]\n"
    "buffer_load_dwordx4 v4, s[92:95], s76 offen offset:1024 lds\n"
    "v_mfma_f32_16x16x32_bf16 v[192:195], a[96:99], a[64:67], v[192:195]\n"
    "ds_read_b64_tr_b16 a[184:185], v21 offset:25088\n"
    "ds_read_b64_tr_b16 a[186:187], v21 offset:27136\n"
    "v_mfma_f32_16x16x32_bf16 v[192:195], a[112:115], a[80:83], v[192:195]\n"
    "ds_read_b64_tr_b16 a[188:189], v22 offset:25088\n"
    "ds_read_b64_tr_b16 a[190:191], v22 offset:27136\n"
    "v_mfma_f32_16x16x32_bf16 v[196:199], a[100:103], a[64:67], v[196:199]\n"
    "v_cvt_pk_bf16_f32 v52, v176, v177\n"
    "v_cvt_pk_bf16_f32 v53, v178, v179\n"
    "v_mfma_f32_16x16x32_bf16 v[196:199], a[116:119], a[80:83], v[196:199]\n"
    "buffer_load_dwordx4 v10, s[92:95], s76 offen offset:2048 lds\n"
    "v_mfma_f32_16x16x32_bf16 v[200:203], a[104:107], a[64:67], v[200:203]\n"
    "v_cvt_pk_bf16_f32 v54, v180, v181\n"
    "v_cvt_pk_bf16_f32 v55, v182, v183\n"
    "v_mfma_f32_16x16x32_bf16 v[200:203], a[120:123], a[80:83], v[200:203]\n"
    "buffer_store_dwordx4 v[52:55], v8, s[40:43], s74 offen offset:0 sc1\n"
    "v_mfma_f32_16x16x32_bf16 v[204:207], a[108:111], a[64:67], v[204:207]\n"
    "v_mfma_f32_16x16x32_bf16 v[204:207], a[124:127], a[80:83], v[204:207]\n"
    "buffer_load_dwordx4 v9, s[92:95], s76 offen offset:3072 lds\n"
    "s_waitcnt lgkmcnt(0)\n"
    "v_mfma_f32_16x16x32_bf16 v[208:211], a[160:163], a[132:135], v[208:211]\n"
    "s_waitcnt vmcnt(18)\n"
    "s_barrier\n"
    "v_mfma_f32_16x16x32_bf16 v[208:211], a[176:179], a[148:151], v[208:211]\n"
    "ds_read_b128 a[64:67], v16 offset:0\n"
    "s_xor_b32 s73, s86, 0x10000\n"
    "v_mfma_f32_16x16x32_bf16 v[212:215], a[164:167], a[132:135], v[212:215]\n"
    "ds_read_b128 a[68:71], v16 offset:1024\n"
    "s_add_u32 m0, s73, s88\n"
    "v_mfma_f32_16x16x32_bf16 v[212:215], a[180:183], a[148:151], v[212:215]\n"
    "buffer_load_dwordx4 v2, s[60:63], s75 offen offset:0 sc1 lds\n"
    "v_mfma_f32_16x16x32_bf16 v[216:219], a[168:171], a[132:135], v[216:219]\n"
    "ds_read_b128 a[72:75], v16 offset:4096\n"
    "s_add_u32 s76, s76, 0x10000\n"
    "v_mfma_f32_16x16x32_bf16 v[216:219], a[184:187], a[148:151], v[216:219]\n"
    "ds_read_b128 a[76:79], v16 offset:5120\n"
    "v_mfma_f32_16x16x32_bf16 v[220:223], a[172:175], a[132:135], v[220:223]\n"
    "ds_read_b64_tr_b16 a[96:97], v21 offset:49152\n"
    "v_mfma_f32_16x16x32_bf16 v[220:223], a[188:191], a[148:151], v[220:223]\n"
    "buffer_load_dwordx4 v3, s[60:63], s75 offen offset:1024 sc1 lds\n"
    "v_mfma_f32_16x16x32_bf16 v[224:227], a[160:163], a[136:139], v[224:227]\n"
    "ds_read_b64_tr_b16 a[98:99], v21 offset:51200\n"
    "ds_read_b64_tr_b16 a[100:101], v22 offset:49152\n"
    "v_mfma_f32_16x16x32_bf16 v[224:227], a[176:179], a[152:155], v[224:227]\n"
    "ds_read_b64_tr_b16 a[102:103], v22 offset:51200\n"
    "v_mfma_f32_16x16x32_bf16 v[228:231], a[164:167], a[136:139], v[228:231]\n"
    "ds_read_b64_tr_b16 a[104:105], v21 offset:49664\n"
    "ds_read_b64_tr_b16 a[106:107], v21 offset:51712\n"
    "v_mfma_f32_16x16x32_bf16 v[228:231], a[180:183], a[152:155], v[228:231]\n"
    "buffer_load_dwordx4 v2, s[60:63], s75 offen offset:2048 sc1 lds\n"
    "v_mfma_f32_16x16x32_bf16 v[232:235], a[168:171], a[136:139], v[232:235]\n"
    "ds_read_b64_tr_b16 a[108:109], v22 offset:49664\n"
    "s_xor_b32 s74, s83, 3\n"
    "v_mfma_f32_16x16x32_bf16 v[232:235], a[184:187], a[152:155], v[232:235]\n"
    "ds_read_b64_tr_b16 a[110:111], v22 offset:51712\n"
    "s_lshl_b32 s74, s74, 16\n"
    "v_mfma_f32_16x16x32_bf16 v[236:239], a[172:175], a[136:139], v[236:239]\n"
    "ds_read_b128 a[80:83], v16 offset:2048\n"
    "s_add_u32 s74, s74, s84\n"
    "v_mfma_f32_16x16x32_bf16 v[236:239], a[188:191], a[152:155], v[236:239]\n"
    "buffer_load_dwordx4 v3, s[60:63], s75 offen offset:3072 sc1 lds\n"
    "v_mfma_f32_16x16x32_bf16 v[240:243], a[160:163], a[140:143], v[240:243]\n"
    "ds_read_b128 a[84:87], v16 offset:3072\n"
    "s_xor_b32 s73, s86, 0x4000\n"
    "v_mfma_f32_16x16x32_bf16 v[240:243], a[176:179], a[156:159], v[240:243]\n"
    "ds_read_b128 a[88:91], v16 offset:6144\n"
    "s_add_u32 m0, s73, s88\n"
    "v_mfma_f32_16x16x32_bf16 v[244:247], a[164:167], a[140:143], v[244:247]\n"
    "ds_read_b128 a[92:95], v16 offset:7168\n"
    "s_add_u32 s75, s75, 0x3000\n"
    "v_mfma_f32_16x16x32_bf16 v[244:247], a[180:183], a[156:159], v[244:247]\n"
    "buffer_load_dwordx4 v121, s[92:95], s76 offen offset:0 lds\n"
    "v_mfma_f32_16x16x32_bf16 v[248:251], a[168:171], a[140:143], v[248:251]\n"
    "ds_read_b64_tr_b16 a[112:113], v21 offset:57344\n"
    "v_mfma_f32_16x16x32_bf16 v[248:251], a[184:187], a[156:159], v[248:251]\n"
    "ds_read_b64_tr_b16 a[114:115], v21 offset:59392\n"
    "ds_read_b64_tr_b16 a[116:117], v22 offset:57344\n"
    "v_mfma_f32_16x16x32_bf16 v[252:255], a[172:175], a[140:143], v[252:255]\n"
    "ds_read_b64_tr_b16 a[118:119], v22 offset:59392\n"
    "v_mfma_f32_16x16x32_bf16 v[252:255], a[188:191], a[156:159], v[252:255]\n"
    "buffer_load_dwordx4 v4, s[92:95], s76 offen offset:1024 lds\n"
    "v_mfma_f32_16x16x32_bf16 v[192:195], a[160:163], a[128:131], v[192:195]\n"
    "ds_read_b64_tr_b16 a[120:121], v21 offset:57856\n"
    "ds_read_b64_tr_b16 a[122:123], v21 offset:59904\n"
    "v_mfma_f32_16x16x32_bf16 v[192:195], a[176:179], a[144:147], v[192:195]\n"
    "ds_read_b64_tr_b16 a[124:125], v22 offset:57856\n"
    "ds_read_b64_tr_b16 a[126:127], v22 offset:59904\n"
    "v_mfma_f32_16x16x32_bf16 v[196:199], a[164:167], a[128:131], v[196:199]\n"
    "v_cvt_pk_bf16_f32 v56, v184, v185\n"
    "v_cvt_pk_bf16_f32 v57, v186, v187\n"
    "v_mfma_f32_16x16x32_bf16 v[196:199], a[180:183], a[144:147], v[196:199]\n"
    "buffer_load_dwordx4 v10, s[92:95], s76 offen offset:2048 lds\n"
    "v_mfma_f32_16x16x32_bf16 v[200:203], a[168:171], a[128:131], v[200:203]\n"
    "v_cvt_pk_bf16_f32 v58, v188, v189\n"
    "v_cvt_pk_bf16_f32 v59, v190, v191\n"
    "v_mfma_f32_16x16x32_bf16 v[200:203], a[184:187], a[144:147], v[200:203]\n"
    "buffer_store_dwordx4 v[56:59], v8, s[40:43], s74 offen offset:1024 sc1\n"
    "v_mfma_f32_16x16x32_bf16 v[204:207], a[172:175], a[128:131], v[204:207]\n"
    "v_mfma_f32_16x16x32_bf16 v[204:207], a[188:191], a[144:147], v[204:207]\n"
    "buffer_load_dwordx4 v9, s[92:95], s76 offen offset:3072 lds\n"
    "s_waitcnt lgkmcnt(0)\n"
    "v_mfma_f32_16x16x32_bf16 v[208:211], a[96:99], a[68:71], v[208:211]\n"
    "s_waitcnt vmcnt(18)\n"
    "s_barrier\n"
    "v_mfma_f32_16x16x32_bf16 v[208:211], a[112:115], a[84:87], v[208:211]\n"
    "ds_read_b128 a[128:131], v18 offset:0\n"
    "s_xor_b32 s73, s86, 0x18000\n"
    "v_mfma_f32_16x16x32_bf16 v[212:215], a[100:103], a[68:71], v[212:215]\n"
    "ds_read_b128 a[132:135], v18 offset:1024\n"
    "s_add_u32 m0, s73, s88\n"
    "v_mfma_f32_16x16x32_bf16 v[212:215], a[116:119], a[84:87], v[212:215]\n"
    "buffer_load_dwordx4 v2, s[60:63], s75 offen offset:0 sc1 lds\n"
    "v_mfma_f32_16x16x32_bf16 v[216:219], a[104:107], a[68:71], v[216:219]\n"
    "ds_read_b128 a[136:139], v18 offset:4096\n"
    "s_add_u32 s76, s76, 0x30000\n"
    "v_mfma_f32_16x16x32_bf16 v[216:219], a[120:123], a[84:87], v[216:219]\n"
    "ds_read_b128 a[140:143], v18 offset:5120\n"
    "v_mfma_f32_16x16x32_bf16 v[220:223], a[108:111], a[68:71], v[220:223]\n"
    "ds_read_b64_tr_b16 a[160:161], v23 offset:16384\n"
    "v_mfma_f32_16x16x32_bf16 v[220:223], a[124:127], a[84:87], v[220:223]\n"
    "buffer_load_dwordx4 v3, s[60:63], s75 offen offset:1024 sc1 lds\n"
    "v_mfma_f32_16x16x32_bf16 v[224:227], a[96:99], a[72:75], v[224:227]\n"
    "ds_read_b64_tr_b16 a[162:163], v23 offset:18432\n"
    "ds_read_b64_tr_b16 a[164:165], v24 offset:16384\n"
    "v_mfma_f32_16x16x32_bf16 v[224:227], a[112:115], a[88:91], v[224:227]\n"
    "ds_read_b64_tr_b16 a[166:167], v24 offset:18432\n"
    "v_mfma_f32_16x16x32_bf16 v[228:231], a[100:103], a[72:75], v[228:231]\n"
    "ds_read_b64_tr_b16 a[168:169], v23 offset:16896\n"
    "ds_read_b64_tr_b16 a[170:171], v23 offset:18944\n"
    "v_mfma_f32_16x16x32_bf16 v[228:231], a[116:119], a[88:91], v[228:231]\n"
    "buffer_load_dwordx4 v2, s[60:63], s75 offen offset:2048 sc1 lds\n"
    "v_mfma_f32_16x16x32_bf16 v[232:235], a[104:107], a[72:75], v[232:235]\n"
    "ds_read_b64_tr_b16 a[172:173], v24 offset:16896\n"
    "v_mfma_f32_16x16x32_bf16 v[232:235], a[120:123], a[88:91], v[232:235]\n"
    "ds_read_b64_tr_b16 a[174:175], v24 offset:18944\n"
    "v_mfma_f32_16x16x32_bf16 v[236:239], a[108:111], a[72:75], v[236:239]\n"
    "ds_read_b128 a[144:147], v18 offset:2048\n"
    "v_mfma_f32_16x16x32_bf16 v[236:239], a[124:127], a[88:91], v[236:239]\n"
    "buffer_load_dwordx4 v3, s[60:63], s75 offen offset:3072 sc1 lds\n"
    "v_mfma_f32_16x16x32_bf16 v[240:243], a[96:99], a[76:79], v[240:243]\n"
    "ds_read_b128 a[148:151], v18 offset:3072\n"
    "s_xor_b32 s73, s86, 0x14000\n"
    "v_mfma_f32_16x16x32_bf16 v[240:243], a[112:115], a[92:95], v[240:243]\n"
    "ds_read_b128 a[152:155], v18 offset:6144\n"
    "s_add_u32 m0, s73, s88\n"
    "v_mfma_f32_16x16x32_bf16 v[244:247], a[100:103], a[76:79], v[244:247]\n"
    "ds_read_b128 a[156:159], v18 offset:7168\n"
    "s_add_u32 s75, s75, 0x1000\n"
    "v_mfma_f32_16x16x32_bf16 v[244:247], a[116:119], a[92:95], v[244:247]\n"
    "buffer_load_dwordx4 v121, s[92:95], s76 offen offset:0 lds\n"
    "v_mfma_f32_16x16x32_bf16 v[248:251], a[104:107], a[76:79], v[248:251]\n"
    "ds_read_b64_tr_b16 a[176:177], v23 offset:24576\n"
    "v_mfma_f32_16x16x32_bf16 v[248:251], a[120:123], a[92:95], v[248:251]\n"
    "ds_read_b64_tr_b16 a[178:179], v23 offset:26624\n"
    "ds_read_b64_tr_b16 a[180:181], v24 offset:24576\n"
    "v_mfma_f32_16x16x32_bf16 v[252:255], a[108:111], a[76:79], v[252:255]\n"
    "ds_read_b64_tr_b16 a[182:183], v24 offset:26624\n"
    "v_mfma_f32_16x16x32_bf16 v[252:255], a[124:127], a[92:95], v[252:255]\n"
    "buffer_load_dwordx4 v4, s[92:95], s76 offen offset:1024 lds\n"
    "v_mfma_f32_16x16x32_bf16 v[192:195], a[96:99], a[64:67], v[192:195]\n"
    "ds_read_b64_tr_b16 a[184:185], v23 offset:25088\n"
    "ds_read_b64_tr_b16 a[186:187], v23 offset:27136\n"
    "v_mfma_f32_16x16x32_bf16 v[192:195], a[112:115], a[80:83], v[192:195]\n"
    "ds_read_b64_tr_b16 a[188:189], v24 offset:25088\n"
    "ds_read_b64_tr_b16 a[190:191], v24 offset:27136\n"
    "v_mfma_f32_16x16x32_bf16 v[196:199], a[100:103], a[64:67], v[196:199]\n"
    "v_mfma_f32_16x16x32_bf16 v[196:199], a[116:119], a[80:83], v[196:199]\n"
    "buffer_load_dwordx4 v10, s[92:95], s76 offen offset:2048 lds\n"
    "v_mfma_f32_16x16x32_bf16 v[200:203], a[104:107], a[64:67], v[200:203]\n"
    "v_mfma_f32_16x16x32_bf16 v[200:203], a[120:123], a[80:83], v[200:203]\n"
    "v_mfma_f32_16x16x32_bf16 v[204:207], a[108:111], a[64:67], v[204:207]\n"
    "v_mfma_f32_16x16x32_bf16 v[204:207], a[124:127], a[80:83], v[204:207]\n"
    "buffer_load_dwordx4 v9, s[92:95], s76 offen offset:3072 lds\n"
    "s_waitcnt lgkmcnt(0)\n"
    "v_mfma_f32_16x16x32_bf16 v[208:211], a[160:163], a[132:135], v[208:211]\n"
    "s_waitcnt vmcnt(17)\n"
    "s_barrier\n"
    "v_mfma_f32_16x16x32_bf16 v[208:211], a[176:179], a[148:151], v[208:211]\n"
    "ds_read_b128 a[64:67], v16 offset:32768\n"
    "s_xor_b32 s73, s86, 0x0\n"
    "v_mfma_f32_16x16x32_bf16 v[212:215], a[164:167], a[132:135], v[212:215]\n"
    "ds_read_b128 a[68:71], v16 offset:33792\n"
    "s_add_u32 m0, s73, s88\n"
    "v_mfma_f32_16x16x32_bf16 v[212:215], a[180:183], a[148:151], v[212:215]\n"
    "buffer_load_dwordx4 v2, s[60:63], s75 offen offset:0 sc1 lds\n"
    "v_mfma_f32_16x16x32_bf16 v[216:219], a[168:171], a[132:135], v[216:219]\n"
    "ds_read_b128 a[72:75], v16 offset:36864\n"
    "s_add_u32 s76, s76, 0x10000\n"
    "v_mfma_f32_16x16x32_bf16 v[216:219], a[184:187], a[148:151], v[216:219]\n"
    "ds_read_b128 a[76:79], v16 offset:37888\n"
    "v_mfma_f32_16x16x32_bf16 v[220:223], a[172:175], a[132:135], v[220:223]\n"
    "ds_read_b64_tr_b16 a[96:97], v19 offset:49152\n"
    "v_mfma_f32_16x16x32_bf16 v[220:223], a[188:191], a[148:151], v[220:223]\n"
    "buffer_load_dwordx4 v3, s[60:63], s75 offen offset:1024 sc1 lds\n"
    "v_mfma_f32_16x16x32_bf16 v[224:227], a[160:163], a[136:139], v[224:227]\n"
    "ds_read_b64_tr_b16 a[98:99], v19 offset:51200\n"
    "ds_read_b64_tr_b16 a[100:101], v20 offset:49152\n"
    "v_mfma_f32_16x16x32_bf16 v[224:227], a[176:179], a[152:155], v[224:227]\n"
    "ds_read_b64_tr_b16 a[102:103], v20 offset:51200\n"
    "v_mfma_f32_16x16x32_bf16 v[228:231], a[164:167], a[136:139], v[228:231]\n"
    "ds_read_b64_tr_b16 a[104:105], v19 offset:49664\n"
    "ds_read_b64_tr_b16 a[106:107], v19 offset:51712\n"
    "v_mfma_f32_16x16x32_bf16 v[228:231], a[180:183], a[152:155], v[228:231]\n"
    "buffer_load_dwordx4 v2, s[60:63], s75 offen offset:2048 sc1 lds\n"
    "v_mfma_f32_16x16x32_bf16 v[232:235], a[168:171], a[136:139], v[232:235]\n"
    "ds_read_b64_tr_b16 a[108:109], v20 offset:49664\n"
    "v_mfma_f32_16x16x32_bf16 v[232:235], a[184:187], a[152:155], v[232:235]\n"
    "ds_read_b64_tr_b16 a[110:111], v20 offset:51712\n"
    "v_mfma_f32_16x16x32_bf16 v[236:239], a[172:175], a[136:139], v[236:239]\n"
    "ds_read_b128 a[80:83], v16 offset:34816\n"
    "v_mfma_f32_16x16x32_bf16 v[236:239], a[188:191], a[152:155], v[236:239]\n"
    "buffer_load_dwordx4 v3, s[60:63], s75 offen offset:3072 sc1 lds\n"
    "v_mfma_f32_16x16x32_bf16 v[240:243], a[160:163], a[140:143], v[240:243]\n"
    "ds_read_b128 a[84:87], v16 offset:35840\n"
    "s_xor_b32 s73, s86, 0x1c000\n"
    "v_mfma_f32_16x16x32_bf16 v[240:243], a[176:179], a[156:159], v[240:243]\n"
    "ds_read_b128 a[88:91], v16 offset:38912\n"
    "s_add_u32 m0, s73, s88\n"
    "v_mfma_f32_16x16x32_bf16 v[244:247], a[164:167], a[140:143], v[244:247]\n"
    "ds_read_b128 a[92:95], v16 offset:39936\n"
    "s_add_u32 s75, s75, 0x3000\n"
    "v_mfma_f32_16x16x32_bf16 v[244:247], a[180:183], a[156:159], v[244:247]\n"
    "buffer_load_dwordx4 v121, s[92:95], s76 offen offset:0 lds\n"
    "v_mfma_f32_16x16x32_bf16 v[248:251], a[168:171], a[140:143], v[248:251]\n"
    "ds_read_b64_tr_b16 a[112:113], v19 offset:57344\n"
    "v_mfma_f32_16x16x32_bf16 v[248:251], a[184:187], a[156:159], v[248:251]\n"
    "ds_read_b64_tr_b16 a[114:115], v19 offset:59392\n"
    "ds_read_b64_tr_b16 a[116:117], v20 offset:57344\n"
    "v_mfma_f32_16x16x32_bf16 v[252:255], a[172:175], a[140:143], v[252:255]\n"
    "ds_read_b64_tr_b16 a[118:119], v20 offset:59392\n"
    "v_mfma_f32_16x16x32_bf16 v[252:255], a[188:191], a[156:159], v[252:255]\n"
    "buffer_load_dwordx4 v4, s[92:95], s76 offen offset:1024 lds\n"
    "v_mfma_f32_16x16x32_bf16 v[192:195], a[160:163], a[128:131], v[192:195]\n"
    "ds_read_b64_tr_b16 a[120:121], v19 offset:57856\n"
    "ds_read_b64_tr_b16 a[122:123], v19 offset:59904\n"
    "v_mfma_f32_16x16x32_bf16 v[192:195], a[176:179], a[144:147], v[192:195]\n"
    "ds_read_b64_tr_b16 a[124:125], v20 offset:57856\n"
    "ds_read_b64_tr_b16 a[126:127], v20 offset:59904\n"
    "v_mfma_f32_16x16x32_bf16 v[196:199], a[164:167], a[128:131], v[196:199]\n"
    "v_mfma_f32_16x16x32_bf16 v[196:199], a[180:183], a[144:147], v[196:199]\n"
    "buffer_load_dwordx4 v10, s[92:95], s76 offen offset:2048 lds\n"
    "v_mfma_f32_16x16x32_bf16 v[200:203], a[168:171], a[128:131], v[200:203]\n"
    "v_mfma_f32_16x16x32_bf16 v[200:203], a[184:187], a[144:147], v[200:203]\n"
    "v_mfma_f32_16x16x32_bf16 v[204:207], a[172:175], a[128:131], v[204:207]\n"
    "v_mfma_f32_16x16x32_bf16 v[204:207], a[188:191], a[144:147], v[204:207]\n"
    "buffer_load_dwordx4 v9, s[92:95], s76 offen offset:3072 lds\n"
    "s_waitcnt lgkmcnt(0)\n"
    "v_mfma_f32_16x16x32_bf16 v[208:211], a[96:99], a[68:71], v[208:211]\n"
    "s_waitcnt vmcnt(16)\n"
    "s_barrier\n"
    "v_mfma_f32_16x16x32_bf16 v[208:211], a[112:115], a[84:87], v[208:211]\n"
    "ds_read_b128 a[128:131], v17 offset:0\n"
    "v_mfma_f32_16x16x32_bf16 v[212:215], a[100:103], a[68:71], v[212:215]\n"
    "ds_read_b128 a[132:135], v17 offset:1024\n"
    "s_add_u32 m0, s88, 0x20000\n"
    "v_mfma_f32_16x16x32_bf16 v[212:215], a[116:119], a[84:87], v[212:215]\n"
    "buffer_load_dwordx4 v2, s[60:63], s75 offen offset:0 sc1 lds\n"
    "v_mfma_f32_16x16x32_bf16 v[216:219], a[104:107], a[68:71], v[216:219]\n"
    "ds_read_b128 a[136:139], v17 offset:4096\n"
    "s_add_u32 s76, s76, 0x30000\n"
    "v_mfma_f32_16x16x32_bf16 v[216:219], a[120:123], a[84:87], v[216:219]\n"
    "ds_read_b128 a[140:143], v17 offset:5120\n"
    "v_mfma_f32_16x16x32_bf16 v[220:223], a[108:111], a[68:71], v[220:223]\n"
    "ds_read_b64_tr_b16 a[160:161], v19 offset:16384\n"
    "v_mfma_f32_16x16x32_bf16 v[220:223], a[124:127], a[84:87], v[220:223]\n"
    "buffer_load_dwordx4 v3, s[60:63], s75 offen offset:1024 sc1 lds\n"
    "v_mfma_f32_16x16x32_bf16 v[224:227], a[96:99], a[72:75], v[224:227]\n"
    "ds_read_b64_tr_b16 a[162:163], v19 offset:18432\n"
    "ds_read_b64_tr_b16 a[164:165], v20 offset:16384\n"
    "v_mfma_f32_16x16x32_bf16 v[224:227], a[112:115], a[88:91], v[224:227]\n"
    "ds_read_b64_tr_b16 a[166:167], v20 offset:18432\n"
    "v_mfma_f32_16x16x32_bf16 v[228:231], a[100:103], a[72:75], v[228:231]\n"
    "ds_read_b64_tr_b16 a[168:169], v19 offset:16896\n"
    "ds_read_b64_tr_b16 a[170:171], v19 offset:18944\n"
    "v_mfma_f32_16x16x32_bf16 v[228:231], a[116:119], a[88:91], v[228:231]\n"
    "buffer_load_dwordx4 v2, s[60:63], s75 offen offset:2048 sc1 lds\n"
    "v_mfma_f32_16x16x32_bf16 v[232:235], a[104:107], a[72:75], v[232:235]\n"
    "ds_read_b64_tr_b16 a[172:173], v20 offset:16896\n"
    "v_mfma_f32_16x16x32_bf16 v[232:235], a[120:123], a[88:91], v[232:235]\n"
    "ds_read_b64_tr_b16 a[174:175], v20 offset:18944\n"
    "v_mfma_f32_16x16x32_bf16 v[236:239], a[108:111], a[72:75], v[236:239]\n"
    "ds_read_b128 a[144:147], v17 offset:2048\n"
    "v_mfma_f32_16x16x32_bf16 v[236:239], a[124:127], a[88:91], v[236:239]\n"
    "buffer_load_dwordx4 v3, s[60:63], s75 offen offset:3072 sc1 lds\n"
    "v_mfma_f32_16x16x32_bf16 v[240:243], a[96:99], a[76:79], v[240:243]\n"
    "ds_read_b128 a[148:151], v17 offset:3072\n"
    "s_add_u32 m0, s88, 0x24000\n"
    "v_mfma_f32_16x16x32_bf16 v[240:243], a[112:115], a[92:95], v[240:243]\n"
    "ds_read_b128 a[152:155], v17 offset:6144\n"
    "s_add_u32 s75, s75, 0x1000\n"
    "v_mfma_f32_16x16x32_bf16 v[244:247], a[100:103], a[76:79], v[244:247]\n"
    "ds_read_b128 a[156:159], v17 offset:7168\n"
    "v_mfma_f32_16x16x32_bf16 v[244:247], a[116:119], a[92:95], v[244:247]\n"
    "buffer_load_dwordx4 v121, s[92:95], s76 offen offset:0 lds\n"
    "v_mfma_f32_16x16x32_bf16 v[248:251], a[104:107], a[76:79], v[248:251]\n"
    "ds_read_b64_tr_b16 a[176:177], v19 offset:24576\n"
    "v_mfma_f32_16x16x32_bf16 v[248:251], a[120:123], a[92:95], v[248:251]\n"
    "ds_read_b64_tr_b16 a[178:179], v19 offset:26624\n"
    "ds_read_b64_tr_b16 a[180:181], v20 offset:24576\n"
    "v_mfma_f32_16x16x32_bf16 v[252:255], a[108:111], a[76:79], v[252:255]\n"
    "ds_read_b64_tr_b16 a[182:183], v20 offset:26624\n"
    "v_mfma_f32_16x16x32_bf16 v[252:255], a[124:127], a[92:95], v[252:255]\n"
    "buffer_load_dwordx4 v4, s[92:95], s76 offen offset:1024 lds\n"
    "v_mfma_f32_16x16x32_bf16 v[192:195], a[96:99], a[64:67], v[192:195]\n"
    "ds_read_b64_tr_b16 a[184:185], v19 offset:25088\n"
    "ds_read_b64_tr_b16 a[186:187], v19 offset:27136\n"
    "v_mfma_f32_16x16x32_bf16 v[192:195], a[112:115], a[80:83], v[192:195]\n"
    "ds_read_b64_tr_b16 a[188:189], v20 offset:25088\n"
    "ds_read_b64_tr_b16 a[190:191], v20 offset:27136\n"
    "v_mfma_f32_16x16x32_bf16 v[196:199], a[100:103], a[64:67], v[196:199]\n"
    "v_mfma_f32_16x16x32_bf16 v[196:199], a[116:119], a[80:83], v[196:199]\n"
    "buffer_load_dwordx4 v10, s[92:95], s76 offen offset:2048 lds\n"
    "v_mfma_f32_16x16x32_bf16 v[200:203], a[104:107], a[64:67], v[200:203]\n"
    "v_mfma_f32_16x16x32_bf16 v[200:203], a[120:123], a[80:83], v[200:203]\n"
    "v_mfma_f32_16x16x32_bf16 v[204:207], a[108:111], a[64:67], v[204:207]\n"
    "v_mfma_f32_16x16x32_bf16 v[204:207], a[124:127], a[80:83], v[204:207]\n"
    "buffer_load_dwordx4 v9, s[92:95], s76 offen offset:3072 lds\n"
    "s_waitcnt lgkmcnt(0)\n"
    "v_mfma_f32_16x16x32_bf16 v[208:211], a[160:163], a[132:135], v[208:211]\n"
    "s_waitcnt vmcnt(16)\n"
    "s_barrier\n"
    "v_mfma_f32_16x16x32_bf16 v[208:211], a[176:179], a[148:151], v[208:211]\n"
    "ds_read_b128 a[64:67], v17 offset:32768\n"
    "s_xor_b32 s73, s86, 0x8000\n"
    "v_mfma_f32_16x16x32_bf16 v[212:215], a[164:167], a[132:135], v[212:215]\n"
    "ds_read_b128 a[68:71], v17 offset:33792\n"
    "s_add_u32 m0, s73, s88\n"
    "v_mfma_f32_16x16x32_bf16 v[212:215], a[180:183], a[148:151], v[212:215]\n"
    "buffer_load_dwordx4 v2, s[60:63], s75 offen offset:0 sc1 lds\n"
    "v_mfma_f32_16x16x32_bf16 v[216:219], a[168:171], a[132:135], v[216:219]\n"
    "ds_read_b128 a[72:75], v17 offset:36864\n"
    "s_add_u32 s76, s76, 0x10000\n"
    "v_mfma_f32_16x16x32_bf16 v[216:219], a[184:187], a[148:151], v[216:219]\n"
    "ds_read_b128 a[76:79], v17 offset:37888\n"
    "v_mfma_f32_16x16x32_bf16 v[220:223], a[172:175], a[132:135], v[220:223]\n"
    "ds_read_b64_tr_b16 a[96:97], v21 offset:16384\n"
    "v_mfma_f32_16x16x32_bf16 v[220:223], a[188:191], a[148:151], v[220:223]\n"
    "buffer_load_dwordx4 v3, s[60:63], s75 offen offset:1024 sc1 lds\n"
    "v_mfma_f32_16x16x32_bf16 v[224:227], a[160:163], a[136:139], v[224:227]\n"
    "ds_read_b64_tr_b16 a[98:99], v21 offset:18432\n"
    "ds_read_b64_tr_b16 a[100:101], v22 offset:16384\n"
    "v_mfma_f32_16x16x32_bf16 v[224:227], a[176:179], a[152:155], v[224:227]\n"
    "ds_read_b64_tr_b16 a[102:103], v22 offset:18432\n"
    "v_mfma_f32_16x16x32_bf16 v[228:231], a[164:167], a[136:139], v[228:231]\n"
    "ds_read_b64_tr_b16 a[104:105], v21 offset:16896\n"
    "ds_read_b64_tr_b16 a[106:107], v21 offset:18944\n"
    "v_mfma_f32_16x16x32_bf16 v[228:231], a[180:183], a[152:155], v[228:231]\n"
    "buffer_load_dwordx4 v2, s[60:63], s75 offen offset:2048 sc1 lds\n"
    "v_mfma_f32_16x16x32_bf16 v[232:235], a[168:171], a[136:139], v[232:235]\n"
    "ds_read_b64_tr_b16 a[108:109], v22 offset:16896\n"
    "v_mfma_f32_16x16x32_bf16 v[232:235], a[184:187], a[152:155], v[232:235]\n"
    "ds_read_b64_tr_b16 a[110:111], v22 offset:18944\n"
    "v_mfma_f32_16x16x32_bf16 v[236:239], a[172:175], a[136:139], v[236:239]\n"
    "ds_read_b128 a[80:83], v17 offset:34816\n"
    "v_mfma_f32_16x16x32_bf16 v[236:239], a[188:191], a[152:155], v[236:239]\n"
    "buffer_load_dwordx4 v3, s[60:63], s75 offen offset:3072 sc1 lds\n"
    "v_mfma_f32_16x16x32_bf16 v[240:243], a[160:163], a[140:143], v[240:243]\n"
    "ds_read_b128 a[84:87], v17 offset:35840\n"
    "s_xor_b32 s73, s86, 0xc000\n"
    "v_mfma_f32_16x16x32_bf16 v[240:243], a[176:179], a[156:159], v[240:243]\n"
    "ds_read_b128 a[88:91], v17 offset:38912\n"
    "s_add_u32 m0, s73, s88\n"
    "v_mfma_f32_16x16x32_bf16 v[244:247], a[164:167], a[140:143], v[244:247]\n"
    "ds_read_b128 a[92:95], v17 offset:39936\n"
    "s_add_u32 s75, s75, 0x3000\n"
    "v_mfma_f32_16x16x32_bf16 v[244:247], a[180:183], a[156:159], v[244:247]\n"
    "buffer_load_dwordx4 v121, s[92:95], s76 offen offset:0 lds\n"
    "v_mfma_f32_16x16x32_bf16 v[248:251], a[168:171], a[140:143], v[248:251]\n"
    "ds_read_b64_tr_b16 a[112:113], v21 offset:24576\n"
    "v_mfma_f32_16x16x32_bf16 v[248:251], a[184:187], a[156:159], v[248:251]\n"
    "ds_read_b64_tr_b16 a[114:115], v21 offset:26624\n"
    "ds_read_b64_tr_b16 a[116:117], v22 offset:24576\n"
    "v_mfma_f32_16x16x32_bf16 v[252:255], a[172:175], a[140:143], v[252:255]\n"
    "ds_read_b64_tr_b16 a[118:119], v22 offset:26624\n"
    "v_mfma_f32_16x16x32_bf16 v[252:255], a[188:191], a[156:159], v[252:255]\n"
    "buffer_load_dwordx4 v4, s[92:95], s76 offen offset:1024 lds\n"
    "v_mfma_f32_16x16x32_bf16 v[192:195], a[160:163], a[128:131], v[192:195]\n"
    "ds_read_b64_tr_b16 a[120:121], v21 offset:25088\n"
    "ds_read_b64_tr_b16 a[122:123], v21 offset:27136\n"
    "v_mfma_f32_16x16x32_bf16 v[192:195], a[176:179], a[144:147], v[192:195]\n"
    "ds_read_b64_tr_b16 a[124:125], v22 offset:25088\n"
    "ds_read_b64_tr_b16 a[126:127], v22 offset:27136\n"
    "v_mfma_f32_16x16x32_bf16 v[196:199], a[164:167], a[128:131], v[196:199]\n"
    "v_mfma_f32_16x16x32_bf16 v[196:199], a[180:183], a[144:147], v[196:199]\n"
    "buffer_load_dwordx4 v10, s[92:95], s76 offen offset:2048 lds\n"
    "v_mfma_f32_16x16x32_bf16 v[200:203], a[168:171], a[128:131], v[200:203]\n"
    "s_waitcnt vmcnt(32)\n"
    "v_mfma_f32_16x16x32_bf16 v[200:203], a[184:187], a[144:147], v[200:203]\n"
    "s_mov_b64 exec, 1\n"
    "buffer_store_dwordx2 v[36:37], v33, s[56:59], 0 offen sc1\n"
    "s_mov_b64 exec, -1\n"
    "v_mfma_f32_16x16x32_bf16 v[204:207], a[172:175], a[128:131], v[204:207]\n"
    "v_mfma_f32_16x16x32_bf16 v[204:207], a[188:191], a[144:147], v[204:207]\n"
    "buffer_load_dwordx4 v9, s[92:95], s76 offen offset:3072 lds\n"
    "s_waitcnt lgkmcnt(0)\n"
    "v_mfma_f32_16x16x32_bf16 v[208:211], a[96:99], a[68:71], v[208:211]\n"
    "s_waitcnt vmcnt(17)\n"
    "s_barrier\n"
    "v_mfma_f32_16x16x32_bf16 v[208:211], a[112:115], a[84:87], v[208:211]\n"
    "ds_read_b128 a[128:131], v16 offset:0\n"
    "s_xor_b32 s73, s86, 0x10000\n"
    "v_mfma_f32_16x16x32_bf16 v[212:215], a[100:103], a[68:71], v[212:215]\n"
    "ds_read_b128 a[132:135], v16 offset:1024\n"
    "s_add_u32 m0, s73, s88\n"
    "v_mfma_f32_16x16x32_bf16 v[212:215], a[116:119], a[84:87], v[212:215]\n"
    "buffer_load_dwordx4 v2, s[60:63], s75 offen offset:0 sc1 lds\n"
    "v_mfma_f32_16x16x32_bf16 v[216:219], a[104:107], a[68:71], v[216:219]\n"
    "ds_read_b128 a[136:139], v16 offset:4096\n"
    "s_add_u32 s76, s76, 0x30000\n"
    "v_mfma_f32_16x16x32_bf16 v[216:219], a[120:123], a[84:87], v[216:219]\n"
    "ds_read_b128 a[140:143], v16 offset:5120\n"
    "v_mfma_f32_16x16x32_bf16 v[220:223], a[108:111], a[68:71], v[220:223]\n"
    "ds_read_b64_tr_b16 a[160:161], v21 offset:49152\n"
    "v_mfma_f32_16x16x32_bf16 v[220:223], a[124:127], a[84:87], v[220:223]\n"
    "buffer_load_dwordx4 v3, s[60:63], s75 offen offset:1024 sc1 lds\n"
    "v_mfma_f32_16x16x32_bf16 v[224:227], a[96:99], a[72:75], v[224:227]\n"
    "ds_read_b64_tr_b16 a[162:163], v21 offset:51200\n"
    "ds_read_b64_tr_b16 a[164:165], v22 offset:49152\n"
    "v_mfma_f32_16x16x32_bf16 v[224:227], a[112:115], a[88:91], v[224:227]\n"
    "ds_read_b64_tr_b16 a[166:167], v22 offset:51200\n"
    "v_mfma_f32_16x16x32_bf16 v[228:231], a[100:103], a[72:75], v[228:231]\n"
    "ds_read_b64_tr_b16 a[168:169], v21 offset:49664\n"
    "ds_read_b64_tr_b16 a[170:171], v21 offset:51712\n"
    "v_mfma_f32_16x16x32_bf16 v[228:231], a[116:119], a[88:91], v[228:231]\n"
    "buffer_load_dwordx4 v2, s[60:63], s75 offen offset:2048 sc1 lds\n"
    "v_mfma_f32_16x16x32_bf16 v[232:235], a[104:107], a[72:75], v[232:235]\n"
    "ds_read_b64_tr_b16 a[172:173], v22 offset:49664\n"
    "v_mfma_f32_16x16x32_bf16 v[232:235], a[120:123], a[88:91], v[232:235]\n"
    "ds_read_b64_tr_b16 a[174:175], v22 offset:51712\n"
    "v_mfma_f32_16x16x32_bf16 v[236:239], a[108:111], a[72:75], v[236:239]\n"
    "ds_read_b128 a[144:147], v16 offset:2048\n"
    "v_mfma_f32_16x16x32_bf16 v[236:239], a[124:127], a[88:91], v[236:239]\n"
    "buffer_load_dwordx4 v3, s[60:63], s75 offen offset:3072 sc1 lds\n"
    "v_mfma_f32_16x16x32_bf16 v[240:243], a[96:99], a[76:79], v[240:243]\n"
    "ds_read_b128 a[148:151], v16 offset:3072\n"
    "s_xor_b32 s73, s86, 0x4000\n"
    "v_mfma_f32_16x16x32_bf16 v[240:243], a[112:115], a[92:95], v[240:243]\n"
    "ds_read_b128 a[152:155], v16 offset:6144\n"
    "s_add_u32 m0, s73, s88\n"
    "v_mfma_f32_16x16x32_bf16 v[244:247], a[100:103], a[76:79], v[244:247]\n"
    "ds_read_b128 a[156:159], v16 offset:7168\n"
    "s_add_u32 s75, s75, 0x1000\n"
    "v_mfma_f32_16x16x32_bf16 v[244:247], a[116:119], a[92:95], v[244:247]\n"
    "buffer_load_dwordx4 v121, s[92:95], s76 offen offset:0 lds\n"
    "v_mfma_f32_16x16x32_bf16 v[248:251], a[104:107], a[76:79], v[248:251]\n"
    "ds_read_b64_tr_b16 a[176:177], v21 offset:57344\n"
    "v_mfma_f32_16x16x32_bf16 v[248:251], a[120:123], a[92:95], v[248:251]\n"
    "ds_read_b64_tr_b16 a[178:179], v21 offset:59392\n"
    "ds_read_b64_tr_b16 a[180:181], v22 offset:57344\n"
    "v_mfma_f32_16x16x32_bf16 v[252:255], a[108:111], a[76:79], v[252:255]\n"
    "ds_read_b64_tr_b16 a[182:183], v22 offset:59392\n"
    "v_mfma_f32_16x16x32_bf16 v[252:255], a[124:127], a[92:95], v[252:255]\n"
    "buffer_load_dwordx4 v4, s[92:95], s76 offen offset:1024 lds\n"
    "v_mfma_f32_16x16x32_bf16 v[192:195], a[96:99], a[64:67], v[192:195]\n"
    "ds_read_b64_tr_b16 a[184:185], v21 offset:57856\n"
    "ds_read_b64_tr_b16 a[186:187], v21 offset:59904\n"
    "v_mfma_f32_16x16x32_bf16 v[192:195], a[112:115], a[80:83], v[192:195]\n"
    "ds_read_b64_tr_b16 a[188:189], v22 offset:57856\n"
    "ds_read_b64_tr_b16 a[190:191], v22 offset:59904\n"
    "v_mfma_f32_16x16x32_bf16 v[196:199], a[100:103], a[64:67], v[196:199]\n"
    "v_mfma_f32_16x16x32_bf16 v[196:199], a[116:119], a[80:83], v[196:199]\n"
    "buffer_load_dwordx4 v10, s[92:95], s76 offen offset:2048 lds\n"
    "v_mfma_f32_16x16x32_bf16 v[200:203], a[104:107], a[64:67], v[200:203]\n"
    "v_mfma_f32_16x16x32_bf16 v[200:203], a[120:123], a[80:83], v[200:203]\n"
    "v_mfma_f32_16x16x32_bf16 v[204:207], a[108:111], a[64:67], v[204:207]\n"
    "v_mfma_f32_16x16x32_bf16 v[204:207], a[124:127], a[80:83], v[204:207]\n"
    "buffer_load_dwordx4 v9, s[92:95], s76 offen offset:3072 lds\n"
    "s_waitcnt lgkmcnt(0)\n"
    "v_mfma_f32_16x16x32_bf16 v[208:211], a[160:163], a[132:135], v[208:211]\n"
    "s_waitcnt vmcnt(17)\n"
    "s_barrier\n"
    "v_mfma_f32_16x16x32_bf16 v[208:211], a[176:179], a[148:151], v[208:211]\n"
    "ds_read_b128 a[64:67], v18 offset:0\n"
    "s_xor_b32 s73, s86, 0x18000\n"
    "v_mfma_f32_16x16x32_bf16 v[212:215], a[164:167], a[132:135], v[212:215]\n"
    "ds_read_b128 a[68:71], v18 offset:1024\n"
    "s_add_u32 m0, s73, s88\n"
    "v_mfma_f32_16x16x32_bf16 v[212:215], a[180:183], a[148:151], v[212:215]\n"
    "buffer_load_dwordx4 v2, s[60:63], s75 offen offset:0 sc1 lds\n"
    "v_mfma_f32_16x16x32_bf16 v[216:219], a[168:171], a[132:135], v[216:219]\n"
    "ds_read_b128 a[72:75], v18 offset:4096\n"
    "s_add_u32 s76, s76, 0x10000\n"
    "v_mfma_f32_16x16x32_bf16 v[216:219], a[184:187], a[148:151], v[216:219]\n"
    "ds_read_b128 a[76:79], v18 offset:5120\n"
    "v_mfma_f32_16x16x32_bf16 v[220:223], a[172:175], a[132:135], v[220:223]\n"
    "ds_read_b64_tr_b16 a[96:97], v23 offset:16384\n"
    "v_mfma_f32_16x16x32_bf16 v[220:223], a[188:191], a[148:151], v[220:223]\n"
    "buffer_load_dwordx4 v3, s[60:63], s75 offen offset:1024 sc1 lds\n"
    "v_mfma_f32_16x16x32_bf16 v[224:227], a[160:163], a[136:139], v[224:227]\n"
    "ds_read_b64_tr_b16 a[98:99], v23 offset:18432\n"
    "ds_read_b64_tr_b16 a[100:101], v24 offset:16384\n"
    "v_mfma_f32_16x16x32_bf16 v[224:227], a[176:179], a[152:155], v[224:227]\n"
    "ds_read_b64_tr_b16 a[102:103], v24 offset:18432\n"
    "v_mfma_f32_16x16x32_bf16 v[228:231], a[164:167], a[136:139], v[228:231]\n"
    "ds_read_b64_tr_b16 a[104:105], v23 offset:16896\n"
    "ds_read_b64_tr_b16 a[106:107], v23 offset:18944\n"
    "v_mfma_f32_16x16x32_bf16 v[228:231], a[180:183], a[152:155], v[228:231]\n"
    "buffer_load_dwordx4 v2, s[60:63], s75 offen offset:2048 sc1 lds\n"
    "v_mfma_f32_16x16x32_bf16 v[232:235], a[168:171], a[136:139], v[232:235]\n"
    "ds_read_b64_tr_b16 a[108:109], v24 offset:16896\n"
    "v_mfma_f32_16x16x32_bf16 v[232:235], a[184:187], a[152:155], v[232:235]\n"
    "ds_read_b64_tr_b16 a[110:111], v24 offset:18944\n"
    "v_mfma_f32_16x16x32_bf16 v[236:239], a[172:175], a[136:139], v[236:239]\n"
    "ds_read_b128 a[80:83], v18 offset:2048\n"
    "v_mfma_f32_16x16x32_bf16 v[236:239], a[188:191], a[152:155], v[236:239]\n"
    "buffer_load_dwordx4 v3, s[60:63], s75 offen offset:3072 sc1 lds\n"
    "v_mfma_f32_16x16x32_bf16 v[240:243], a[160:163], a[140:143], v[240:243]\n"
    "ds_read_b128 a[84:87], v18 offset:3072\n"
    "s_xor_b32 s73, s86, 0x14000\n"
    "v_mfma_f32_16x16x32_bf16 v[240:243], a[176:179], a[156:159], v[240:243]\n"
    "ds_read_b128 a[88:91], v18 offset:6144\n"
    "s_add_u32 m0, s73, s88\n"
    "v_mfma_f32_16x16x32_bf16 v[244:247], a[164:167], a[140:143], v[244:247]\n"
    "ds_read_b128 a[92:95], v18 offset:7168\n"
    "s_add_u32 s75, s75, 0x3000\n"
    "v_mfma_f32_16x16x32_bf16 v[244:247], a[180:183], a[156:159], v[244:247]\n"
    "buffer_load_dwordx4 v121, s[92:95], s76 offen offset:0 lds\n"
    "v_mfma_f32_16x16x32_bf16 v[248:251], a[168:171], a[140:143], v[248:251]\n"
    "ds_read_b64_tr_b16 a[112:113], v23 offset:24576\n"
    "v_mfma_f32_16x16x32_bf16 v[248:251], a[184:187], a[156:159], v[248:251]\n"
    "ds_read_b64_tr_b16 a[114:115], v23 offset:26624\n"
    "ds_read_b64_tr_b16 a[116:117], v24 offset:24576\n"
    "v_mfma_f32_16x16x32_bf16 v[252:255], a[172:175], a[140:143], v[252:255]\n"
    "ds_read_b64_tr_b16 a[118:119], v24 offset:26624\n"
    "v_mfma_f32_16x16x32_bf16 v[252:255], a[188:191], a[156:159], v[252:255]\n"
    "buffer_load_dwordx4 v4, s[92:95], s76 offen offset:1024 lds\n"
    "v_mfma_f32_16x16x32_bf16 v[192:195], a[160:163], a[128:131], v[192:195]\n"
    "ds_read_b64_tr_b16 a[120:121], v23 offset:25088\n"
    "ds_read_b64_tr_b16 a[122:123], v23 offset:27136\n"
    "v_mfma_f32_16x16x32_bf16 v[192:195], a[176:179], a[144:147], v[192:195]\n"
    "ds_read_b64_tr_b16 a[124:125], v24 offset:25088\n"
    "ds_read_b64_tr_b16 a[126:127], v24 offset:27136\n"
    "v_mfma_f32_16x16x32_bf16 v[196:199], a[164:167], a[128:131], v[196:199]\n"
    "v_mfma_f32_16x16x32_bf16 v[196:199], a[180:183], a[144:147], v[196:199]\n"
    "buffer_load_dwordx4 v10, s[92:95], s76 offen offset:2048 lds\n"
    "v_mfma_f32_16x16x32_bf16 v[200:203], a[168:171], a[128:131], v[200:203]\n"
    "v_mfma_f32_16x16x32_bf16 v[200:203], a[184:187], a[144:147], v[200:203]\n"
    "v_mfma_f32_16x16x32_bf16 v[204:207], a[172:175], a[128:131], v[204:207]\n"
    "v_mfma_f32_16x16x32_bf16 v[204:207], a[188:191], a[144:147], v[204:207]\n"
    "buffer_load_dwordx4 v9, s[92:95], s76 offen offset:3072 lds\n"
    "s_waitcnt lgkmcnt(0)\n"
    "v_mfma_f32_16x16x32_bf16 v[208:211], a[96:99], a[68:71], v[208:211]\n"
    "s_waitcnt vmcnt(16)\n"
    "s_barrier\n"
    "v_mfma_f32_16x16x32_bf16 v[208:211], a[112:115], a[84:87], v[208:211]\n"
    "ds_read_b128 a[128:131], v16 offset:32768\n"
    "v_mfma_f32_16x16x32_bf16 v[212:215], a[100:103], a[68:71], v[212:215]\n"
    "ds_read_b128 a[132:135], v16 offset:33792\n"
    "v_mfma_f32_16x16x32_bf16 v[212:215], a[116:119], a[84:87], v[212:215]\n"
    "ds_read_b128 a[136:139], v16 offset:36864\n"
    "v_mfma_f32_16x16x32_bf16 v[216:219], a[104:107], a[68:71], v[216:219]\n"
    "ds_read_b128 a[140:143], v16 offset:37888\n"
    "s_add_u32 s76, s76, 0x30000\n"
    "v_mfma_f32_16x16x32_bf16 v[216:219], a[120:123], a[84:87], v[216:219]\n"
    "ds_read_b64_tr_b16 a[160:161], v19 offset:49152\n"
    "v_mov_b32 v5, v32\n"
    "v_mfma_f32_16x16x32_bf16 v[220:223], a[108:111], a[68:71], v[220:223]\n"
    "ds_read_b64_tr_b16 a[162:163], v19 offset:51200\n"
    "v_mfma_f32_16x16x32_bf16 v[220:223], a[124:127], a[84:87], v[220:223]\n"
    "ds_read_b64_tr_b16 a[164:165], v20 offset:49152\n"
    "v_mfma_f32_16x16x32_bf16 v[224:227], a[96:99], a[72:75], v[224:227]\n"
    "ds_read_b64_tr_b16 a[166:167], v20 offset:51200\n"
    "v_mfma_f32_16x16x32_bf16 v[224:227], a[112:115], a[88:91], v[224:227]\n"
    "ds_read_b64_tr_b16 a[168:169], v19 offset:49664\n"
    "v_mfma_f32_16x16x32_bf16 v[228:231], a[100:103], a[72:75], v[228:231]\n"
    "ds_read_b64_tr_b16 a[170:171], v19 offset:51712\n"
    "v_mfma_f32_16x16x32_bf16 v[228:231], a[116:119], a[88:91], v[228:231]\n"
    "ds_read_b64_tr_b16 a[172:173], v20 offset:49664\n"
    "v_mfma_f32_16x16x32_bf16 v[232:235], a[104:107], a[72:75], v[232:235]\n"
    "ds_read_b64_tr_b16 a[174:175], v20 offset:51712\n"
    "v_mfma_f32_16x16x32_bf16 v[232:235], a[120:123], a[88:91], v[232:235]\n"
    "ds_read_b128 a[144:147], v16 offset:34816\n"
    "v_mfma_f32_16x16x32_bf16 v[236:239], a[108:111], a[72:75], v[236:239]\n"
    "ds_read_b128 a[148:151], v16 offset:35840\n"
    "v_mfma_f32_16x16x32_bf16 v[236:239], a[124:127], a[88:91], v[236:239]\n"
    "ds_read_b128 a[152:155], v16 offset:38912\n"
    "v_mfma_f32_16x16x32_bf16 v[240:243], a[96:99], a[76:79], v[240:243]\n"
    "ds_read_b128 a[156:159], v16 offset:39936\n"
    "v_mfma_f32_16x16x32_bf16 v[240:243], a[112:115], a[92:95], v[240:243]\n"
    "ds_read_b64_tr_b16 a[176:177], v19 offset:57344\n"
    "v_mfma_f32_16x16x32_bf16 v[244:247], a[100:103], a[76:79], v[244:247]\n"
    "ds_read_b64_tr_b16 a[178:179], v19 offset:59392\n"
    "v_mfma_f32_16x16x32_bf16 v[244:247], a[116:119], a[92:95], v[244:247]\n"
    "ds_read_b64_tr_b16 a[180:181], v20 offset:57344\n"
    "v_mfma_f32_16x16x32_bf16 v[248:251], a[104:107], a[76:79], v[248:251]\n"
    "ds_read_b64_tr_b16 a[182:183], v20 offset:59392\n"
    "v_mfma_f32_16x16x32_bf16 v[248:251], a[120:123], a[92:95], v[248:251]\n"
    "ds_read_b64_tr_b16 a[184:185], v19 offset:57856\n"
    "v_mfma_f32_16x16x32_bf16 v[252:255], a[108:111], a[76:79], v[252:255]\n"
    "ds_read_b64_tr_b16 a[186:187], v19 offset:59904\n"
    "v_mfma_f32_16x16x32_bf16 v[252:255], a[124:127], a[92:95], v[252:255]\n"
    "ds_read_b64_tr_b16 a[188:189], v20 offset:57856\n"
    "v_mfma_f32_16x16x32_bf16 v[192:195], a[96:99], a[64:67], v[192:195]\n"
    "ds_read_b64_tr_b16 a[190:191], v20 offset:59904\n"
    "v_mfma_f32_16x16x32_bf16 v[192:195], a[112:115], a[80:83], v[192:195]\n"
    "buffer_load_dwordx2 v[6:7], v5, s[56:59], 0 offen sc1\n"
    "v_mfma_f32_16x16x32_bf16 v[196:199], a[100:103], a[64:67], v[196:199]\n"
    "buffer_load_dwordx2 v[60:61], v27, s[48:51], 0 offen offset:0\n"
    "v_mfma_f32_16x16x32_bf16 v[196:199], a[116:119], a[80:83], v[196:199]\n"
    "buffer_load_dwordx2 v[62:63], v27, s[48:51], 0 offen offset:32\n"
    "v_mfma_f32_16x16x32_bf16 v[200:203], a[104:107], a[64:67], v[200:203]\n"
    "buffer_load_dwordx2 v[64:65], v27, s[48:51], 0 offen offset:64\n"
    "v_mfma_f32_16x16x32_bf16 v[200:203], a[120:123], a[80:83], v[200:203]\n"
    "buffer_load_dwordx2 v[66:67], v27, s[48:51], 0 offen offset:96\n"
    "v_mfma_f32_16x16x32_bf16 v[204:207], a[108:111], a[64:67], v[204:207]\n"
    "v_mfma_f32_16x16x32_bf16 v[204:207], a[124:127], a[80:83], v[204:207]\n"
    "s_waitcnt lgkmcnt(0)\n"
    "v_mfma_f32_16x16x32_bf16 v[208:211], a[160:163], a[132:135], v[208:211]\n"
    "s_waitcnt vmcnt(13)\n"
    "s_barrier\n"
    "v_mfma_f32_16x16x32_bf16 v[208:211], a[176:179], a[148:151], v[208:211]\n"
    "ds_read_b128 a[64:67], v17 offset:0\n"
    "v_mfma_f32_16x16x32_bf16 v[212:215], a[164:167], a[132:135], v[212:215]\n"
    "ds_read_b128 a[68:71], v17 offset:1024\n"
    "v_mfma_f32_16x16x32_bf16 v[212:215], a[180:183], a[148:151], v[212:215]\n"
    "ds_read_b128 a[72:75], v17 offset:4096\n"
    "v_mfma_f32_16x16x32_bf16 v[216:219], a[168:171], a[132:135], v[216:219]\n"
    "ds_read_b128 a[76:79], v17 offset:5120\n"
    "v_mfma_f32_16x16x32_bf16 v[216:219], a[184:187], a[148:151], v[216:219]\n"
    "ds_read_b64_tr_b16 a[96:97], v19 offset:16384\n"
    "v_mfma_f32_16x16x32_bf16 v[220:223], a[172:175], a[132:135], v[220:223]\n"
    "ds_read_b64_tr_b16 a[98:99], v19 offset:18432\n"
    "v_mfma_f32_16x16x32_bf16 v[220:223], a[188:191], a[148:151], v[220:223]\n"
    "ds_read_b64_tr_b16 a[100:101], v20 offset:16384\n"
    "v_mfma_f32_16x16x32_bf16 v[224:227], a[160:163], a[136:139], v[224:227]\n"
    "ds_read_b64_tr_b16 a[102:103], v20 offset:18432\n"
    "v_mfma_f32_16x16x32_bf16 v[224:227], a[176:179], a[152:155], v[224:227]\n"
    "ds_read_b64_tr_b16 a[104:105], v19 offset:16896\n"
    "v_mfma_f32_16x16x32_bf16 v[228:231], a[164:167], a[136:139], v[228:231]\n"
    "ds_read_b64_tr_b16 a[106:107], v19 offset:18944\n"
    "v_mfma_f32_16x16x32_bf16 v[228:231], a[180:183], a[152:155], v[228:231]\n"
    "ds_read_b64_tr_b16 a[108:109], v20 offset:16896\n"
    "v_mfma_f32_16x16x32_bf16 v[232:235], a[168:171], a[136:139], v[232:235]\n"
    "ds_read_b64_tr_b16 a[110:111], v20 offset:18944\n"
    "v_mfma_f32_16x16x32_bf16 v[232:235], a[184:187], a[152:155], v[232:235]\n"
    "ds_read_b128 a[80:83], v17 offset:2048\n"
    "v_mfma_f32_16x16x32_bf16 v[236:239], a[172:175], a[136:139], v[236:239]\n"
    "ds_read_b128 a[84:87], v17 offset:3072\n"
    "v_mfma_f32_16x16x32_bf16 v[236:239], a[188:191], a[152:155], v[236:239]\n"
    "ds_read_b128 a[88:91], v17 offset:6144\n"
    "v_mfma_f32_16x16x32_bf16 v[240:243], a[160:163], a[140:143], v[240:243]\n"
    "ds_read_b128 a[92:95], v17 offset:7168\n"
    "v_mfma_f32_16x16x32_bf16 v[240:243], a[176:179], a[156:159], v[240:243]\n"
    "ds_read_b64_tr_b16 a[112:113], v19 offset:24576\n"
    "v_mfma_f32_16x16x32_bf16 v[244:247], a[164:167], a[140:143], v[244:247]\n"
    "ds_read_b64_tr_b16 a[114:115], v19 offset:26624\n"
    "v_mfma_f32_16x16x32_bf16 v[244:247], a[180:183], a[156:159], v[244:247]\n"
    "ds_read_b64_tr_b16 a[116:117], v20 offset:24576\n"
    "v_mfma_f32_16x16x32_bf16 v[248:251], a[168:171], a[140:143], v[248:251]\n"
    "ds_read_b64_tr_b16 a[118:119], v20 offset:26624\n"
    "v_mfma_f32_16x16x32_bf16 v[248:251], a[184:187], a[156:159], v[248:251]\n"
    "ds_read_b64_tr_b16 a[120:121], v19 offset:25088\n"
    "v_mfma_f32_16x16x32_bf16 v[252:255], a[172:175], a[140:143], v[252:255]\n"
    "ds_read_b64_tr_b16 a[122:123], v19 offset:27136\n"
    "v_mfma_f32_16x16x32_bf16 v[252:255], a[188:191], a[156:159], v[252:255]\n"
    "ds_read_b64_tr_b16 a[124:125], v20 offset:25088\n"
    "v_mfma_f32_16x16x32_bf16 v[192:195], a[160:163], a[128:131], v[192:195]\n"
    "ds_read_b64_tr_b16 a[126:127], v20 offset:27136\n"
    "v_mfma_f32_16x16x32_bf16 v[192:195], a[176:179], a[144:147], v[192:195]\n"
    "v_mfma_f32_16x16x32_bf16 v[196:199], a[164:167], a[128:131], v[196:199]\n"
    "v_mfma_f32_16x16x32_bf16 v[196:199], a[180:183], a[144:147], v[196:199]\n"
    "v_mfma_f32_16x16x32_bf16 v[200:203], a[168:171], a[128:131], v[200:203]\n"
    "v_mfma_f32_16x16x32_bf16 v[200:203], a[184:187], a[144:147], v[200:203]\n"
    "v_mfma_f32_16x16x32_bf16 v[204:207], a[172:175], a[128:131], v[204:207]\n"
    "v_mfma_f32_16x16x32_bf16 v[204:207], a[188:191], a[144:147], v[204:207]\n"
    "s_waitcnt lgkmcnt(0)\n"
    "v_mfma_f32_16x16x32_bf16 v[208:211], a[96:99], a[68:71], v[208:211]\n"
    "s_waitcnt vmcnt(5)\n"
    "s_barrier\n"
    "v_mfma_f32_16x16x32_bf16 v[208:211], a[112:115], a[84:87], v[208:211]\n"
    "ds_read_b128 a[128:131], v17 offset:32768\n"
    "v_mfma_f32_16x16x32_bf16 v[212:215], a[100:103], a[68:71], v[212:215]\n"
    "ds_read_b128 a[132:135], v17 offset:33792\n"
    "v_mfma_f32_16x16x32_bf16 v[212:215], a[116:119], a[84:87], v[212:215]\n"
    "ds_read_b128 a[136:139], v17 offset:36864\n"
    "v_mfma_f32_16x16x32_bf16 v[216:219], a[104:107], a[68:71], v[216:219]\n"
    "ds_read_b128 a[140:143], v17 offset:37888\n"
    "v_mfma_f32_16x16x32_bf16 v[216:219], a[120:123], a[84:87], v[216:219]\n"
    "ds_read_b64_tr_b16 a[160:161], v21 offset:16384\n"
    "v_mfma_f32_16x16x32_bf16 v[220:223], a[108:111], a[68:71], v[220:223]\n"
    "ds_read_b64_tr_b16 a[162:163], v21 offset:18432\n"
    "s_waitcnt vmcnt(4)\n"
    "v_mfma_f32_16x16x32_bf16 v[220:223], a[124:127], a[84:87], v[220:223]\n"
    "ds_read_b64_tr_b16 a[164:165], v22 offset:16384\n"
    "v_mfma_f32_16x16x32_bf16 v[224:227], a[96:99], a[72:75], v[224:227]\n"
    "ds_read_b64_tr_b16 a[166:167], v22 offset:18432\n"
    "v_cmp_ne_u64 vcc, s[64:65], v[6:7]\n"
    "v_mfma_f32_16x16x32_bf16 v[224:227], a[112:115], a[88:91], v[224:227]\n"
    "ds_read_b64_tr_b16 a[168:169], v21 offset:16896\n"
    "v_mfma_f32_16x16x32_bf16 v[228:231], a[100:103], a[72:75], v[228:231]\n"
    "ds_read_b64_tr_b16 a[170:171], v21 offset:18944\n"
    "v_mfma_f32_16x16x32_bf16 v[228:231], a[116:119], a[88:91], v[228:231]\n"
    "ds_read_b64_tr_b16 a[172:173], v22 offset:16896\n"
    "v_mfma_f32_16x16x32_bf16 v[232:235], a[104:107], a[72:75], v[232:235]\n"
    "ds_read_b64_tr_b16 a[174:175], v22 offset:18944\n"
    "s_cbranch_vccnz .Lc24_%=\n"
    ".Lb24_%=:\n"
    "v_mfma_f32_16x16x32_bf16 v[232:235], a[120:123], a[88:91], v[232:235]\n"
    "ds_read_b128 a[144:147], v17 offset:34816\n"
    "v_mfma_f32_16x16x32_bf16 v[236:239], a[108:111], a[72:75], v[236:239]\n"
    "ds_read_b128 a[148:151], v17 offset:35840\n"
    "v_mfma_f32_16x16x32_bf16 v[236:239], a[124:127], a[88:91], v[236:239]\n"
    "ds_read_b128 a[152:155], v17 offset:38912\n"
    "v_mfma_f32_16x16x32_bf16 v[240:243], a[96:99], a[76:79], v[240:243]\n"
    "ds_read_b128 a[156:159], v17 offset:39936\n"
    "v_mfma_f32_16x16x32_bf16 v[240:243], a[112:115], a[92:95], v[240:243]\n"
    "ds_read_b64_tr_b16 a[176:177], v21 offset:24576\n"
    "v_mfma_f32_16x16x32_bf16 v[244:247], a[100:103], a[76:79], v[244:247]\n"
    "ds_read_b64_tr_b16 a[178:179], v21 offset:26624\n"
    "v_mfma_f32_16x16x32_bf16 v[244:247], a[116:119], a[92:95], v[244:247]\n"
    "ds_read_b64_tr_b16 a[180:181], v22 offset:24576\n"
    "v_mfma_f32_16x16x32_bf16 v[248:251], a[104:107], a[76:79], v[248:251]\n"
    "ds_read_b64_tr_b16 a[182:183], v22 offset:26624\n"
    "v_mfma_f32_16x16x32_bf16 v[248:251], a[120:123], a[92:95], v[248:251]\n"
    "ds_read_b64_tr_b16 a[184:185], v21 offset:25088\n"
    "v_mfma_f32_16x16x32_bf16 v[252:255], a[108:111], a[76:79], v[252:255]\n"
    "ds_read_b64_tr_b16 a[186:187], v21 offset:27136\n"
    "v_mfma_f32_16x16x32_bf16 v[252:255], a[124:127], a[92:95], v[252:255]\n"
    "ds_read_b64_tr_b16 a[188:189], v22 offset:25088\n"
    "v_mfma_f32_16x16x32_bf16 v[192:195], a[96:99], a[64:67], v[192:195]\n"
    "ds_read_b64_tr_b16 a[190:191], v22 offset:27136\n"
    "v_mfma_f32_16x16x32_bf16 v[192:195], a[112:115], a[80:83], v[192:195]\n"
    "s_xor_b32 s73, s83, 1\n"
    "s_lshl_b32 s73, s73, 14\n"
    "s_add_u32 s73, s73, s87\n"
    "buffer_load_dwordx4 v[144:147], v8, s[40:43], s73 offen offset:0 sc1\n"
    "buffer_load_dwordx4 v[148:151], v8, s[40:43], s73 offen offset:1024 sc1\n"
    "v_mfma_f32_16x16x32_bf16 v[196:199], a[100:103], a[64:67], v[196:199]\n"
    "v_mfma_f32_16x16x32_bf16 v[196:199], a[116:119], a[80:83], v[196:199]\n"
    "s_xor_b32 s73, s83, 2\n"
    "s_lshl_b32 s73, s73, 14\n"
    "s_add_u32 s73, s73, s87\n"
    "buffer_load_dwordx4 v[152:155], v8, s[40:43], s73 offen offset:0 sc1\n"
    "buffer_load_dwordx4 v[156:159], v8, s[40:43], s73 offen offset:1024 sc1\n"
    "v_mfma_f32_16x16x32_bf16 v[200:203], a[104:107], a[64:67], v[200:203]\n"
    "v_mfma_f32_16x16x32_bf16 v[200:203], a[120:123], a[80:83], v[200:203]\n"
    "s_xor_b32 s73, s83, 3\n"
    "s_lshl_b32 s73, s73, 14\n"
    "s_add_u32 s73, s73, s87\n"
    "buffer_load_dwordx4 v[160:163], v8, s[40:43], s73 offen offset:0 sc1\n"
    "buffer_load_dwordx4 v[164:167], v8, s[40:43], s73 offen offset:1024 sc1\n"
    "v_mfma_f32_16x16x32_bf16 v[204:207], a[108:111], a[64:67], v[204:207]\n"
    "v_mfma_f32_16x16x32_bf16 v[204:207], a[124:127], a[80:83], v[204:207]\n"
    "s_waitcnt lgkmcnt(0)\n"
    "v_mfma_f32_16x16x32_bf16 v[208:211], a[160:163], a[132:135], v[208:211]\n"
    "s_waitcnt vmcnt(6)\n"
    "v_lshlrev_b32 v68, 16, v60\n"
    "v_mfma_f32_16x16x32_bf16 v[208:211], a[176:179], a[148:151], v[208:211]\n"
    "v_and_b32 v69, 0xffff0000, v60\n"
    "v_lshlrev_b32 v70, 16, v61\n"
    "v_mfma_f32_16x16x32_bf16 v[212:215], a[164:167], a[132:135], v[212:215]\n"
    "v_and_b32 v71, 0xffff0000, v61\n"
    "v_mfma_f32_16x16x32_bf16 v[212:215], a[180:183], a[148:151], v[212:215]\n"
    "v_lshlrev_b32 v72, 16, v62\n"
    "v_and_b32 v73, 0xffff0000, v62\n"
    "v_mfma_f32_16x16x32_bf16 v[216:219], a[168:171], a[132:135], v[216:219]\n"
    "v_lshlrev_b32 v74, 16, v63\n"
    "v_mfma_f32_16x16x32_bf16 v[216:219], a[184:187], a[148:151], v[216:219]\n"
    "v_and_b32 v75, 0xffff0000, v63\n"
    "v_lshlrev_b32 v76, 16, v64\n"
    "v_mfma_f32_16x16x32_bf16 v[220:223], a[172:175], a[132:135], v[220:223]\n"
    "v_and_b32 v77, 0xffff0000, v64\n"
    "v_mfma_f32_16x16x32_bf16 v[220:223], a[188:191], a[148:151], v[220:223]\n"
    "v_lshlrev_b32 v78, 16, v65\n"
    "v_and_b32 v79, 0xffff0000, v65\n"
    "v_mfma_f32_16x16x32_bf16 v[224:227], a[160:163], a[136:139], v[224:227]\n"
    "v_lshlrev_b32 v80, 16, v66\n"
    "v_mfma_f32_16x16x32_bf16 v[224:227], a[176:179], a[152:155], v[224:227]\n"
    "v_and_b32 v81, 0xffff0000, v66\n"
    "v_lshlrev_b32 v82, 16, v67\n"
    "v_mfma_f32_16x16x32_bf16 v[228:231], a[164:167], a[136:139], v[228:231]\n"
    "v_and_b32 v83, 0xffff0000, v67\n"
    "v_mfma_f32_16x16x32_bf16 v[228:231], a[180:183], a[152:155], v[228:231]\n"
    "v_cvt_pk_bf16_f32 v52, v208, v209\n"
    "v_cvt_pk_bf16_f32 v53, v210, v211\n"
    "v_cvt_pk_bf16_f32 v54, v212, v213\n"
    "v_mfma_f32_16x16x32_bf16 v[232:235], a[168:171], a[136:139], v[232:235]\n"
    "v_cvt_pk_bf16_f32 v55, v214, v215\n"
    "v_min_u32 v52, -2, v52\n"
    "v_mfma_f32_16x16x32_bf16 v[232:235], a[184:187], a[152:155], v[232:235]\n"
    "v_min_u32 v53, -2, v53\n"
    "v_min_u32 v54, -2, v54\n"
    "v_mfma_f32_16x16x32_bf16 v[236:239], a[172:175], a[136:139], v[236:239]\n"
    "v_min_u32 v55, -2, v55\n"
    "s_xor_b32 s73, s83, 1\n"
    "s_lshl_b32 s73, s73, 16\n"
    "s_add_u32 s73, s73, s84\n"
    "s_add_u32 s73, s73, 0x2000\n"
    "buffer_store_dwordx4 v[52:55], v8, s[40:43], s73 offen offset:0 sc1\n"
    "v_mfma_f32_16x16x32_bf16 v[236:239], a[188:191], a[152:155], v[236:239]\n"
    "v_cvt_pk_bf16_f32 v56, v216, v217\n"
    "v_cvt_pk_bf16_f32 v57, v218, v219\n"
    "v_cvt_pk_bf16_f32 v58, v220, v221\n"
    "v_mfma_f32_16x16x32_bf16 v[240:243], a[160:163], a[140:143], v[240:243]\n"
    "v_cvt_pk_bf16_f32 v59, v222, v223\n"
    "v_min_u32 v56, -2, v56\n"
    "v_mfma_f32_16x16x32_bf16 v[240:243], a[176:179], a[156:159], v[240:243]\n"
    "v_min_u32 v57, -2, v57\n"
    "v_min_u32 v58, -2, v58\n"
    "v_mfma_f32_16x16x32_bf16 v[244:247], a[164:167], a[140:143], v[244:247]\n"
    "v_min_u32 v59, -2, v59\n"
    "s_xor_b32 s73, s83, 1\n"
    "s_lshl_b32 s73, s73, 16\n"
    "s_add_u32 s73, s73, s84\n"
    "s_add_u32 s73, s73, 0x2000\n"
    "buffer_store_dwordx4 v[56:59], v8, s[40:43], s73 offen offset:1024 sc1\n"
    "v_mfma_f32_16x16x32_bf16 v[244:247], a[180:183], a[156:159], v[244:247]\n"
    "v_cvt_pk_bf16_f32 v44, v224, v225\n"
    "v_cvt_pk_bf16_f32 v45, v226, v227\n"
    "v_cvt_pk_bf16_f32 v46, v228, v229\n"
    "v_mfma_f32_16x16x32_bf16 v[248:251], a[168:171], a[140:143], v[248:251]\n"
    "v_cvt_pk_bf16_f32 v47, v230, v231\n"
    "v_min_u32 v44, -2, v44\n"
    "v_mfma_f32_16x16x32_bf16 v[248:251], a[184:187], a[156:159], v[248:251]\n"
    "v_min_u32 v45, -2, v45\n"
    "v_min_u32 v46, -2, v46\n"
    "v_mfma_f32_16x16x32_bf16 v[252:255], a[172:175], a[140:143], v[252:255]\n"
    "v_min_u32 v47, -2, v47\n"
    "s_xor_b32 s73, s83, 2\n"
    "s_lshl_b32 s73, s73, 16\n"
    "s_add_u32 s73, s73, s84\n"
    "s_add_u32 s73, s73, 0x2000\n"
    "buffer_store_dwordx4 v[44:47], v8, s[40:43], s73 offen offset:0 sc1\n"
    "v_mfma_f32_16x16x32_bf16 v[252:255], a[188:191], a[156:159], v[252:255]\n"
    "v_cvt_pk_bf16_f32 v48, v232, v233\n"
    "v_cvt_pk_bf16_f32 v49, v234, v235\n"
    "v_cvt_pk_bf16_f32 v50, v236, v237\n"
    "v_mfma_f32_16x16x32_bf16 v[192:195], a[160:163], a[128:131], v[192:195]\n"
    "v_cvt_pk_bf16_f32 v51, v238, v239\n"
    "v_min_u32 v48, -2, v48\n"
    "v_mfma_f32_16x16x32_bf16 v[192:195], a[176:179], a[144:147], v[192:195]\n"
    "v_min_u32 v49, -2, v49\n"
    "v_min_u32 v50, -2, v50\n"
    "v_mfma_f32_16x16x32_bf16 v[196:199], a[164:167], a[128:131], v[196:199]\n"
    "v_min_u32 v51, -2, v51\n"
    "s_xor_b32 s73, s83, 2\n"
    "s_lshl_b32 s73, s73, 16\n"
    "s_add_u32 s73, s73, s84\n"
    "s_add_u32 s73, s73, 0x2000\n"
    "buffer_store_dwordx4 v[48:51], v8, s[40:43], s73 offen offset:1024 sc1\n"
    "v_mfma_f32_16x16x32_bf16 v[196:199], a[180:183], a[144:147], v[196:199]\n"
    "v_cvt_pk_bf16_f32 v52, v240, v241\n"
    "v_cvt_pk_bf16_f32 v53, v242, v243\n"
    "v_cvt_pk_bf16_f32 v54, v244, v245\n"
    "v_cvt_pk_bf16_f32 v55, v246, v247\n"
    "v_mfma_f32_16x16x32_bf16 v[200:203], a[168:171], a[128:131], v[200:203]\n"
    "v_min_u32 v52, -2, v52\n"
    "v_min_u32 v53, -2, v53\n"
    "v_min_u32 v54, -2, v54\n"
    "v_min_u32 v55, -2, v55\n"
    "v_mfma_f32_16x16x32_bf16 v[200:203], a[184:187], a[144:147], v[200:203]\n"
    "s_xor_b32 s73, s83, 3\n"
    "s_lshl_b32 s73, s73, 16\n"
    "s_add_u32 s73, s73, s84\n"
    "s_add_u32 s73, s73, 0x2000\n"
    "buffer_store_dwordx4 v[52:55], v8, s[40:43], s73 offen offset:0 sc1\n"
    "v_cvt_pk_bf16_f32 v56, v248, v249\n"
    "v_cvt_pk_bf16_f32 v57, v250, v251\n"
    "v_mfma_f32_16x16x32_bf16 v[204:207], a[172:175], a[128:131], v[204:207]\n"
    "v_cvt_pk_bf16_f32 v58, v252, v253\n"
    "v_cvt_pk_bf16_f32 v59, v254, v255\n"
    "v_min_u32 v56, -2, v56\n"
    "v_min_u32 v57, -2, v57\n"
    "v_mfma_f32_16x16x32_bf16 v[204:207], a[188:191], a[144:147], v[204:207]\n"
    "v_min_u32 v58, -2, v58\n"
    "v_min_u32 v59, -2, v59\n"
    "s_xor_b32 s73, s83, 3\n"
    "s_lshl_b32 s73, s73, 16\n"
    "s_add_u32 s73, s73, s84\n"
    "s_add_u32 s73, s73, 0x2000\n"
    "buffer_store_dwordx4 v[56:59], v8, s[40:43], s73 offen offset:1024 sc1\n"
    "s_xor_b32 s73, s83, 1\n"
    "s_lshl_b32 s73, s73, 14\n"
    "s_add_u32 s73, s73, s87\n"
    "s_add_u32 s73, s73, 0x2000\n"
    "buffer_load_dwordx4 v[168:171], v8, s[40:43], s73 offen offset:0 sc1\n"
    "buffer_load_dwordx4 v[172:175], v8, s[40:43], s73 offen offset:1024 sc1\n"
    "s_xor_b32 s73, s83, 2\n"
    "s_lshl_b32 s73, s73, 14\n"
    "s_add_u32 s73, s73, s87\n"
    "s_add_u32 s73, s73, 0x2000\n"
    "buffer_load_dwordx4 v[176:179], v8, s[40:43], s73 offen offset:0 sc1\n"
    "buffer_load_dwordx4 v[180:183], v8, s[40:43], s73 offen offset:1024 sc1\n"
    "s_xor_b32 s73, s83, 3\n"
    "s_lshl_b32 s73, s73, 14\n"
    "s_add_u32 s73, s73, s87\n"
    "s_add_u32 s73, s73, 0x2000\n"
    "buffer_load_dwordx4 v[184:187], v8, s[40:43], s73 offen offset:0 sc1\n"
    "buffer_load_dwordx4 v[188:191], v8, s[40:43], s73 offen offset:1024 sc1\n"
    "s_waitcnt vmcnt(16)\n"
    "v_lshlrev_b32 v100, 16, v144\n"
    "v_and_b32 v101, 0xffff0000, v144\n"
    "v_pk_add_f32 v[128:129], v[128:129], v[100:101]\n"
    "v_lshlrev_b32 v102, 16, v145\n"
    "v_and_b32 v103, 0xffff0000, v145\n"
    "v_pk_add_f32 v[130:131], v[130:131], v[102:103]\n"
    "v_lshlrev_b32 v100, 16, v146\n"
    "v_and_b32 v101, 0xffff0000, v146\n"
    "v_pk_add_f32 v[132:133], v[132:133], v[100:101]\n"
    "v_lshlrev_b32 v102, 16, v147\n"
    "v_and_b32 v103, 0xffff0000, v147\n"
    "v_pk_add_f32 v[134:135], v[134:135], v[102:103]\n"
    "v_lshlrev_b32 v100, 16, v148\n"
    "v_and_b32 v101, 0xffff0000, v148\n"
    "v_pk_add_f32 v[136:137], v[136:137], v[100:101]\n"
    "v_lshlrev_b32 v102, 16, v149\n"
    "v_and_b32 v103, 0xffff0000, v149\n"
    "v_pk_add_f32 v[138:139], v[138:139], v[102:103]\n"
    "v_lshlrev_b32 v100, 16, v150\n"
    "v_and_b32 v101, 0xffff0000, v150\n"
    "v_pk_add_f32 v[140:141], v[140:141], v[100:101]\n"
    "v_lshlrev_b32 v102, 16, v151\n"
    "v_and_b32 v103, 0xffff0000, v151\n"
    "v_pk_add_f32 v[142:143], v[142:143], v[102:103]\n"
    "s_waitcnt vmcnt(14)\n"
    "v_lshlrev_b32 v100, 16, v152\n"
    "v_and_b32 v101, 0xffff0000, v152\n"
    "v_pk_add_f32 v[128:129], v[128:129], v[100:101]\n"
    "v_lshlrev_b32 v102, 16, v153\n"
    "v_and_b32 v103, 0xffff0000, v153\n"
    "v_pk_add_f32 v[130:131], v[130:131], v[102:103]\n"
    "v_lshlrev_b32 v100, 16, v154\n"
    "v_and_b32 v101, 0xffff0000, v154\n"
    "v_pk_add_f32 v[132:133], v[132:133], v[100:101]\n"
    "v_lshlrev_b32 v102, 16, v155\n"
    "v_and_b32 v103, 0xffff0000, v155\n"
    "v_pk_add_f32 v[134:135], v[134:135], v[102:103]\n"
    "v_lshlrev_b32 v100, 16, v156\n"
    "v_and_b32 v101, 0xffff0000, v156\n"
    "v_pk_add_f32 v[136:137], v[136:137], v[100:101]\n"
    "v_lshlrev_b32 v102, 16, v157\n"
    "v_and_b32 v103, 0xffff0000, v157\n"
    "v_pk_add_f32 v[138:139], v[138:139], v[102:103]\n"
    "v_lshlrev_b32 v100, 16, v158\n"
    "v_and_b32 v101, 0xffff0000, v158\n"
    "v_pk_add_f32 v[140:141], v[140:141], v[100:101]\n"
    "v_lshlrev_b32 v102, 16, v159\n"
    "v_and_b32 v103, 0xffff0000, v159\n"
    "v_pk_add_f32 v[142:143], v[142:143], v[102:103]\n"
    "s_waitcnt vmcnt(12)\n"
    "v_lshlrev_b32 v100, 16, v160\n"
    "v_and_b32 v101, 0xffff0000, v160\n"
    "v_pk_add_f32 v[128:129], v[128:129], v[100:101]\n"
    "v_lshlrev_b32 v102, 16, v161\n"
    "v_and_b32 v103, 0xffff0000, v161\n"
    "v_pk_add_f32 v[130:131], v[130:131], v[102:103]\n"
    "v_lshlrev_b32 v100, 16, v162\n"
    "v_and_b32 v101, 0xffff0000, v162\n"
    "v_pk_add_f32 v[132:133], v[132:133], v[100:101]\n"
    "v_lshlrev_b32 v102, 16, v163\n"
    "v_and_b32 v103, 0xffff0000, v163\n"
    "v_pk_add_f32 v[134:135], v[134:135], v[102:103]\n"
    "v_lshlrev_b32 v100, 16, v164\n"
    "v_and_b32 v101, 0xffff0000, v164\n"
    "v_pk_add_f32 v[136:137], v[136:137], v[100:101]\n"
    "v_lshlrev_b32 v102, 16, v165\n"
    "v_and_b32 v103, 0xffff0000, v165\n"
    "v_pk_add_f32 v[138:139], v[138:139], v[102:103]\n"
    "v_lshlrev_b32 v100, 16, v166\n"
    "v_and_b32 v101, 0xffff0000, v166\n"
    "v_pk_add_f32 v[140:141], v[140:141], v[100:101]\n"
    "v_lshlrev_b32 v102, 16, v167\n"
    "v_and_b32 v103, 0xffff0000, v167\n"
    "v_pk_add_f32 v[142:143], v[142:143], v[102:103]\n"
    "v_mul_f32 v84, 0xbfb8aa3b, v128\n"
    "v_mul_f32 v85, 0xbfb8aa3b, v129\n"
    "v_mul_f32 v86, 0xbfb8aa3b, v130\n"
    "v_mul_f32 v87, 0xbfb8aa3b, v131\n"
    "v_mul_f32 v88, 0xbfb8aa3b, v132\n"
    "v_mul_f32 v89, 0xbfb8aa3b, v133\n"
    "v_mul_f32 v90, 0xbfb8aa3b, v134\n"
    "v_mul_f32 v91, 0xbfb8aa3b, v135\n"
    "v_mul_f32 v92, 0xbfb8aa3b, v136\n"
    "v_mul_f32 v93, 0xbfb8aa3b, v137\n"
    "v_mul_f32 v94, 0xbfb8aa3b, v138\n"
    "v_mul_f32 v95, 0xbfb8aa3b, v139\n"
    "v_mul_f32 v96, 0xbfb8aa3b, v140\n"
    "v_mul_f32 v97, 0xbfb8aa3b, v141\n"
    "v_mul_f32 v98, 0xbfb8aa3b, v142\n"
    "v_mul_f32 v99, 0xbfb8aa3b, v143\n"
    "v_exp_f32 v84, v84\n"
    "v_exp_f32 v85, v85\n"
    "v_exp_f32 v86, v86\n"
    "v_exp_f32 v87, v87\n"
    "v_exp_f32 v88, v88\n"
    "v_exp_f32 v89, v89\n"
    "v_exp_f32 v90, v90\n"
    "v_exp_f32 v91, v91\n"
    "v_exp_f32 v92, v92\n"
    "v_exp_f32 v93, v93\n"
    "v_exp_f32 v94, v94\n"
    "v_exp_f32 v95, v95\n"
    "v_exp_f32 v96, v96\n"
    "v_exp_f32 v97, v97\n"
    "v_exp_f32 v98, v98\n"
    "v_exp_f32 v99, v99\n"
    "v_add_f32 v84, 1.0, v84\n"
    "v_add_f32 v85, 1.0, v85\n"
    "v_add_f32 v86, 1.0, v86\n"
    "v_add_f32 v87, 1.0, v87\n"
    "v_add_f32 v88, 1.0, v88\n"
    "v_add_f32 v89, 1.0, v89\n"
    "v_add_f32 v90, 1.0, v90\n"
    "v_add_f32 v91, 1.0, v91\n"
    "v_add_f32 v92, 1.0, v92\n"
    "v_add_f32 v93, 1.0, v93\n"
    "v_add_f32 v94, 1.0, v94\n"
    "v_add_f32 v95, 1.0, v95\n"
    "v_add_f32 v96, 1.0, v96\n"
    "v_add_f32 v97, 1.0, v97\n"
    "v_add_f32 v98, 1.0, v98\n"
    "v_add_f32 v99, 1.0, v99\n"
    "v_rcp_f32 v84, v84\n"
    "v_rcp_f32 v85, v85\n"
    "v_rcp_f32 v86, v86\n"
    "v_rcp_f32 v87, v87\n"
    "v_rcp_f32 v88, v88\n"
    "v_rcp_f32 v89, v89\n"
    "v_rcp_f32 v90, v90\n"
    "v_rcp_f32 v91, v91\n"
    "v_rcp_f32 v92, v92\n"
    "v_rcp_f32 v93, v93\n"
    "v_rcp_f32 v94, v94\n"
    "v_rcp_f32 v95, v95\n"
    "v_rcp_f32 v96, v96\n"
    "v_rcp_f32 v97, v97\n"
    "v_rcp_f32 v98, v98\n"
    "v_rcp_f32 v99, v99\n"
    "v_mul_f32 v128, v128, v84\n"
    "v_mul_f32 v129, v129, v85\n"
    "v_mul_f32 v130, v130, v86\n"
    "v_mul_f32 v131, v131, v87\n"
    "v_mul_f32 v132, v132, v88\n"
    "v_mul_f32 v133, v133, v89\n"
    "v_mul_f32 v134, v134, v90\n"
    "v_mul_f32 v135, v135, v91\n"
    "v_mul_f32 v136, v136, v92\n"
    "v_mul_f32 v137, v137, v93\n"
    "v_mul_f32 v138, v138, v94\n"
    "v_mul_f32 v139, v139, v95\n"
    "v_mul_f32 v140, v140, v96\n"
    "v_mul_f32 v141, v141, v97\n"
    "v_mul_f32 v142, v142, v98\n"
    "v_mul_f32 v143, v143, v99\n"
    "s_waitcnt vmcnt(4)\n"
    "s_mov_b32 s89, 0x40000\n"
    ".Lp26_%=:\n"
    "v_max3_u32 v28, v168, v169, v170\n"
    "v_max3_u32 v30, v171, v172, v173\n"
    "v_max3_u32 v28, v28, v174, v175\n"
    "v_max_u32 v28, v28, v30\n"
    "v_cmp_eq_u32 vcc, -1, v28\n"
    "s_nop 4\n"
    "s_cbranch_vccz .Lq26_%=\n"
    "s_waitcnt vmcnt(0)\n"
    "s_xor_b32 s73, s83, 1\n"
    "s_lshl_b32 s73, s73, 14\n"
    "s_add_u32 s73, s73, s87\n"
    "s_add_u32 s73, s73, 0x2000\n"
    "buffer_load_dwordx4 v[168:171], v8, s[40:43], s73 offen offset:0 sc1\n"
    "buffer_load_dwordx4 v[172:175], v8, s[40:43], s73 offen offset:1024 sc1\n"
    "s_xor_b32 s73, s83, 2\n"
    "s_lshl_b32 s73, s73, 14\n"
    "s_add_u32 s73, s73, s87\n"
    "s_add_u32 s73, s73, 0x2000\n"
    "buffer_load_dwordx4 v[176:179], v8, s[40:43], s73 offen offset:0 sc1\n"
    "buffer_load_dwordx4 v[180:183], v8, s[40:43], s73 offen offset:1024 sc1\n"
    "s_xor_b32 s73, s83, 3\n"
    "s_lshl_b32 s73, s73, 14\n"
    "s_add_u32 s73, s73, s87\n"
    "s_add_u32 s73, s73, 0x2000\n"
    "buffer_load_dwordx4 v[184:187], v8, s[40:43], s73 offen offset:0 sc1\n"
    "buffer_load_dwordx4 v[188:191], v8, s[40:43], s73 offen offset:1024 sc1\n"
    "s_waitcnt vmcnt(0)\n"
    "s_sub_u32 s89, s89, 1\n"
    "s_cbranch_scc0 .Lp26_%=\n"
    ".Lq26_%=:\n"
    "v_lshlrev_b32 v100, 16, v168\n"
    "v_and_b32 v101, 0xffff0000, v168\n"
    "v_pk_add_f32 v[192:193], v[192:193], v[100:101]\n"
    "v_lshlrev_b32 v102, 16, v169\n"
    "v_and_b32 v103, 0xffff0000, v169\n"
    "v_pk_add_f32 v[194:195], v[194:195], v[102:103]\n"
    "v_lshlrev_b32 v100, 16, v170\n"
    "v_and_b32 v101, 0xffff0000, v170\n"
    "v_pk_add_f32 v[196:197], v[196:197], v[100:101]\n"
    "v_lshlrev_b32 v102, 16, v171\n"
    "v_and_b32 v103, 0xffff0000, v171\n"
    "v_pk_add_f32 v[198:199], v[198:199], v[102:103]\n"
    "v_lshlrev_b32 v100, 16, v172\n"
    "v_and_b32 v101, 0xffff0000, v172\n"
    "v_pk_add_f32 v[200:201], v[200:201], v[100:101]\n"
    "v_lshlrev_b32 v102, 16, v173\n"
    "v_and_b32 v103, 0xffff0000, v173\n"
    "v_pk_add_f32 v[202:203], v[202:203], v[102:103]\n"
    "v_lshlrev_b32 v100, 16, v174\n"
    "v_and_b32 v101, 0xffff0000, v174\n"
    "v_pk_add_f32 v[204:205], v[204:205], v[100:101]\n"
    "v_lshlrev_b32 v102, 16, v175\n"
    "v_and_b32 v103, 0xffff0000, v175\n"
    "v_pk_add_f32 v[206:207], v[206:207], v[102:103]\n"
    "s_waitcnt vmcnt(2)\n"
    "s_mov_b32 s89, 0x40000\n"
    ".Lp27_%=:\n"
    "v_max3_u32 v28, v176, v177, v178\n"
    "v_max3_u32 v30, v179, v180, v181\n"
    "v_max3_u32 v28, v28, v182, v183\n"
    "v_max_u32 v28, v28, v30\n"
    "v_cmp_eq_u32 vcc, -1, v28\n"
    "s_nop 4\n"
    "s_cbranch_vccz .Lq27_%=\n"
    "s_waitcnt vmcnt(0)\n"
    "s_xor_b32 s73, s83, 2\n"
    "s_lshl_b32 s73, s73, 14\n"
    "s_add_u32 s73, s73, s87\n"
    "s_add_u32 s73, s73, 0x2000\n"
    "buffer_load_dwordx4 v[176:179], v8, s[40:43], s73 offen offset:0 sc1\n"
    "buffer_load_dwordx4 v[180:183], v8, s[40:43], s73 offen offset:1024 sc1\n"
    "s_xor_b32 s73, s83, 3\n"
    "s_lshl_b32 s73, s73, 14\n"
    "s_add_u32 s73, s73, s87\n"
    "s_add_u32 s73, s73, 0x2000\n"
    "buffer_load_dwordx4 v[184:187], v8, s[40:43], s73 offen offset:0 sc1\n"
    "buffer_load_dwordx4 v[188:191], v8, s[40:43], s73 offen offset:1024 sc1\n"
    "s_waitcnt vmcnt(0)\n"
    "s_sub_u32 s89, s89, 1\n"
    "s_cbranch_scc0 .Lp27_%=\n"
    ".Lq27_%=:\n"
    "v_lshlrev_b32 v100, 16, v176\n"
    "v_and_b32 v101, 0xffff0000, v176\n"
    "v_pk_add_f32 v[192:193], v[192:193], v[100:101]\n"
    "v_lshlrev_b32 v102, 16, v177\n"
    "v_and_b32 v103, 0xffff0000, v177\n"
    "v_pk_add_f32 v[194:195], v[194:195], v[102:103]\n"
    "v_lshlrev_b32 v100, 16, v178\n"
    "v_and_b32 v101, 0xffff0000, v178\n"
    "v_pk_add_f32 v[196:197], v[196:197], v[100:101]\n"
    "v_lshlrev_b32 v102, 16, v179\n"
    "v_and_b32 v103, 0xffff0000, v179\n"
    "v_pk_add_f32 v[198:199], v[198:199], v[102:103]\n"
    "v_lshlrev_b32 v100, 16, v180\n"
    "v_and_b32 v101, 0xffff0000, v180\n"
    "v_pk_add_f32 v[200:201], v[200:201], v[100:101]\n"
    "v_lshlrev_b32 v102, 16, v181\n"
    "v_and_b32 v103, 0xffff0000, v181\n"
    "v_pk_add_f32 v[202:203], v[202:203], v[102:103]\n"
    "v_lshlrev_b32 v100, 16, v182\n"
    "v_and_b32 v101, 0xffff0000, v182\n"
    "v_pk_add_f32 v[204:205], v[204:205], v[100:101]\n"
    "v_lshlrev_b32 v102, 16, v183\n"
    "v_and_b32 v103, 0xffff0000, v183\n"
    "v_pk_add_f32 v[206:207], v[206:207], v[102:103]\n"
    "s_waitcnt vmcnt(0)\n"
    "s_mov_b32 s89, 0x40000\n"
    ".Lp28_%=:\n"
    "v_max3_u32 v28, v184, v185, v186\n"
    "v_max3_u32 v30, v187, v188, v189\n"
    "v_max3_u32 v28, v28, v190, v191\n"
    "v_max_u32 v28, v28, v30\n"
    "v_cmp_eq_u32 vcc, -1, v28\n"
    "s_nop 4\n"
    "s_cbranch_vccz .Lq28_%=\n"
    "s_waitcnt vmcnt(0)\n"
    "s_xor_b32 s73, s83, 3\n"
    "s_lshl_b32 s73, s73, 14\n"
    "s_add_u32 s73, s73, s87\n"
    "s_add_u32 s73, s73, 0x2000\n"
    "buffer_load_dwordx4 v[184:187], v8, s[40:43], s73 offen offset:0 sc1\n"
    "buffer_load_dwordx4 v[188:191], v8, s[40:43], s73 offen offset:1024 sc1\n"
    "s_waitcnt vmcnt(0)\n"
    "s_sub_u32 s89, s89, 1\n"
    "s_cbranch_scc0 .Lp28_%=\n"
    ".Lq28_%=:\n"
    "v_lshlrev_b32 v100, 16, v184\n"
    "v_and_b32 v101, 0xffff0000, v184\n"
    "v_pk_add_f32 v[192:193], v[192:193], v[100:101]\n"
    "v_lshlrev_b32 v102, 16, v185\n"
    "v_and_b32 v103, 0xffff0000, v185\n"
    "v_pk_add_f32 v[194:195], v[194:195], v[102:103]\n"
    "v_lshlrev_b32 v100, 16, v186\n"
    "v_and_b32 v101, 0xffff0000, v186\n"
    "v_pk_add_f32 v[196:197], v[196:197], v[100:101]\n"
    "v_lshlrev_b32 v102, 16, v187\n"
    "v_and_b32 v103, 0xffff0000, v187\n"
    "v_pk_add_f32 v[198:199], v[198:199], v[102:103]\n"
    "v_lshlrev_b32 v100, 16, v188\n"
    "v_and_b32 v101, 0xffff0000, v188\n"
    "v_pk_add_f32 v[200:201], v[200:201], v[100:101]\n"
    "v_lshlrev_b32 v102, 16, v189\n"
    "v_and_b32 v103, 0xffff0000, v189\n"
    "v_pk_add_f32 v[202:203], v[202:203], v[102:103]\n"
    "v_lshlrev_b32 v100, 16, v190\n"
    "v_and_b32 v101, 0xffff0000, v190\n"
    "v_pk_add_f32 v[204:205], v[204:205], v[100:101]\n"
    "v_lshlrev_b32 v102, 16, v191\n"
    "v_and_b32 v103, 0xffff0000, v191\n"
    "v_pk_add_f32 v[206:207], v[206:207], v[102:103]\n"
    "v_pk_fma_f32 v[192:193], v[192:193], v[128:129], v[68:69]\n"
    "v_pk_fma_f32 v[194:195], v[194:195], v[130:131], v[70:71]\n"
    "v_pk_fma_f32 v[196:197], v[196:197], v[132:133], v[72:73]\n"
    "v_pk_fma_f32 v[198:199], v[198:199], v[134:135], v[74:75]\n"
    "v_pk_fma_f32 v[200:201], v[200:201], v[136:137], v[76:77]\n"
    "v_pk_fma_f32 v[202:203], v[202:203], v[138:139], v[78:79]\n"
    "v_pk_fma_f32 v[204:205], v[204:205], v[140:141], v[80:81]\n"
    "v_pk_fma_f32 v[206:207], v[206:207], v[142:143], v[82:83]\n"
    "v_cvt_pk_bf16_f32 v60, v192, v193\n"
    "v_cvt_pk_bf16_f32 v61, v194, v195\n"
    "v_cvt_pk_bf16_f32 v62, v196, v197\n"
    "v_cvt_pk_bf16_f32 v63, v198, v199\n"
    "v_cvt_pk_bf16_f32 v64, v200, v201\n"
    "v_cvt_pk_bf16_f32 v65, v202, v203\n"
    "v_cvt_pk_bf16_f32 v66, v204, v205\n"
    "v_cvt_pk_bf16_f32 v67, v206, v207\n"
    "s_nop 0\n"
    "v_permlane16_swap_b32 v60, v62\n"
    "v_permlane16_swap_b32 v61, v63\n"
    "v_permlane16_swap_b32 v64, v66\n"
    "v_permlane16_swap_b32 v65, v67\n"
    "buffer_store_dwordx4 v[60:63], v26, s[44:47], 0 offen offset:0 sc1\n"
    "buffer_store_dwordx4 v[64:67], v26, s[44:47], 0 offen offset:64 sc1\n"
    "s_endpgm\n"
    ".Lc20_%=:\n"
    "s_mov_b32 s73, 0x40000\n"
    ".Lp21_%=:\n"
    "v_cmp_ne_u64 vcc, s[64:65], v[6:7]\n"
    "s_nop 4\n"
    "s_cbranch_vccz .Lq21_%=\n"
    "s_sleep 1\n"
    "buffer_load_dwordx2 v[6:7], v5, s[56:59], 0 offen sc1\n"
    "s_waitcnt vmcnt(0)\n"
    "s_sub_u32 s73, s73, 1\n"
    "s_cbranch_scc0 .Lp21_%=\n"
    ".Lq21_%=:\n"
    "s_branch .Lb20_%=\n"
    ".Lc22_%=:\n"
    "s_mov_b32 s73, 0x40000\n"
    ".Lp23_%=:\n"
    "v_cmp_ne_u64 vcc, s[64:65], v[6:7]\n"
    "s_nop 4\n"
    "s_cbranch_vccz .Lq23_%=\n"
    "s_sleep 1\n"
    "buffer_load_dwordx2 v[6:7], v5, s[56:59], 0 offen sc1\n"
    "s_waitcnt vmcnt(0)\n"
    "s_sub_u32 s73, s73, 1\n"
    "s_cbranch_scc0 .Lp23_%=\n"
    ".Lq23_%=:\n"
    "s_branch .Lb22_%=\n"
    ".Lc24_%=:\n"
    "s_mov_b32 s73, 0x40000\n"
    ".Lp25_%=:\n"
    "v_cmp_ne_u64 vcc, s[64:65], v[6:7]\n"
    "s_nop 4\n"
    "s_cbranch_vccz .Lq25_%=\n"
    "s_sleep 1\n"
    "buffer_load_dwordx2 v[6:7], v5, s[56:59], 0 offen sc1\n"
    "s_waitcnt vmcnt(0)\n"
    "s_sub_u32 s73, s73, 1\n"
    "s_cbranch_scc0 .Lp25_%=\n"
    ".Lq25_%=:\n"
    "s_branch .Lb24_%=\n"
    ".Lat_redo0_%=:\n"
    "s_and_b32 s73, s94, 1\n"
    "s_cbranch_scc1 .Lat_ok0_%=\n"
    "s_or_b32 s94, s94, 1\n"
    "s_waitcnt vmcnt(0) lgkmcnt(0)\n"
    "s_mov_b32 s42, 0x80000\n"
    "s_mov_b32 s46, 0x80000\n"
    "v_mov_b32 v124, 0xff800000\n"
    "v_mov_b32 v13, 0xff800000\n"
    "v_mov_b32 v113, 0xff800000\n"
    "v_mov_b32 v14, 0xff800000\n"
    "s_lshl_b32 s93, s38, 15\n"
    "s_lshl_b32 s73, s71, 13\n"
    "s_add_u32 s93, s93, s73\n"
    "s_mov_b32 s91, 1\n"
    "s_nop 7\n"
    ".Lat_mx0_%=:\n"
    "buffer_load_dwordx4 a[64:67], v8, s[40:43], s93 offen offset:0 sc1\n"
    "buffer_load_dwordx4 a[68:71], v8, s[40:43], s93 offen offset:1024 sc1\n"
    "buffer_load_dwordx4 a[72:75], v8, s[40:43], s93 offen offset:2048 sc1\n"
    "buffer_load_dwordx4 a[76:79], v8, s[40:43], s93 offen offset:3072 sc1\n"
    "buffer_load_dwordx4 a[80:83], v120, s[40:43], s93 offen offset:0 sc1\n"
    "buffer_load_dwordx4 a[84:87], v120, s[40:43], s93 offen offset:1024 sc1\n"
    "buffer_load_dwordx4 a[88:91], v120, s[40:43], s93 offen offset:2048 sc1\n"
    "buffer_load_dwordx4 a[92:95], v120, s[40:43], s93 offen offset:3072 sc1\n"
    "s_waitcnt vmcnt(0)\n"
    "v_mfma_f32_16x16x32_bf16 v[16:19], a[64:67], a[128:131], 0\n"
    "v_mfma_f32_16x16x32_bf16 v[16:19], a[68:71], a[132:135], v[16:19]\n"
    "v_mfma_f32_16x16x32_bf16 v[16:19], a[72:75], a[136:139], v[16:19]\n"
    "v_mfma_f32_16x16x32_bf16 v[16:19], a[76:79], a[140:143], v[16:19]\n"
    "v_mfma_f32_16x16x32_bf16 v[20:23], a[80:83], a[128:131], 0\n"
    "v_mfma_f32_16x16x32_bf16 v[20:23], a[84:87], a[132:135], v[20:23]\n"
    "v_mfma_f32_16x16x32_bf16 v[20:23], a[88:91], a[136:139], v[20:23]\n"
    "v_mfma_f32_16x16x32_bf16 v[20:23], a[92:95], a[140:143], v[20:23]\n"
    "v_mfma_f32_16x16x32_bf16 v[24:27], a[64:67], a[144:147], 0\n"
    "v_mfma_f32_16x16x32_bf16 v[24:27], a[68:71], a[148:151], v[24:27]\n"
    "v_mfma_f32_16x16x32_bf16 v[24:27], a[72:75], a[152:155], v[24:27]\n"
    "v_mfma_f32_16x16x32_bf16 v[24:27], a[76:79], a[156:159], v[24:27]\n"
    "v_mfma_f32_16x16x32_bf16 v[28:31], a[80:83], a[144:147], 0\n"
    "v_mfma_f32_16x16x32_bf16 v[28:31], a[84:87], a[148:151], v[28:31]\n"
    "v_mfma_f32_16x16x32_bf16 v[28:31], a[88:91], a[152:155], v[28:31]\n"
    "v_mfma_f32_16x16x32_bf16 v[28:31], a[92:95], a[156:159], v[28:31]\n"
    "v_mfma_f32_16x16x32_bf16 v[32:35], a[64:67], a[160:163], 0\n"
    "v_mfma_f32_16x16x32_bf16 v[32:35], a[68:71], a[164:167], v[32:35]\n"
    "v_mfma_f32_16x16x32_bf16 v[32:35], a[72:75], a[168:171], v[32:35]\n"
    "v_mfma_f32_16x16x32_bf16 v[32:35], a[76:79], a[172:175], v[32:35]\n"
    "v_mfma_f32_16x16x32_bf16 v[36:39], a[80:83], a[160:163], 0\n"
    "v_mfma_f32_16x16x32_bf16 v[36:39], a[84:87], a[164:167], v[36:39]\n"
    "v_mfma_f32_16x16x32_bf16 v[36:39], a[88:91], a[168:171], v[36:39]\n"
    "v_mfma_f32_16x16x32_bf16 v[36:39], a[92:95], a[172:175], v[36:39]\n"
    "v_mfma_f32_16x16x32_bf16 v[40:43], a[64:67], a[176:179], 0\n"
    "v_mfma_f32_16x16x32_bf16 v[40:43], a[68:71], a[180:183], v[40:43]\n"
    "v_mfma_f32_16x16x32_bf16 v[40:43], a[72:75], a[184:187], v[40:43]\n"
    "v_mfma_f32_16x16x32_bf16 v[40:43], a[76:79], a[188:191], v[40:43]\n"
    "v_mfma_f32_16x16x32_bf16 v[44:47], a[80:83], a[176:179], 0\n"
    "v_mfma_f32_16x16x32_bf16 v[44:47], a[84:87], a[180:183], v[44:47]\n"
    "v_mfma_f32_16x16x32_bf16 v[44:47], a[88:91], a[184:187], v[44:47]\n"
    "v_mfma_f32_16x16x32_bf16 v[44:47], a[92:95], a[188:191], v[44:47]\n"
    "s_nop 7\n"
    "v_max3_f32 v112, v32, v33, v34\n"
    "v_max3_f32 v112, v112, v35, v36\n"
    "v_max3_f32 v112, v112, v37, v38\n"
    "v_max_f32 v112, v112, v39\n"
    "v_max_f32 v113, v113, v112\n"
    "v_max3_f32 v112, v40, v41, v42\n"
    "v_max3_f32 v112, v112, v43, v44\n"
    "v_max3_f32 v112, v112, v45, v46\n"
    "v_max_f32 v112, v112, v47\n"
    "v_max_f32 v14, v14, v112\n"
    "s_cmp_lg_u32 s91, 0\n"
    "s_cbranch_scc1 .Lat_my0_%=\n"
    "v_max3_f32 v112, v16, v17, v18\n"
    "v_max3_f32 v112, v112, v19, v20\n"
    "v_max3_f32 v112, v112, v21, v22\n"
    "v_max_f32 v112, v112, v23\n"
    "v_max_f32 v124, v124, v112\n"
    "v_max3_f32 v112, v24, v25, v26\n"
    "v_max3_f32 v112, v112, v27, v28\n"
    "v_max3_f32 v112, v112, v29, v30\n"
    "v_max_f32 v112, v112, v31\n"
    "v_max_f32 v13, v13, v112\n"
    ".Lat_my0_%=:\n"
    "s_mov_b32 s91, 0\n"
    "s_sub_u32 s93, s93, 0x4000\n"
    "s_cbranch_scc0 .Lat_mx0_%=\n"
    "s_nop 1\n"
    "v_permlane32_swap_b32 v124, v113\n"
    "v_permlane32_swap_b32 v13, v14\n"
    "v_max_f32 v124, v124, v113\n"
    "v_max_f32 v13, v13, v14\n"
    "s_nop 1\n"
    "v_permlane16_swap_b32 v124, v13\n"
    "v_max_f32 v125, v124, v13\n"
    "s_lshl_b32 s89, s38, 15\n"
    "s_add_u32 s89, s89, 0x4000\n"
    "s_mov_b32 s90, s89\n"
    "s_mov_b32 s81, 0x80000\n"
    "s_mov_b32 s82, s81\n"
    "s_lshl_b32 s73, s71, 13\n"
    "s_add_u32 s73, s73, s69\n"
    "v_add_u32 v9, s73, v8\n"
    "s_barrier\n"
    "s_add_u32 m0, s86, s88\n"
    "s_nop 0\n"
    "buffer_load_dwordx4 v12, s[40:43], s89 offen offset:0 sc1 lds\n"
    "buffer_load_dwordx4 v12, s[40:43], s89 offen offset:1024 sc1 lds\n"
    "buffer_load_dwordx4 v12, s[40:43], s89 offen offset:2048 sc1 lds\n"
    "buffer_load_dwordx4 v12, s[40:43], s89 offen offset:3072 sc1 lds\n"
    "s_sub_u32 s89, s89, 0x4000\n"
    "s_cselect_b32 s89, s87, s89\n"
    "s_cselect_b32 s42, s81, s42\n"
    "s_cselect_b32 s81, 0, s81\n"
    "s_add_u32 s73, s86, 0x8000\n"
    "s_and_b32 s73, s73, 0x1ffff\n"
    "s_add_u32 m0, s73, s88\n"
    "s_nop 0\n"
    "buffer_load_dwordx4 v12, s[40:43], s89 offen offset:0 sc1 lds\n"
    "buffer_load_dwordx4 v12, s[40:43], s89 offen offset:1024 sc1 lds\n"
    "buffer_load_dwordx4 v12, s[40:43], s89 offen offset:2048 sc1 lds\n"
    "buffer_load_dwordx4 v12, s[40:43], s89 offen offset:3072 sc1 lds\n"
    "s_sub_u32 s89, s89, 0x4000\n"
    "s_cselect_b32 s89, s87, s89\n"
    "s_cselect_b32 s42, s81, s42\n"
    "s_cselect_b32 s81, 0, s81\n"
    "s_add_u32 s73, s86, 0x4000\n"
    "s_add_u32 m0, s73, s88\n"
    "s_nop 0\n"
    "buffer_load_dwordx4 v12, s[44:47], s90 offen offset:0 sc1 lds\n"
    "buffer_load_dwordx4 v12, s[44:47], s90 offen offset:1024 sc1 lds\n"
    "buffer_load_dwordx4 v12, s[44:47], s90 offen offset:2048 sc1 lds\n"
    "buffer_load_dwordx4 v12, s[44:47], s90 offen offset:3072 sc1 lds\n"
    "s_sub_u32 s90, s90, 0x4000\n"
    "s_cselect_b32 s90, s87, s90\n"
    "s_cselect_b32 s46, s82, s46\n"
    "s_cselect_b32 s82, 0, s82\n"
    "s_add_u32 s73, s86, 0x10000\n"
    "s_and_b32 s73, s73, 0x1ffff\n"
    "s_add_u32 m0, s73, s88\n"
    "s_nop 0\n"
    "buffer_load_dwordx4 v12, s[40:43], s89 offen offset:0 sc1 lds\n"
    "buffer_load_dwordx4 v12, s[40:43], s89 offen offset:1024 sc1 lds\n"
    "buffer_load_dwordx4 v12, s[40:43], s89 offen offset:2048 sc1 lds\n"
    "buffer_load_dwordx4 v12, s[40:43], s89 offen offset:3072 sc1 lds\n"
    "s_sub_u32 s89, s89, 0x4000\n"
    "s_cselect_b32 s89, s87, s89\n"
    "s_cselect_b32 s42, s81, s42\n"
    "s_cselect_b32 s81, 0, s81\n"
    "s_add_u32 s73, s86, 0x8000\n"
    "s_and_b32 s73, s73, 0x1ffff\n"
    "s_add_u32 s73, s73, 0x4000\n"
    "s_add_u32 m0, s73, s88\n"
    "s_nop 0\n"
    "buffer_load_dwordx4 v12, s[44:47], s90 offen offset:0 sc1 lds\n"
    "buffer_load_dwordx4 v12, s[44:47], s90 offen offset:1024 sc1 lds\n"
    "buffer_load_dwordx4 v12, s[44:47], s90 offen offset:2048 sc1 lds\n"
    "buffer_load_dwordx4 v12, s[44:47], s90 offen offset:3072 sc1 lds\n"
    "s_sub_u32 s90, s90, 0x4000\n"
    "s_cselect_b32 s90, s87, s90\n"
    "s_cselect_b32 s46, s82, s46\n"
    "s_cselect_b32 s82, 0, s82\n"
    "v_add_u32 v10, s86, v9\n"
    "v_accvgpr_read_b32 v48, a0\n"
    "v_accvgpr_read_b32 v49, a1\n"
    "v_accvgpr_read_b32 v50, a2\n"
    "v_accvgpr_read_b32 v51, a3\n"
    "v_accvgpr_read_b32 v52, a4\n"
    "v_accvgpr_read_b32 v53, a5\n"
    "v_accvgpr_read_b32 v54, a6\n"
    "v_accvgpr_read_b32 v55, a7\n"
    "v_accvgpr_read_b32 v56, a8\n"
    "v_accvgpr_read_b32 v57, a9\n"
    "v_accvgpr_read_b32 v58, a10\n"
    "v_accvgpr_read_b32 v59, a11\n"
    "v_accvgpr_read_b32 v60, a12\n"
    "v_accvgpr_read_b32 v61, a13\n"
    "v_accvgpr_read_b32 v62, a14\n"
    "v_accvgpr_read_b32 v63, a15\n"
    "v_accvgpr_read_b32 v16, a16\n"
    "v_accvgpr_read_b32 v17, a17\n"
    "v_accvgpr_read_b32 v18, a18\n"
    "v_accvgpr_read_b32 v19, a19\n"
    "v_accvgpr_read_b32 v20, a20\n"
    "v_accvgpr_read_b32 v21, a21\n"
    "v_accvgpr_read_b32 v22, a22\n"
    "v_accvgpr_read_b32 v23, a23\n"
    "v_accvgpr_read_b32 v24, a24\n"
    "v_accvgpr_read_b32 v25, a25\n"
    "v_accvgpr_read_b32 v26, a26\n"
    "v_accvgpr_read_b32 v27, a27\n"
    "v_accvgpr_read_b32 v28, a28\n"
    "v_accvgpr_read_b32 v29, a29\n"
    "v_accvgpr_read_b32 v30, a30\n"
    "v_accvgpr_read_b32 v31, a31\n"
    "s_waitcnt vmcnt(0)\n"
    "s_barrier\n"
    "ds_read_b128 a[64:67], v10 offset:0\n"
    "ds_read_b128 a[68:71], v10 offset:1024\n"
    "ds_read_b128 a[72:75], v10 offset:2048\n"
    "ds_read_b128 a[76:79], v10 offset:3072\n"
    "ds_read_b128 a[80:83], v10 offset:4096\n"
    "ds_read_b128 a[84:87], v10 offset:5120\n"
    "ds_read_b128 a[88:91], v10 offset:6144\n"
    "ds_read_b128 a[92:95], v10 offset:7168\n"
    "s_waitcnt lgkmcnt(0)\n"
    "v_add_u32 v10, s86, v9\n"
    "s_add_u32 s73, s86, 0x8000\n"
    "s_and_b32 s73, s73, 0x1ffff\n"
    "v_add_u32 v11, s73, v9\n"
    "v_mfma_f32_16x16x32_bf16 v[48:51], a[64:67], a[160:163], v[48:51]\n"
    "v_mfma_f32_16x16x32_bf16 v[48:51], a[68:71], a[164:167], v[48:51]\n"
    "v_mfma_f32_16x16x32_bf16 v[48:51], a[72:75], a[168:171], v[48:51]\n"
    "v_mfma_f32_16x16x32_bf16 v[48:51], a[76:79], a[172:175], v[48:51]\n"
    "v_mfma_f32_16x16x32_bf16 v[56:59], a[64:67], a[176:179], v[56:59]\n"
    "ds_read_b128 a[64:67], v11 offset:0\n"
    "v_mfma_f32_16x16x32_bf16 v[56:59], a[68:71], a[180:183], v[56:59]\n"
    "ds_read_b128 a[68:71], v11 offset:1024\n"
    "v_mfma_f32_16x16x32_bf16 v[56:59], a[72:75], a[184:187], v[56:59]\n"
    "ds_read_b128 a[72:75], v11 offset:2048\n"
    "v_mfma_f32_16x16x32_bf16 v[56:59], a[76:79], a[188:191], v[56:59]\n"
    "ds_read_b128 a[76:79], v11 offset:3072\n"
    "v_mfma_f32_16x16x32_bf16 v[52:55], a[80:83], a[160:163], v[52:55]\n"
    "v_mfma_f32_16x16x32_bf16 v[52:55], a[84:87], a[164:167], v[52:55]\n"
    "v_mfma_f32_16x16x32_bf16 v[52:55], a[88:91], a[168:171], v[52:55]\n"
    "v_mfma_f32_16x16x32_bf16 v[52:55], a[92:95], a[172:175], v[52:55]\n"
    "v_mfma_f32_16x16x32_bf16 v[60:63], a[80:83], a[176:179], v[60:63]\n"
    "ds_read_b128 a[80:83], v11 offset:4096\n"
    "v_mfma_f32_16x16x32_bf16 v[60:63], a[84:87], a[180:183], v[60:63]\n"
    "ds_read_b128 a[84:87], v11 offset:5120\n"
    "v_mfma_f32_16x16x32_bf16 v[60:63], a[88:91], a[184:187], v[60:63]\n"
    "ds_read_b128 a[88:91], v11 offset:6144\n"
    "v_mfma_f32_16x16x32_bf16 v[60:63], a[92:95], a[188:191], v[60:63]\n"
    "ds_read_b128 a[92:95], v11 offset:7168\n"
    "s_waitcnt lgkmcnt(7)\n"
    "v_mfma_f32_16x16x32_bf16 v[16:19], a[64:67], a[128:131], v[16:19]\n"
    "s_waitcnt lgkmcnt(6)\n"
    "v_mfma_f32_16x16x32_bf16 v[16:19], a[68:71], a[132:135], v[16:19]\n"
    "s_waitcnt lgkmcnt(5)\n"
    "v_mfma_f32_16x16x32_bf16 v[16:19], a[72:75], a[136:139], v[16:19]\n"
    "s_waitcnt lgkmcnt(4)\n"
    "v_mfma_f32_16x16x32_bf16 v[16:19], a[76:79], a[140:143], v[16:19]\n"
    "v_mfma_f32_16x16x32_bf16 v[24:27], a[64:67], a[144:147], v[24:27]\n"
    "s_nop 3\n"
    "v_mfma_f32_16x16x32_bf16 v[24:27], a[68:71], a[148:151], v[24:27]\n"
    "v_mfma_f32_16x16x32_bf16 v[24:27], a[72:75], a[152:155], v[24:27]\n"
    "v_mfma_f32_16x16x32_bf16 v[24:27], a[76:79], a[156:159], v[24:27]\n"
    "s_waitcnt lgkmcnt(3)\n"
    "v_mfma_f32_16x16x32_bf16 v[20:23], a[80:83], a[128:131], v[20:23]\n"
    "v_max3_f32 v113, v48, v49, v50\n"
    "s_waitcnt lgkmcnt(2)\n"
    "v_mfma_f32_16x16x32_bf16 v[20:23], a[84:87], a[132:135], v[20:23]\n"
    "v_max3_f32 v113, v113, v51, v52\n"
    "s_waitcnt lgkmcnt(1)\n"
    "v_mfma_f32_16x16x32_bf16 v[20:23], a[88:91], a[136:139], v[20:23]\n"
    "v_max3_f32 v113, v113, v53, v54\n"
    "s_waitcnt lgkmcnt(0)\n"
    "v_mfma_f32_16x16x32_bf16 v[20:23], a[92:95], a[140:143], v[20:23]\n"
    "v_max_f32 v113, v113, v55\n"
    "v_mfma_f32_16x16x32_bf16 v[28:31], a[80:83], a[144:147], v[28:31]\n"
    "v_max3_f32 v14, v56, v57, v58\n"
    "v_mfma_f32_16x16x32_bf16 v[28:31], a[84:87], a[148:151], v[28:31]\n"
    "v_max3_f32 v14, v14, v59, v60\n"
    "v_mfma_f32_16x16x32_bf16 v[28:31], a[88:91], a[152:155], v[28:31]\n"
    "v_max3_f32 v14, v14, v61, v62\n"
    "v_mfma_f32_16x16x32_bf16 v[28:31], a[92:95], a[156:159], v[28:31]\n"
    "v_max_f32 v14, v14, v63\n"
    "v_mfma_f32_16x16x32_bf16 v[32:35], a[64:67], a[160:163], 0\n"
    "v_mfma_f32_16x16x32_bf16 v[32:35], a[68:71], a[164:167], v[32:35]\n"
    "v_mfma_f32_16x16x32_bf16 v[32:35], a[72:75], a[168:171], v[32:35]\n"
    "v_mfma_f32_16x16x32_bf16 v[32:35], a[76:79], a[172:175], v[32:35]\n"
    "s_nop 3\n"
    "v_mfma_f32_16x16x32_bf16 v[36:39], a[80:83], a[160:163], 0\n"
    "v_max3_f32 v124, v16, v17, v18\n"
    "v_max3_f32 v124, v124, v19, v20\n"
    "v_mfma_f32_16x16x32_bf16 v[36:39], a[84:87], a[164:167], v[36:39]\n"
    "v_max3_f32 v124, v124, v21, v22\n"
    "v_max_f32 v124, v124, v23\n"
    "v_mfma_f32_16x16x32_bf16 v[36:39], a[88:91], a[168:171], v[36:39]\n"
    "v_max3_f32 v13, v24, v25, v26\n"
    "v_max3_f32 v13, v13, v27, v28\n"
    "v_mfma_f32_16x16x32_bf16 v[36:39], a[92:95], a[172:175], v[36:39]\n"
    "v_max3_f32 v13, v13, v29, v30\n"
    "v_max_f32 v13, v13, v31\n"
    "v_mfma_f32_16x16x32_bf16 v[40:43], a[64:67], a[176:179], 0\n"
    "s_nop 1\n"
    "v_permlane32_swap_b32 v124, v113\n"
    "v_permlane32_swap_b32 v13, v14\n"
    "v_mfma_f32_16x16x32_bf16 v[40:43], a[68:71], a[180:183], v[40:43]\n"
    "v_max_f32 v124, v124, v113\n"
    "v_max_f32 v13, v13, v14\n"
    "v_mfma_f32_16x16x32_bf16 v[40:43], a[72:75], a[184:187], v[40:43]\n"
    "s_nop 1\n"
    "v_permlane16_swap_b32 v124, v13\n"
    "v_mfma_f32_16x16x32_bf16 v[40:43], a[76:79], a[188:191], v[40:43]\n"
    "v_max3_f32 v124, v124, v13, v125\n"
    "ds_write_b32 v122, v124 offset:1024\n"
    "v_mfma_f32_16x16x32_bf16 v[44:47], a[80:83], a[176:179], 0\n"
    "v_mfma_f32_16x16x32_bf16 v[44:47], a[84:87], a[180:183], v[44:47]\n"
    "s_waitcnt vmcnt(0) lgkmcnt(0)\n"
    "s_barrier\n"
    "ds_read_b32 v113, v123 offset:1024\n"
    "v_mfma_f32_16x16x32_bf16 v[44:47], a[88:91], a[184:187], v[44:47]\n"
    "v_mfma_f32_16x16x32_bf16 v[44:47], a[92:95], a[188:191], v[44:47]\n"
    "ds_read_b128 a[96:99], v10 offset:16384\n"
    "ds_read_b128 a[100:103], v10 offset:17408\n"
    "ds_read_b128 a[104:107], v10 offset:18432\n"
    "ds_read_b128 a[108:111], v10 offset:19456\n"
    "ds_read_b128 a[112:115], v10 offset:20480\n"
    "ds_read_b128 a[116:119], v10 offset:21504\n"
    "ds_read_b128 a[120:123], v10 offset:22528\n"
    "ds_read_b128 a[124:127], v10 offset:23552\n"
    "s_add_u32 s73, s86, 0x18000\n"
    "s_and_b32 s73, s73, 0x1ffff\n"
    "s_add_u32 m0, s73, s88\n"
    "s_nop 0\n"
    "buffer_load_dwordx4 v12, s[40:43], s89 offen offset:0 sc1 lds\n"
    "buffer_load_dwordx4 v12, s[40:43], s89 offen offset:1024 sc1 lds\n"
    "buffer_load_dwordx4 v12, s[40:43], s89 offen offset:2048 sc1 lds\n"
    "buffer_load_dwordx4 v12, s[40:43], s89 offen offset:3072 sc1 lds\n"
    "s_sub_u32 s89, s89, 0x4000\n"
    "s_cselect_b32 s89, s87, s89\n"
    "s_cselect_b32 s42, s81, s42\n"
    "s_cselect_b32 s81, 0, s81\n"
    "s_waitcnt lgkmcnt(8)\n"
    "v_max_f32 v124, v124, v113\n"
    "v_mov_b32 v13, v124\n"
    "s_nop 1\n"
    "v_permlane16_swap_b32 v124, v13\n"
    "v_mov_b32 v113, v124\n"
    "v_mov_b32 v14, v13\n"
    "s_nop 0\n"
    "v_permlane32_swap_b32 v124, v113\n"
    "v_permlane32_swap_b32 v13, v14\n"
    "s_add_u32 s73, s86, 0x10000\n"
    "s_and_b32 s73, s73, 0x1ffff\n"
    "v_add_u32 v10, s73, v9\n"
    "v_sub_f32 v48, v48, v113\n"
    "ds_read_b128 a[64:67], v10 offset:0\n"
    "v_sub_f32 v49, v49, v113\n"
    "v_sub_f32 v50, v50, v113\n"
    "ds_read_b128 a[68:71], v10 offset:1024\n"
    "v_sub_f32 v51, v51, v113\n"
    "v_sub_f32 v52, v52, v113\n"
    "ds_read_b128 a[72:75], v10 offset:2048\n"
    "v_sub_f32 v53, v53, v113\n"
    "v_sub_f32 v54, v54, v113\n"
    "ds_read_b128 a[76:79], v10 offset:3072\n"
    "v_sub_f32 v55, v55, v113\n"
    "v_exp_f32 v48, v48\n"
    "ds_read_b128 a[80:83], v10 offset:4096\n"
    "v_exp_f32 v49, v49\n"
    "v_exp_f32 v50, v50\n"
    "ds_read_b128 a[84:87], v10 offset:5120\n"
    "v_exp_f32 v51, v51\n"
    "v_exp_f32 v52, v52\n"
    "ds_read_b128 a[88:91], v10 offset:6144\n"
    "v_exp_f32 v53, v53\n"
    "v_exp_f32 v54, v54\n"
    "ds_read_b128 a[92:95], v10 offset:7168\n"
    "v_exp_f32 v55, v55\n"
    "v_cvt_pk_bf16_f32 v104, v48, v49\n"
    "v_cvt_pk_bf16_f32 v105, v50, v51\n"
    "v_cvt_pk_bf16_f32 v106, v52, v53\n"
    "v_cvt_pk_bf16_f32 v107, v54, v55\n"
    "s_nop 1\n"
    "s_branch .Lat_prob0_%=\n"
    ".Lat_redo1_%=:\n"
    "s_and_b32 s73, s94, 2\n"
    "s_cbranch_scc1 .Lat_ok1_%=\n"
    "s_or_b32 s94, s94, 2\n"
    "s_waitcnt vmcnt(0) lgkmcnt(0)\n"
    "s_mov_b32 s42, 0x80000\n"
    "s_mov_b32 s46, 0x80000\n"
    "v_mov_b32 v124, 0xff800000\n"
    "v_mov_b32 v13, 0xff800000\n"
    "v_mov_b32 v113, 0xff800000\n"
    "v_mov_b32 v14, 0xff800000\n"
    "s_lshl_b32 s93, s39, 15\n"
    "s_lshl_b32 s73, s71, 13\n"
    "s_add_u32 s93, s93, s73\n"
    "s_mov_b32 s91, 1\n"
    "s_nop 7\n"
    ".Lat_mx1_%=:\n"
    "buffer_load_dwordx4 a[64:67], v8, s[40:43], s93 offen offset:0 sc1\n"
    "buffer_load_dwordx4 a[68:71], v8, s[40:43], s93 offen offset:1024 sc1\n"
    "buffer_load_dwordx4 a[72:75], v8, s[40:43], s93 offen offset:2048 sc1\n"
    "buffer_load_dwordx4 a[76:79], v8, s[40:43], s93 offen offset:3072 sc1\n"
    "buffer_load_dwordx4 a[80:83], v120, s[40:43], s93 offen offset:0 sc1\n"
    "buffer_load_dwordx4 a[84:87], v120, s[40:43], s93 offen offset:1024 sc1\n"
    "buffer_load_dwordx4 a[88:91], v120, s[40:43], s93 offen offset:2048 sc1\n"
    "buffer_load_dwordx4 a[92:95], v120, s[40:43], s93 offen offset:3072 sc1\n"
    "s_waitcnt vmcnt(0)\n"
    "v_mfma_f32_16x16x32_bf16 v[16:19], a[64:67], a[192:195], 0\n"
    "v_mfma_f32_16x16x32_bf16 v[16:19], a[68:71], a[196:199], v[16:19]\n"
    "v_mfma_f32_16x16x32_bf16 v[16:19], a[72:75], a[200:203], v[16:19]\n"
    "v_mfma_f32_16x16x32_bf16 v[16:19], a[76:79], a[204:207], v[16:19]\n"
    "v_mfma_f32_16x16x32_bf16 v[20:23], a[80:83], a[192:195], 0\n"
    "v_mfma_f32_16x16x32_bf16 v[20:23], a[84:87], a[196:199], v[20:23]\n"
    "v_mfma_f32_16x16x32_bf16 v[20:23], a[88:91], a[200:203], v[20:23]\n"
    "v_mfma_f32_16x16x32_bf16 v[20:23], a[92:95], a[204:207], v[20:23]\n"
    "v_mfma_f32_16x16x32_bf16 v[24:27], a[64:67], a[208:211], 0\n"
    "v_mfma_f32_16x16x32_bf16 v[24:27], a[68:71], a[212:215], v[24:27]\n"
    "v_mfma_f32_16x16x32_bf16 v[24:27], a[72:75], a[216:219], v[24:27]\n"
    "v_mfma_f32_16x16x32_bf16 v[24:27], a[76:79], a[220:223], v[24:27]\n"
    "v_mfma_f32_16x16x32_bf16 v[28:31], a[80:83], a[208:211], 0\n"
    "v_mfma_f32_16x16x32_bf16 v[28:31], a[84:87], a[212:215], v[28:31]\n"
    "v_mfma_f32_16x16x32_bf16 v[28:31], a[88:91], a[216:219], v[28:31]\n"
    "v_mfma_f32_16x16x32_bf16 v[28:31], a[92:95], a[220:223], v[28:31]\n"
    "v_mfma_f32_16x16x32_bf16 v[32:35], a[64:67], a[224:227], 0\n"
    "v_mfma_f32_16x16x32_bf16 v[32:35], a[68:71], a[228:231], v[32:35]\n"
    "v_mfma_f32_16x16x32_bf16 v[32:35], a[72:75], a[232:235], v[32:35]\n"
    "v_mfma_f32_16x16x32_bf16 v[32:35], a[76:79], a[236:239], v[32:35]\n"
    "v_mfma_f32_16x16x32_bf16 v[36:39], a[80:83], a[224:227], 0\n"
    "v_mfma_f32_16x16x32_bf16 v[36:39], a[84:87], a[228:231], v[36:39]\n"
    "v_mfma_f32_16x16x32_bf16 v[36:39], a[88:91], a[232:235], v[36:39]\n"
    "v_mfma_f32_16x16x32_bf16 v[36:39], a[92:95], a[236:239], v[36:39]\n"
    "v_mfma_f32_16x16x32_bf16 v[40:43], a[64:67], a[240:243], 0\n"
    "v_mfma_f32_16x16x32_bf16 v[40:43], a[68:71], a[244:247], v[40:43]\n"
    "v_mfma_f32_16x16x32_bf16 v[40:43], a[72:75], a[248:251], v[40:43]\n"
    "v_mfma_f32_16x16x32_bf16 v[40:43], a[76:79], a[252:255], v[40:43]\n"
    "v_mfma_f32_16x16x32_bf16 v[44:47], a[80:83], a[240:243], 0\n"
    "v_mfma_f32_16x16x32_bf16 v[44:47], a[84:87], a[244:247], v[44:47]\n"
    "v_mfma_f32_16x16x32_bf16 v[44:47], a[88:91], a[248:251], v[44:47]\n"
    "v_mfma_f32_16x16x32_bf16 v[44:47], a[92:95], a[252:255], v[44:47]\n"
    "s_nop 7\n"
    "v_max3_f32 v112, v32, v33, v34\n"
    "v_max3_f32 v112, v112, v35, v36\n"
    "v_max3_f32 v112, v112, v37, v38\n"
    "v_max_f32 v112, v112, v39\n"
    "v_max_f32 v113, v113, v112\n"
    "v_max3_f32 v112, v40, v41, v42\n"
    "v_max3_f32 v112, v112, v43, v44\n"
    "v_max3_f32 v112, v112, v45, v46\n"
    "v_max_f32 v112, v112, v47\n"
    "v_max_f32 v14, v14, v112\n"
    "s_cmp_lg_u32 s91, 0\n"
    "s_cbranch_scc1 .Lat_my1_%=\n"
    "v_max3_f32 v112, v16, v17, v18\n"
    "v_max3_f32 v112, v112, v19, v20\n"
    "v_max3_f32 v112, v112, v21, v22\n"
    "v_max_f32 v112, v112, v23\n"
    "v_max_f32 v124, v124, v112\n"
    "v_max3_f32 v112, v24, v25, v26\n"
    "v_max3_f32 v112, v112, v27, v28\n"
    "v_max3_f32 v112, v112, v29, v30\n"
    "v_max_f32 v112, v112, v31\n"
    "v_max_f32 v13, v13, v112\n"
    ".Lat_my1_%=:\n"
    "s_mov_b32 s91, 0\n"
    "s_sub_u32 s93, s93, 0x4000\n"
    "s_cbranch_scc0 .Lat_mx1_%=\n"
    "s_nop 1\n"
    "v_permlane32_swap_b32 v124, v113\n"
    "v_permlane32_swap_b32 v13, v14\n"
    "v_max_f32 v124, v124, v113\n"
    "v_max_f32 v13, v13, v14\n"
    "s_nop 1\n"
    "v_permlane16_swap_b32 v124, v13\n"
    "v_max_f32 v125, v124, v13\n"
    "s_lshl_b32 s89, s39, 15\n"
    "s_add_u32 s89, s89, 0x4000\n"
    "s_mov_b32 s90, s89\n"
    "s_mov_b32 s81, 0x0\n"
    "s_mov_b32 s82, s81\n"
    "s_lshl_b32 s73, s71, 13\n"
    "s_add_u32 s73, s73, s69\n"
    "v_add_u32 v9, s73, v8\n"
    "s_barrier\n"
    "s_add_u32 m0, s86, s88\n"
    "s_nop 0\n"
    "buffer_load_dwordx4 v12, s[40:43], s89 offen offset:0 sc1 lds\n"
    "buffer_load_dwordx4 v12, s[40:43], s89 offen offset:1024 sc1 lds\n"
    "buffer_load_dwordx4 v12, s[40:43], s89 offen offset:2048 sc1 lds\n"
    "buffer_load_dwordx4 v12, s[40:43], s89 offen offset:3072 sc1 lds\n"
    "s_sub_u32 s89, s89, 0x4000\n"
    "s_cselect_b32 s89, s87, s89\n"
    "s_cselect_b32 s42, s81, s42\n"
    "s_cselect_b32 s81, 0, s81\n"
    "s_add_u32 s73, s86, 0x8000\n"
    "s_and_b32 s73, s73, 0x1ffff\n"
    "s_add_u32 m0, s73, s88\n"
    "s_nop 0\n"
    "buffer_load_dwordx4 v12, s[40:43], s89 offen offset:0 sc1 lds\n"
    "buffer_load_dwordx4 v12, s[40:43], s89 offen offset:1024 sc1 lds\n"
    "buffer_load_dwordx4 v12, s[40:43], s89 offen offset:2048 sc1 lds\n"
    "buffer_load_dwordx4 v12, s[40:43], s89 offen offset:3072 sc1 lds\n"
    "s_sub_u32 s89, s89, 0x4000\n"
    "s_cselect_b32 s89, s87, s89\n"
    "s_cselect_b32 s42, s81, s42\n"
    "s_cselect_b32 s81, 0, s81\n"
    "s_add_u32 s73, s86, 0x4000\n"
    "s_add_u32 m0, s73, s88\n"
    "s_nop 0\n"
    "buffer_load_dwordx4 v12, s[44:47], s90 offen offset:0 sc1 lds\n"
    "buffer_load_dwordx4 v12, s[44:47], s90 offen offset:1024 sc1 lds\n"
    "buffer_load_dwordx4 v12, s[44:47], s90 offen offset:2048 sc1 lds\n"
    "buffer_load_dwordx4 v12, s[44:47], s90 offen offset:3072 sc1 lds\n"
    "s_sub_u32 s90, s90, 0x4000\n"
    "s_cselect_b32 s90, s87, s90\n"
    "s_cselect_b32 s46, s82, s46\n"
    "s_cselect_b32 s82, 0, s82\n"
    "s_add_u32 s73, s86, 0x10000\n"
    "s_and_b32 s73, s73, 0x1ffff\n"
    "s_add_u32 m0, s73, s88\n"
    "s_nop 0\n"
    "buffer_load_dwordx4 v12, s[40:43], s89 offen offset:0 sc1 lds\n"
    "buffer_load_dwordx4 v12, s[40:43], s89 offen offset:1024 sc1 lds\n"
    "buffer_load_dwordx4 v12, s[40:43], s89 offen offset:2048 sc1 lds\n"
    "buffer_load_dwordx4 v12, s[40:43], s89 offen offset:3072 sc1 lds\n"
    "s_sub_u32 s89, s89, 0x4000\n"
    "s_cselect_b32 s89, s87, s89\n"
    "s_cselect_b32 s42, s81, s42\n"
    "s_cselect_b32 s81, 0, s81\n"
    "s_add_u32 s73, s86, 0x8000\n"
    "s_and_b32 s73, s73, 0x1ffff\n"
    "s_add_u32 s73, s73, 0x4000\n"
    "s_add_u32 m0, s73, s88\n"
    "s_nop 0\n"
    "buffer_load_dwordx4 v12, s[44:47], s90 offen offset:0 sc1 lds\n"
    "buffer_load_dwordx4 v12, s[44:47], s90 offen offset:1024 sc1 lds\n"
    "buffer_load_dwordx4 v12, s[44:47], s90 offen offset:2048 sc1 lds\n"
    "buffer_load_dwordx4 v12, s[44:47], s90 offen offset:3072 sc1 lds\n"
    "s_sub_u32 s90, s90, 0x4000\n"
    "s_cselect_b32 s90, s87, s90\n"
    "s_cselect_b32 s46, s82, s46\n"
    "s_cselect_b32 s82, 0, s82\n"
    "v_add_u32 v10, s86, v9\n"
    "v_accvgpr_read_b32 v48, a0\n"
    "v_accvgpr_read_b32 v49, a1\n"
    "v_accvgpr_read_b32 v50, a2\n"
    "v_accvgpr_read_b32 v51, a3\n"
    "v_accvgpr_read_b32 v52, a4\n"
    "v_accvgpr_read_b32 v53, a5\n"
    "v_accvgpr_read_b32 v54, a6\n"
    "v_accvgpr_read_b32 v55, a7\n"
    "v_accvgpr_read_b32 v56, a8\n"
    "v_accvgpr_read_b32 v57, a9\n"
    "v_accvgpr_read_b32 v58, a10\n"
    "v_accvgpr_read_b32 v59, a11\n"
    "v_accvgpr_read_b32 v60, a12\n"
    "v_accvgpr_read_b32 v61, a13\n"
    "v_accvgpr_read_b32 v62, a14\n"
    "v_accvgpr_read_b32 v63, a15\n"
    "v_accvgpr_read_b32 v16, a16\n"
    "v_accvgpr_read_b32 v17, a17\n"
    "v_accvgpr_read_b32 v18, a18\n"
    "v_accvgpr_read_b32 v19, a19\n"
    "v_accvgpr_read_b32 v20, a20\n"
    "v_accvgpr_read_b32 v21, a21\n"
    "v_accvgpr_read_b32 v22, a22\n"
    "v_accvgpr_read_b32 v23, a23\n"
    "v_accvgpr_read_b32 v24, a24\n"
    "v_accvgpr_read_b32 v25, a25\n"
    "v_accvgpr_read_b32 v26, a26\n"
    "v_accvgpr_read_b32 v27, a27\n"
    "v_accvgpr_read_b32 v28, a28\n"
    "v_accvgpr_read_b32 v29, a29\n"
    "v_accvgpr_read_b32 v30, a30\n"
    "v_accvgpr_read_b32 v31, a31\n"
    "s_waitcnt vmcnt(0)\n"
    "s_barrier\n"
    "ds_read_b128 a[64:67], v10 offset:0\n"
    "ds_read_b128 a[68:71], v10 offset:1024\n"
    "ds_read_b128 a[72:75], v10 offset:2048\n"
    "ds_read_b128 a[76:79], v10 offset:3072\n"
    "ds_read_b128 a[80:83], v10 offset:4096\n"
    "ds_read_b128 a[84:87], v10 offset:5120\n"
    "ds_read_b128 a[88:91], v10 offset:6144\n"
    "ds_read_b128 a[92:95], v10 offset:7168\n"
    "s_waitcnt lgkmcnt(0)\n"
    "v_add_u32 v10, s86, v9\n"
    "s_add_u32 s73, s86, 0x8000\n"
    "s_and_b32 s73, s73, 0x1ffff\n"
    "v_add_u32 v11, s73, v9\n"
    "v_mfma_f32_16x16x32_bf16 v[48:51], a[64:67], a[224:227], v[48:51]\n"
    "v_mfma_f32_16x16x32_bf16 v[48:51], a[68:71], a[228:231], v[48:51]\n"
    "v_mfma_f32_16x16x32_bf16 v[48:51], a[72:75], a[232:235], v[48:51]\n"
    "v_mfma_f32_16x16x32_bf16 v[48:51], a[76:79], a[236:239], v[48:51]\n"
    "v_mfma_f32_16x16x32_bf16 v[56:59], a[64:67], a[240:243], v[56:59]\n"
    "ds_read_b128 a[64:67], v11 offset:0\n"
    "v_mfma_f32_16x16x32_bf16 v[56:59], a[68:71], a[244:247], v[56:59]\n"
    "ds_read_b128 a[68:71], v11 offset:1024\n"
    "v_mfma_f32_16x16x32_bf16 v[56:59], a[72:75], a[248:251], v[56:59]\n"
    "ds_read_b128 a[72:75], v11 offset:2048\n"
    "v_mfma_f32_16x16x32_bf16 v[56:59], a[76:79], a[252:255], v[56:59]\n"
    "ds_read_b128 a[76:79], v11 offset:3072\n"
    "v_mfma_f32_16x16x32_bf16 v[52:55], a[80:83], a[224:227], v[52:55]\n"
    "v_mfma_f32_16x16x32_bf16 v[52:55], a[84:87], a[228:231], v[52:55]\n"
    "v_mfma_f32_16x16x32_bf16 v[52:55], a[88:91], a[232:235], v[52:55]\n"
    "v_mfma_f32_16x16x32_bf16 v[52:55], a[92:95], a[236:239], v[52:55]\n"
    "v_mfma_f32_16x16x32_bf16 v[60:63], a[80:83], a[240:243], v[60:63]\n"
    "ds_read_b128 a[80:83], v11 offset:4096\n"
    "v_mfma_f32_16x16x32_bf16 v[60:63], a[84:87], a[244:247], v[60:63]\n"
    "ds_read_b128 a[84:87], v11 offset:5120\n"
    "v_mfma_f32_16x16x32_bf16 v[60:63], a[88:91], a[248:251], v[60:63]\n"
    "ds_read_b128 a[88:91], v11 offset:6144\n"
    "v_mfma_f32_16x16x32_bf16 v[60:63], a[92:95], a[252:255], v[60:63]\n"
    "ds_read_b128 a[92:95], v11 offset:7168\n"
    "s_waitcnt lgkmcnt(7)\n"
    "v_mfma_f32_16x16x32_bf16 v[16:19], a[64:67], a[192:195], v[16:19]\n"
    "s_waitcnt lgkmcnt(6)\n"
    "v_mfma_f32_16x16x32_bf16 v[16:19], a[68:71], a[196:199], v[16:19]\n"
    "s_waitcnt lgkmcnt(5)\n"
    "v_mfma_f32_16x16x32_bf16 v[16:19], a[72:75], a[200:203], v[16:19]\n"
    "s_waitcnt lgkmcnt(4)\n"
    "v_mfma_f32_16x16x32_bf16 v[16:19], a[76:79], a[204:207], v[16:19]\n"
    "v_mfma_f32_16x16x32_bf16 v[24:27], a[64:67], a[208:211], v[24:27]\n"
    "s_nop 3\n"
    "v_mfma_f32_16x16x32_bf16 v[24:27], a[68:71], a[212:215], v[24:27]\n"
    "v_mfma_f32_16x16x32_bf16 v[24:27], a[72:75], a[216:219], v[24:27]\n"
    "v_mfma_f32_16x16x32_bf16 v[24:27], a[76:79], a[220:223], v[24:27]\n"
    "s_waitcnt lgkmcnt(3)\n"
    "v_mfma_f32_16x16x32_bf16 v[20:23], a[80:83], a[192:195], v[20:23]\n"
    "v_max3_f32 v113, v48, v49, v50\n"
    "s_waitcnt lgkmcnt(2)\n"
    "v_mfma_f32_16x16x32_bf16 v[20:23], a[84:87], a[196:199], v[20:23]\n"
    "v_max3_f32 v113, v113, v51, v52\n"
    "s_waitcnt lgkmcnt(1)\n"
    "v_mfma_f32_16x16x32_bf16 v[20:23], a[88:91], a[200:203], v[20:23]\n"
    "v_max3_f32 v113, v113, v53, v54\n"
    "s_waitcnt lgkmcnt(0)\n"
    "v_mfma_f32_16x16x32_bf16 v[20:23], a[92:95], a[204:207], v[20:23]\n"
    "v_max_f32 v113, v113, v55\n"
    "v_mfma_f32_16x16x32_bf16 v[28:31], a[80:83], a[208:211], v[28:31]\n"
    "v_max3_f32 v14, v56, v57, v58\n"
    "v_mfma_f32_16x16x32_bf16 v[28:31], a[84:87], a[212:215], v[28:31]\n"
    "v_max3_f32 v14, v14, v59, v60\n"
    "v_mfma_f32_16x16x32_bf16 v[28:31], a[88:91], a[216:219], v[28:31]\n"
    "v_max3_f32 v14, v14, v61, v62\n"
    "v_mfma_f32_16x16x32_bf16 v[28:31], a[92:95], a[220:223], v[28:31]\n"
    "v_max_f32 v14, v14, v63\n"
    "v_mfma_f32_16x16x32_bf16 v[32:35], a[64:67], a[224:227], 0\n"
    "v_mfma_f32_16x16x32_bf16 v[32:35], a[68:71], a[228:231], v[32:35]\n"
    "v_mfma_f32_16x16x32_bf16 v[32:35], a[72:75], a[232:235], v[32:35]\n"
    "v_mfma_f32_16x16x32_bf16 v[32:35], a[76:79], a[236:239], v[32:35]\n"
    "s_nop 3\n"
    "v_mfma_f32_16x16x32_bf16 v[36:39], a[80:83], a[224:227], 0\n"
    "v_max3_f32 v124, v16, v17, v18\n"
    "v_max3_f32 v124, v124, v19, v20\n"
    "v_mfma_f32_16x16x32_bf16 v[36:39], a[84:87], a[228:231], v[36:39]\n"
    "v_max3_f32 v124, v124, v21, v22\n"
    "v_max_f32 v124, v124, v23\n"
    "v_mfma_f32_16x16x32_bf16 v[36:39], a[88:91], a[232:235], v[36:39]\n"
    "v_max3_f32 v13, v24, v25, v26\n"
    "v_max3_f32 v13, v13, v27, v28\n"
    "v_mfma_f32_16x16x32_bf16 v[36:39], a[92:95], a[236:239], v[36:39]\n"
    "v_max3_f32 v13, v13, v29, v30\n"
    "v_max_f32 v13, v13, v31\n"
    "v_mfma_f32_16x16x32_bf16 v[40:43], a[64:67], a[240:243], 0\n"
    "s_nop 1\n"
    "v_permlane32_swap_b32 v124, v113\n"
    "v_permlane32_swap_b32 v13, v14\n"
    "v_mfma_f32_16x16x32_bf16 v[40:43], a[68:71], a[244:247], v[40:43]\n"
    "v_max_f32 v124, v124, v113\n"
    "v_max_f32 v13, v13, v14\n"
    "v_mfma_f32_16x16x32_bf16 v[40:43], a[72:75], a[248:251], v[40:43]\n"
    "s_nop 1\n"
    "v_permlane16_swap_b32 v124, v13\n"
    "v_mfma_f32_16x16x32_bf16 v[40:43], a[76:79], a[252:255], v[40:43]\n"
    "v_max3_f32 v124, v124, v13, v125\n"
    "ds_write_b32 v122, v124 offset:1024\n"
    "v_mfma_f32_16x16x32_bf16 v[44:47], a[80:83], a[240:243], 0\n"
    "v_mfma_f32_16x16x32_bf16 v[44:47], a[84:87], a[244:247], v[44:47]\n"
    "s_waitcnt vmcnt(0) lgkmcnt(0)\n"
    "s_barrier\n"
    "ds_read_b32 v113, v123 offset:1024\n"
    "v_mfma_f32_16x16x32_bf16 v[44:47], a[88:91], a[248:251], v[44:47]\n"
    "v_mfma_f32_16x16x32_bf16 v[44:47], a[92:95], a[252:255], v[44:47]\n"
    "ds_read_b128 a[96:99], v10 offset:16384\n"
    "ds_read_b128 a[100:103], v10 offset:17408\n"
    "ds_read_b128 a[104:107], v10 offset:18432\n"
    "ds_read_b128 a[108:111], v10 offset:19456\n"
    "ds_read_b128 a[112:115], v10 offset:20480\n"
    "ds_read_b128 a[116:119], v10 offset:21504\n"
    "ds_read_b128 a[120:123], v10 offset:22528\n"
    "ds_read_b128 a[124:127], v10 offset:23552\n"
    "s_add_u32 s73, s86, 0x18000\n"
    "s_and_b32 s73, s73, 0x1ffff\n"
    "s_add_u32 m0, s73, s88\n"
    "s_nop 0\n"
    "buffer_load_dwordx4 v12, s[40:43], s89 offen offset:0 sc1 lds\n"
    "buffer_load_dwordx4 v12, s[40:43], s89 offen offset:1024 sc1 lds\n"
    "buffer_load_dwordx4 v12, s[40:43], s89 offen offset:2048 sc1 lds\n"
    "buffer_load_dwordx4 v12, s[40:43], s89 offen offset:3072 sc1 lds\n"
    "s_sub_u32 s89, s89, 0x4000\n"
    "s_cselect_b32 s89, s87, s89\n"
    "s_cselect_b32 s42, s81, s42\n"
    "s_cselect_b32 s81, 0, s81\n"
    "s_waitcnt lgkmcnt(8)\n"
    "v_max_f32 v124, v124, v113\n"
    "v_mov_b32 v13, v124\n"
    "s_nop 1\n"
    "v_permlane16_swap_b32 v124, v13\n"
    "v_mov_b32 v113, v124\n"
    "v_mov_b32 v14, v13\n"
    "s_nop 0\n"
    "v_permlane32_swap_b32 v124, v113\n"
    "v_permlane32_swap_b32 v13, v14\n"
    "s_add_u32 s73, s86, 0x10000\n"
    "s_and_b32 s73, s73, 0x1ffff\n"
    "v_add_u32 v10, s73, v9\n"
    "v_sub_f32 v48, v48, v113\n"
    "ds_read_b128 a[64:67], v10 offset:0\n"
    "v_sub_f32 v49, v49, v113\n"
    "v_sub_f32 v50, v50, v113\n"
    "ds_read_b128 a[68:71], v10 offset:1024\n"
    "v_sub_f32 v51, v51, v113\n"
    "v_sub_f32 v52, v52, v113\n"
    "ds_read_b128 a[72:75], v10 offset:2048\n"
    "v_sub_f32 v53, v53, v113\n"
    "v_sub_f32 v54, v54, v113\n"
    "ds_read_b128 a[76:79], v10 offset:3072\n"
    "v_sub_f32 v55, v55, v113\n"
    "v_exp_f32 v48, v48\n"
    "ds_read_b128 a[80:83], v10 offset:4096\n"
    "v_exp_f32 v49, v49\n"
    "v_exp_f32 v50, v50\n"
    "ds_read_b128 a[84:87], v10 offset:5120\n"
    "v_exp_f32 v51, v51\n"
    "v_exp_f32 v52, v52\n"
    "ds_read_b128 a[88:91], v10 offset:6144\n"
    "v_exp_f32 v53, v53\n"
    "v_exp_f32 v54, v54\n"
    "ds_read_b128 a[92:95], v10 offset:7168\n"
    "v_exp_f32 v55, v55\n"
    "v_cvt_pk_bf16_f32 v104, v48, v49\n"
    "v_cvt_pk_bf16_f32 v105, v50, v51\n"
    "v_cvt_pk_bf16_f32 v106, v52, v53\n"
    "v_cvt_pk_bf16_f32 v107, v54, v55\n"
    "s_nop 1\n"
    "s_or_b32 s94, s94, 1\n"
    "s_sub_u32 s92, s85, 0x5000\n"
    "s_lshl_b32 s92, s92, 10\n"
    "s_branch .Lat_prob1_%=\n"
    ".Lcode1_%=:\n"
    :
    : "s"(x), "s"(c), "s"(wq), "s"(wuk), "s"(wuv), "s"(wo), "s"(wg), "s"(wu), "s"(wd), "s"(ws), "s"(out), "s"(head), "s"(xq), "s"(wid), "s"(lds0)
    : "v0","v1","v2","v3","v4","v5","v6","v7","v8","v9","v10","v11","v12","v13","v14","v15","v16","v17","v18","v19","v20","v21","v22","v23","v24","v25",
      "v26","v27","v28","v29","v30","v31","v32","v33","v34","v35","v36","v37","v38","v39","v40","v41","v42","v43","v44","v45","v46","v47","v48","v49","v50",
      "v51","v52","v53","v54","v55","v56","v57","v58","v59","v60","v61","v62","v63","v64","v65","v66","v67","v68","v69","v70","v71","v72","v73","v74","v75",
      "v76","v77","v78","v79","v80","v81","v82","v83","v84","v85","v86","v87","v88","v89","v90","v91","v92","v93","v94","v95","v96","v97","v98","v99","v100",
      "v101","v102","v103","v104","v105","v106","v107","v108","v109","v110","v111","v112","v113","v114","v115","v116","v117","v118","v119","v120","v121",
      "v122","v123","v124","v125","v126","v127","v128","v129","v130","v131","v132","v133","v134","v135","v136","v137","v138","v139","v140","v141","v142",
      "v143","v144","v145","v146","v147","v148","v149","v150","v151","v152","v153","v154","v155","v156","v157","v158","v159","v160","v161","v162","v163",
      "v164","v165","v166","v167","v168","v169","v170","v171","v172","v173","v174","v175","v176","v177","v178","v179","v180","v181","v182","v183","v184",
      "v185","v186","v187","v188","v189","v190","v191","v192","v193","v194","v195","v196","v197","v198","v199","v200","v201","v202","v203","v204","v205",
      "v206","v207","v208","v209","v210","v211","v212","v213","v214","v215","v216","v217","v218","v219","v220","v221","v222","v223","v224","v225","v226",
      "v227","v228","v229","v230","v231","v232","v233","v234","v235","v236","v237","v238","v239","v240","v241","v242","v243","v244","v245","v246","v247",
      "v248","v249","v250","v251","v252","v253","v254","v255","a0","a1","a2","a3","a4","a5","a6","a7","a8","a9","a10","a11","a12","a13","a14","a15","a16",
      "a17","a18","a19","a20","a21","a22","a23","a24","a25","a26","a27","a28","a29","a30","a31","a32","a33","a34","a35","a36","a37","a38","a39","a40","a41",
      "a42","a43","a44","a45","a46","a47","a48","a49","a50","a51","a52","a53","a54","a55","a56","a57","a58","a59","a60","a61","a62","a63","a64","a65","a66",
      "a67","a68","a69","a70","a71","a72","a73","a74","a75","a76","a77","a78","a79","a80","a81","a82","a83","a84","a85","a86","a87","a88","a89","a90","a91",
      "a92","a93","a94","a95","a96","a97","a98","a99","a100","a101","a102","a103","a104","a105","a106","a107","a108","a109","a110","a111","a112","a113",
      "a114","a115","a116","a117","a118","a119","a120","a121","a122","a123","a124","a125","a126","a127","a128","a129","a130","a131","a132","a133","a134",
      "a135","a136","a137","a138","a139","a140","a141","a142","a143","a144","a145","a146","a147","a148","a149","a150","a151","a152","a153","a154","a155",
      "a156","a157","a158","a159","a160","a161","a162","a163","a164","a165","a166","a167","a168","a169","a170","a171","a172","a173","a174","a175","a176",
      "a177","a178","a179","a180","a181","a182","a183","a184","a185","a186","a187","a188","a189","a190","a191","a192","a193","a194","a195","a196","a197",
      "a198","a199","a200","a201","a202","a203","a204","a205","a206","a207","a208","a209","a210","a211","a212","a213","a214","a215","a216","a217","a218",
      "a219","a220","a221","a222","a223","a224","a225","a226","a227","a228","a229","a230","a231","a232","a233","a234","a235","a236","a237","a238","a239",
      "a240","a241","a242","a243","a244","a245","a246","a247","a248","a249","a250","a251","a252","a253","a254","a255","s36","s37","s38","s39","s40","s41",
      "s42","s43","s44","s45","s46","s47","s48","s49","s50","s51","s52","s53","s54","s55","s56","s57","s58","s59","s60","s61","s62","s63","s64","s65","s66",
      "s67","s68","s69","s70","s71","s72","s73","s74","s75","s76","s77","s78","s79","s80","s81","s82","s83","s84","s85","s86","s87","s88","s89","s90","s91",
      "s92","s93","s94","s95","s96","s97","s98","s99","vcc","scc","memory");
}

extern "C" void kernel_launch(void* const* d_in, const int* in_sizes, int n_in, void* d_out, int out_size, void* d_ws,
                              size_t ws_size, hipStream_t stream) {
  const u16* x = (const u16*)d_in[0];
  const u16* c = (const u16*)d_in[1];
  const u16* wq = (const u16*)d_in[2];
  const u16* wuk = (const u16*)d_in[3];
  const u16* wuv = (const u16*)d_in[4];
  const u16* wo = (const u16*)d_in[5];
  const u16* wg = (const u16*)d_in[7];
  const u16* wu = (const u16*)d_in[8];
  const u16* wd = (const u16*)d_in[9];
  char* ws = (char*)d_ws;
  if (n_in != 10 || out_size != 2048 * 512 || ws_size < WS_END) return;
  (void)hipFuncSetAttribute((const void*)k_mega, hipFuncAttributeMaxDynamicSharedMemorySize, 163840);
  k_mega<<<256, 256, 163840, stream>>>(x, c, wq, wuk, wuv, wo, wg, wu, wd, ws, (u16*)d_out);
}
```
